```python
import torch

def _softmax(x, axis=-1):
    return torch.softmax(x, dim=axis)
def _gelu(x):
    return torch.nn.functional.gelu(x, approximate="tanh")
def _silu(x):
    return torch.nn.functional.silu(x)
def _sigmoid(x):
    return torch.sigmoid(x)
def _layernorm(x, axis=-1, eps=1e-5):
    m = x.mean(dim=axis, keepdim=True)
    v = x.var(dim=axis, keepdim=True, unbiased=False)
    return (x - m) / torch.sqrt(v + eps)
def _scatter_add(tgt, idx, src, axis=0):
    return tgt.clone().index_add_(axis, idx, src)
def _segment_sum(src, seg, n):
    out = torch.zeros((n,) + src.shape[1:], dtype=src.dtype)
    return out.index_add_(0, seg, src)
def _pad(x, pw):
    flat = tuple(v for pair in reversed(pw) for v in pair)
    return torch.nn.functional.pad(x, flat)

kernel_name = 'gated_mix__linear_attn_block__mha_block__prompt'

def setup_inputs():

    _w = (lambda a: (lambda t: t.to(torch.bfloat16) if t.is_floating_point() else t)(torch.as_tensor(a)))
    def _A_setup():
        B, S, D = 1, 2048, 640
        return {
            "x": _w((lambda *s: torch.randn(*s, dtype=torch.bfloat16) * 0.05)(B, S, D)),
            "wqkv": _w((lambda *s: torch.randn(*s, dtype=torch.bfloat16) * 0.8)(D, 3 * D)),
            "wo": _w((lambda *s: torch.randn(*s, dtype=torch.bfloat16) * 0.2)(D, D)),
        }
    _A_in = _A_setup()

    def _B_setup():
        B, S, D, H = 1, 2048, 640, 32
        return {
            "x": _w((lambda *s: torch.randn(*s, dtype=torch.bfloat16) * 0.05)(B, S, D)),
            "wqkv": _w((lambda *s: torch.randn(*s, dtype=torch.bfloat16) * 0.8)(D, 3 * D)),
            "wo": _w((lambda *s: torch.randn(*s, dtype=torch.bfloat16) * 0.05)(D, D)),
        }
    _B_in = _B_setup()

    return {
        "A_x": _A_in["x"],
        "A_wqkv": _A_in["wqkv"],
        "A_wo": _A_in["wo"],
        "B_x": _B_in["x"],
        "B_wqkv": _B_in["wqkv"],
        "B_wo": _B_in["wo"],
        "gate_w": _w((lambda *s: torch.randn(*s, dtype=torch.bfloat16) * 0.05)(640) * 16.0),
    }

def reference(A_x, A_wqkv, A_wo, B_x, B_wqkv, B_wo, gate_w):
    def _A_ref(x, wqkv, wo):
        B, S, D = x.shape
        H = 32; Dh = D // H
        qkv = (lambda x, *d: x.permute(*d))((x @ wqkv).reshape(B, S, 3, H, Dh), 2, 0, 3, 1, 4)
        q, k, v = qkv[0], qkv[1], qkv[2]
        phi = lambda t: torch.where(t > 0, t + 1, torch.exp(t)) + 1e-6
        pq, pk = phi(q), phi(k)
        kv = torch.einsum("bhsd,bhse->bhde", pk, v)
        z = torch.einsum("bhsd->bhd", pk)
        num = torch.einsum("bhqd,bhde->bhqe", pq, kv)
        den = torch.einsum("bhqd,bhd->bhq", pq, z)[..., None] + 1e-6
        o = num / den
        return (lambda x, *d: x.permute(*d))(o, 0, 2, 1, 3).reshape(B, S, D) @ wo

    def _B_ref(x, wqkv, wo):
        B, S, D = x.shape
        H = 32
        Dh = D // H
        qkv = (lambda x, *d: x.permute(*d))((x @ wqkv).reshape(B, S, 3, H, Dh), 2, 0, 3, 1, 4)
        q, k, v = qkv[0], qkv[1], qkv[2]
        scores = torch.einsum("bhqd,bhkd->bhqk", q, k) * (Dh ** -0.5)
        p = _softmax(scores, axis=-1)
        o = torch.einsum("bhqk,bhkd->bhqd", p, v)
        return (lambda x, *d: x.permute(*d))(o, 0, 2, 1, 3).reshape(B, S, D) @ wo

    x = A_x
    g = _sigmoid((x * gate_w).sum(-1, keepdims=True))
    a = _A_ref(x, A_wqkv, A_wo)
    b = _B_ref(x, B_wqkv, B_wo)
    return g * a + (1 - g) * b
```

```cpp
#include <hip/hip_runtime.h>
#include <hip/hip_bf16.h>
#include <stdint.h>

typedef short    bf16x8 __attribute__((ext_vector_type(8)));
typedef short    s16x4  __attribute__((ext_vector_type(4)));
typedef float    f32x2  __attribute__((ext_vector_type(2)));
typedef float    f32x4  __attribute__((ext_vector_type(4)));
typedef float    f32x16 __attribute__((ext_vector_type(16)));
typedef unsigned u32x4  __attribute__((ext_vector_type(4)));
typedef unsigned u32x2  __attribute__((ext_vector_type(2)));
typedef __bf16   bf2_t  __attribute__((ext_vector_type(2)));
typedef unsigned short u16;
#define LAS __attribute__((address_space(3)))
#define GAS __attribute__((address_space(1)))
#define DI __device__ __forceinline__
#define SBAR() __builtin_amdgcn_sched_barrier(0)

constexpr int S_ = 2048, D_ = 640, H_ = 32, DH = 20;
constexpr size_t OFF_QB  = 0;
constexpr size_t OFF_PQA = OFF_QB + (size_t)H_ * S_ * DH * 2;
constexpr size_t OFF_KVB = OFF_PQA + (size_t)H_ * S_ * DH * 2;
constexpr size_t OFF_KVA = OFF_KVB + (size_t)H_ * 163840;
constexpr size_t OFF_O   = OFF_KVA + (size_t)H_ * 163840;
constexpr size_t OFF_WOT = OFF_O + (size_t)128 * 40 * 1024;
constexpr size_t OFF_G   = OFF_WOT + (size_t)40 * 40 * 1024;

DI unsigned pk2(float lo, float hi) { f32x2 v = {lo, hi}; return __builtin_bit_cast(unsigned, __builtin_convertvector(v, bf2_t)); }
DI float bf2f(u16 v) { return __uint_as_float((unsigned)v << 16); }
DI int rfl(int x) { return __builtin_amdgcn_readfirstlane(x); }
DI void glds16(const void* g, unsigned lds_base) {
    unsigned sv;
    asm volatile("s_mov_b32 %0, m0\n\ts_mov_b32 m0, %2\n\ts_nop 0\n\tglobal_load_lds_dwordx4 %1, off\n\ts_mov_b32 m0, %0"
                 : "=&s"(sv) : "v"(g), "s"(lds_base) : "memory");
}
#define MFMA16(a, b, c) __builtin_amdgcn_mfma_f32_16x16x32_bf16((a), (b), (c), 0, 0, 0)
#define MFMA32(a, b, c) __builtin_amdgcn_mfma_f32_32x32x16_bf16((a), (b), (c), 0, 0, 0)
DI float phi(float t) { return (t > 0.f ? t + 1.f : __expf(t)) + 1e-6f; }

constexpr int K1_STAGE = 49152;
constexpr int K1_LDS = 3 * K1_STAGE;

template <bool SWAP>
DI void k1_main(const u16* __restrict__ x, const u16* __restrict__ W, int m0, int col0, char* lds, f32x4 (&acc)[4][4]) {
    const int tid = threadIdx.x, lane = tid & 63, wave = rfl(tid >> 6);
    const int wm = wave & 3, wn = wave >> 2;
    const unsigned lds0 = (unsigned)(uintptr_t)lds;
    const u16* xs[4]; const u16* wsrc[2];
#pragma unroll
    for (int i = 0; i < 4; ++i) { const int c = i * 512 + tid, row = c >> 3, sl = c & 7; xs[i] = x + (size_t)(m0 + row) * D_ + ((sl ^ ((row >> 1) & 7)) << 3); }
#pragma unroll
    for (int i = 0; i < 2; ++i) { const int c = i * 512 + tid, k = c >> 4, s16 = c & 15; const int f = (k & 3) | (((k >> 3) & 1) << 2);
        wsrc[i] = W + (size_t)k * 1920 + col0 + (((((s16 >> 1) ^ f) << 1) | (s16 & 1)) << 3); }
    auto stage = [&](int kt, int slot) {
#pragma unroll
        for (int i = 0; i < 4; ++i) glds16(xs[i] + kt * 64, lds0 + slot * K1_STAGE + (i * 512 + wave * 64) * 16);
#pragma unroll
        for (int i = 0; i < 2; ++i) glds16(wsrc[i] + (size_t)kt * 64 * 1920, lds0 + slot * K1_STAGE + 32768 + (i * 512 + wave * 64) * 16);
    };
    const int fr = lane & 15, g = lane >> 4, q = (lane >> 2) & 3, p = lane & 3;
    const LAS char* la[2]; const LAS char* lb[4];
#pragma unroll
    for (int ks = 0; ks < 2; ++ks) la[ks] = (const LAS char*)lds + (wm * 64 + fr) * 128 + (((ks * 4 + g) ^ (fr >> 1)) << 4);
    { const int f = q | ((g & 1) << 2);
#pragma unroll
      for (int ni = 0; ni < 4; ++ni) lb[ni] = (const LAS char*)lds + 32768 + (8 * g + q) * 256 + (((wn * 4 + ni) ^ f) << 5) + p * 8; }
    stage(0, 0); stage(1, 1);
#pragma unroll
    for (int kt = 0; kt < 10; ++kt) {
        if (kt < 9) asm volatile("s_waitcnt vmcnt(6) lgkmcnt(0)\n\ts_barrier" ::: "memory");
        else        asm volatile("s_waitcnt vmcnt(0) lgkmcnt(0)\n\ts_barrier" ::: "memory");
        if (kt + 2 < 10) stage(kt + 2, (kt + 2) % 3);
        const int sb = (kt % 3) * K1_STAGE;
#pragma unroll
        for (int ks = 0; ks < 2; ++ks) {
            bf16x8 af[4], bfr[4];
#pragma unroll
            for (int mi = 0; mi < 4; ++mi) af[mi] = *(const LAS bf16x8*)(la[ks] + sb + mi * 2048);
#pragma unroll
            for (int ni = 0; ni < 4; ++ni) {
                s16x4 lo = __builtin_amdgcn_ds_read_tr16_b64_v4i16((LAS s16x4*)(lb[ni] + sb + ks * 8192));
                s16x4 hi = __builtin_amdgcn_ds_read_tr16_b64_v4i16((LAS s16x4*)(lb[ni] + sb + ks * 8192 + 1024));
                bfr[ni] = __builtin_shufflevector(lo, hi, 0, 1, 2, 3, 4, 5, 6, 7);
            }
#pragma unroll
            for (int mi = 0; mi < 4; ++mi)
#pragma unroll
                for (int ni = 0; ni < 4; ++ni)
                    acc[mi][ni] = SWAP ? MFMA16(bfr[ni], af[mi], acc[mi][ni]) : MFMA16(af[mi], bfr[ni], acc[mi][ni]);
        }
    }
}

__global__ __launch_bounds__(512, 2) void k1_qkv(const u16* __restrict__ x, const u16* __restrict__ wA, const u16* __restrict__ wB,
                                                 const u16* __restrict__ woA, const u16* __restrict__ woB, const u16* __restrict__ gate_w,
                                                 char* __restrict__ ws) {
    extern __shared__ __attribute__((aligned(16))) char lds[];
    const int bid = blockIdx.x, tid = threadIdx.x, lane = tid & 63, wave = rfl(tid >> 6);
    if (bid >= 240) {
        const int aw = (bid - 240) * 8 + wave;
        unsigned gw[5];
#pragma unroll
        for (int i = 0; i < 5; ++i) gw[i] = ((const unsigned*)gate_w)[lane + 64 * i];
        for (int rr = 0; rr < 16; ++rr) {
            const int row = aw * 16 + rr; float s = 0.f;
#pragma unroll
            for (int i = 0; i < 5; ++i) { const unsigned xv = ((const unsigned*)(x + (size_t)row * D_))[lane + 64 * i];
                s += __uint_as_float(xv << 16) * __uint_as_float(gw[i] << 16) + __uint_as_float(xv & 0xffff0000u) * __uint_as_float(gw[i] & 0xffff0000u); }
#pragma unroll
            for (int o = 32; o > 0; o >>= 1) s += __shfl_xor(s, o);
            if (lane == 0) ((float*)(ws + OFF_G))[row] = 1.f / (1.f + __expf(-s));
        }
        for (int b = aw; b < 1600; b += 128) {
            const int kst = b / 40, nt = b % 40;
            const int k0 = kst * 32 + (lane >> 4) * 8, n = nt * 16 + (lane & 15);
            const u16* src = (k0 < 640 ? woA + (size_t)k0 * D_ : woB + (size_t)(k0 - 640) * D_) + n;
            u16 v[8];
#pragma unroll
            for (int j = 0; j < 8; ++j) v[j] = src[(size_t)j * D_];
            u32x4 o = {v[0] | ((unsigned)v[1] << 16), v[2] | ((unsigned)v[3] << 16), v[4] | ((unsigned)v[5] << 16), v[6] | ((unsigned)v[7] << 16)};
            *(u32x4*)(ws + OFF_WOT + (size_t)b * 1024 + lane * 16) = o;
        }
        return;
    }
    const int xcd = bid & 7, idx = (xcd >> 1) * 30 + (bid >> 3);
    const int nt = idx >> 2, mt = (xcd & 1) * 4 + (idx & 3);
    const int brB = nt >= 15, ct = brB ? nt - 15 : nt, type = ct / 5;
    const int m0 = mt * 256, col0 = ct * 128;
    const u16* W = brB ? wB : wA;
    const bool swap = (type == 0) || (type == 1 && brB);
    f32x4 acc[4][4];
#pragma unroll
    for (int i = 0; i < 4; ++i)
#pragma unroll
        for (int j = 0; j < 4; ++j) acc[i][j] = f32x4{0.f, 0.f, 0.f, 0.f};
    const int wm = wave & 3, wn = wave >> 2, fr = lane & 15, g = lane >> 4;
    const int cseg = col0 - type * 640 + wn * 64;
    if (swap) {
        k1_main<true>(x, W, m0, col0, lds, acc);
        constexpr float QS = 0.22360679774997896f * 1.4426950408889634f;
#pragma unroll
        for (int ni = 0; ni < 4; ++ni) {
            const int c = cseg + ni * 16 + g * 4, h = c / 20, d = c - h * 20;
#pragma unroll
            for (int mi = 0; mi < 4; ++mi) {
                const int s = m0 + wm * 64 + mi * 16 + fr;
                f32x4 v = acc[mi][ni];
                if (type == 0) {
                    if (brB) v *= QS; else { v[0] = phi(v[0]); v[1] = phi(v[1]); v[2] = phi(v[2]); v[3] = phi(v[3]); }
                    u32x2 o = {pk2(v[0], v[1]), pk2(v[2], v[3])};
                    *(u32x2*)(ws + (brB ? OFF_QB : OFF_PQA) + ((size_t)(h * S_ + s) * DH + d) * 2) = o;
                } else {
                    char* tb = ws + OFF_KVB + (size_t)h * 163840 + (s >> 5) * 2560; const int key = s & 31;
                    u32x2 o = {pk2(v[0], v[1]), pk2(v[2], v[3])};
                    if (d < 16) *(u32x2*)(tb + (d >> 3) * 512 + key * 16 + (d & 7) * 2) = o;
                    else { *(unsigned*)(tb + 1024 + key * 4) = o[0]; *(unsigned*)(tb + 1152 + key * 4) = o[1]; }
                }
            }
        }
    } else {
        k1_main<false>(x, W, m0, col0, lds, acc);
        char* base = ws + (brB ? OFF_KVB + 1280 : (type == 1 ? OFF_KVA : OFF_KVA + 1280));
#pragma unroll
        for (int ni = 0; ni < 4; ++ni) {
            const int c = cseg + ni * 16 + fr, h = c / 20, d = c - h * 20;
#pragma unroll
            for (int mi = 0; mi < 4; ++mi) {
                const int s = m0 + wm * 64 + mi * 16;
                f32x4 v = acc[mi][ni];
                if (type == 1) { v[0] = phi(v[0]); v[1] = phi(v[1]); v[2] = phi(v[2]); v[3] = phi(v[3]); }
                u32x2 o = {pk2(v[0], v[1]), pk2(v[2], v[3])};
                *(u32x2*)(base + (size_t)h * 163840 + (s >> 5) * 2560 + (((s >> 4) & 1) * 2 + (g & 1)) * 320 + d * 16 + (g >> 1) * 8) = o;
            }
        }
    }
}

constexpr int K2_LDS = 163840;

DI void store_o(char* ws, const f32x16& o, float f, int s, int cb, int hh) {
    char* rb = ws + OFF_O + (size_t)(s >> 4) * 40960 + (s & 15) * 16;
#pragma unroll
    for (int i = 0; i < 3; ++i) {
        if (i < 2 || hh == 0) {
            const int c = cb + 8 * i + 4 * hh;
            u32x2 v = {pk2(o[4 * i] * f, o[4 * i + 1] * f), pk2(o[4 * i + 2] * f, o[4 * i + 3] * f)};
            *(u32x2*)(rb + (c >> 3) * 256 + ((c >> 2) & 1) * 8) = v;
        }
    }
}
DI float xhalf_max(float m) { auto rr = __builtin_amdgcn_permlane32_swap(__float_as_uint(m), __float_as_uint(m), false, false);
    return fmaxf(__uint_as_float(rr[0]), __uint_as_float(rr[1])); }
DI float hi_half(float v) {
    auto rr = __builtin_amdgcn_permlane32_swap(__float_as_uint(v), __float_as_uint(v), false, false);
    return __uint_as_float(rr[1]); }

__global__ __launch_bounds__(512, 2) void k2_attn(char* __restrict__ ws) {
    extern __shared__ __attribute__((aligned(16))) char lds[];
    const int bid = blockIdx.x, tid = threadIdx.x, lane = tid & 63, wave = rfl(tid >> 6);
    const int h = (bid & 7) * 4 + ((bid >> 3) & 3), qb = bid >> 5;
    const int r = lane & 31, hh = lane >> 5;
    const unsigned lds0 = (unsigned)(uintptr_t)lds;
    const char* kvb = ws + OFF_KVB + (size_t)h * 163840;
    const char* kva = ws + OFF_KVA + (size_t)h * 163840;
    const int srow = qb * 256 + wave * 32 + r;
#pragma unroll
    for (int rd = 0; rd < 4; ++rd) glds16(kvb + (rd * 8 + wave) * 1024 + lane * 16, lds0 + (rd * 8 + wave) * 1024);
    const char* qp = ws + OFF_QB + (size_t)(h * S_ + srow) * 40;
    const u32x2 q0 = *(const u32x2*)(qp + 16 * hh), q1 = *(const u32x2*)(qp + 16 * hh + 8);
    const unsigned q2 = *(const unsigned*)(qp + 32 + 4 * hh);
    const float gate = ((const float*)(ws + OFF_G))[srow];
    asm volatile("s_waitcnt vmcnt(0)\n\ts_barrier" ::: "memory");
#pragma unroll
    for (int rd = 4; rd < 20; ++rd) glds16(kvb + (rd * 8 + wave) * 1024 + lane * 16, lds0 + (rd * 8 + wave) * 1024);
    const bf16x8 qm = __builtin_bit_cast(bf16x8, (u32x4){q0[0], q0[1], q1[0], q1[1]});
    const f32x16 Z16 = {};
    const LAS char* kmp = (const LAS char*)lds + hh * 512 + r * 16;
    const LAS char* ktp = (const LAS char*)lds + 1024 + hh * 128 + r * 4;
    const LAS char* vp  = (const LAS char*)lds + 1280 + hh * 320 + r * 16;
#define KM(off) (*(const LAS bf16x8*)(kmp + (off)))
#define KT(off) __builtin_bit_cast(bf16x8, (u32x4){*(const LAS unsigned*)(ktp + (off)), 0x3F80u, 0u, 0u})
    bf16x8 qt;
    {
        f32x16 s0 = MFMA32(KM(0), qm, Z16);
        s0 = MFMA32(KT(0), __builtin_bit_cast(bf16x8, (u32x4){q2, 0u, 0u, 0u}), s0);
        float m = s0[0];
#pragma unroll
        for (int i = 1; i < 16; ++i) m = fmaxf(m, s0[i]);
        m = xhalf_max(m);
        qt = __builtin_bit_cast(bf16x8, (u32x4){q2, hh == 0 ? (pk2(-m, 0.f) & 0xffffu) : 0u, 0u, 0u});
    }
    f32x16 O = {}, SA, SB, kvacc = {};
    const u32x4 ONES = {0x3F803F80u, 0x3F803F80u, 0x3F803F80u, 0x3F803F80u};
    u32x4 V0 = ONES, V1 = ONES, P0, P1 = {0u, 0u, 0u, 0u};
    bf16x8 km, kt;
    SA = MFMA32(KM(0), qm, Z16); SA = MFMA32(KT(0), qt, SA);
    km = KM(2560); kt = KT(2560);
    if (r < 20) V0 = *(const LAS u32x4*)(vp);
#define EX(x) __builtin_amdgcn_exp2f(x)
#define B8(x) __builtin_bit_cast(bf16x8, x)
#define STEP(SC, SN, T) do { \
        SN = MFMA32(km, qm, Z16); SBAR(); \
        { float e0 = EX(SC[0]), e1 = EX(SC[1]), e2 = EX(SC[2]), e3 = EX(SC[3]); P0[0] = pk2(e0, e1); P0[1] = pk2(e2, e3); } SBAR(); \
        O = MFMA32(B8(V1), B8(P1), O); \
        if (r < 20) V1 = *(const LAS u32x4*)(vp + (T) * 2560 + 640); \
        km = KM(((T) + 2) * 2560); SBAR(); \
        { float e0 = EX(SC[4]), e1 = EX(SC[5]), e2 = EX(SC[6]), e3 = EX(SC[7]); P0[2] = pk2(e0, e1); P0[3] = pk2(e2, e3); } SBAR(); \
        SN = MFMA32(kt, qt, SN); \
        kt = KT(((T) + 2) * 2560); SBAR(); \
        { float e0 = EX(SC[8]), e1 = EX(SC[9]), e2 = EX(SC[10]), e3 = EX(SC[11]); P1[0] = pk2(e0, e1); P1[1] = pk2(e2, e3); } SBAR(); \
        O = MFMA32(B8(V0), B8(P0), O); \
        if (r < 20) V0 = *(const LAS u32x4*)(vp + ((T) + 1) * 2560); SBAR(); \
        { float e0 = EX(SC[12]), e1 = EX(SC[13]), e2 = EX(SC[14]), e3 = EX(SC[15]); P1[2] = pk2(e0, e1); P1[3] = pk2(e2, e3); } SBAR(); \
    } while (0)
    const int rc = r < 20 ? r : 19;
    for (int c = 0; c < 8; ++c) {
        const char* ap = kva + (c * 8 + wave) * 2560 + hh * 320 + rc * 16;
        u32x4 a0 = *(const u32x4*)(ap), a1 = *(const u32x4*)(ap + 640), b0 = *(const u32x4*)(ap + 1280), b1 = *(const u32x4*)(ap + 1920);
        STEP(SA, SB, 0); STEP(SB, SA, 1); STEP(SA, SB, 2); STEP(SB, SA, 3);
        STEP(SA, SB, 4); STEP(SB, SA, 5);
        if (c == 0) asm volatile("s_waitcnt vmcnt(0)\n\ts_barrier" ::: "memory");
        STEP(SA, SB, 6); STEP(SB, SA, 7);
        kmp += 8 * 2560; ktp += 8 * 2560; vp += 8 * 2560;
        if (r >= 20) { a0 = u32x4{0u, 0u, 0u, 0u}; a1 = a0; b0 = ONES; b1 = ONES; }
        kvacc = MFMA32(B8(a0), B8(b0), kvacc); kvacc = MFMA32(B8(a1), B8(b1), kvacc);
    }
    O = MFMA32(B8(V1), B8(P1), O);
    if (r <= 20) {
        LAS f32x4* dst = (LAS f32x4*)(lds + ((wave * 2 + hh) * 21 + r) * 48);
        dst[0] = f32x4{kvacc[0], kvacc[1], kvacc[2], kvacc[3]};
        dst[1] = f32x4{kvacc[4], kvacc[5], kvacc[6], kvacc[7]};
        dst[2] = f32x4{kvacc[8], kvacc[9], kvacc[10], kvacc[11]};
    }
    const char* pqp = ws + OFF_PQA + (size_t)(h * S_ + srow) * 40;
    const u32x2 pq0 = *(const u32x2*)(pqp + 8 * hh), pq1 = *(const u32x2*)(pqp + 16 + 8 * hh);
    u32x2 pq2 = *(const u32x2*)(pqp + 32); if (hh) pq2 = u32x2{0u, 0u};
    float l = hi_half(O[8]);
    {
        float am = 0.f;
#pragma unroll
        for (int i = 0; i < 12; ++i) am = fmaxf(am, fabsf(O[i]) * 0.f + (fabsf(O[i]) <= 3.0e38f ? 0.f : 1.f));
        const bool bad = !(l >= 1e-18f && l <= 3.0e38f) || ((am != 0.f) && !(hh == 1 && false));
        if (__builtin_expect(__any(bad), 0)) {
            const bf16x8 qt0 = __builtin_bit_cast(bf16x8, (u32x4){q2, 0u, 0u, 0u});
            float m = -INFINITY; O = Z16;
            for (int t = 0; t < 64; ++t) {
                const char* tb = kvb + t * 2560;
                const bf16x8 fkm = *(const bf16x8*)(tb + hh * 512 + r * 16);
                const bf16x8 fkt = __builtin_bit_cast(bf16x8, (u32x4){*(const unsigned*)(tb + 1024 + hh * 128 + r * 4), 0u, 0u, 0u});
                u32x4 f0 = *(const u32x4*)(tb + 1280 + hh * 320 + rc * 16), f1 = *(const u32x4*)(tb + 1920 + hh * 320 + rc * 16);
                if (r >= 20) { f0 = ONES; f1 = ONES; }
                f32x16 s = MFMA32(fkm, qm, Z16); s = MFMA32(fkt, qt0, s);
                float tm = s[0];
#pragma unroll
                for (int i = 1; i < 16; ++i) tm = fmaxf(tm, s[i]);
                tm = xhalf_max(tm);
                const float mn = fmaxf(m, tm);
                const float al = (m == -INFINITY) ? 0.f : EX(m - mn);
                m = mn;
#pragma unroll
                for (int i = 0; i < 16; ++i) { O[i] *= al; s[i] = EX(s[i] - mn); }
                u32x4 p0 = {pk2(s[0], s[1]), pk2(s[2], s[3]), pk2(s[4], s[5]), pk2(s[6], s[7])};
                u32x4 p1 = {pk2(s[8], s[9]), pk2(s[10], s[11]), pk2(s[12], s[13]), pk2(s[14], s[15])};
                O = MFMA32(B8(f0), B8(p0), O); O = MFMA32(B8(f1), B8(p1), O);
            }
            l = hi_half(O[8]);
        }
    }
    store_o(ws, O, (1.f - gate) / l, srow, 640 + h * 20, hh);
    __syncthreads();
    f32x4 ks0 = {}, ks1 = {}, ks2 = {};
    if (r <= 20) {
#pragma unroll
        for (int w = 0; w < 8; ++w) {
            const LAS f32x4* src = (const LAS f32x4*)(lds + ((w * 2 + hh) * 21 + r) * 48);
            ks0 += src[0]; ks1 += src[1]; ks2 += src[2];
        }
    }
    const u32x4 ka0 = {pk2(ks0[0], ks0[1]), pk2(ks0[2], ks0[3]), pk2(ks1[0], ks1[1]), pk2(ks1[2], ks1[3])};
    const u32x4 ka1 = {pk2(ks2[0], ks2[1]), pk2(ks2[2], ks2[3]), 0u, 0u};
    f32x16 oa = MFMA32(B8(ka0), B8(((u32x4){pq0[0], pq0[1], pq1[0], pq1[1]})), Z16);
    oa = MFMA32(B8(ka1), B8(((u32x4){pq2[0], pq2[1], 0u, 0u})), oa);
    const float den = hi_half(oa[8]) + 1e-6f;
    store_o(ws, oa, gate / den, srow, h * 20, hh);
}

__global__ __launch_bounds__(256, 1) void k3_out(const char* __restrict__ ws, u16* __restrict__ out) {
    __shared__ __attribute__((aligned(16))) f32x4 red[4][20][64];
    const int bid = blockIdx.x, tid = threadIdx.x, lane = tid & 63, wave = rfl(tid >> 6);
    const int xcd = bid & 7, i = bid >> 3;
    const int rb = xcd * 4 + (i & 3), cb = i >> 2;
    const char* ap = ws + OFF_O + (size_t)(rb * 4) * 40960 + (wave * 10) * 1024 + lane * 16;
    const char* bp = ws + OFF_WOT + (size_t)(wave * 10) * 40960 + (cb * 5) * 1024 + lane * 16;
    bf16x8 af[10][4], bfr[10][5];
#pragma unroll
    for (int k = 0; k < 10; ++k) {
#pragma unroll
        for (int mi = 0; mi < 4; ++mi) af[k][mi] = *(const bf16x8*)(ap + mi * 40960 + k * 1024);
#pragma unroll
        for (int ni = 0; ni < 5; ++ni) bfr[k][ni] = *(const bf16x8*)(bp + k * 40960 + ni * 1024);
    }
    SBAR();
    f32x4 acc[4][5];
#pragma unroll
    for (int mi = 0; mi < 4; ++mi)
#pragma unroll
        for (int ni = 0; ni < 5; ++ni) acc[mi][ni] = f32x4{0.f, 0.f, 0.f, 0.f};
#pragma unroll
    for (int k = 0; k < 10; ++k)
#pragma unroll
        for (int mi = 0; mi < 4; ++mi)
#pragma unroll
            for (int ni = 0; ni < 5; ++ni) acc[mi][ni] = MFMA16(bfr[k][ni], af[k][mi], acc[mi][ni]);
#pragma unroll
    for (int mi = 0; mi < 4; ++mi)
#pragma unroll
        for (int ni = 0; ni < 5; ++ni) red[wave][mi * 5 + ni][lane] = acc[mi][ni];
    __syncthreads();
    const int s = rb * 64 + wave * 16 + (lane & 15);
#pragma unroll
    for (int ni = 0; ni < 5; ++ni) {
        f32x4 v = red[0][wave * 5 + ni][lane] + red[1][wave * 5 + ni][lane] + red[2][wave * 5 + ni][lane] + red[3][wave * 5 + ni][lane];
        u32x2 o = {pk2(v[0], v[1]), pk2(v[2], v[3])};
        *(u32x2*)(out + (size_t)s * D_ + cb * 80 + ni * 16 + (lane >> 4) * 4) = o;
    }
}

extern "C" void kernel_launch(void* const* d_in, const int* in_sizes, int n_in, void* d_out, int out_size, void* d_ws, size_t ws_size,
                              hipStream_t stream) {
    const u16* x = (const u16*)d_in[0];
    const u16* wA = (const u16*)d_in[1];
    const u16* woA = (const u16*)d_in[2];
    const u16* wB = (const u16*)d_in[4];
    const u16* woB = (const u16*)d_in[5];
    const u16* gw = (const u16*)d_in[6];
    char* ws = (char*)d_ws;
    (void)hipFuncSetAttribute((const void*)k1_qkv, hipFuncAttributeMaxDynamicSharedMemorySize, K1_LDS);
    (void)hipFuncSetAttribute((const void*)k2_attn, hipFuncAttributeMaxDynamicSharedMemorySize, K2_LDS);
    k1_qkv<<<256, 512, K1_LDS, stream>>>(x, wA, wB, woA, woB, gw, ws);
    k2_attn<<<256, 512, K2_LDS, stream>>>(ws);
    k3_out<<<256, 256, 0, stream>>>(ws, (u16*)d_out);
}
```

```cpp
#include <hip/hip_runtime.h>
#include <hip/hip_bf16.h>
#include <stdint.h>

typedef short    bf16x8 __attribute__((ext_vector_type(8)));
typedef short    s16x4  __attribute__((ext_vector_type(4)));
typedef float    f32x2  __attribute__((ext_vector_type(2)));
typedef float    f32x4  __attribute__((ext_vector_type(4)));
typedef float    f32x16 __attribute__((ext_vector_type(16)));
typedef float    f32x32 __attribute__((ext_vector_type(32)));
typedef unsigned u32x2  __attribute__((ext_vector_type(2)));
typedef unsigned u32x4  __attribute__((ext_vector_type(4)));
typedef unsigned u32x8  __attribute__((ext_vector_type(8)));
typedef unsigned u32x32 __attribute__((ext_vector_type(32)));
typedef __bf16   bf2_t  __attribute__((ext_vector_type(2)));
typedef unsigned short u16;
#define LAS __attribute__((address_space(3)))
#define DI __device__ __forceinline__

constexpr int S_ = 2048, D_ = 640;
constexpr size_t IMG     = (size_t)32 * 163840;
constexpr size_t OFF_IQ  = 0;
constexpr size_t OFF_IB  = IMG;
constexpr size_t OFF_IA  = 2 * IMG;
constexpr size_t OFF_O   = 3 * IMG;
constexpr size_t OFF_WOT = 4 * IMG;
constexpr size_t OFF_G   = OFF_WOT + (size_t)1600 * 1024;

#define PSTAMP(k, i)
#define WSTAMP(i)

DI unsigned pk2(float lo, float hi) { f32x2 v = {lo, hi}; return __builtin_bit_cast(unsigned, __builtin_convertvector(v, bf2_t)); }
DI float bflo(unsigned v) { return __uint_as_float(v << 16); }
DI float bfhi(unsigned v) { return __uint_as_float(v & 0xffff0000u); }
DI int rfl(int x) { return __builtin_amdgcn_readfirstlane(x); }
DI void glds16(const void* sbase, unsigned voff, unsigned lds_base) {
    asm volatile("s_mov_b32 m0, %0\n\ts_nop 0\n\tglobal_load_lds_dwordx4 %1, %2" :: "s"(lds_base), "v"(voff), "s"(sbase) : "memory");
}
#define MFMA16(a, b, c) __builtin_amdgcn_mfma_f32_16x16x32_bf16((a), (b), (c), 0, 0, 0)
#define MFMA32(a, b, c) __builtin_amdgcn_mfma_f32_32x32x16_bf16((a), (b), (c), 0, 0, 0)
#define B8(x) __builtin_bit_cast(bf16x8, x)
DI u32x2 trd(const LAS char* p) { return __builtin_bit_cast(u32x2, __builtin_amdgcn_ds_read_tr16_b64_v4i16((LAS s16x4*)p)); }
DI float phi(float t) { return (t > 0.f ? t + 1.f : __expf(t)) + 1e-6f; }
template <int N> DI void wait_vm_bar() { asm volatile("s_waitcnt vmcnt(%0) lgkmcnt(0)\n\ts_barrier" :: "n"(N) : "memory"); }

constexpr int LDS_BYTES = 163840;

DI void p1_aux(const u16* __restrict__ x, const u16* __restrict__ woA, const u16* __restrict__ woB, const u16* __restrict__ gate_w,
               char* __restrict__ ws, char* lds, int a) {
    const int tid = threadIdx.x, lane = tid & 63, wave = rfl(tid >> 6);
    const unsigned lds0 = (unsigned)(uintptr_t)lds;
    const int br = a >> 3, u = a & 7;
    const char* wsrc = (const char*)(br ? woB : woA) + (size_t)u * 102400;
    for (int i = wave; i < 100; i += 8) glds16(wsrc + i * 1024, lane * 16, lds0 + i * 1024);
    const u32x4* xs = (const u32x4*)(x + (size_t)(a * 128 + wave * 16) * D_);
    u32x4 xv[20], gw[5];
#pragma unroll
    for (int i = 0; i < 20; ++i) xv[i] = xs[i * 64 + lane];
#pragma unroll
    for (int i = 0; i < 5; ++i) gw[i] = ((const u32x4*)gate_w)[(i * 64 + lane) % 80];
    LAS float* part = (LAS float*)(lds + 102400) + wave * 1280;
#pragma unroll
    for (int i = 0; i < 20; ++i) {
        float s = 0.f;
#pragma unroll
        for (int j = 0; j < 4; ++j) s += bflo(xv[i][j]) * bflo(gw[i % 5][j]) + bfhi(xv[i][j]) * bfhi(gw[i % 5][j]);
        part[i * 64 + lane] = s;
    }
    wait_vm_bar<0>();
    {
        float s = 0.f;
#pragma unroll
        for (int j = 0; j < 20; ++j) s += part[lane * 20 + j];
        s += __shfl_xor(s, 1); s += __shfl_xor(s, 2);
        if ((lane & 3) == 0) ((float*)(ws + OFF_G))[a * 128 + wave * 16 + (lane >> 2)] = 1.f / (1.f + __expf(-s));
    }
    const int g = lane >> 4, hh = g & 1, ns = g >> 1, q = (lane >> 2) & 3, p = lane & 3;
    for (int un = wave; un < 100; un += 8) {
        const int e = un / 20, np = un - e * 20, nt = np * 2 + ns;
        const int r0 = e < 4 ? e * 20 + 4 * hh : (2 * hh) * 20 + 16;
        const int r1 = e < 4 ? r0 + 8 : r0 + 20;
        const int ks16 = br * 40 + (e < 4 ? u * 4 + e : 32 + u);
        const u32x2 lo = trd((const LAS char*)lds + (r0 + q) * 1280 + nt * 32 + p * 8);
        const u32x2 hi = trd((const LAS char*)lds + (r1 + q) * 1280 + nt * 32 + p * 8);
        *(u32x4*)(ws + OFF_WOT + (size_t)((ks16 >> 1) * 40 + nt) * 1024 + ((ks16 & 1) * 2 + hh) * 256 + (lane & 15) * 16) = u32x4{lo[0], lo[1], hi[0], hi[1]};
    }
}

constexpr int p1_nld(int s) { return s < 10 ? 4 : (s < 20 ? 2 : 0); }

template <int S> struct P1Step {
    template <class F, class G, class E>
    static DI void run(F&& stage, G&& compute, E&& epi) {
        constexpr int N = p1_nld(S + 1) + p1_nld(S + 2) + p1_nld(S + 3) + ((S >= 10 && S <= 13) ? 4 : 0);
        wait_vm_bar<N>();
        if (S + 4 < 20) stage(S + 4);
        compute(S);
        if (S == 9) epi(0);
        if (S == 19) epi(1);
        P1Step<S + 1>::run(stage, compute, epi);
    }
};
template <> struct P1Step<20> { template <class F, class G, class E> static DI void run(F&&, G&&, E&&) {} };

DI void phase1(const u16* __restrict__ x, const u16* __restrict__ wA, const u16* __restrict__ wB, char* __restrict__ ws, char* lds, int bid) {
    const int tid = threadIdx.x, lane = tid & 63, wave = rfl(tid >> 6);
    const unsigned lds0 = (unsigned)(uintptr_t)lds;
    const int xcd = bid & 7, j = (xcd >> 1) * 30 + (bid >> 3);
    const int ct = j >> 3, mt = (xcd & 1) * 8 + (j & 7);
    const int type = ct / 5, rot = (3 * ct + mt) % 10;
    const int wm = wave & 3, wn = wave >> 2;
    unsigned xo[2], wo[2];
#pragma unroll
    for (int i = 0; i < 2; ++i) {
        const int c = i * 512 + tid, row = c >> 3, sl = c & 7;
        xo[i] = (unsigned)((mt * 128 + row) * 1280 + ((sl ^ ((row >> 1) & 7)) << 4));
        const int k = c >> 4, s16 = c & 15;
        wo[i] = (unsigned)(k * 3840 + ct * 256 + ((((s16 >> 2) ^ (k & 3)) << 6) | ((s16 & 3) << 4)));
    }
    auto stage = [&](int s) {
        int kk = (s % 10) + rot; kk -= kk >= 10 ? 10 : 0;
        const unsigned slot = lds0 + (s % 5) * 32768 + wave * 1024;
        if (s < 10) {
            glds16((const char*)x + kk * 128, xo[0], slot);
            glds16((const char*)x + kk * 128, xo[1], slot + 8192);
        }
        const char* W = (const char*)(s < 10 ? wB : wA) + kk * (64 * 3840);
        glds16(W, wo[0], slot + 16384);
        glds16(W, wo[1], slot + 16384 + 8192);
    };
    const int r = lane & 31, hh = lane >> 5, g = lane >> 4, q = (lane >> 2) & 3, p = lane & 3;
    const LAS char* la = (const LAS char*)lds + (wm * 32 + r) * 128;
    const int fx = (r >> 1) & 7;
    const LAS char* lb[2];
#pragma unroll
    for (int jn = 0; jn < 2; ++jn) lb[jn] = (const LAS char*)lds + 16384 + (8 * (g >> 1) + q) * 256 + (((wn * 2 + jn) ^ q) << 6) + (g & 1) * 32 + p * 8;
    bf16x8 xf[40];
    f32x16 acc[2] = {};
    auto compute = [&](int s) {
        const int sb = (s % 5) * 32768;
#pragma unroll
        for (int ks = 0; ks < 4; ++ks) {
            if (s < 10) xf[s * 4 + ks] = *(const LAS bf16x8*)(la + sb + (((ks * 2 + hh) ^ fx) << 4));
#pragma unroll
            for (int jn = 0; jn < 2; ++jn) {
                const u32x2 lo = trd(lb[jn] + sb + ks * 4096), hi = trd(lb[jn] + sb + ks * 4096 + 1024);
                acc[jn] = MFMA32(xf[(s % 10) * 4 + ks], B8(((u32x4){lo[0], lo[1], hi[0], hi[1]})), acc[jn]);
            }
        }
    };
    auto epi = [&](int pass) {
        constexpr float QS = 0.22360679774997896f * 1.4426950408889634f;
        const size_t ib = type == 0 ? OFF_IQ + pass * 1280 : (pass ? OFF_IA : OFF_IB) + (type - 1) * 1280;
#pragma unroll
        for (int jn = 0; jn < 2; ++jn) {
            const int c = (ct - type * 5) * 128 + wn * 64 + jn * 32 + r, h = c / 20, d = c - h * 20;
            f32x16 v = acc[jn];
            if (pass == 0) { if (type == 0) v *= QS; }
            else if (type < 2) {
#pragma unroll
                for (int i = 0; i < 16; ++i) v[i] = phi(v[i]);
            }
            char* dst = ws + ib + (size_t)h * 163840 + (mt * 4 + wm) * 2560 + hh * 320 + d * 16;
            *(u32x4*)(dst)       = u32x4{pk2(v[0], v[1]), pk2(v[2], v[3]), pk2(v[4], v[5]), pk2(v[6], v[7])};
            *(u32x4*)(dst + 640) = u32x4{pk2(v[8], v[9]), pk2(v[10], v[11]), pk2(v[12], v[13]), pk2(v[14], v[15])};
            acc[jn] = f32x16{};
        }
        asm volatile("" ::: "memory");
    };
    stage(0); stage(1); stage(2); stage(3);
    P1Step<0>::run(stage, compute, epi);
}

__global__ __launch_bounds__(512, 2) void k1_qkv(const u16* __restrict__ x, const u16* __restrict__ wA, const u16* __restrict__ wB,
                                                 const u16* __restrict__ woA, const u16* __restrict__ woB, const u16* __restrict__ gate_w,
                                                 char* __restrict__ ws) {
    extern __shared__ __attribute__((aligned(16))) char lds[];
    PSTAMP(0, 0);
    if (blockIdx.x >= 240) p1_aux(x, woA, woB, gate_w, ws, lds, blockIdx.x - 240);
    else phase1(x, wA, wB, ws, lds, blockIdx.x);
}

#define STR(x) #x
#define XS(x) STR(x)
#define A_E(i) "v_exp_f32 v" XS(i) ", v" XS(i) "\n"
#define A_C(d, a, b) "v_cvt_pk_bf16_f32 v" XS(d) ", v" XS(a) ", v" XS(b) "\n"
#define A_G(a, b, c, d, p0, p1) A_E(a) A_E(b) A_E(c) A_E(d) A_C(p0, a, b) A_C(p1, c, d)
#define A_M(d, a, b, c) "v_mfma_f32_32x32x16_bf16 " d ", " a ", " b ", " c "\n"
#define A_STEP(C0,C1,C2,C3,C4,C5,C6,C7,C8,C9,C10,C11,C12,C13,C14,C15, SN, KM, KT, V1, V0, NKMa, NKMb, NKT, NV1, NV0, OK0, OK1, OV1, OV0, PRIO) \
    "s_waitcnt lgkmcnt(0)\n" \
    A_M(SN, KM, "v[120:123]", "0") \
    "ds_read_b64_tr_b16 " NKMa ", v128 offset:" XS(OK0) "\n" \
    "ds_read_b64_tr_b16 " NKMb ", v128 offset:" XS(OK1) "\n" \
    "ds_read_b64_tr_b16 " NKT ", v129 offset:" XS(OK0) "\n" \
    A_G(C0, C1, C2, C3, 112, 113) \
    A_M("v[64:79]", V1, "v[116:119]", "v[64:79]") \
    "s_mov_b64 exec, %[vm]\n" \
    "ds_read_b128 " NV1 ", v130 offset:" XS(OV1) "\n" \
    "ds_read_b128 " NV0 ", v130 offset:" XS(OV0) "\n" \
    "s_mov_b64 exec, -1\n" \
    A_G(C4, C5, C6, C7, 114, 115) \
    A_M(SN, KT, "v[124:127]", SN) \
    A_G(C8, C9, C10, C11, 116, 117) \
    A_M("v[64:79]", V0, "v[112:115]", "v[64:79]") \
    "s_setprio " XS(PRIO) "\n" \
    A_G(C12, C13, C14, C15, 118, 119)
#define A_EVEN(OK0, OK1, OV1, OV0, PRIO) A_STEP(32,33,34,35,36,37,38,39,40,41,42,43,44,45,46,47, "v[48:63]", "v[80:83]", "v[84:87]", "v[88:91]", "v[92:95]", \
    "v[96:97]", "v[98:99]", "v[100:101]", "v[104:107]", "v[108:111]", OK0, OK1, OV1, OV0, PRIO)
#define A_ODD(OK0, OK1, OV1, OV0, PRIO) A_STEP(48,49,50,51,52,53,54,55,56,57,58,59,60,61,62,63, "v[32:47]", "v[96:99]", "v[100:103]", "v[104:107]", "v[108:111]", \
    "v[80:81]", "v[82:83]", "v[84:85]", "v[88:91]", "v[92:95]", OK0, OK1, OV1, OV0, PRIO)

DI void attn_chunk(f32x32& S, f32x16& O, u32x32& OPS, u32x8& P, u32x4& AD, const u32x8& Q, unsigned long long vmask) {
    asm volatile(
        "s_nop 7\n"
        A_EVEN(5120, 5184, 640, 2560, 2)     A_ODD(7680, 7744, 3200, 5120, 1)
        A_EVEN(10240, 10304, 5760, 7680, 0)  A_ODD(12800, 12864, 8320, 10240, 3)
        A_EVEN(15360, 15424, 10880, 12800, 2) A_ODD(17920, 17984, 13440, 15360, 1)
        A_EVEN(20480, 20544, 16000, 17920, 0) A_ODD(23040, 23104, 18560, 20480, 3)
        "v_add_u32 v128, 0x5000, v128\n v_add_u32 v129, 0x5000, v129\n v_add_u32 v130, 0x5000, v130\n"
        "s_waitcnt lgkmcnt(0)\n s_nop 4\n"
        : "+{v[32:63]}"(S), "+{v[64:79]}"(O), "+{v[80:111]}"(OPS), "+{v[112:119]}"(P), "+{v[128:131]}"(AD)
        : "{v[120:127]}"(Q), [vm] "s"(vmask)
        : "memory");
}

DI float xhalf_max(float m) { auto rr = __builtin_amdgcn_permlane32_swap(__float_as_uint(m), __float_as_uint(m), false, false);
    return fmaxf(__uint_as_float(rr[0]), __uint_as_float(rr[1])); }
DI float hi_half(float v) {
    auto rr = __builtin_amdgcn_permlane32_swap(__float_as_uint(v), __float_as_uint(v), false, false);
    return __uint_as_float(rr[1]); }
DI void store_o(char* ws, const f32x16& o, float f, int rt, int br, int h, int lane) {
    char* rb = ws + OFF_O + (size_t)(rt * 80 + br * 40) * 1024;
    *(u32x4*)(rb + h * 1024 + lane * 16) = u32x4{pk2(o[0] * f, o[1] * f), pk2(o[2] * f, o[3] * f), pk2(o[4] * f, o[5] * f), pk2(o[6] * f, o[7] * f)};
    if (lane < 32) *(u32x2*)(rb + (32 + (h >> 2)) * 1024 + ((h >> 1) & 1) * 512 + lane * 16 + (h & 1) * 8) = u32x2{pk2(o[8] * f, o[9] * f), pk2(o[10] * f, o[11] * f)};
}

DI void phase2(char* __restrict__ ws, char* lds, int bid) {
    const int tid = threadIdx.x, lane = tid & 63, wave = rfl(tid >> 6);
    const int h = (bid & 7) * 4 + ((bid >> 3) & 3), qb = bid >> 5;
    const int r = lane & 31, hh = lane >> 5;
    const unsigned lds0 = (unsigned)(uintptr_t)lds;
    const char* kvb = ws + OFF_IB + (size_t)h * 163840;
    const char* kva = ws + OFF_IA + (size_t)h * 163840;
    const char* qim = ws + OFF_IQ + (size_t)h * 163840 + qb * 20480;
    const int rt = qb * 8 + wave;
    unsigned qo[2];
#pragma unroll
    for (int i = 0; i < 2; ++i) { const int pc = i * 512 + tid, t = pc / 80; qo[i] = (unsigned)(t * 2560 + (pc - t * 80) * 16); }
    glds16(qim, qo[0], lds0 + 153600 + wave * 1024);
    if (wave < 2) glds16(qim, qo[1], lds0 + 153600 + 8192 + wave * 1024);
#pragma unroll
    for (int i = 0; i < 3; ++i) glds16(kvb + (i * 8 + wave) * 1024, lane * 16, lds0 + (i * 8 + wave) * 1024);
    if (wave == 2) glds16(kvb + 24 * 1024, lane * 16, lds0 + 24 * 1024);
    const float gate = ((const float*)(ws + OFF_G))[rt * 32 + r];
    asm volatile("" ::: "memory");
#pragma unroll
    for (int i = 0; i < 16; ++i) { int pc = 25 + i * 8 + wave; pc = pc < 150 ? pc : pc - 8; glds16(kvb + pc * 1024, lane * 16, lds0 + pc * 1024); }
    wait_vm_bar<16>();
    PSTAMP(1, 1); WSTAMP(1);
    const int g = lane >> 4, q = (lane >> 2) & 3, p = lane & 3;
    const int tro = (g & 1) * 640 + (p & 1) * 320 + q * 16 + (p >> 1) * 8;
    const LAS char* L = (const LAS char*)lds;
    const LAS char* kmp = L + tro + hh * 128;
    const LAS char* ktp = L + tro + 256;
    const LAS char* vp  = L + 1280 + hh * 320 + r * 16;
    u32x8 Q;
    {
        const u32x2 a = trd(kmp + 153600 + wave * 1280), b = trd(kmp + 153600 + wave * 1280 + 64), c = trd(ktp + 153600 + wave * 1280);
        Q[0] = a[0]; Q[1] = a[1]; Q[2] = b[0]; Q[3] = b[1]; Q[4] = hh ? 0u : c[0]; Q[5] = hh ? 0u : c[1]; Q[6] = 0u; Q[7] = 0u;
    }
    const f32x16 Z16 = {};
    const bf16x8 qm = B8(((u32x4){Q[0], Q[1], Q[2], Q[3]}));
    f32x32 S; f32x16 O = {}; u32x32 OPS; u32x8 P = {}; u32x4 AD;
    {
        const u32x2 a = trd(kmp), b = trd(kmp + 64), c = trd(ktp);
        const bf16x8 k0 = B8(((u32x4){a[0], a[1], b[0], b[1]})), k1 = B8(((u32x4){c[0], c[1], 0x3F80u, 0u}));
        const f32x16 sm = MFMA32(k0, qm, Z16);
        f32x16 s0 = MFMA32(k1, B8(((u32x4){Q[4], Q[5], 0u, 0u})), sm);
        float m = s0[0];
#pragma unroll
        for (int i = 1; i < 16; ++i) m = fmaxf(m, s0[i]);
        m = xhalf_max(m);
        Q[6] = hh ? 0u : (pk2(-m, 0.f) & 0xffffu);
        s0 = MFMA32(k1, B8(((u32x4){Q[4], Q[5], Q[6], 0u})), sm);
#pragma unroll
        for (int i = 0; i < 16; ++i) { S[i] = s0[i]; S[16 + i] = 0.f; }
        const unsigned vc = r == 20 ? 0x3F803F80u : 0u;
#pragma unroll
        for (int i = 0; i < 32; ++i) OPS[i] = (i & 8) ? vc : 0u;
        const u32x2 a1 = trd(kmp + 2560), b1 = trd(kmp + 2560 + 64), c1 = trd(ktp + 2560);
        OPS[0] = a1[0]; OPS[1] = a1[1]; OPS[2] = b1[0]; OPS[3] = b1[1]; OPS[4] = c1[0]; OPS[5] = c1[1]; OPS[6] = 0x3F80u; OPS[22] = 0x3F80u;
        if (r < 20) { const u32x4 v0 = *(const LAS u32x4*)vp; OPS[12] = v0[0]; OPS[13] = v0[1]; OPS[14] = v0[2]; OPS[15] = v0[3]; }
        AD[0] = (unsigned)(uintptr_t)kmp; AD[1] = (unsigned)(uintptr_t)ktp; AD[2] = (unsigned)(uintptr_t)vp; AD[3] = 0u;
    }
    const unsigned long long vmask = 0x000FFFFF000FFFFFull;
    const u32x4 ONES = {0x3F803F80u, 0x3F803F80u, 0x3F803F80u, 0x3F803F80u};
    const int rc = r < 20 ? r : 19;
    f32x16 kvacc = {};
    for (int c = 0; c < 8; ++c) {
        const char* ap = kva + (c * 8 + wave) * 2560 + hh * 320 + rc * 16;
        u32x4 a0 = *(const u32x4*)(ap), a1 = *(const u32x4*)(ap + 640), b0 = *(const u32x4*)(ap + 1280), b1 = *(const u32x4*)(ap + 1920);
        if (c == 7) wait_vm_bar<4>();
        attn_chunk(S, O, OPS, P, AD, Q, vmask);
        if (r >= 20) { a0 = u32x4{0u, 0u, 0u, 0u}; a1 = a0; b0 = ONES; b1 = ONES; }
        kvacc = MFMA32(B8(a0), B8(b0), kvacc); kvacc = MFMA32(B8(a1), B8(b1), kvacc);
        if (c == 0) {
            wait_vm_bar<0>();
            glds16(kvb + (150 + wave) * 1024, lane * 16, lds0 + (150 + wave) * 1024);
            if (wave < 2) glds16(kvb + (158 + wave) * 1024, lane * 16, lds0 + (158 + wave) * 1024);
            glds16(qim + 1280, qo[0], lds0 + wave * 1024);
            if (wave < 2) glds16(qim + 1280, qo[1], lds0 + 8192 + wave * 1024);
        }
    }
    __builtin_amdgcn_s_setprio(0);
    O = MFMA32(B8(((u32x4){OPS[8], OPS[9], OPS[10], OPS[11]})), B8(((u32x4){P[4], P[5], P[6], P[7]})), O);
    PSTAMP(1, 2); WSTAMP(2);
    if (r <= 20) {
        LAS f32x4* dst = (LAS f32x4*)(lds + 10240 + ((wave * 2 + hh) * 21 + r) * 48);
        dst[0] = f32x4{kvacc[0], kvacc[1], kvacc[2], kvacc[3]};
        dst[1] = f32x4{kvacc[4], kvacc[5], kvacc[6], kvacc[7]};
        dst[2] = f32x4{kvacc[8], kvacc[9], kvacc[10], kvacc[11]};
    }
    float l = hi_half(O[8]);
    {
        bool bad = !(l >= 1e-18f && l <= 3.0e38f);
#pragma unroll
        for (int i = 0; i < 12; ++i) bad = bad || !(fabsf(O[i]) <= 3.0e38f);
        if (__builtin_expect(__any(bad), 0)) {
            const bf16x8 qt0 = B8(((u32x4){Q[4], Q[5], 0u, 0u}));
            float m = -INFINITY; O = Z16;
            const int ko = (r >> 4) * 640 + ((r >> 2) & 1) * 320 + ((r >> 3) & 1) * 8 + (r & 3) * 2;
            for (int t = 0; t < 64; ++t) {
                const char* tb = kvb + t * 2560;
                unsigned kw[6];
#pragma unroll
                for (int i = 0; i < 6; ++i) {
                    const int d = i < 4 ? 8 * hh + 2 * i : 16 + 2 * (i - 4);
                    kw[i] = (unsigned)*(const u16*)(tb + ko + d * 16) | ((unsigned)*(const u16*)(tb + ko + d * 16 + 16) << 16);
                }
                u32x4 f0 = *(const u32x4*)(tb + 1280 + hh * 320 + rc * 16), f1 = *(const u32x4*)(tb + 1920 + hh * 320 + rc * 16);
                if (r >= 20) { f0 = ONES; f1 = ONES; }
                f32x16 s = MFMA32(B8(((u32x4){kw[0], kw[1], kw[2], kw[3]})), qm, Z16);
                s = MFMA32(B8(((u32x4){kw[4], kw[5], 0u, 0u})), qt0, s);
                float tm = s[0];
#pragma unroll
                for (int i = 1; i < 16; ++i) tm = fmaxf(tm, s[i]);
                tm = xhalf_max(tm);
                const float mn = fmaxf(m, tm);
                const float al = (m == -INFINITY) ? 0.f : __builtin_amdgcn_exp2f(m - mn);
                m = mn;
#pragma unroll
                for (int i = 0; i < 16; ++i) { O[i] *= al; s[i] = __builtin_amdgcn_exp2f(s[i] - mn); }
                u32x4 p0 = {pk2(s[0], s[1]), pk2(s[2], s[3]), pk2(s[4], s[5]), pk2(s[6], s[7])};
                u32x4 p1 = {pk2(s[8], s[9]), pk2(s[10], s[11]), pk2(s[12], s[13]), pk2(s[14], s[15])};
                O = MFMA32(B8(f0), B8(p0), O); O = MFMA32(B8(f1), B8(p1), O);
            }
            l = hi_half(O[8]);
        }
    }
    store_o(ws, O, (1.f - gate) / l, rt, 1, h, lane);
    const u32x2 pq0 = trd(L + wave * 1280 + tro + hh * 64), pq1 = trd(L + wave * 1280 + tro + 128 + hh * 64);
    u32x2 pq2 = trd(L + wave * 1280 + tro + 256); if (hh) pq2 = u32x2{0u, 0u};
    WSTAMP(4);
    __syncthreads();
    WSTAMP(5);
    f32x4 ks0 = {}, ks1 = {}, ks2 = {};
    if (r <= 20) {
#pragma unroll
        for (int w = 0; w < 8; ++w) {
            const LAS f32x4* src = (const LAS f32x4*)(lds + 10240 + ((w * 2 + hh) * 21 + r) * 48);
            ks0 += src[0]; ks1 += src[1]; ks2 += src[2];
        }
    }
    const u32x4 ka0 = {pk2(ks0[0], ks0[1]), pk2(ks0[2], ks0[3]), pk2(ks1[0], ks1[1]), pk2(ks1[2], ks1[3])};
    const u32x4 ka1 = {pk2(ks2[0], ks2[1]), pk2(ks2[2], ks2[3]), 0u, 0u};
    f32x16 oa = MFMA32(B8(ka0), B8(((u32x4){pq0[0], pq0[1], pq1[0], pq1[1]})), Z16);
    oa = MFMA32(B8(ka1), B8(((u32x4){pq2[0], pq2[1], 0u, 0u})), oa);
    const float den = hi_half(oa[8]) + 1e-6f;
    store_o(ws, oa, gate / den, rt, 0, h, lane);
}

__global__ __launch_bounds__(512, 2) void k2_attn(char* __restrict__ ws) {
    extern __shared__ __attribute__((aligned(16))) char lds[];
    PSTAMP(1, 0); WSTAMP(0);
    phase2(ws, lds, blockIdx.x);
}

DI void phase3(const char* __restrict__ ws, u16* __restrict__ out, char* lds, int bid) {
    const int tid = threadIdx.x, lane = tid & 63, wave = rfl(tid >> 6);
    const int xcd = bid & 7, i = bid >> 3;
    const int rb = xcd * 4 + (i & 3), cb = i >> 2;
    const int g = lane >> 4;
    const char* ap = ws + OFF_O + (size_t)(rb * 160 + wave * 10 + (g >> 1)) * 1024 + (g & 1) * 512 + (lane & 15) * 16;
    const char* bp = ws + OFF_WOT + (size_t)(wave * 5 * 40 + cb * 5) * 1024 + lane * 16;
    bf16x8 af[5][4], bfr[5][5];
#pragma unroll
    for (int k = 0; k < 5; ++k) {
#pragma unroll
        for (int ni = 0; ni < 5; ++ni) bfr[k][ni] = *(const bf16x8*)(bp + k * 40960 + ni * 1024);
#pragma unroll
        for (int mi = 0; mi < 4; ++mi) af[k][mi] = *(const bf16x8*)(ap + (mi >> 1) * 81920 + k * 2048 + (mi & 1) * 256);
    }
    __builtin_amdgcn_sched_barrier(0);
    f32x4 acc[4][5] = {};
#pragma unroll
    for (int k = 0; k < 5; ++k)
#pragma unroll
        for (int mi = 0; mi < 4; ++mi)
#pragma unroll
            for (int ni = 0; ni < 5; ++ni) acc[mi][ni] = MFMA16(bfr[k][ni], af[k][mi], acc[mi][ni]);
    LAS f32x4* red = (LAS f32x4*)lds;
#pragma unroll
    for (int mi = 0; mi < 4; ++mi)
#pragma unroll
        for (int ni = 0; ni < 5; ++ni) red[(wave * 20 + mi * 5 + ni) * 64 + lane] = acc[mi][ni];
    PSTAMP(2, 1);
    __syncthreads();
#pragma unroll
    for (int it = 0; it < 2; ++it) {
        const int id = it * 512 + tid;
        if (id < 640) {
            const int row = id / 10, pc = id - row * 10;
            const int ix = ((row >> 4) * 5 + (pc >> 1)) * 64 + (pc & 1) * 32 + (row & 15);
            f32x4 v0 = {}, v1 = {};
#pragma unroll
            for (int w = 0; w < 8; ++w) { v0 += red[w * 1280 + ix]; v1 += red[w * 1280 + ix + 16]; }
            *(u32x4*)(out + (size_t)(rb * 64 + row) * D_ + cb * 80 + pc * 8) = u32x4{pk2(v0[0], v0[1]), pk2(v0[2], v0[3]), pk2(v1[0], v1[1]), pk2(v1[2], v1[3])};
        }
    }
}

__global__ __launch_bounds__(512, 2) void k3_out(char* __restrict__ ws, u16* __restrict__ out) {
    extern __shared__ __attribute__((aligned(16))) char lds[];
    PSTAMP(2, 0);
    phase3(ws, out, lds, blockIdx.x);
}

extern "C" void kernel_launch(void* const* d_in, const int* in_sizes, int n_in, void* d_out, int out_size, void* d_ws, size_t ws_size,
                              hipStream_t stream) {
    const u16* x = (const u16*)d_in[0];
    const u16* wA = (const u16*)d_in[1];
    const u16* woA = (const u16*)d_in[2];
    const u16* wB = (const u16*)d_in[4];
    const u16* woB = (const u16*)d_in[5];
    const u16* gw = (const u16*)d_in[6];
    char* ws = (char*)d_ws;
    (void)hipFuncSetAttribute((const void*)k1_qkv, hipFuncAttributeMaxDynamicSharedMemorySize, LDS_BYTES);
    (void)hipFuncSetAttribute((const void*)k2_attn, hipFuncAttributeMaxDynamicSharedMemorySize, LDS_BYTES);
    (void)hipFuncSetAttribute((const void*)k3_out, hipFuncAttributeMaxDynamicSharedMemorySize, LDS_BYTES);
    k1_qkv<<<256, 512, LDS_BYTES, stream>>>(x, wA, wB, woA, woB, gw, ws);
    k2_attn<<<256, 512, LDS_BYTES, stream>>>(ws);
    k3_out<<<256, 512, LDS_BYTES, stream>>>(ws, (u16*)d_out);
}
```

```cpp
#include <hip/hip_runtime.h>
#include <hip/hip_bf16.h>
#include <stdint.h>

typedef short    bf16x8 __attribute__((ext_vector_type(8)));
typedef short    s16x4  __attribute__((ext_vector_type(4)));
typedef float    f32x2  __attribute__((ext_vector_type(2)));
typedef float    f32x4  __attribute__((ext_vector_type(4)));
typedef float    f32x16 __attribute__((ext_vector_type(16)));
typedef float    f32x32 __attribute__((ext_vector_type(32)));
typedef unsigned u32x2  __attribute__((ext_vector_type(2)));
typedef unsigned u32x4  __attribute__((ext_vector_type(4)));
typedef unsigned u32x8  __attribute__((ext_vector_type(8)));
typedef unsigned u32x32 __attribute__((ext_vector_type(32)));
typedef __bf16   bf2_t  __attribute__((ext_vector_type(2)));
typedef unsigned short u16;
#define LAS __attribute__((address_space(3)))
#define DI __device__ __forceinline__

constexpr int S_ = 2048, D_ = 640;
constexpr size_t IMG     = (size_t)32 * 163840;
constexpr size_t OFF_IQ  = 0;
constexpr size_t OFF_IB  = IMG;
constexpr size_t OFF_IA  = 2 * IMG;
constexpr size_t OFF_O   = 3 * IMG;
constexpr size_t OFF_WOT = 4 * IMG;
constexpr size_t OFF_G   = OFF_WOT + (size_t)1600 * 1024;

#define PSTAMP(k, i)
#define WSTAMP(i)

DI unsigned pk2(float lo, float hi) { f32x2 v = {lo, hi}; return __builtin_bit_cast(unsigned, __builtin_convertvector(v, bf2_t)); }
DI float bflo(unsigned v) { return __uint_as_float(v << 16); }
DI float bfhi(unsigned v) { return __uint_as_float(v & 0xffff0000u); }
DI int rfl(int x) { return __builtin_amdgcn_readfirstlane(x); }
DI void glds16(const void* sbase, unsigned voff, unsigned lds_base) {
    const unsigned long long a = (unsigned long long)(uintptr_t)sbase;
    const unsigned long long u = (unsigned long long)(unsigned)rfl((int)(unsigned)a) | ((unsigned long long)(unsigned)rfl((int)(unsigned)(a >> 32)) << 32);
    asm volatile("s_mov_b32 m0, %0\n\ts_nop 0\n\tglobal_load_lds_dwordx4 %1, %2" :: "s"(rfl((int)lds_base)), "v"(voff), "s"(u) : "memory");
}
#define MFMA16(a, b, c) __builtin_amdgcn_mfma_f32_16x16x32_bf16((a), (b), (c), 0, 0, 0)
#define MFMA32(a, b, c) __builtin_amdgcn_mfma_f32_32x32x16_bf16((a), (b), (c), 0, 0, 0)
#define B8(x) __builtin_bit_cast(bf16x8, x)
DI u32x2 trd(const LAS char* p) { return __builtin_bit_cast(u32x2, __builtin_amdgcn_ds_read_tr16_b64_v4i16((LAS s16x4*)p)); }
DI float phi(float t) { return (t > 0.f ? t + 1.f : __expf(t)) + 1e-6f; }
template <int N> DI void wait_vm_bar() { asm volatile("s_waitcnt vmcnt(%0) lgkmcnt(0)\n\ts_barrier" :: "n"(N) : "memory"); }

constexpr int LDS_BYTES = 163840;

DI void p1_aux(const u16* __restrict__ x, const u16* __restrict__ woA, const u16* __restrict__ woB, const u16* __restrict__ gate_w,
               char* __restrict__ ws, char* lds, int a) {
    const int tid = threadIdx.x, lane = tid & 63, wave = rfl(tid >> 6);
    const unsigned lds0 = (unsigned)(uintptr_t)lds;
    const int br = a >> 3, u = a & 7;
    const char* wsrc = (const char*)(br ? woB : woA) + (size_t)u * 102400;
    for (int i = wave; i < 100; i += 8) glds16(wsrc + i * 1024, lane * 16, lds0 + i * 1024);
    const u32x4* xs = (const u32x4*)(x + (size_t)(a * 128 + wave * 16) * D_);
    u32x4 xv[20], gw[5];
#pragma unroll
    for (int i = 0; i < 20; ++i) xv[i] = xs[i * 64 + lane];
#pragma unroll
    for (int i = 0; i < 5; ++i) gw[i] = ((const u32x4*)gate_w)[(i * 64 + lane) % 80];
    LAS float* part = (LAS float*)(lds + 102400) + wave * 1280;
#pragma unroll
    for (int i = 0; i < 20; ++i) {
        float s = 0.f;
#pragma unroll
        for (int j = 0; j < 4; ++j) s += bflo(xv[i][j]) * bflo(gw[i % 5][j]) + bfhi(xv[i][j]) * bfhi(gw[i % 5][j]);
        part[i * 64 + lane] = s;
    }
    wait_vm_bar<0>();
    {
        float s = 0.f;
#pragma unroll
        for (int j = 0; j < 20; ++j) s += part[lane * 20 + j];
        s += __shfl_xor(s, 1); s += __shfl_xor(s, 2);
        if ((lane & 3) == 0) ((float*)(ws + OFF_G))[a * 128 + wave * 16 + (lane >> 2)] = 1.f / (1.f + __expf(-s));
    }
    const int g = lane >> 4, hh = g & 1, ns = g >> 1, q = (lane >> 2) & 3, p = lane & 3;
    for (int un = wave; un < 100; un += 8) {
        const int e = un / 20, np = un - e * 20, nt = np * 2 + ns;
        const int r0 = e < 4 ? e * 20 + 4 * hh : (2 * hh) * 20 + 16;
        const int r1 = e < 4 ? r0 + 8 : r0 + 20;
        const int ks16 = br * 40 + (e < 4 ? u * 4 + e : 32 + u);
        const u32x2 lo = trd((const LAS char*)lds + (r0 + q) * 1280 + nt * 32 + p * 8);
        const u32x2 hi = trd((const LAS char*)lds + (r1 + q) * 1280 + nt * 32 + p * 8);
        *(u32x4*)(ws + OFF_WOT + (size_t)((ks16 >> 1) * 40 + nt) * 1024 + ((ks16 & 1) * 2 + hh) * 256 + (lane & 15) * 16) = u32x4{lo[0], lo[1], hi[0], hi[1]};
    }
}

constexpr int p1_nld(int s) { return s < 10 ? 4 : (s < 20 ? 2 : 0); }

template <int S> struct P1Step {
    template <class F, class G, class E>
    static DI void run(F&& stage, G&& compute, E&& epi) {
        constexpr int N = p1_nld(S + 1) + p1_nld(S + 2) + p1_nld(S + 3) + ((S >= 10 && S <= 13) ? 4 : 0);
        wait_vm_bar<N>();
        if (S + 4 < 20) stage(S + 4);
        compute(S);
        if (S == 9) epi(0);
        if (S == 19) epi(1);
        P1Step<S + 1>::run(stage, compute, epi);
    }
};
template <> struct P1Step<20> { template <class F, class G, class E> static DI void run(F&&, G&&, E&&) {} };

DI void phase1(const u16* __restrict__ x, const u16* __restrict__ wA, const u16* __restrict__ wB, char* __restrict__ ws, char* lds, int bid) {
    const int tid = threadIdx.x, lane = tid & 63, wave = rfl(tid >> 6);
    const unsigned lds0 = (unsigned)(uintptr_t)lds;
    const int xcd = bid & 7, j = (xcd >> 1) * 30 + (bid >> 3);
    const int ct = j >> 3, mt = (xcd & 1) * 8 + (j & 7);
    const int type = rfl(ct / 5), rot = rfl((3 * ct + mt) % 10);
    const int wm = wave & 3, wn = wave >> 2;
    unsigned xo[2], wo[2];
#pragma unroll
    for (int i = 0; i < 2; ++i) {
        const int c = i * 512 + tid, row = c >> 3, sl = c & 7;
        xo[i] = (unsigned)((mt * 128 + row) * 1280 + ((sl ^ ((row >> 1) & 7)) << 4));
        const int k = c >> 4, s16 = c & 15;
        wo[i] = (unsigned)(k * 3840 + ct * 256 + ((((s16 >> 2) ^ (k & 3)) << 6) | ((s16 & 3) << 4)));
    }
    auto stage = [&](int s) {
        int kk = (s % 10) + rot; kk -= kk >= 10 ? 10 : 0;
        const unsigned slot = lds0 + (s % 5) * 32768 + wave * 1024;
        if (s < 10) {
            glds16((const char*)x + kk * 128, xo[0], slot);
            glds16((const char*)x + kk * 128, xo[1], slot + 8192);
        }
        const char* W = (const char*)(s < 10 ? wB : wA) + kk * (64 * 3840);
        glds16(W, wo[0], slot + 16384);
        glds16(W, wo[1], slot + 16384 + 8192);
    };
    const int r = lane & 31, hh = lane >> 5, g = lane >> 4, q = (lane >> 2) & 3, p = lane & 3;
    const LAS char* la = (const LAS char*)lds + (wm * 32 + r) * 128;
    const int fx = (r >> 1) & 7;
    const LAS char* lb[2];
#pragma unroll
    for (int jn = 0; jn < 2; ++jn) lb[jn] = (const LAS char*)lds + 16384 + (8 * (g >> 1) + q) * 256 + (((wn * 2 + jn) ^ q) << 6) + (g & 1) * 32 + p * 8;
    bf16x8 xf[40];
    f32x16 acc[2] = {};
    auto compute = [&](int s) {
        const int sb = (s % 5) * 32768;
#pragma unroll
        for (int ks = 0; ks < 4; ++ks) {
            if (s < 10) xf[s * 4 + ks] = *(const LAS bf16x8*)(la + sb + (((ks * 2 + hh) ^ fx) << 4));
#pragma unroll
            for (int jn = 0; jn < 2; ++jn) {
                const u32x2 lo = trd(lb[jn] + sb + ks * 4096), hi = trd(lb[jn] + sb + ks * 4096 + 1024);
                acc[jn] = MFMA32(xf[(s % 10) * 4 + ks], B8(((u32x4){lo[0], lo[1], hi[0], hi[1]})), acc[jn]);
            }
        }
    };
    auto epi = [&](int pass) {
        constexpr float QS = 0.22360679774997896f * 1.4426950408889634f;
        const size_t ib = type == 0 ? OFF_IQ + pass * 1280 : (pass ? OFF_IA : OFF_IB) + (type - 1) * 1280;
#pragma unroll
        for (int jn = 0; jn < 2; ++jn) {
            const int c = (ct - type * 5) * 128 + wn * 64 + jn * 32 + r, h = c / 20, d = c - h * 20;
            f32x16 v = acc[jn];
            if (pass == 0) { if (type == 0) v *= QS; }
            else if (type < 2) {
#pragma unroll
                for (int i = 0; i < 16; ++i) v[i] = phi(v[i]);
            }
            char* dst = ws + ib + (size_t)h * 163840 + (mt * 4 + wm) * 2560 + hh * 320 + d * 16;
            *(u32x4*)(dst)       = u32x4{pk2(v[0], v[1]), pk2(v[2], v[3]), pk2(v[4], v[5]), pk2(v[6], v[7])};
            *(u32x4*)(dst + 640) = u32x4{pk2(v[8], v[9]), pk2(v[10], v[11]), pk2(v[12], v[13]), pk2(v[14], v[15])};
            acc[jn] = f32x16{};
        }
        asm volatile("" ::: "memory");
    };
    stage(0); stage(1); stage(2); stage(3);
    P1Step<0>::run(stage, compute, epi);
}

__global__ __launch_bounds__(512, 2) void k1_qkv(const u16* __restrict__ x, const u16* __restrict__ wA, const u16* __restrict__ wB,
                                                 const u16* __restrict__ woA, const u16* __restrict__ woB, const u16* __restrict__ gate_w,
                                                 char* __restrict__ ws) {
    extern __shared__ __attribute__((aligned(16))) char lds[];
    PSTAMP(0, 0);
    if (blockIdx.x >= 240) p1_aux(x, woA, woB, gate_w, ws, lds, blockIdx.x - 240);
    else phase1(x, wA, wB, ws, lds, blockIdx.x);
}

#define STR(x) #x
#define XS(x) STR(x)
#define A_E(i) "v_exp_f32 v" XS(i) ", v" XS(i) "\n"
#define A_C(d, a, b) "v_cvt_pk_bf16_f32 v" XS(d) ", v" XS(a) ", v" XS(b) "\n"
#define A_G(a, b, c, d, p0, p1) A_E(a) A_E(b) A_E(c) A_E(d) A_C(p0, a, b) A_C(p1, c, d)
#define A_M(d, a, b, c) "v_mfma_f32_32x32x16_bf16 " d ", " a ", " b ", " c "\n"
#define A_STEP(C0,C1,C2,C3,C4,C5,C6,C7,C8,C9,C10,C11,C12,C13,C14,C15, SN, KM, KT, V1, V0, NKMa, NKMb, NKT, NV1, NV0, OK0, OK1, OV1, OV0, PRIO) \
    "s_waitcnt lgkmcnt(0)\n" \
    A_M(SN, KM, "v[120:123]", "0") \
    "ds_read_b64_tr_b16 " NKMa ", v128 offset:" XS(OK0) "\n" \
    "ds_read_b64_tr_b16 " NKMb ", v128 offset:" XS(OK1) "\n" \
    "ds_read_b64_tr_b16 " NKT ", v129 offset:" XS(OK0) "\n" \
    A_G(C0, C1, C2, C3, 112, 113) \
    A_M("v[64:79]", V1, "v[116:119]", "v[64:79]") \
    "s_mov_b64 exec, %[vm]\n" \
    "ds_read_b128 " NV1 ", v130 offset:" XS(OV1) "\n" \
    "ds_read_b128 " NV0 ", v130 offset:" XS(OV0) "\n" \
    "s_mov_b64 exec, -1\n" \
    A_G(C4, C5, C6, C7, 114, 115) \
    A_M(SN, KT, "v[124:127]", SN) \
    A_G(C8, C9, C10, C11, 116, 117) \
    A_M("v[64:79]", V0, "v[112:115]", "v[64:79]") \
    "s_setprio " XS(PRIO) "\n" \
    A_G(C12, C13, C14, C15, 118, 119)
#define A_EVEN(OK0, OK1, OV1, OV0, PRIO) A_STEP(32,33,34,35,36,37,38,39,40,41,42,43,44,45,46,47, "v[48:63]", "v[80:83]", "v[84:87]", "v[88:91]", "v[92:95]", \
    "v[96:97]", "v[98:99]", "v[100:101]", "v[104:107]", "v[108:111]", OK0, OK1, OV1, OV0, PRIO)
#define A_ODD(OK0, OK1, OV1, OV0, PRIO) A_STEP(48,49,50,51,52,53,54,55,56,57,58,59,60,61,62,63, "v[32:47]", "v[96:99]", "v[100:103]", "v[104:107]", "v[108:111]", \
    "v[80:81]", "v[82:83]", "v[84:85]", "v[88:91]", "v[92:95]", OK0, OK1, OV1, OV0, PRIO)

DI void attn_chunk(f32x32& S, f32x16& O, u32x32& OPS, u32x8& P, u32x4& AD, const u32x8& Q, unsigned long long vmask) {
    asm volatile(
        "s_nop 7\n"
        A_EVEN(5120, 5184, 640, 2560, 2)     A_ODD(7680, 7744, 3200, 5120, 1)
        A_EVEN(10240, 10304, 5760, 7680, 0)  A_ODD(12800, 12864, 8320, 10240, 3)
        A_EVEN(15360, 15424, 10880, 12800, 2) A_ODD(17920, 17984, 13440, 15360, 1)
        A_EVEN(20480, 20544, 16000, 17920, 0) A_ODD(23040, 23104, 18560, 20480, 3)
        "v_add_u32 v128, 0x5000, v128\n v_add_u32 v129, 0x5000, v129\n v_add_u32 v130, 0x5000, v130\n"
        "s_waitcnt lgkmcnt(0)\n s_nop 4\n"
        : "+{v[32:63]}"(S), "+{v[64:79]}"(O), "+{v[80:111]}"(OPS), "+{v[112:119]}"(P), "+{v[128:131]}"(AD)
        : "{v[120:127]}"(Q), [vm] "s"(vmask)
        : "memory");
}

DI float xhalf_max(float m) { auto rr = __builtin_amdgcn_permlane32_swap(__float_as_uint(m), __float_as_uint(m), false, false);
    return fmaxf(__uint_as_float(rr[0]), __uint_as_float(rr[1])); }
DI float hi_half(float v) {
    auto rr = __builtin_amdgcn_permlane32_swap(__float_as_uint(v), __float_as_uint(v), false, false);
    return __uint_as_float(rr[1]); }
DI void store_o(char* ws, const f32x16& o, float f, int rt, int br, int h, int lane) {
    char* rb = ws + OFF_O + (size_t)(rt * 80 + br * 40) * 1024;
    *(u32x4*)(rb + h * 1024 + lane * 16) = u32x4{pk2(o[0] * f, o[1] * f), pk2(o[2] * f, o[3] * f), pk2(o[4] * f, o[5] * f), pk2(o[6] * f, o[7] * f)};
    if (lane < 32) *(u32x2*)(rb + (32 + (h >> 2)) * 1024 + ((h >> 1) & 1) * 512 + lane * 16 + (h & 1) * 8) = u32x2{pk2(o[8] * f, o[9] * f), pk2(o[10] * f, o[11] * f)};
}

DI void phase2(char* __restrict__ ws, char* lds, int bid) {
    const int tid = threadIdx.x, lane = tid & 63, wave = rfl(tid >> 6);
    const int h = (bid & 7) * 4 + ((bid >> 3) & 3), qb = bid >> 5;
    const int r = lane & 31, hh = lane >> 5;
    const unsigned lds0 = (unsigned)(uintptr_t)lds;
    const char* kvb = ws + OFF_IB + (size_t)h * 163840;
    const char* kva = ws + OFF_IA + (size_t)h * 163840;
    const char* qim = ws + OFF_IQ + (size_t)h * 163840 + qb * 20480;
    const int rt = qb * 8 + wave;
    unsigned qo[2];
#pragma unroll
    for (int i = 0; i < 2; ++i) { const int pc = i * 512 + tid, t = pc / 80; qo[i] = (unsigned)(t * 2560 + (pc - t * 80) * 16); }
    glds16(qim, qo[0], lds0 + 153600 + wave * 1024);
    if (wave < 2) glds16(qim, qo[1], lds0 + 153600 + 8192 + wave * 1024);
#pragma unroll
    for (int i = 0; i < 3; ++i) glds16(kvb + (i * 8 + wave) * 1024, lane * 16, lds0 + (i * 8 + wave) * 1024);
    if (wave == 2) glds16(kvb + 24 * 1024, lane * 16, lds0 + 24 * 1024);
    const float gate = ((const float*)(ws + OFF_G))[rt * 32 + r];
    asm volatile("" ::: "memory");
#pragma unroll
    for (int i = 0; i < 16; ++i) { int pc = 25 + i * 8 + wave; pc = pc < 150 ? pc : pc - 8; glds16(kvb + pc * 1024, lane * 16, lds0 + pc * 1024); }
    wait_vm_bar<16>();
    PSTAMP(1, 1); WSTAMP(1);
    const int g = lane >> 4, q = (lane >> 2) & 3, p = lane & 3;
    const int tro = (g & 1) * 640 + (p & 1) * 320 + q * 16 + (p >> 1) * 8;
    const LAS char* L = (const LAS char*)lds;
    const LAS char* kmp = L + tro + hh * 128;
    const LAS char* ktp = L + tro + 256;
    const LAS char* vp  = L + 1280 + hh * 320 + r * 16;
    u32x8 Q;
    {
        const u32x2 a = trd(kmp + 153600 + wave * 1280), b = trd(kmp + 153600 + wave * 1280 + 64), c = trd(ktp + 153600 + wave * 1280);
        Q[0] = a[0]; Q[1] = a[1]; Q[2] = b[0]; Q[3] = b[1]; Q[4] = hh ? 0u : c[0]; Q[5] = hh ? 0u : c[1]; Q[6] = 0u; Q[7] = 0u;
    }
    const f32x16 Z16 = {};
    const bf16x8 qm = B8(((u32x4){Q[0], Q[1], Q[2], Q[3]}));
    f32x32 S; f32x16 O = {}; u32x32 OPS; u32x8 P = {}; u32x4 AD;
    {
        const u32x2 a = trd(kmp), b = trd(kmp + 64), c = trd(ktp);
        const bf16x8 k0 = B8(((u32x4){a[0], a[1], b[0], b[1]})), k1 = B8(((u32x4){c[0], c[1], 0x3F80u, 0u}));
        const f32x16 sm = MFMA32(k0, qm, Z16);
        f32x16 s0 = MFMA32(k1, B8(((u32x4){Q[4], Q[5], 0u, 0u})), sm);
        float m = s0[0];
#pragma unroll
        for (int i = 1; i < 16; ++i) m = fmaxf(m, s0[i]);
        m = xhalf_max(m);
        Q[6] = hh ? 0u : (pk2(-m, 0.f) & 0xffffu);
        s0 = MFMA32(k1, B8(((u32x4){Q[4], Q[5], Q[6], 0u})), sm);
#pragma unroll
        for (int i = 0; i < 16; ++i) { S[i] = s0[i]; S[16 + i] = 0.f; }
        const unsigned vc = r == 20 ? 0x3F803F80u : 0u;
#pragma unroll
        for (int i = 0; i < 32; ++i) OPS[i] = (i & 8) ? vc : 0u;
        const u32x2 a1 = trd(kmp + 2560), b1 = trd(kmp + 2560 + 64), c1 = trd(ktp + 2560);
        OPS[0] = a1[0]; OPS[1] = a1[1]; OPS[2] = b1[0]; OPS[3] = b1[1]; OPS[4] = c1[0]; OPS[5] = c1[1]; OPS[6] = 0x3F80u; OPS[22] = 0x3F80u;
        if (r < 20) { const u32x4 v0 = *(const LAS u32x4*)vp; OPS[12] = v0[0]; OPS[13] = v0[1]; OPS[14] = v0[2]; OPS[15] = v0[3]; }
        AD[0] = (unsigned)(uintptr_t)kmp; AD[1] = (unsigned)(uintptr_t)ktp; AD[2] = (unsigned)(uintptr_t)vp; AD[3] = 0u;
    }
    const unsigned long long vmask = 0x000FFFFF000FFFFFull;
    const u32x4 ONES = {0x3F803F80u, 0x3F803F80u, 0x3F803F80u, 0x3F803F80u};
    const int rc = r < 20 ? r : 19;
    f32x16 kvacc = {};
    for (int c = 0; c < 8; ++c) {
        const char* ap = kva + (c * 8 + wave) * 2560 + hh * 320 + rc * 16;
        u32x4 a0 = *(const u32x4*)(ap), a1 = *(const u32x4*)(ap + 640), b0 = *(const u32x4*)(ap + 1280), b1 = *(const u32x4*)(ap + 1920);
        if (c == 7) wait_vm_bar<4>();
        attn_chunk(S, O, OPS, P, AD, Q, vmask);
        if (r >= 20) { a0 = u32x4{0u, 0u, 0u, 0u}; a1 = a0; b0 = ONES; b1 = ONES; }
        kvacc = MFMA32(B8(a0), B8(b0), kvacc); kvacc = MFMA32(B8(a1), B8(b1), kvacc);
        if (c == 0) {
            wait_vm_bar<0>();
            glds16(kvb + (150 + wave) * 1024, lane * 16, lds0 + (150 + wave) * 1024);
            if (wave < 2) glds16(kvb + (158 + wave) * 1024, lane * 16, lds0 + (158 + wave) * 1024);
            glds16(qim + 1280, qo[0], lds0 + wave * 1024);
            if (wave < 2) glds16(qim + 1280, qo[1], lds0 + 8192 + wave * 1024);
        }
    }
    __builtin_amdgcn_s_setprio(0);
    O = MFMA32(B8(((u32x4){OPS[8], OPS[9], OPS[10], OPS[11]})), B8(((u32x4){P[4], P[5], P[6], P[7]})), O);
    PSTAMP(1, 2); WSTAMP(2);
    if (r <= 20) {
        LAS f32x4* dst = (LAS f32x4*)(lds + 10240 + ((wave * 2 + hh) * 21 + r) * 48);
        dst[0] = f32x4{kvacc[0], kvacc[1], kvacc[2], kvacc[3]};
        dst[1] = f32x4{kvacc[4], kvacc[5], kvacc[6], kvacc[7]};
        dst[2] = f32x4{kvacc[8], kvacc[9], kvacc[10], kvacc[11]};
    }
    float l = hi_half(O[8]);
    {
        bool bad = !(l >= 1e-18f && l <= 3.0e38f);
#pragma unroll
        for (int i = 0; i < 12; ++i) bad = bad || !(fabsf(O[i]) <= 3.0e38f);
        if (__builtin_expect(__any(bad), 0)) {
            const bf16x8 qt0 = B8(((u32x4){Q[4], Q[5], 0u, 0u}));
            float m = -INFINITY; O = Z16;
            const int ko = (r >> 4) * 640 + ((r >> 2) & 1) * 320 + ((r >> 3) & 1) * 8 + (r & 3) * 2;
            for (int t = 0; t < 64; ++t) {
                const char* tb = kvb + t * 2560;
                unsigned kw[6];
#pragma unroll
                for (int i = 0; i < 6; ++i) {
                    const int d = i < 4 ? 8 * hh + 2 * i : 16 + 2 * (i - 4);
                    kw[i] = (unsigned)*(const u16*)(tb + ko + d * 16) | ((unsigned)*(const u16*)(tb + ko + d * 16 + 16) << 16);
                }
                u32x4 f0 = *(const u32x4*)(tb + 1280 + hh * 320 + rc * 16), f1 = *(const u32x4*)(tb + 1920 + hh * 320 + rc * 16);
                if (r >= 20) { f0 = ONES; f1 = ONES; }
                f32x16 s = MFMA32(B8(((u32x4){kw[0], kw[1], kw[2], kw[3]})), qm, Z16);
                s = MFMA32(B8(((u32x4){kw[4], kw[5], 0u, 0u})), qt0, s);
                float tm = s[0];
#pragma unroll
                for (int i = 1; i < 16; ++i) tm = fmaxf(tm, s[i]);
                tm = xhalf_max(tm);
                const float mn = fmaxf(m, tm);
                const float al = (m == -INFINITY) ? 0.f : __builtin_amdgcn_exp2f(m - mn);
                m = mn;
#pragma unroll
                for (int i = 0; i < 16; ++i) { O[i] *= al; s[i] = __builtin_amdgcn_exp2f(s[i] - mn); }
                u32x4 p0 = {pk2(s[0], s[1]), pk2(s[2], s[3]), pk2(s[4], s[5]), pk2(s[6], s[7])};
                u32x4 p1 = {pk2(s[8], s[9]), pk2(s[10], s[11]), pk2(s[12], s[13]), pk2(s[14], s[15])};
                O = MFMA32(B8(f0), B8(p0), O); O = MFMA32(B8(f1), B8(p1), O);
            }
            l = hi_half(O[8]);
        }
    }
    store_o(ws, O, (1.f - gate) / l, rt, 1, h, lane);
    const u32x2 pq0 = trd(L + wave * 1280 + tro + hh * 64), pq1 = trd(L + wave * 1280 + tro + 128 + hh * 64);
    u32x2 pq2 = trd(L + wave * 1280 + tro + 256); if (hh) pq2 = u32x2{0u, 0u};
    WSTAMP(4);
    __syncthreads();
    WSTAMP(5);
    f32x4 ks0 = {}, ks1 = {}, ks2 = {};
    if (r <= 20) {
#pragma unroll
        for (int w = 0; w < 8; ++w) {
            const LAS f32x4* src = (const LAS f32x4*)(lds + 10240 + ((w * 2 + hh) * 21 + r) * 48);
            ks0 += src[0]; ks1 += src[1]; ks2 += src[2];
        }
    }
    const u32x4 ka0 = {pk2(ks0[0], ks0[1]), pk2(ks0[2], ks0[3]), pk2(ks1[0], ks1[1]), pk2(ks1[2], ks1[3])};
    const u32x4 ka1 = {pk2(ks2[0], ks2[1]), pk2(ks2[2], ks2[3]), 0u, 0u};
    f32x16 oa = MFMA32(B8(ka0), B8(((u32x4){pq0[0], pq0[1], pq1[0], pq1[1]})), Z16);
    oa = MFMA32(B8(ka1), B8(((u32x4){pq2[0], pq2[1], 0u, 0u})), oa);
    const float den = hi_half(oa[8]) + 1e-6f;
    store_o(ws, oa, gate / den, rt, 0, h, lane);
}

__global__ __launch_bounds__(512, 2) void k2_attn(char* __restrict__ ws) {
    extern __shared__ __attribute__((aligned(16))) char lds[];
    PSTAMP(1, 0); WSTAMP(0);
    phase2(ws, lds, blockIdx.x);
}

DI void phase3(const char* __restrict__ ws, u16* __restrict__ out, char* lds, int bid) {
    const int tid = threadIdx.x, lane = tid & 63, wave = rfl(tid >> 6);
    const int xcd = bid & 7, i = bid >> 3;
    const int rb = xcd * 4 + (i & 3), cb = i >> 2;
    const int g = lane >> 4;
    const char* ap = ws + OFF_O + (size_t)(rb * 160 + wave * 10 + (g >> 1)) * 1024 + (g & 1) * 512 + (lane & 15) * 16;
    const char* bp = ws + OFF_WOT + (size_t)(wave * 5 * 40 + cb * 5) * 1024 + lane * 16;
    bf16x8 af[5][4], bfr[5][5];
#pragma unroll
    for (int k = 0; k < 5; ++k) {
#pragma unroll
        for (int ni = 0; ni < 5; ++ni) bfr[k][ni] = *(const bf16x8*)(bp + k * 40960 + ni * 1024);
#pragma unroll
        for (int mi = 0; mi < 4; ++mi) af[k][mi] = *(const bf16x8*)(ap + (mi >> 1) * 81920 + k * 2048 + (mi & 1) * 256);
    }
    __builtin_amdgcn_sched_barrier(0);
    f32x4 acc[4][5] = {};
#pragma unroll
    for (int k = 0; k < 5; ++k)
#pragma unroll
        for (int mi = 0; mi < 4; ++mi)
#pragma unroll
            for (int ni = 0; ni < 5; ++ni) acc[mi][ni] = MFMA16(bfr[k][ni], af[k][mi], acc[mi][ni]);
    LAS f32x4* red = (LAS f32x4*)lds;
#pragma unroll
    for (int mi = 0; mi < 4; ++mi)
#pragma unroll
        for (int ni = 0; ni < 5; ++ni) red[(wave * 20 + mi * 5 + ni) * 64 + lane] = acc[mi][ni];
    PSTAMP(2, 1);
    __syncthreads();
#pragma unroll
    for (int it = 0; it < 2; ++it) {
        const int id = it * 512 + tid;
        if (id < 640) {
            const int row = id / 10, pc = id - row * 10;
            const int ix = ((row >> 4) * 5 + (pc >> 1)) * 64 + (pc & 1) * 32 + (row & 15);
            f32x4 v0 = {}, v1 = {};
#pragma unroll
            for (int w = 0; w < 8; ++w) { v0 += red[w * 1280 + ix]; v1 += red[w * 1280 + ix + 16]; }
            *(u32x4*)(out + (size_t)(rb * 64 + row) * D_ + cb * 80 + pc * 8) = u32x4{pk2(v0[0], v0[1]), pk2(v0[2], v0[3]), pk2(v1[0], v1[1]), pk2(v1[2], v1[3])};
        }
    }
}

__global__ __launch_bounds__(512, 2) void k3_out(char* __restrict__ ws, u16* __restrict__ out) {
    extern __shared__ __attribute__((aligned(16))) char lds[];
    PSTAMP(2, 0);
    phase3(ws, out, lds, blockIdx.x);
}

extern "C" void kernel_launch(void* const* d_in, const int* in_sizes, int n_in, void* d_out, int out_size, void* d_ws, size_t ws_size,
                              hipStream_t stream) {
    const u16* x = (const u16*)d_in[0];
    const u16* wA = (const u16*)d_in[1];
    const u16* woA = (const u16*)d_in[2];
    const u16* wB = (const u16*)d_in[4];
    const u16* woB = (const u16*)d_in[5];
    const u16* gw = (const u16*)d_in[6];
    char* ws = (char*)d_ws;
    (void)hipFuncSetAttribute((const void*)k1_qkv, hipFuncAttributeMaxDynamicSharedMemorySize, LDS_BYTES);
    (void)hipFuncSetAttribute((const void*)k2_attn, hipFuncAttributeMaxDynamicSharedMemorySize, LDS_BYTES);
    (void)hipFuncSetAttribute((const void*)k3_out, hipFuncAttributeMaxDynamicSharedMemorySize, LDS_BYTES);
    k1_qkv<<<256, 512, LDS_BYTES, stream>>>(x, wA, wB, woA, woB, gw, ws);
    k2_attn<<<256, 512, LDS_BYTES, stream>>>(ws);
    k3_out<<<256, 512, LDS_BYTES, stream>>>(ws, (u16*)d_out);
}
```

```cpp
#include <hip/hip_runtime.h>
#include <hip/hip_bf16.h>
#include <stdint.h>

typedef short    bf16x8 __attribute__((ext_vector_type(8)));
typedef short    s16x4  __attribute__((ext_vector_type(4)));
typedef float    f32x2  __attribute__((ext_vector_type(2)));
typedef float    f32x4  __attribute__((ext_vector_type(4)));
typedef float    f32x16 __attribute__((ext_vector_type(16)));
typedef float    f32x32 __attribute__((ext_vector_type(32)));
typedef unsigned u32x2  __attribute__((ext_vector_type(2)));
typedef unsigned u32x4  __attribute__((ext_vector_type(4)));
typedef unsigned u32x8  __attribute__((ext_vector_type(8)));
typedef unsigned u32x32 __attribute__((ext_vector_type(32)));
typedef __bf16   bf2_t  __attribute__((ext_vector_type(2)));
typedef unsigned short u16;
#define LAS __attribute__((address_space(3)))
#define DI __device__ __forceinline__

constexpr int S_ = 2048, D_ = 640;
constexpr size_t IMG     = (size_t)32 * 163840;
constexpr size_t OFF_IQ  = 0;
constexpr size_t OFF_IB  = IMG;
constexpr size_t OFF_IA  = 2 * IMG;
constexpr size_t OFF_O   = 3 * IMG;
constexpr size_t OFF_WOT = 4 * IMG;
constexpr size_t OFF_G   = OFF_WOT + (size_t)1600 * 1024;

#define PSTAMP(k, i)
#define WSTAMP(i)

DI unsigned pk2(float lo, float hi) { f32x2 v = {lo, hi}; return __builtin_bit_cast(unsigned, __builtin_convertvector(v, bf2_t)); }
DI float bflo(unsigned v) { return __uint_as_float(v << 16); }
DI float bfhi(unsigned v) { return __uint_as_float(v & 0xffff0000u); }
DI int rfl(int x) { return __builtin_amdgcn_readfirstlane(x); }
DI void glds16(const void* sbase, unsigned voff, unsigned lds_base) {
    const unsigned long long a = (unsigned long long)(uintptr_t)sbase;
    const unsigned long long u = (unsigned long long)(unsigned)rfl((int)(unsigned)a) | ((unsigned long long)(unsigned)rfl((int)(unsigned)(a >> 32)) << 32);
    asm volatile("s_mov_b32 m0, %0\n\ts_nop 0\n\tglobal_load_lds_dwordx4 %1, %2" :: "s"(rfl((int)lds_base)), "v"(voff), "s"(u) : "memory");
}
#define MFMA16(a, b, c) __builtin_amdgcn_mfma_f32_16x16x32_bf16((a), (b), (c), 0, 0, 0)
#define MFMA32(a, b, c) __builtin_amdgcn_mfma_f32_32x32x16_bf16((a), (b), (c), 0, 0, 0)
#define B8(x) __builtin_bit_cast(bf16x8, x)
DI u32x2 trd(const LAS char* p) { return __builtin_bit_cast(u32x2, __builtin_amdgcn_ds_read_tr16_b64_v4i16((LAS s16x4*)p)); }
DI float phi(float t) { return (t > 0.f ? t + 1.f : __expf(t)) + 1e-6f; }
template <int N> DI void wait_vm_bar() { asm volatile("s_waitcnt vmcnt(%0) lgkmcnt(0)\n\ts_barrier" :: "n"(N) : "memory"); }

constexpr int LDS_BYTES = 163840;

DI void p1_aux(const u16* __restrict__ x, const u16* __restrict__ woA, const u16* __restrict__ woB, const u16* __restrict__ gate_w,
               char* __restrict__ ws, char* lds, int a) {
    const int tid = threadIdx.x, lane = tid & 63, wave = rfl(tid >> 6);
    const unsigned lds0 = (unsigned)(uintptr_t)lds;
    const int br = a >> 3, u = a & 7;
    const char* wsrc = (const char*)(br ? woB : woA) + (size_t)u * 102400;
    for (int i = wave; i < 100; i += 8) glds16(wsrc + i * 1024, lane * 16, lds0 + i * 1024);
    const u32x4* xs = (const u32x4*)(x + (size_t)(a * 128 + wave * 16) * D_);
    u32x4 xv[20], gw[5];
#pragma unroll
    for (int i = 0; i < 20; ++i) xv[i] = xs[i * 64 + lane];
#pragma unroll
    for (int i = 0; i < 5; ++i) gw[i] = ((const u32x4*)gate_w)[(i * 64 + lane) % 80];
    LAS float* part = (LAS float*)(lds + 102400) + wave * 1280;
#pragma unroll
    for (int i = 0; i < 20; ++i) {
        float s = 0.f;
#pragma unroll
        for (int j = 0; j < 4; ++j) s += bflo(xv[i][j]) * bflo(gw[i % 5][j]) + bfhi(xv[i][j]) * bfhi(gw[i % 5][j]);
        part[i * 64 + lane] = s;
    }
    wait_vm_bar<0>();
    {
        float s = 0.f;
#pragma unroll
        for (int j = 0; j < 20; ++j) s += part[lane * 20 + j];
        s += __shfl_xor(s, 1); s += __shfl_xor(s, 2);
        if ((lane & 3) == 0) ((float*)(ws + OFF_G))[a * 128 + wave * 16 + (lane >> 2)] = 1.f / (1.f + __expf(-s));
    }
    const int g = lane >> 4, hh = g & 1, ns = g >> 1, q = (lane >> 2) & 3, p = lane & 3;
    for (int un = wave; un < 100; un += 8) {
        const int e = un / 20, np = un - e * 20, nt = np * 2 + ns;
        const int r0 = e < 4 ? e * 20 + 4 * hh : (2 * hh) * 20 + 16;
        const int r1 = e < 4 ? r0 + 8 : r0 + 20;
        const int ks16 = br * 40 + (e < 4 ? u * 4 + e : 32 + u);
        const u32x2 lo = trd((const LAS char*)lds + (r0 + q) * 1280 + nt * 32 + p * 8);
        const u32x2 hi = trd((const LAS char*)lds + (r1 + q) * 1280 + nt * 32 + p * 8);
        *(u32x4*)(ws + OFF_WOT + (size_t)((ks16 >> 1) * 40 + nt) * 1024 + ((ks16 & 1) * 2 + hh) * 256 + (lane & 15) * 16) = u32x4{lo[0], lo[1], hi[0], hi[1]};
    }
}

constexpr int g1_issued(int t) {
    int n = 10;
    for (int b = 1; b < t; ++b) { if (!(b & 1) && b / 2 + 2 <= 9) ++n; if (b + 6 <= 19) ++n; }
    return n;
}
constexpr int g1_posX(int u) { return u < 3 ? 3 * u : g1_issued(2 * (u - 2)); }
constexpr int g1_posW(int t) { return t < 7 ? (t < 6 ? 3 * (t / 2) + 1 + (t & 1) : 9) : g1_issued(t - 6) + ((!((t - 6) & 1) && (t - 6) / 2 + 2 <= 9) ? 1 : 0); }
constexpr int g1_wait(int t) { const int p = g1_posX(t >> 1) > g1_posW(t) ? g1_posX(t >> 1) : g1_posW(t); return 4 * (g1_issued(t) - 1 - p); }
constexpr int G1_WOFF = 49152;

template <int T> struct G1Load {
    template <class FX, class FW> static DI void run(FX&& ldx, FW&& ldw) {
        asm volatile("s_waitcnt vmcnt(%0)\n\ts_barrier" :: "n"(g1_wait(T)) : "memory");
        if (T >= 1) {
            if (!(T & 1) && T / 2 + 2 <= 9) ldx(T / 2 + 2);
            if (T + 6 <= 19) ldw(T + 6);
        }
        G1Load<T + 1>::run(ldx, ldw);
    }
};
template <> struct G1Load<20> { template <class FX, class FW> static DI void run(FX&&, FW&&) {} };

struct G1Frag { bf16x8 x[2]; u32x2 w[4][2]; };
#define SB0() __builtin_amdgcn_sched_barrier(0)
template <int J> struct G1Step {
    static DI void run(f32x16 (&acc)[8], G1Frag& cur, G1Frag& nxt, const LAS char* const (&xa)[4], const LAS char* const (&wa)[4]) {
        constexpr int N = J + 1, t = N >> 1, xo = ((N >> 2) % 3) * 16384, sl = t % 7, wo = (sl & 3) * 16384 + (N & 1) * 4096, wi = (sl >> 2) * 2;
#define G1_M(i) acc[i] = MFMA32(cur.x[(i) & 1], B8(((u32x4){cur.w[(i) >> 1][0][0], cur.w[(i) >> 1][0][1], cur.w[(i) >> 1][1][0], cur.w[(i) >> 1][1][1]})), acc[i]); SB0();
#define G1_X(i) if (N < 40) nxt.x[i] = *(const LAS bf16x8*)(xa[N & 3] + xo + (i) * 4096);
#define G1_W(i) if (N < 40) { nxt.w[i][0] = trd(wa[wi + ((i) & 1)] + wo + ((i) >> 1) * 8192); nxt.w[i][1] = trd(wa[wi + ((i) & 1)] + wo + ((i) >> 1) * 8192 + 1024); } SB0();
        if (J & 1) {
            G1_M(0) G1_M(1)
            if (N < 40) asm volatile("s_waitcnt lgkmcnt(0)\n\ts_barrier" ::: "memory");
            SB0();
            G1_X(0) G1_W(0) G1_M(2) G1_X(1) G1_W(1) G1_M(3) G1_W(2) G1_M(4) G1_W(3) G1_M(5) G1_M(6) G1_M(7)
        } else {
            G1_M(0) G1_X(0) G1_W(0) G1_M(1) G1_X(1) G1_W(1) G1_M(2) G1_W(2) G1_M(3) G1_W(3) G1_M(4) G1_M(5) G1_M(6) G1_M(7)
        }
        G1Step<J + 1>::run(acc, nxt, cur, xa, wa);
    }
};
template <> struct G1Step<40> { static DI void run(f32x16 (&)[8], G1Frag&, G1Frag&, const LAS char* const (&)[4], const LAS char* const (&)[4]) {} };

DI void phase1(const u16* __restrict__ x, const u16* __restrict__ wA, const u16* __restrict__ wB, char* __restrict__ ws, char* lds, int bid) {
    const int tid = threadIdx.x, lane = tid & 63, wave = rfl(tid >> 6);
    const unsigned lds0 = (unsigned)(uintptr_t)lds;
    const int xcd = bid & 7, j = (xcd >> 1) * 30 + (bid >> 3);
    const int ct = j >> 3, mt = (xcd & 1) * 8 + (j & 7);
    const int type = rfl(ct / 5), rot = rfl((3 * ct + mt) % 10);
    if (wave >= 4) {
        const int lw = wave - 4;
        unsigned xo[4], wo[2];
#pragma unroll
        for (int i = 0; i < 4; ++i) { const int row = (lw * 4 + i) * 8 + (lane >> 3); xo[i] = (unsigned)((mt * 128 + row) * 1280 + (((lane & 7) ^ ((row >> 1) & 7)) << 4)); }
#pragma unroll
        for (int i = 0; i < 2; ++i) { const int k = (lw * 2 + i) * 4 + (lane >> 4), s16 = lane & 15;
            wo[i] = (unsigned)(k * 3840 + ct * 256 + ((((s16 >> 2) ^ (k & 3)) << 6) | ((s16 & 3) << 4))); }
        auto ldx = [&](int u) {
            int kk = u + rot; kk -= kk >= 10 ? 10 : 0;
            const unsigned slot = lds0 + (u % 3) * 16384 + lw * 4096;
#pragma unroll
            for (int i = 0; i < 4; ++i) glds16((const char*)x + kk * 128, xo[i], slot + i * 1024);
        };
        auto ldw = [&](int t) {
            int kk = (t >> 1) + rot; kk -= kk >= 10 ? 10 : 0;
            const unsigned slot = lds0 + G1_WOFF + (t % 7) * 16384 + lw * 2048;
            const int go = (kk * 64 + (t & 1) * 32) * 3840;
            glds16((const char*)wB + go, wo[0], slot);        glds16((const char*)wB + go, wo[1], slot + 1024);
            glds16((const char*)wA + go, wo[0], slot + 8192); glds16((const char*)wA + go, wo[1], slot + 8192 + 1024);
        };
        ldx(0); ldw(0); ldw(1); ldx(1); ldw(2); ldw(3); ldx(2); ldw(4); ldw(5); ldw(6);
        G1Load<0>::run(ldx, ldw);
        return;
    }
    const int wm = wave & 1, wn = wave >> 1;
    const int r = lane & 31, hh = lane >> 5, g = lane >> 4, q = (lane >> 2) & 3, p = lane & 3;
    const LAS char* xa[4]; const LAS char* wa[4];
#pragma unroll
    for (int ks = 0; ks < 4; ++ks) { xa[ks] = (const LAS char*)lds + (wm * 64 + r) * 128 + (((ks * 2 + hh) ^ ((r >> 1) & 7)) << 4); asm volatile("" : "+v"(xa[ks])); }
#pragma unroll
    for (int i = 0; i < 4; ++i) { wa[i] = (const LAS char*)lds + G1_WOFF + (i >> 1) * 65536 + (8 * (g >> 1) + q) * 256 + (((wn * 2 + (i & 1)) ^ q) << 6) + (g & 1) * 32 + p * 8;
        asm volatile("" : "+v"(wa[i])); }
    f32x16 acc[8] = {};
    G1Frag f0, f1;
    __builtin_amdgcn_s_setprio(2);
    asm volatile("s_barrier" ::: "memory");
    f0.x[0] = *(const LAS bf16x8*)(xa[0]); f0.x[1] = *(const LAS bf16x8*)(xa[0] + 4096);
#pragma unroll
    for (int i = 0; i < 4; ++i) { f0.w[i][0] = trd(wa[i & 1] + (i >> 1) * 8192); f0.w[i][1] = trd(wa[i & 1] + (i >> 1) * 8192 + 1024); }
    SB0();
    G1Step<0>::run(acc, f0, f1, xa, wa);
    constexpr float QS = 0.22360679774997896f * 1.4426950408889634f;
#pragma unroll
    for (int i = 0; i < 8; ++i) {
        const int mi = i & 1, jn = (i >> 1) & 1, pass = i >> 2;
        const size_t ib = type == 0 ? OFF_IQ + pass * 1280 : (pass ? OFF_IA : OFF_IB) + (type - 1) * 1280;
        const int c = (ct - type * 5) * 128 + wn * 64 + jn * 32 + r, h = c / 20, d = c - h * 20;
        f32x16 v = acc[i];
        if (pass == 0) { if (type == 0) v *= QS; }
        else if (type < 2) {
#pragma unroll
            for (int e = 0; e < 16; ++e) v[e] = phi(v[e]);
        }
        char* dst = ws + ib + (size_t)h * 163840 + (mt * 4 + wm * 2 + mi) * 2560 + hh * 320 + d * 16;
        *(u32x4*)(dst)       = u32x4{pk2(v[0], v[1]), pk2(v[2], v[3]), pk2(v[4], v[5]), pk2(v[6], v[7])};
        *(u32x4*)(dst + 640) = u32x4{pk2(v[8], v[9]), pk2(v[10], v[11]), pk2(v[12], v[13]), pk2(v[14], v[15])};
    }
}

__global__ __launch_bounds__(512, 2) void k1_qkv(const u16* __restrict__ x, const u16* __restrict__ wA, const u16* __restrict__ wB,
                                                 const u16* __restrict__ woA, const u16* __restrict__ woB, const u16* __restrict__ gate_w,
                                                 char* __restrict__ ws) {
    extern __shared__ __attribute__((aligned(16))) char lds[];
    PSTAMP(0, 0);
    if (blockIdx.x >= 240) p1_aux(x, woA, woB, gate_w, ws, lds, blockIdx.x - 240);
    else phase1(x, wA, wB, ws, lds, blockIdx.x);
}

#define STR(x) #x
#define XS(x) STR(x)
#define A_E(i) "v_exp_f32 v" XS(i) ", v" XS(i) "\n"
#define A_C(d, a, b) "v_cvt_pk_bf16_f32 v" XS(d) ", v" XS(a) ", v" XS(b) "\n"
#define A_G(a, b, c, d, p0, p1) A_E(a) A_E(b) A_E(c) A_E(d) A_C(p0, a, b) A_C(p1, c, d)
#define A_M(d, a, b, c) "v_mfma_f32_32x32x16_bf16 " d ", " a ", " b ", " c "\n"
#define A_STEP(C0,C1,C2,C3,C4,C5,C6,C7,C8,C9,C10,C11,C12,C13,C14,C15, SN, KM, KT, V1, V0, NKMa, NKMb, NKT, NV1, NV0, OK0, OK1, OV1, OV0, PRIO) \
    "s_waitcnt lgkmcnt(0)\n" \
    A_M(SN, KM, "v[120:123]", "0") \
    "ds_read_b64_tr_b16 " NKMa ", v128 offset:" XS(OK0) "\n" \
    "ds_read_b64_tr_b16 " NKMb ", v128 offset:" XS(OK1) "\n" \
    "ds_read_b64_tr_b16 " NKT ", v129 offset:" XS(OK0) "\n" \
    A_G(C0, C1, C2, C3, 112, 113) \
    A_M("v[64:79]", V1, "v[116:119]", "v[64:79]") \
    "s_mov_b64 exec, %[vm]\n" \
    "ds_read_b128 " NV1 ", v130 offset:" XS(OV1) "\n" \
    "ds_read_b128 " NV0 ", v130 offset:" XS(OV0) "\n" \
    "s_mov_b64 exec, -1\n" \
    A_G(C4, C5, C6, C7, 114, 115) \
    A_M(SN, KT, "v[124:127]", SN) \
    A_G(C8, C9, C10, C11, 116, 117) \
    A_M("v[64:79]", V0, "v[112:115]", "v[64:79]") \
    "s_setprio " XS(PRIO) "\n" \
    A_G(C12, C13, C14, C15, 118, 119)
#define A_EVEN(OK0, OK1, OV1, OV0, PRIO) A_STEP(32,33,34,35,36,37,38,39,40,41,42,43,44,45,46,47, "v[48:63]", "v[80:83]", "v[84:87]", "v[88:91]", "v[92:95]", \
    "v[96:97]", "v[98:99]", "v[100:101]", "v[104:107]", "v[108:111]", OK0, OK1, OV1, OV0, PRIO)
#define A_ODD(OK0, OK1, OV1, OV0, PRIO) A_STEP(48,49,50,51,52,53,54,55,56,57,58,59,60,61,62,63, "v[32:47]", "v[96:99]", "v[100:103]", "v[104:107]", "v[108:111]", \
    "v[80:81]", "v[82:83]", "v[84:85]", "v[88:91]", "v[92:95]", OK0, OK1, OV1, OV0, PRIO)

DI void attn_chunk(f32x32& S, f32x16& O, u32x32& OPS, u32x8& P, u32x4& AD, const u32x8& Q, unsigned long long vmask) {
    asm volatile(
        "s_nop 7\n"
        A_EVEN(5120, 5184, 640, 2560, 2)     A_ODD(7680, 7744, 3200, 5120, 1)
        A_EVEN(10240, 10304, 5760, 7680, 0)  A_ODD(12800, 12864, 8320, 10240, 3)
        A_EVEN(15360, 15424, 10880, 12800, 2) A_ODD(17920, 17984, 13440, 15360, 1)
        A_EVEN(20480, 20544, 16000, 17920, 0) A_ODD(23040, 23104, 18560, 20480, 3)
        "v_add_u32 v128, 0x5000, v128\n v_add_u32 v129, 0x5000, v129\n v_add_u32 v130, 0x5000, v130\n"
        "s_waitcnt lgkmcnt(0)\n s_nop 4\n"
        : "+{v[32:63]}"(S), "+{v[64:79]}"(O), "+{v[80:111]}"(OPS), "+{v[112:119]}"(P), "+{v[128:131]}"(AD)
        : "{v[120:127]}"(Q), [vm] "s"(vmask)
        : "memory");
}

DI float xhalf_max(float m) { auto rr = __builtin_amdgcn_permlane32_swap(__float_as_uint(m), __float_as_uint(m), false, false);
    return fmaxf(__uint_as_float(rr[0]), __uint_as_float(rr[1])); }
DI float hi_half(float v) {
    auto rr = __builtin_amdgcn_permlane32_swap(__float_as_uint(v), __float_as_uint(v), false, false);
    return __uint_as_float(rr[1]); }
DI void store_o(char* ws, const f32x16& o, float f, int rt, int br, int h, int lane) {
    char* rb = ws + OFF_O + (size_t)(rt * 80 + br * 40) * 1024;
    *(u32x4*)(rb + h * 1024 + lane * 16) = u32x4{pk2(o[0] * f, o[1] * f), pk2(o[2] * f, o[3] * f), pk2(o[4] * f, o[5] * f), pk2(o[6] * f, o[7] * f)};
    if (lane < 32) *(u32x2*)(rb + (32 + (h >> 2)) * 1024 + ((h >> 1) & 1) * 512 + lane * 16 + (h & 1) * 8) = u32x2{pk2(o[8] * f, o[9] * f), pk2(o[10] * f, o[11] * f)};
}

DI void phase2(char* __restrict__ ws, char* lds, int bid) {
    const int tid = threadIdx.x, lane = tid & 63, wave = rfl(tid >> 6);
    const int h = (bid & 7) * 4 + ((bid >> 3) & 3), qb = bid >> 5;
    const int r = lane & 31, hh = lane >> 5;
    const unsigned lds0 = (unsigned)(uintptr_t)lds;
    const char* kvb = ws + OFF_IB + (size_t)h * 163840;
    const char* kva = ws + OFF_IA + (size_t)h * 163840;
    const char* qim = ws + OFF_IQ + (size_t)h * 163840 + qb * 20480;
    const int rt = qb * 8 + wave;
    unsigned qo[2];
#pragma unroll
    for (int i = 0; i < 2; ++i) { const int pc = i * 512 + tid, t = pc / 80; qo[i] = (unsigned)(t * 2560 + (pc - t * 80) * 16); }
    glds16(qim, qo[0], lds0 + 153600 + wave * 1024);
    if (wave < 2) glds16(qim, qo[1], lds0 + 153600 + 8192 + wave * 1024);
#pragma unroll
    for (int i = 0; i < 3; ++i) glds16(kvb + (i * 8 + wave) * 1024, lane * 16, lds0 + (i * 8 + wave) * 1024);
    if (wave == 2) glds16(kvb + 24 * 1024, lane * 16, lds0 + 24 * 1024);
    const float gate = ((const float*)(ws + OFF_G))[rt * 32 + r];
    asm volatile("" ::: "memory");
#pragma unroll
    for (int i = 0; i < 16; ++i) { int pc = 25 + i * 8 + wave; pc = pc < 150 ? pc : pc - 8; glds16(kvb + pc * 1024, lane * 16, lds0 + pc * 1024); }
    wait_vm_bar<16>();
    PSTAMP(1, 1); WSTAMP(1);
    const int g = lane >> 4, q = (lane >> 2) & 3, p = lane & 3;
    const int tro = (g & 1) * 640 + (p & 1) * 320 + q * 16 + (p >> 1) * 8;
    const LAS char* L = (const LAS char*)lds;
    const LAS char* kmp = L + tro + hh * 128;
    const LAS char* ktp = L + tro + 256;
    const LAS char* vp  = L + 1280 + hh * 320 + r * 16;
    u32x8 Q;
    {
        const u32x2 a = trd(kmp + 153600 + wave * 1280), b = trd(kmp + 153600 + wave * 1280 + 64), c = trd(ktp + 153600 + wave * 1280);
        Q[0] = a[0]; Q[1] = a[1]; Q[2] = b[0]; Q[3] = b[1]; Q[4] = hh ? 0u : c[0]; Q[5] = hh ? 0u : c[1]; Q[6] = 0u; Q[7] = 0u;
    }
    const f32x16 Z16 = {};
    const bf16x8 qm = B8(((u32x4){Q[0], Q[1], Q[2], Q[3]}));
    f32x32 S; f32x16 O = {}; u32x32 OPS; u32x8 P = {}; u32x4 AD;
    {
        const u32x2 a = trd(kmp), b = trd(kmp + 64), c = trd(ktp);
        const bf16x8 k0 = B8(((u32x4){a[0], a[1], b[0], b[1]})), k1 = B8(((u32x4){c[0], c[1], 0x3F80u, 0u}));
        const f32x16 sm = MFMA32(k0, qm, Z16);
        f32x16 s0 = MFMA32(k1, B8(((u32x4){Q[4], Q[5], 0u, 0u})), sm);
        float m = s0[0];
#pragma unroll
        for (int i = 1; i < 16; ++i) m = fmaxf(m, s0[i]);
        m = xhalf_max(m);
        Q[6] = hh ? 0u : (pk2(-m, 0.f) & 0xffffu);
        s0 = MFMA32(k1, B8(((u32x4){Q[4], Q[5], Q[6], 0u})), sm);
#pragma unroll
        for (int i = 0; i < 16; ++i) { S[i] = s0[i]; S[16 + i] = 0.f; }
        const unsigned vc = r == 20 ? 0x3F803F80u : 0u;
#pragma unroll
        for (int i = 0; i < 32; ++i) OPS[i] = (i & 8) ? vc : 0u;
        const u32x2 a1 = trd(kmp + 2560), b1 = trd(kmp + 2560 + 64), c1 = trd(ktp + 2560);
        OPS[0] = a1[0]; OPS[1] = a1[1]; OPS[2] = b1[0]; OPS[3] = b1[1]; OPS[4] = c1[0]; OPS[5] = c1[1]; OPS[6] = 0x3F80u; OPS[22] = 0x3F80u;
        if (r < 20) { const u32x4 v0 = *(const LAS u32x4*)vp; OPS[12] = v0[0]; OPS[13] = v0[1]; OPS[14] = v0[2]; OPS[15] = v0[3]; }
        AD[0] = (unsigned)(uintptr_t)kmp; AD[1] = (unsigned)(uintptr_t)ktp; AD[2] = (unsigned)(uintptr_t)vp; AD[3] = 0u;
    }
    const unsigned long long vmask = 0x000FFFFF000FFFFFull;
    const u32x4 ONES = {0x3F803F80u, 0x3F803F80u, 0x3F803F80u, 0x3F803F80u};
    const int rc = r < 20 ? r : 19;
    f32x16 kvacc = {};
    for (int c = 0; c < 8; ++c) {
        const char* ap = kva + (c * 8 + wave) * 2560 + hh * 320 + rc * 16;
        u32x4 a0 = *(const u32x4*)(ap), a1 = *(const u32x4*)(ap + 640), b0 = *(const u32x4*)(ap + 1280), b1 = *(const u32x4*)(ap + 1920);
        if (c == 7) wait_vm_bar<4>();
        attn_chunk(S, O, OPS, P, AD, Q, vmask);
        if (r >= 20) { a0 = u32x4{0u, 0u, 0u, 0u}; a1 = a0; b0 = ONES; b1 = ONES; }
        kvacc = MFMA32(B8(a0), B8(b0), kvacc); kvacc = MFMA32(B8(a1), B8(b1), kvacc);
        if (c == 0) {
            wait_vm_bar<0>();
            glds16(kvb + (150 + wave) * 1024, lane * 16, lds0 + (150 + wave) * 1024);
            if (wave < 2) glds16(kvb + (158 + wave) * 1024, lane * 16, lds0 + (158 + wave) * 1024);
            glds16(qim + 1280, qo[0], lds0 + wave * 1024);
            if (wave < 2) glds16(qim + 1280, qo[1], lds0 + 8192 + wave * 1024);
        }
    }
    __builtin_amdgcn_s_setprio(0);
    O = MFMA32(B8(((u32x4){OPS[8], OPS[9], OPS[10], OPS[11]})), B8(((u32x4){P[4], P[5], P[6], P[7]})), O);
    PSTAMP(1, 2); WSTAMP(2);
    if (r <= 20) {
        LAS f32x4* dst = (LAS f32x4*)(lds + 10240 + ((wave * 2 + hh) * 21 + r) * 48);
        dst[0] = f32x4{kvacc[0], kvacc[1], kvacc[2], kvacc[3]};
        dst[1] = f32x4{kvacc[4], kvacc[5], kvacc[6], kvacc[7]};
        dst[2] = f32x4{kvacc[8], kvacc[9], kvacc[10], kvacc[11]};
    }
    float l = hi_half(O[8]);
    {
        bool bad = !(l >= 1e-18f && l <= 3.0e38f);
#pragma unroll
        for (int i = 0; i < 12; ++i) bad = bad || !(fabsf(O[i]) <= 3.0e38f);
        if (__builtin_expect(__any(bad), 0)) {
            const bf16x8 qt0 = B8(((u32x4){Q[4], Q[5], 0u, 0u}));
            float m = -INFINITY; O = Z16;
            const int ko = (r >> 4) * 640 + ((r >> 2) & 1) * 320 + ((r >> 3) & 1) * 8 + (r & 3) * 2;
            for (int t = 0; t < 64; ++t) {
                const char* tb = kvb + t * 2560;
                unsigned kw[6];
#pragma unroll
                for (int i = 0; i < 6; ++i) {
                    const int d = i < 4 ? 8 * hh + 2 * i : 16 + 2 * (i - 4);
                    kw[i] = (unsigned)*(const u16*)(tb + ko + d * 16) | ((unsigned)*(const u16*)(tb + ko + d * 16 + 16) << 16);
                }
                u32x4 f0 = *(const u32x4*)(tb + 1280 + hh * 320 + rc * 16), f1 = *(const u32x4*)(tb + 1920 + hh * 320 + rc * 16);
                if (r >= 20) { f0 = ONES; f1 = ONES; }
                f32x16 s = MFMA32(B8(((u32x4){kw[0], kw[1], kw[2], kw[3]})), qm, Z16);
                s = MFMA32(B8(((u32x4){kw[4], kw[5], 0u, 0u})), qt0, s);
                float tm = s[0];
#pragma unroll
                for (int i = 1; i < 16; ++i) tm = fmaxf(tm, s[i]);
                tm = xhalf_max(tm);
                const float mn = fmaxf(m, tm);
                const float al = (m == -INFINITY) ? 0.f : __builtin_amdgcn_exp2f(m - mn);
                m = mn;
#pragma unroll
                for (int i = 0; i < 16; ++i) { O[i] *= al; s[i] = __builtin_amdgcn_exp2f(s[i] - mn); }
                u32x4 p0 = {pk2(s[0], s[1]), pk2(s[2], s[3]), pk2(s[4], s[5]), pk2(s[6], s[7])};
                u32x4 p1 = {pk2(s[8], s[9]), pk2(s[10], s[11]), pk2(s[12], s[13]), pk2(s[14], s[15])};
                O = MFMA32(B8(f0), B8(p0), O); O = MFMA32(B8(f1), B8(p1), O);
            }
            l = hi_half(O[8]);
        }
    }
    store_o(ws, O, (1.f - gate) / l, rt, 1, h, lane);
    const u32x2 pq0 = trd(L + wave * 1280 + tro + hh * 64), pq1 = trd(L + wave * 1280 + tro + 128 + hh * 64);
    u32x2 pq2 = trd(L + wave * 1280 + tro + 256); if (hh) pq2 = u32x2{0u, 0u};
    WSTAMP(4);
    __syncthreads();
    WSTAMP(5);
    f32x4 ks0 = {}, ks1 = {}, ks2 = {};
    if (r <= 20) {
#pragma unroll
        for (int w = 0; w < 8; ++w) {
            const LAS f32x4* src = (const LAS f32x4*)(lds + 10240 + ((w * 2 + hh) * 21 + r) * 48);
            ks0 += src[0]; ks1 += src[1]; ks2 += src[2];
        }
    }
    const u32x4 ka0 = {pk2(ks0[0], ks0[1]), pk2(ks0[2], ks0[3]), pk2(ks1[0], ks1[1]), pk2(ks1[2], ks1[3])};
    const u32x4 ka1 = {pk2(ks2[0], ks2[1]), pk2(ks2[2], ks2[3]), 0u, 0u};
    f32x16 oa = MFMA32(B8(ka0), B8(((u32x4){pq0[0], pq0[1], pq1[0], pq1[1]})), Z16);
    oa = MFMA32(B8(ka1), B8(((u32x4){pq2[0], pq2[1], 0u, 0u})), oa);
    const float den = hi_half(oa[8]) + 1e-6f;
    store_o(ws, oa, gate / den, rt, 0, h, lane);
}

__global__ __launch_bounds__(512, 2) void k2_attn(char* __restrict__ ws) {
    extern __shared__ __attribute__((aligned(16))) char lds[];
    PSTAMP(1, 0); WSTAMP(0);
    phase2(ws, lds, blockIdx.x);
}

DI void phase3(const char* __restrict__ ws, u16* __restrict__ out, char* lds, int bid) {
    const int tid = threadIdx.x, lane = tid & 63, wave = rfl(tid >> 6);
    const int xcd = bid & 7, i = bid >> 3;
    const int rb = xcd * 4 + (i & 3), cb = i >> 2;
    const int g = lane >> 4;
    const char* ap = ws + OFF_O + (size_t)(rb * 160 + wave * 10 + (g >> 1)) * 1024 + (g & 1) * 512 + (lane & 15) * 16;
    const char* bp = ws + OFF_WOT + (size_t)(wave * 5 * 40 + cb * 5) * 1024 + lane * 16;
    bf16x8 af[5][4], bfr[5][5];
#pragma unroll
    for (int k = 0; k < 5; ++k) {
#pragma unroll
        for (int ni = 0; ni < 5; ++ni) bfr[k][ni] = *(const bf16x8*)(bp + k * 40960 + ni * 1024);
#pragma unroll
        for (int mi = 0; mi < 4; ++mi) af[k][mi] = *(const bf16x8*)(ap + (mi >> 1) * 81920 + k * 2048 + (mi & 1) * 256);
    }
    __builtin_amdgcn_sched_barrier(0);
    f32x4 acc[4][5] = {};
#pragma unroll
    for (int k = 0; k < 5; ++k)
#pragma unroll
        for (int mi = 0; mi < 4; ++mi)
#pragma unroll
            for (int ni = 0; ni < 5; ++ni) acc[mi][ni] = MFMA16(bfr[k][ni], af[k][mi], acc[mi][ni]);
    LAS f32x4* red = (LAS f32x4*)lds;
#pragma unroll
    for (int mi = 0; mi < 4; ++mi)
#pragma unroll
        for (int ni = 0; ni < 5; ++ni) red[(wave * 20 + mi * 5 + ni) * 64 + lane] = acc[mi][ni];
    PSTAMP(2, 1);
    __syncthreads();
#pragma unroll
    for (int it = 0; it < 2; ++it) {
        const int id = it * 512 + tid;
        if (id < 640) {
            const int row = id / 10, pc = id - row * 10;
            const int ix = ((row >> 4) * 5 + (pc >> 1)) * 64 + (pc & 1) * 32 + (row & 15);
            f32x4 v0 = {}, v1 = {};
#pragma unroll
            for (int w = 0; w < 8; ++w) { v0 += red[w * 1280 + ix]; v1 += red[w * 1280 + ix + 16]; }
            *(u32x4*)(out + (size_t)(rb * 64 + row) * D_ + cb * 80 + pc * 8) = u32x4{pk2(v0[0], v0[1]), pk2(v0[2], v0[3]), pk2(v1[0], v1[1]), pk2(v1[2], v1[3])};
        }
    }
}

__global__ __launch_bounds__(512, 2) void k3_out(char* __restrict__ ws, u16* __restrict__ out) {
    extern __shared__ __attribute__((aligned(16))) char lds[];
    PSTAMP(2, 0);
    phase3(ws, out, lds, blockIdx.x);
}

extern "C" void kernel_launch(void* const* d_in, const int* in_sizes, int n_in, void* d_out, int out_size, void* d_ws, size_t ws_size,
                              hipStream_t stream) {
    const u16* x = (const u16*)d_in[0];
    const u16* wA = (const u16*)d_in[1];
    const u16* woA = (const u16*)d_in[2];
    const u16* wB = (const u16*)d_in[4];
    const u16* woB = (const u16*)d_in[5];
    const u16* gw = (const u16*)d_in[6];
    char* ws = (char*)d_ws;
    (void)hipFuncSetAttribute((const void*)k1_qkv, hipFuncAttributeMaxDynamicSharedMemorySize, LDS_BYTES);
    (void)hipFuncSetAttribute((const void*)k2_attn, hipFuncAttributeMaxDynamicSharedMemorySize, LDS_BYTES);
    (void)hipFuncSetAttribute((const void*)k3_out, hipFuncAttributeMaxDynamicSharedMemorySize, LDS_BYTES);
    k1_qkv<<<256, 512, LDS_BYTES, stream>>>(x, wA, wB, woA, woB, gw, ws);
    k2_attn<<<256, 512, LDS_BYTES, stream>>>(ws);
    k3_out<<<256, 512, LDS_BYTES, stream>>>(ws, (u16*)d_out);
}
```

```cpp
#define G1_ST 1
#define G1_ROT xcd
#include <hip/hip_runtime.h>
#include <hip/hip_bf16.h>
#include <stdint.h>

typedef short    bf16x8 __attribute__((ext_vector_type(8)));
typedef short    s16x4  __attribute__((ext_vector_type(4)));
typedef float    f32x2  __attribute__((ext_vector_type(2)));
typedef float    f32x4  __attribute__((ext_vector_type(4)));
typedef float    f32x16 __attribute__((ext_vector_type(16)));
typedef float    f32x32 __attribute__((ext_vector_type(32)));
typedef unsigned u32x2  __attribute__((ext_vector_type(2)));
typedef unsigned u32x4  __attribute__((ext_vector_type(4)));
typedef unsigned u32x8  __attribute__((ext_vector_type(8)));
typedef unsigned u32x32 __attribute__((ext_vector_type(32)));
typedef __bf16   bf2_t  __attribute__((ext_vector_type(2)));
typedef unsigned short u16;
#define LAS __attribute__((address_space(3)))
#define DI __device__ __forceinline__

constexpr int S_ = 2048, D_ = 640;
constexpr size_t IMG     = (size_t)32 * 163840;
constexpr size_t OFF_IQ  = 0;
constexpr size_t OFF_IB  = IMG;
constexpr size_t OFF_IA  = 2 * IMG;
constexpr size_t OFF_O   = 3 * IMG;
constexpr size_t OFF_WOT = 4 * IMG;
constexpr size_t OFF_G   = OFF_WOT + (size_t)1600 * 1024;

#define PSTAMP(k, i)
#define WSTAMP(i)

DI unsigned pk2(float lo, float hi) { f32x2 v = {lo, hi}; return __builtin_bit_cast(unsigned, __builtin_convertvector(v, bf2_t)); }
DI float bflo(unsigned v) { return __uint_as_float(v << 16); }
DI float bfhi(unsigned v) { return __uint_as_float(v & 0xffff0000u); }
DI int rfl(int x) { return __builtin_amdgcn_readfirstlane(x); }
DI void glds16(const void* sbase, unsigned voff, unsigned lds_base) {
    const unsigned long long a = (unsigned long long)(uintptr_t)sbase;
    const unsigned long long u = (unsigned long long)(unsigned)rfl((int)(unsigned)a) | ((unsigned long long)(unsigned)rfl((int)(unsigned)(a >> 32)) << 32);
    asm volatile("s_mov_b32 m0, %0\n\ts_nop 0\n\tglobal_load_lds_dwordx4 %1, %2" :: "s"(rfl((int)lds_base)), "v"(voff), "s"(u) : "memory");
}
#define MFMA16(a, b, c) __builtin_amdgcn_mfma_f32_16x16x32_bf16((a), (b), (c), 0, 0, 0)
#define MFMA32(a, b, c) __builtin_amdgcn_mfma_f32_32x32x16_bf16((a), (b), (c), 0, 0, 0)
#define B8(x) __builtin_bit_cast(bf16x8, x)
DI u32x2 trd(const LAS char* p) { return __builtin_bit_cast(u32x2, __builtin_amdgcn_ds_read_tr16_b64_v4i16((LAS s16x4*)p)); }
DI float phi(float t) { return (t > 0.f ? t + 1.f : __expf(t)) + 1e-6f; }
template <int N> DI void wait_vm_bar() { asm volatile("s_waitcnt vmcnt(%0) lgkmcnt(0)\n\ts_barrier" :: "n"(N) : "memory"); }

constexpr int LDS_BYTES = 163840;

DI void p1_aux(const u16* __restrict__ x, const u16* __restrict__ woA, const u16* __restrict__ woB, const u16* __restrict__ gate_w,
               char* __restrict__ ws, char* lds, int a) {
    const int tid = threadIdx.x, lane = tid & 63, wave = rfl(tid >> 6);
    const unsigned lds0 = (unsigned)(uintptr_t)lds;
    const int br = a >> 3, u = a & 7;
    const char* wsrc = (const char*)(br ? woB : woA) + (size_t)u * 102400;
    for (int i = wave; i < 100; i += 8) glds16(wsrc + i * 1024, lane * 16, lds0 + i * 1024);
    const u32x4* xs = (const u32x4*)(x + (size_t)(a * 128 + wave * 16) * D_);
    u32x4 xv[20], gw[5];
#pragma unroll
    for (int i = 0; i < 20; ++i) xv[i] = xs[i * 64 + lane];
#pragma unroll
    for (int i = 0; i < 5; ++i) gw[i] = ((const u32x4*)gate_w)[(i * 64 + lane) % 80];
    LAS float* part = (LAS float*)(lds + 102400) + wave * 1280;
#pragma unroll
    for (int i = 0; i < 20; ++i) {
        float s = 0.f;
#pragma unroll
        for (int j = 0; j < 4; ++j) s += bflo(xv[i][j]) * bflo(gw[i % 5][j]) + bfhi(xv[i][j]) * bfhi(gw[i % 5][j]);
        part[i * 64 + lane] = s;
    }
    wait_vm_bar<0>();
    {
        float s = 0.f;
#pragma unroll
        for (int j = 0; j < 20; ++j) s += part[lane * 20 + j];
        s += __shfl_xor(s, 1); s += __shfl_xor(s, 2);
        if ((lane & 3) == 0) ((float*)(ws + OFF_G))[a * 128 + wave * 16 + (lane >> 2)] = 1.f / (1.f + __expf(-s));
    }
    const int g = lane >> 4, hh = g & 1, ns = g >> 1, q = (lane >> 2) & 3, p = lane & 3;
    for (int un = wave; un < 100; un += 8) {
        const int e = un / 20, np = un - e * 20, nt = np * 2 + ns;
        const int r0 = e < 4 ? e * 20 + 4 * hh : (2 * hh) * 20 + 16;
        const int r1 = e < 4 ? r0 + 8 : r0 + 20;
        const int ks16 = br * 40 + (e < 4 ? u * 4 + e : 32 + u);
        const u32x2 lo = trd((const LAS char*)lds + (r0 + q) * 1280 + nt * 32 + p * 8);
        const u32x2 hi = trd((const LAS char*)lds + (r1 + q) * 1280 + nt * 32 + p * 8);
        *(u32x4*)(ws + OFF_WOT + (size_t)((ks16 >> 1) * 40 + nt) * 1024 + ((ks16 & 1) * 2 + hh) * 256 + (lane & 15) * 16) = u32x4{lo[0], lo[1], hi[0], hi[1]};
    }
}

#define G1_P0 4
#define G1_P1 2
constexpr int g1_free(int i) { const int u = i / 3, m = i % 3, t = 2 * u + m - 1;
    return m == 0 ? (u < 3 ? -1 : 2 * (u - 2)) : (t < 7 ? -1 : t - 6); }
struct G1Plan { int seq[30]; int cnt[22]; int pos[30]; };
constexpr G1Plan g1_plan() {
    G1Plan p = {}; bool done[30] = {}; int n = 0;
    for (int t = 0; t <= 20; ++t) {
        int room = t == 0 ? G1_P0 : G1_P1;
        for (int i = 0; i < 30 && room > 0; ++i) if (!done[i] && g1_free(i) < t) { done[i] = true; p.pos[i] = n; p.seq[n++] = i; --room; }
        p.cnt[t] = n;
    }
    return p;
}
constexpr G1Plan G1P = g1_plan();
constexpr int g1_wait(int t) { const int a = G1P.pos[3 * (t >> 1)], b = G1P.pos[3 * (t >> 1) + 1 + (t & 1)]; return 4 * (G1P.cnt[t] - 1 - (a > b ? a : b)); }
constexpr bool g1_ok() { for (int t = 0; t < 20; ++t) if (g1_wait(t) < 0 || g1_wait(t) > 60) return false; return G1P.cnt[19] == 30; }
static_assert(g1_ok(), "loader plan");
constexpr int G1_WOFF = 49152;

#define LST_P
#define LST_A
template <int T> struct G1Load {
    template <class FX, class FW> static DI void run(FX&& ldx, FW&& ldw LST_P) {
        asm volatile("s_waitcnt vmcnt(%0)\n\ts_barrier" :: "n"(g1_wait(T)) : "memory");
#pragma unroll
        for (int n = G1P.cnt[T]; n < G1P.cnt[T + 1]; ++n) { const int i = G1P.seq[n]; if (i % 3 == 0) ldx(i / 3); else ldw(2 * (i / 3) + i % 3 - 1); }
        G1Load<T + 1>::run(ldx, ldw LST_A);
    }
};
template <> struct G1Load<20> { template <class FX, class FW> static DI void run(FX&&, FW&& LST_P) {} };

struct G1Frag { bf16x8 x[2]; u32x2 w[4][2]; };
#define SB0() __builtin_amdgcn_sched_barrier(0)
template <int J> struct G1Step {
    static DI void run(f32x16 (&acc)[8], G1Frag& cur, G1Frag& nxt, const LAS char* const (&xa)[4], const LAS char* const (&wa)[4]) {
        constexpr int N = J + 1, t = N >> 1, xo = ((N >> 2) % 3) * 16384, sl = t % 7, wo = (sl & 3) * 16384 + (N & 1) * 4096, wi = (sl >> 2) * 2;
#define G1_M(i) acc[i] = MFMA32(cur.x[(i) & 1], B8(((u32x4){cur.w[(i) >> 1][0][0], cur.w[(i) >> 1][0][1], cur.w[(i) >> 1][1][0], cur.w[(i) >> 1][1][1]})), acc[i]); SB0();
#define G1_X(i) if (N < 40) nxt.x[i] = *(const LAS bf16x8*)(xa[N & 3] + xo + (i) * 4096);
#define G1_W(i) if (N < 40) { nxt.w[i][0] = trd(wa[wi + ((i) & 1)] + wo + ((i) >> 1) * 8192); nxt.w[i][1] = trd(wa[wi + ((i) & 1)] + wo + ((i) >> 1) * 8192 + 1024); } SB0();
        if (J & 1) {
            G1_M(0) G1_M(1)
            if (N < 40) asm volatile("s_waitcnt lgkmcnt(0)\n\ts_barrier" ::: "memory");
            SB0();
            G1_X(0) G1_W(0) G1_M(2) G1_X(1) G1_W(1) G1_M(3) G1_W(2) G1_M(4) G1_W(3) G1_M(5) G1_M(6) G1_M(7)
        } else {
            G1_M(0) G1_X(0) G1_W(0) G1_M(1) G1_X(1) G1_W(1) G1_M(2) G1_W(2) G1_M(3) G1_W(3) G1_M(4) G1_M(5) G1_M(6) G1_M(7)
        }
        G1Step<J + 1>::run(acc, nxt, cur, xa, wa);
    }
};
template <> struct G1Step<40> { static DI void run(f32x16 (&)[8], G1Frag&, G1Frag&, const LAS char* const (&)[4], const LAS char* const (&)[4]) {} };

DI void phase1(const u16* __restrict__ x, const u16* __restrict__ wA, const u16* __restrict__ wB, char* __restrict__ ws, char* lds, int bid) {
    const int tid = threadIdx.x, lane = tid & 63, wave = rfl(tid >> 6);
    const unsigned lds0 = (unsigned)(uintptr_t)lds;
    const int xcd = bid & 7, j = (xcd >> 1) * 30 + (bid >> 3);
    const int ct = j >> 3, mt = (xcd & 1) * 8 + (j & 7);
    const int type = rfl(ct / 5), rot = rfl(G1_ROT);
    if (wave >= 4) {
        const int lw = wave - 4;
        unsigned xo[4], wo[2];
#pragma unroll
        for (int i = 0; i < 4; ++i) { const int row = (lw * 4 + i) * 8 + (lane >> 3); xo[i] = (unsigned)((mt * 128 + row) * 1280 + (((lane & 7) ^ ((row >> 1) & 7)) << 4)); }
#pragma unroll
        for (int i = 0; i < 2; ++i) { const int k = (lw * 2 + i) * 4 + (lane >> 4), s16 = lane & 15;
            wo[i] = (unsigned)(k * 3840 + ct * 256 + ((((s16 >> 2) ^ (k & 3)) << 6) | ((s16 & 3) << 4))); }
        auto ldx = [&](int u) {
            int kk = u + rot; kk -= kk >= 10 ? 10 : 0;
            const unsigned slot = lds0 + (u % 3) * 16384 + lw * 4096;
#pragma unroll
            for (int i = 0; i < 4; ++i) glds16((const char*)x + kk * 128, xo[i], slot + i * 1024);
        };
        auto ldw = [&](int t) {
            int kk = (t >> 1) + rot; kk -= kk >= 10 ? 10 : 0;
            const unsigned slot = lds0 + G1_WOFF + (t % 7) * 16384 + lw * 2048;
            const int go = (kk * 64 + (t & 1) * 32) * 3840;
            glds16((const char*)wB + go, wo[0], slot);        glds16((const char*)wB + go, wo[1], slot + 1024);
            glds16((const char*)wA + go, wo[0], slot + 8192); glds16((const char*)wA + go, wo[1], slot + 8192 + 1024);
        };
#pragma unroll
        for (int n = 0; n < G1P.cnt[0]; ++n) { const int i = G1P.seq[n]; if (i % 3 == 0) ldx(i / 3); else ldw(2 * (i / 3) + i % 3 - 1); }
        G1Load<0>::run(ldx, ldw LST_A);
        return;
    }
    const int wm = wave & 1, wn = wave >> 1;
    const int r = lane & 31, hh = lane >> 5, g = lane >> 4, q = (lane >> 2) & 3, p = lane & 3;
    const LAS char* xa[4]; const LAS char* wa[4];
#pragma unroll
    for (int ks = 0; ks < 4; ++ks) { xa[ks] = (const LAS char*)lds + (wm * 64 + r) * 128 + (((ks * 2 + hh) ^ ((r >> 1) & 7)) << 4); asm volatile("" : "+v"(xa[ks])); }
#pragma unroll
    for (int i = 0; i < 4; ++i) { wa[i] = (const LAS char*)lds + G1_WOFF + (i >> 1) * 65536 + (8 * (g >> 1) + q) * 256 + (((wn * 2 + (i & 1)) ^ q) << 6) + (g & 1) * 32 + p * 8;
        asm volatile("" : "+v"(wa[i])); }
    f32x16 acc[8] = {};
    G1Frag f0, f1;
    __builtin_amdgcn_s_setprio(2);
    asm volatile("s_barrier" ::: "memory");
    f0.x[0] = *(const LAS bf16x8*)(xa[0]); f0.x[1] = *(const LAS bf16x8*)(xa[0] + 4096);
#pragma unroll
    for (int i = 0; i < 4; ++i) { f0.w[i][0] = trd(wa[i & 1] + (i >> 1) * 8192); f0.w[i][1] = trd(wa[i & 1] + (i >> 1) * 8192 + 1024); }
    SB0();
    G1Step<0>::run(acc, f0, f1, xa, wa);
    PSTAMP(0, 2);
    constexpr float QS = 0.22360679774997896f * 1.4426950408889634f;
#pragma unroll
    for (int i = 0; i < 8; ++i) {
        const int mi = i & 1, jn = (i >> 1) & 1, pass = i >> 2;
        const size_t ib = type == 0 ? OFF_IQ + pass * 1280 : (pass ? OFF_IA : OFF_IB) + (type - 1) * 1280;
        const int c = (ct - type * 5) * 128 + wn * 64 + jn * 32 + r, h = c / 20, d = c - h * 20;
        f32x16 v = acc[i];
        if (pass == 0) { if (type == 0) v *= QS; }
        else if (type < 2) {
#pragma unroll
            for (int e = 0; e < 16; ++e) v[e] = phi(v[e]);
        }
        char* dst = ws + ib + (size_t)h * 163840 + (mt * 4 + wm * 2 + mi) * 2560 + hh * 320 + d * 16;
        const u32x4 s0 = u32x4{pk2(v[0], v[1]), pk2(v[2], v[3]), pk2(v[4], v[5]), pk2(v[6], v[7])};
        const u32x4 s1 = u32x4{pk2(v[8], v[9]), pk2(v[10], v[11]), pk2(v[12], v[13]), pk2(v[14], v[15])};
        asm volatile("global_store_dwordx4 %0, %1, off sc1\n\tglobal_store_dwordx4 %0, %2, off offset:640 sc1\n\ts_nop 1" :: "v"(dst), "v"(s0), "v"(s1) : "memory");
    }
}

__global__ __launch_bounds__(512, 2) void k1_qkv(const u16* __restrict__ x, const u16* __restrict__ wA, const u16* __restrict__ wB,
                                                 const u16* __restrict__ woA, const u16* __restrict__ woB, const u16* __restrict__ gate_w,
                                                 char* __restrict__ ws) {
    extern __shared__ __attribute__((aligned(16))) char lds[];
    PSTAMP(0, 0);
    if (blockIdx.x >= 240) p1_aux(x, woA, woB, gate_w, ws, lds, blockIdx.x - 240);
    else phase1(x, wA, wB, ws, lds, blockIdx.x);
}

#define STR(x) #x
#define XS(x) STR(x)
#define A_E(i) "v_exp_f32 v" XS(i) ", v" XS(i) "\n"
#define A_C(d, a, b) "v_cvt_pk_bf16_f32 v" XS(d) ", v" XS(a) ", v" XS(b) "\n"
#define A_G(a, b, c, d, p0, p1) A_E(a) A_E(b) A_E(c) A_E(d) A_C(p0, a, b) A_C(p1, c, d)
#define A_M(d, a, b, c) "v_mfma_f32_32x32x16_bf16 " d ", " a ", " b ", " c "\n"
#define A_STEP(C0,C1,C2,C3,C4,C5,C6,C7,C8,C9,C10,C11,C12,C13,C14,C15, SN, KM, KT, V1, V0, NKMa, NKMb, NKT, NV1, NV0, OK0, OK1, OV1, OV0, PRIO) \
    "s_waitcnt lgkmcnt(0)\n" \
    A_M(SN, KM, "v[120:123]", "0") \
    "ds_read_b64_tr_b16 " NKMa ", v128 offset:" XS(OK0) "\n" \
    "ds_read_b64_tr_b16 " NKMb ", v128 offset:" XS(OK1) "\n" \
    "ds_read_b64_tr_b16 " NKT ", v129 offset:" XS(OK0) "\n" \
    A_G(C0, C1, C2, C3, 112, 113) \
    A_M("v[64:79]", V1, "v[116:119]", "v[64:79]") \
    "s_mov_b64 exec, %[vm]\n" \
    "ds_read_b128 " NV1 ", v130 offset:" XS(OV1) "\n" \
    "ds_read_b128 " NV0 ", v130 offset:" XS(OV0) "\n" \
    "s_mov_b64 exec, -1\n" \
    A_G(C4, C5, C6, C7, 114, 115) \
    A_M(SN, KT, "v[124:127]", SN) \
    A_G(C8, C9, C10, C11, 116, 117) \
    A_M("v[64:79]", V0, "v[112:115]", "v[64:79]") \
    "s_setprio " XS(PRIO) "\n" \
    A_G(C12, C13, C14, C15, 118, 119)
#define A_EVEN(OK0, OK1, OV1, OV0, PRIO) A_STEP(32,33,34,35,36,37,38,39,40,41,42,43,44,45,46,47, "v[48:63]", "v[80:83]", "v[84:87]", "v[88:91]", "v[92:95]", \
    "v[96:97]", "v[98:99]", "v[100:101]", "v[104:107]", "v[108:111]", OK0, OK1, OV1, OV0, PRIO)
#define A_ODD(OK0, OK1, OV1, OV0, PRIO) A_STEP(48,49,50,51,52,53,54,55,56,57,58,59,60,61,62,63, "v[32:47]", "v[96:99]", "v[100:103]", "v[104:107]", "v[108:111]", \
    "v[80:81]", "v[82:83]", "v[84:85]", "v[88:91]", "v[92:95]", OK0, OK1, OV1, OV0, PRIO)

DI void attn_chunk(f32x32& S, f32x16& O, u32x32& OPS, u32x8& P, u32x4& AD, const u32x8& Q, unsigned long long vmask) {
    asm volatile(
        "s_nop 7\n"
        A_EVEN(5120, 5184, 640, 2560, 2)     A_ODD(7680, 7744, 3200, 5120, 1)
        A_EVEN(10240, 10304, 5760, 7680, 0)  A_ODD(12800, 12864, 8320, 10240, 3)
        A_EVEN(15360, 15424, 10880, 12800, 2) A_ODD(17920, 17984, 13440, 15360, 1)
        A_EVEN(20480, 20544, 16000, 17920, 0) A_ODD(23040, 23104, 18560, 20480, 3)
        "v_add_u32 v128, 0x5000, v128\n v_add_u32 v129, 0x5000, v129\n v_add_u32 v130, 0x5000, v130\n"
        "s_waitcnt lgkmcnt(0)\n s_nop 4\n"
        : "+{v[32:63]}"(S), "+{v[64:79]}"(O), "+{v[80:111]}"(OPS), "+{v[112:119]}"(P), "+{v[128:131]}"(AD)
        : "{v[120:127]}"(Q), [vm] "s"(vmask)
        : "memory");
}

DI float xhalf_max(float m) { auto rr = __builtin_amdgcn_permlane32_swap(__float_as_uint(m), __float_as_uint(m), false, false);
    return fmaxf(__uint_as_float(rr[0]), __uint_as_float(rr[1])); }
DI float hi_half(float v) {
    auto rr = __builtin_amdgcn_permlane32_swap(__float_as_uint(v), __float_as_uint(v), false, false);
    return __uint_as_float(rr[1]); }
DI void store_o(char* ws, const f32x16& o, float f, int rt, int br, int h, int lane) {
    char* rb = ws + OFF_O + (size_t)(rt * 80 + br * 40) * 1024;
    *(u32x4*)(rb + h * 1024 + lane * 16) = u32x4{pk2(o[0] * f, o[1] * f), pk2(o[2] * f, o[3] * f), pk2(o[4] * f, o[5] * f), pk2(o[6] * f, o[7] * f)};
    if (lane < 32) *(u32x2*)(rb + (32 + (h >> 2)) * 1024 + ((h >> 1) & 1) * 512 + lane * 16 + (h & 1) * 8) = u32x2{pk2(o[8] * f, o[9] * f), pk2(o[10] * f, o[11] * f)};
}

DI void phase2(char* __restrict__ ws, char* lds, int bid) {
    const int tid = threadIdx.x, lane = tid & 63, wave = rfl(tid >> 6);
    const int h = (bid & 7) * 4 + ((bid >> 3) & 3), qb = bid >> 5;
    const int r = lane & 31, hh = lane >> 5;
    const unsigned lds0 = (unsigned)(uintptr_t)lds;
    const char* kvb = ws + OFF_IB + (size_t)h * 163840;
    const char* kva = ws + OFF_IA + (size_t)h * 163840;
    const char* qim = ws + OFF_IQ + (size_t)h * 163840 + qb * 20480;
    const int rt = qb * 8 + wave;
    unsigned qo[2];
#pragma unroll
    for (int i = 0; i < 2; ++i) { const int pc = i * 512 + tid, t = pc / 80; qo[i] = (unsigned)(t * 2560 + (pc - t * 80) * 16); }
    glds16(qim, qo[0], lds0 + 153600 + wave * 1024);
    if (wave < 2) glds16(qim, qo[1], lds0 + 153600 + 8192 + wave * 1024);
#pragma unroll
    for (int i = 0; i < 3; ++i) glds16(kvb + (i * 8 + wave) * 1024, lane * 16, lds0 + (i * 8 + wave) * 1024);
    if (wave == 2) glds16(kvb + 24 * 1024, lane * 16, lds0 + 24 * 1024);
    const float gate = ((const float*)(ws + OFF_G))[rt * 32 + r];
    asm volatile("" ::: "memory");
#pragma unroll
    for (int i = 0; i < 16; ++i) { int pc = 25 + i * 8 + wave; pc = pc < 150 ? pc : pc - 8; glds16(kvb + pc * 1024, lane * 16, lds0 + pc * 1024); }
    wait_vm_bar<16>();
    PSTAMP(1, 1); WSTAMP(1);
    const int g = lane >> 4, q = (lane >> 2) & 3, p = lane & 3;
    const int tro = (g & 1) * 640 + (p & 1) * 320 + q * 16 + (p >> 1) * 8;
    const LAS char* L = (const LAS char*)lds;
    const LAS char* kmp = L + tro + hh * 128;
    const LAS char* ktp = L + tro + 256;
    const LAS char* vp  = L + 1280 + hh * 320 + r * 16;
    u32x8 Q;
    {
        const u32x2 a = trd(kmp + 153600 + wave * 1280), b = trd(kmp + 153600 + wave * 1280 + 64), c = trd(ktp + 153600 + wave * 1280);
        Q[0] = a[0]; Q[1] = a[1]; Q[2] = b[0]; Q[3] = b[1]; Q[4] = hh ? 0u : c[0]; Q[5] = hh ? 0u : c[1]; Q[6] = 0u; Q[7] = 0u;
    }
    const f32x16 Z16 = {};
    const bf16x8 qm = B8(((u32x4){Q[0], Q[1], Q[2], Q[3]}));
    f32x32 S; f32x16 O = {}; u32x32 OPS; u32x8 P = {}; u32x4 AD;
    {
        const u32x2 a = trd(kmp), b = trd(kmp + 64), c = trd(ktp);
        const bf16x8 k0 = B8(((u32x4){a[0], a[1], b[0], b[1]})), k1 = B8(((u32x4){c[0], c[1], 0x3F80u, 0u}));
        const f32x16 sm = MFMA32(k0, qm, Z16);
        f32x16 s0 = MFMA32(k1, B8(((u32x4){Q[4], Q[5], 0u, 0u})), sm);
        float m = s0[0];
#pragma unroll
        for (int i = 1; i < 16; ++i) m = fmaxf(m, s0[i]);
        m = xhalf_max(m);
        Q[6] = hh ? 0u : (pk2(-m, 0.f) & 0xffffu);
        s0 = MFMA32(k1, B8(((u32x4){Q[4], Q[5], Q[6], 0u})), sm);
#pragma unroll
        for (int i = 0; i < 16; ++i) { S[i] = s0[i]; S[16 + i] = 0.f; }
        const unsigned vc = r == 20 ? 0x3F803F80u : 0u;
#pragma unroll
        for (int i = 0; i < 32; ++i) OPS[i] = (i & 8) ? vc : 0u;
        const u32x2 a1 = trd(kmp + 2560), b1 = trd(kmp + 2560 + 64), c1 = trd(ktp + 2560);
        OPS[0] = a1[0]; OPS[1] = a1[1]; OPS[2] = b1[0]; OPS[3] = b1[1]; OPS[4] = c1[0]; OPS[5] = c1[1]; OPS[6] = 0x3F80u; OPS[22] = 0x3F80u;
        if (r < 20) { const u32x4 v0 = *(const LAS u32x4*)vp; OPS[12] = v0[0]; OPS[13] = v0[1]; OPS[14] = v0[2]; OPS[15] = v0[3]; }
        AD[0] = (unsigned)(uintptr_t)kmp; AD[1] = (unsigned)(uintptr_t)ktp; AD[2] = (unsigned)(uintptr_t)vp; AD[3] = 0u;
    }
    const unsigned long long vmask = 0x000FFFFF000FFFFFull;
    const u32x4 ONES = {0x3F803F80u, 0x3F803F80u, 0x3F803F80u, 0x3F803F80u};
    const int rc = r < 20 ? r : 19;
    f32x16 kvacc = {};
    for (int c = 0; c < 8; ++c) {
        const char* ap = kva + (c * 8 + wave) * 2560 + hh * 320 + rc * 16;
        u32x4 a0 = *(const u32x4*)(ap), a1 = *(const u32x4*)(ap + 640), b0 = *(const u32x4*)(ap + 1280), b1 = *(const u32x4*)(ap + 1920);
        if (c == 7) wait_vm_bar<4>();
        attn_chunk(S, O, OPS, P, AD, Q, vmask);
        if (r >= 20) { a0 = u32x4{0u, 0u, 0u, 0u}; a1 = a0; b0 = ONES; b1 = ONES; }
        kvacc = MFMA32(B8(a0), B8(b0), kvacc); kvacc = MFMA32(B8(a1), B8(b1), kvacc);
        if (c == 0) {
            wait_vm_bar<0>();
            glds16(kvb + (150 + wave) * 1024, lane * 16, lds0 + (150 + wave) * 1024);
            if (wave < 2) glds16(kvb + (158 + wave) * 1024, lane * 16, lds0 + (158 + wave) * 1024);
            glds16(qim + 1280, qo[0], lds0 + wave * 1024);
            if (wave < 2) glds16(qim + 1280, qo[1], lds0 + 8192 + wave * 1024);
        }
    }
    __builtin_amdgcn_s_setprio(0);
    O = MFMA32(B8(((u32x4){OPS[8], OPS[9], OPS[10], OPS[11]})), B8(((u32x4){P[4], P[5], P[6], P[7]})), O);
    PSTAMP(1, 2); WSTAMP(2);
    if (r <= 20) {
        LAS f32x4* dst = (LAS f32x4*)(lds + 10240 + ((wave * 2 + hh) * 21 + r) * 48);
        dst[0] = f32x4{kvacc[0], kvacc[1], kvacc[2], kvacc[3]};
        dst[1] = f32x4{kvacc[4], kvacc[5], kvacc[6], kvacc[7]};
        dst[2] = f32x4{kvacc[8], kvacc[9], kvacc[10], kvacc[11]};
    }
    float l = hi_half(O[8]);
    {
        bool bad = !(l >= 1e-18f && l <= 3.0e38f);
#pragma unroll
        for (int i = 0; i < 12; ++i) bad = bad || !(fabsf(O[i]) <= 3.0e38f);
        if (__builtin_expect(__any(bad), 0)) {
            const bf16x8 qt0 = B8(((u32x4){Q[4], Q[5], 0u, 0u}));
            float m = -INFINITY; O = Z16;
            const int ko = (r >> 4) * 640 + ((r >> 2) & 1) * 320 + ((r >> 3) & 1) * 8 + (r & 3) * 2;
            for (int t = 0; t < 64; ++t) {
                const char* tb = kvb + t * 2560;
                unsigned kw[6];
#pragma unroll
                for (int i = 0; i < 6; ++i) {
                    const int d = i < 4 ? 8 * hh + 2 * i : 16 + 2 * (i - 4);
                    kw[i] = (unsigned)*(const u16*)(tb + ko + d * 16) | ((unsigned)*(const u16*)(tb + ko + d * 16 + 16) << 16);
                }
                u32x4 f0 = *(const u32x4*)(tb + 1280 + hh * 320 + rc * 16), f1 = *(const u32x4*)(tb + 1920 + hh * 320 + rc * 16);
                if (r >= 20) { f0 = ONES; f1 = ONES; }
                f32x16 s = MFMA32(B8(((u32x4){kw[0], kw[1], kw[2], kw[3]})), qm, Z16);
                s = MFMA32(B8(((u32x4){kw[4], kw[5], 0u, 0u})), qt0, s);
                float tm = s[0];
#pragma unroll
                for (int i = 1; i < 16; ++i) tm = fmaxf(tm, s[i]);
                tm = xhalf_max(tm);
                const float mn = fmaxf(m, tm);
                const float al = (m == -INFINITY) ? 0.f : __builtin_amdgcn_exp2f(m - mn);
                m = mn;
#pragma unroll
                for (int i = 0; i < 16; ++i) { O[i] *= al; s[i] = __builtin_amdgcn_exp2f(s[i] - mn); }
                u32x4 p0 = {pk2(s[0], s[1]), pk2(s[2], s[3]), pk2(s[4], s[5]), pk2(s[6], s[7])};
                u32x4 p1 = {pk2(s[8], s[9]), pk2(s[10], s[11]), pk2(s[12], s[13]), pk2(s[14], s[15])};
                O = MFMA32(B8(f0), B8(p0), O); O = MFMA32(B8(f1), B8(p1), O);
            }
            l = hi_half(O[8]);
        }
    }
    store_o(ws, O, (1.f - gate) / l, rt, 1, h, lane);
    const u32x2 pq0 = trd(L + wave * 1280 + tro + hh * 64), pq1 = trd(L + wave * 1280 + tro + 128 + hh * 64);
    u32x2 pq2 = trd(L + wave * 1280 + tro + 256); if (hh) pq2 = u32x2{0u, 0u};
    WSTAMP(4);
    __syncthreads();
    WSTAMP(5);
    f32x4 ks0 = {}, ks1 = {}, ks2 = {};
    if (r <= 20) {
#pragma unroll
        for (int w = 0; w < 8; ++w) {
            const LAS f32x4* src = (const LAS f32x4*)(lds + 10240 + ((w * 2 + hh) * 21 + r) * 48);
            ks0 += src[0]; ks1 += src[1]; ks2 += src[2];
        }
    }
    const u32x4 ka0 = {pk2(ks0[0], ks0[1]), pk2(ks0[2], ks0[3]), pk2(ks1[0], ks1[1]), pk2(ks1[2], ks1[3])};
    const u32x4 ka1 = {pk2(ks2[0], ks2[1]), pk2(ks2[2], ks2[3]), 0u, 0u};
    f32x16 oa = MFMA32(B8(ka0), B8(((u32x4){pq0[0], pq0[1], pq1[0], pq1[1]})), Z16);
    oa = MFMA32(B8(ka1), B8(((u32x4){pq2[0], pq2[1], 0u, 0u})), oa);
    const float den = hi_half(oa[8]) + 1e-6f;
    store_o(ws, oa, gate / den, rt, 0, h, lane);
}

__global__ __launch_bounds__(512, 2) void k2_attn(char* __restrict__ ws) {
    extern __shared__ __attribute__((aligned(16))) char lds[];
    PSTAMP(1, 0); WSTAMP(0);
    phase2(ws, lds, blockIdx.x);
}

DI void phase3(const char* __restrict__ ws, u16* __restrict__ out, char* lds, int bid) {
    const int tid = threadIdx.x, lane = tid & 63, wave = rfl(tid >> 6);
    const int xcd = bid & 7, i = bid >> 3;
    const int rb = xcd * 4 + (i & 3), cb = i >> 2;
    const int g = lane >> 4;
    const char* ap = ws + OFF_O + (size_t)(rb * 160 + wave * 10 + (g >> 1)) * 1024 + (g & 1) * 512 + (lane & 15) * 16;
    const char* bp = ws + OFF_WOT + (size_t)(wave * 5 * 40 + cb * 5) * 1024 + lane * 16;
    bf16x8 af[5][4], bfr[5][5];
#pragma unroll
    for (int k = 0; k < 5; ++k) {
#pragma unroll
        for (int ni = 0; ni < 5; ++ni) bfr[k][ni] = *(const bf16x8*)(bp + k * 40960 + ni * 1024);
#pragma unroll
        for (int mi = 0; mi < 4; ++mi) af[k][mi] = *(const bf16x8*)(ap + (mi >> 1) * 81920 + k * 2048 + (mi & 1) * 256);
    }
    __builtin_amdgcn_sched_barrier(0);
    f32x4 acc[4][5] = {};
#pragma unroll
    for (int k = 0; k < 5; ++k)
#pragma unroll
        for (int mi = 0; mi < 4; ++mi)
#pragma unroll
            for (int ni = 0; ni < 5; ++ni) acc[mi][ni] = MFMA16(bfr[k][ni], af[k][mi], acc[mi][ni]);
    LAS f32x4* red = (LAS f32x4*)lds;
#pragma unroll
    for (int mi = 0; mi < 4; ++mi)
#pragma unroll
        for (int ni = 0; ni < 5; ++ni) red[(wave * 20 + mi * 5 + ni) * 64 + lane] = acc[mi][ni];
    PSTAMP(2, 1);
    __syncthreads();
#pragma unroll
    for (int it = 0; it < 2; ++it) {
        const int id = it * 512 + tid;
        if (id < 640) {
            const int row = id / 10, pc = id - row * 10;
            const int ix = ((row >> 4) * 5 + (pc >> 1)) * 64 + (pc & 1) * 32 + (row & 15);
            f32x4 v0 = {}, v1 = {};
#pragma unroll
            for (int w = 0; w < 8; ++w) { v0 += red[w * 1280 + ix]; v1 += red[w * 1280 + ix + 16]; }
            *(u32x4*)(out + (size_t)(rb * 64 + row) * D_ + cb * 80 + pc * 8) = u32x4{pk2(v0[0], v0[1]), pk2(v0[2], v0[3]), pk2(v1[0], v1[1]), pk2(v1[2], v1[3])};
        }
    }
}

__global__ __launch_bounds__(512, 2) void k3_out(char* __restrict__ ws, u16* __restrict__ out) {
    extern __shared__ __attribute__((aligned(16))) char lds[];
    PSTAMP(2, 0);
    phase3(ws, out, lds, blockIdx.x);
}

extern "C" void kernel_launch(void* const* d_in, const int* in_sizes, int n_in, void* d_out, int out_size, void* d_ws, size_t ws_size,
                              hipStream_t stream) {
    const u16* x = (const u16*)d_in[0];
    const u16* wA = (const u16*)d_in[1];
    const u16* woA = (const u16*)d_in[2];
    const u16* wB = (const u16*)d_in[4];
    const u16* woB = (const u16*)d_in[5];
    const u16* gw = (const u16*)d_in[6];
    char* ws = (char*)d_ws;
    (void)hipFuncSetAttribute((const void*)k1_qkv, hipFuncAttributeMaxDynamicSharedMemorySize, LDS_BYTES);
    (void)hipFuncSetAttribute((const void*)k2_attn, hipFuncAttributeMaxDynamicSharedMemorySize, LDS_BYTES);
    (void)hipFuncSetAttribute((const void*)k3_out, hipFuncAttributeMaxDynamicSharedMemorySize, LDS_BYTES);
    k1_qkv<<<256, 512, LDS_BYTES, stream>>>(x, wA, wB, woA, woB, gw, ws);
    k2_attn<<<256, 512, LDS_BYTES, stream>>>(ws);
    k3_out<<<256, 512, LDS_BYTES, stream>>>(ws, (u16*)d_out);
}
```

```cpp
#include <hip/hip_runtime.h>
#include <hip/hip_bf16.h>
#include <stdint.h>

typedef short    bf16x8 __attribute__((ext_vector_type(8)));
typedef short    s16x4  __attribute__((ext_vector_type(4)));
typedef float    f32x2  __attribute__((ext_vector_type(2)));
typedef float    f32x4  __attribute__((ext_vector_type(4)));
typedef float    f32x16 __attribute__((ext_vector_type(16)));
typedef float    f32x32 __attribute__((ext_vector_type(32)));
typedef unsigned u32x2  __attribute__((ext_vector_type(2)));
typedef unsigned u32x4  __attribute__((ext_vector_type(4)));
typedef unsigned u32x8  __attribute__((ext_vector_type(8)));
typedef unsigned u32x32 __attribute__((ext_vector_type(32)));
typedef __bf16   bf2_t  __attribute__((ext_vector_type(2)));
typedef unsigned short u16;
#define LAS __attribute__((address_space(3)))
#define DI __device__ __forceinline__

constexpr int S_ = 2048, D_ = 640;
constexpr size_t IMG     = (size_t)32 * 163840;
constexpr size_t OFF_IQ  = 0;
constexpr size_t OFF_IB  = IMG;
constexpr size_t OFF_IA  = 2 * IMG;
constexpr size_t OFF_O   = 3 * IMG;
constexpr size_t OFF_WOT = 4 * IMG;
constexpr size_t OFF_G   = OFF_WOT + (size_t)1600 * 1024;
constexpr size_t OFF_KVP = OFF_G + 8192;
constexpr size_t OFF_FL  = OFF_KVP + (size_t)256 * 2048;
constexpr int    WS_USED = (int)(OFF_FL + 96 * 128);
typedef __amdgpu_buffer_rsrc_t rsrc_t;
constexpr int SC1 = 16;

#define PSTAMP(k, i)
#define WSTAMP(i)

DI unsigned pk2(float lo, float hi) { f32x2 v = {lo, hi}; return __builtin_bit_cast(unsigned, __builtin_convertvector(v, bf2_t)); }
DI float bflo(unsigned v) { return __uint_as_float(v << 16); }
DI float bfhi(unsigned v) { return __uint_as_float(v & 0xffff0000u); }
DI int rfl(int x) { return __builtin_amdgcn_readfirstlane(x); }
DI void glds16(const void* sbase, unsigned voff, unsigned lds_base) {
    const unsigned long long a = (unsigned long long)(uintptr_t)sbase;
    const unsigned long long u = (unsigned long long)(unsigned)rfl((int)(unsigned)a) | ((unsigned long long)(unsigned)rfl((int)(unsigned)(a >> 32)) << 32);
    asm volatile("s_mov_b32 m0, %0\n\ts_nop 0\n\tglobal_load_lds_dwordx4 %1, %2" :: "s"(rfl((int)lds_base)), "v"(voff), "s"(u) : "memory");
}
#define MFMA16(a, b, c) __builtin_amdgcn_mfma_f32_16x16x32_bf16((a), (b), (c), 0, 0, 0)
#define MFMA32(a, b, c) __builtin_amdgcn_mfma_f32_32x32x16_bf16((a), (b), (c), 0, 0, 0)
#define B8(x) __builtin_bit_cast(bf16x8, x)
DI u32x2 trd(const LAS char* p) { return __builtin_bit_cast(u32x2, __builtin_amdgcn_ds_read_tr16_b64_v4i16((LAS s16x4*)p)); }
DI float phi(float t) { return (t > 0.f ? t + 1.f : __expf(t)) + 1e-6f; }
template <int N> DI void wait_vm_bar() { asm volatile("s_waitcnt vmcnt(%0) lgkmcnt(0)\n\ts_barrier" :: "n"(N) : "memory"); }

constexpr int LDS_BYTES = 163840;

DI void p1_aux(const u16* __restrict__ x, const u16* __restrict__ woA, const u16* __restrict__ woB, const u16* __restrict__ gate_w,
               char* __restrict__ ws, char* lds, int a) {
    const int tid = threadIdx.x, lane = tid & 63, wave = rfl(tid >> 6);
    const unsigned lds0 = (unsigned)(uintptr_t)lds;
    const int br = a >> 3, u = a & 7;
    if (tid < 48) *(u32x4*)(ws + OFF_FL + (a * 48 + tid) * 16) = u32x4{0u, 0u, 0u, 0u};
    const char* wsrc = (const char*)(br ? woB : woA) + (size_t)u * 102400;
    for (int i = wave; i < 100; i += 8) glds16(wsrc + i * 1024, lane * 16, lds0 + i * 1024);
    const u32x4* xs = (const u32x4*)(x + (size_t)(a * 128 + wave * 16) * D_);
    u32x4 xv[20], gw[5];
#pragma unroll
    for (int i = 0; i < 20; ++i) xv[i] = xs[i * 64 + lane];
#pragma unroll
    for (int i = 0; i < 5; ++i) gw[i] = ((const u32x4*)gate_w)[(i * 64 + lane) % 80];
    LAS float* part = (LAS float*)(lds + 102400) + wave * 1280;
#pragma unroll
    for (int i = 0; i < 20; ++i) {
        float s = 0.f;
#pragma unroll
        for (int j = 0; j < 4; ++j) s += bflo(xv[i][j]) * bflo(gw[i % 5][j]) + bfhi(xv[i][j]) * bfhi(gw[i % 5][j]);
        part[i * 64 + lane] = s;
    }
    wait_vm_bar<0>();
    {
        float s = 0.f;
#pragma unroll
        for (int j = 0; j < 20; ++j) s += part[lane * 20 + j];
        s += __shfl_xor(s, 1); s += __shfl_xor(s, 2);
        if ((lane & 3) == 0) ((float*)(ws + OFF_G))[a * 128 + wave * 16 + (lane >> 2)] = 1.f / (1.f + __expf(-s));
    }
    const int g = lane >> 4, hh = g & 1, ns = g >> 1, q = (lane >> 2) & 3, p = lane & 3;
    for (int un = wave; un < 100; un += 8) {
        const int e = un / 20, np = un - e * 20, nt = np * 2 + ns;
        const int r0 = e < 4 ? e * 20 + 4 * hh : (2 * hh) * 20 + 16;
        const int r1 = e < 4 ? r0 + 8 : r0 + 20;
        const int ks16 = e < 4 ? br * 4 + e : 8 + br, cbk = nt / 5;
        const u32x2 lo = trd((const LAS char*)lds + (r0 + q) * 1280 + nt * 32 + p * 8);
        const u32x2 hi = trd((const LAS char*)lds + (r1 + q) * 1280 + nt * 32 + p * 8);
        *(u32x4*)(ws + OFF_WOT + (size_t)((cbk * 8 + u) * 25 + (ks16 >> 1) * 5 + nt - cbk * 5) * 1024 + ((ks16 & 1) * 2 + hh) * 256 + (lane & 15) * 16) = u32x4{lo[0], lo[1], hi[0], hi[1]};
    }
}

#define G1_P0 4
#define G1_P1 2
constexpr int g1_free(int i) { const int u = i / 3, m = i % 3, t = 2 * u + m - 1;
    return m == 0 ? (u < 3 ? -1 : 2 * (u - 2)) : (t < 7 ? -1 : t - 6); }
struct G1Plan { int seq[30]; int cnt[22]; int pos[30]; };
constexpr G1Plan g1_plan() {
    G1Plan p = {}; bool done[30] = {}; int n = 0;
    for (int t = 0; t <= 20; ++t) {
        int room = t == 0 ? G1_P0 : G1_P1;
        for (int i = 0; i < 30 && room > 0; ++i) if (!done[i] && g1_free(i) < t) { done[i] = true; p.pos[i] = n; p.seq[n++] = i; --room; }
        p.cnt[t] = n;
    }
    return p;
}
constexpr G1Plan G1P = g1_plan();
constexpr int g1_wait(int t) { const int a = G1P.pos[3 * (t >> 1)], b = G1P.pos[3 * (t >> 1) + 1 + (t & 1)]; return 4 * (G1P.cnt[t] - 1 - (a > b ? a : b)); }
constexpr bool g1_ok() { for (int t = 0; t < 20; ++t) if (g1_wait(t) < 0 || g1_wait(t) > 60) return false; return G1P.cnt[19] == 30; }
static_assert(g1_ok(), "loader plan");
#define G1_ROT xcd
#define G1_PF 0
#define G1_PFD 4
#define G1_PFD0 4
#define G1_ST 1
constexpr int G1_WOFF = 49152;

#define LST_P
#define LST_A
template <int T> struct G1Load {
    template <class FX, class FW> static DI void run(FX&& ldx, FW&& ldw LST_P) {
        asm volatile("s_waitcnt vmcnt(%0)\n\ts_barrier" :: "n"(g1_wait(T)) : "memory");
#pragma unroll
        for (int n = G1P.cnt[T]; n < G1P.cnt[T + 1]; ++n) { const int i = G1P.seq[n]; if (i % 3 == 0) ldx(i / 3); else ldw(2 * (i / 3) + i % 3 - 1); }
        G1Load<T + 1>::run(ldx, ldw LST_A);
    }
};
template <> struct G1Load<20> { template <class FX, class FW> static DI void run(FX&&, FW&& LST_P) {} };

struct G1Frag { bf16x8 x[2]; u32x2 w[4][2]; };
#define SB0() __builtin_amdgcn_sched_barrier(0)
template <int J> struct G1Step {
    static DI void run(f32x16 (&acc)[8], G1Frag& cur, G1Frag& nxt, const LAS char* const (&xa)[4], const LAS char* const (&wa)[4]) {
        constexpr int N = J + 1, t = N >> 1, xo = ((N >> 2) % 3) * 16384, sl = t % 7, wo = (sl & 3) * 16384 + (N & 1) * 4096, wi = (sl >> 2) * 2;
#define G1_M(i) acc[i] = MFMA32(cur.x[(i) & 1], B8(((u32x4){cur.w[(i) >> 1][0][0], cur.w[(i) >> 1][0][1], cur.w[(i) >> 1][1][0], cur.w[(i) >> 1][1][1]})), acc[i]); SB0();
#define G1_X(i) if (N < 40) nxt.x[i] = *(const LAS bf16x8*)(xa[N & 3] + xo + (i) * 4096);
#define G1_W(i) if (N < 40) { nxt.w[i][0] = trd(wa[wi + ((i) & 1)] + wo + ((i) >> 1) * 8192); nxt.w[i][1] = trd(wa[wi + ((i) & 1)] + wo + ((i) >> 1) * 8192 + 1024); } SB0();
        if (J & 1) {
            G1_M(0) G1_M(1)
            if (N < 40) asm volatile("s_waitcnt lgkmcnt(0)\n\ts_barrier" ::: "memory");
            SB0();
            G1_X(0) G1_W(0) G1_M(2) G1_X(1) G1_W(1) G1_M(3) G1_W(2) G1_M(4) G1_W(3) G1_M(5) G1_M(6) G1_M(7)
        } else {
            G1_M(0) G1_X(0) G1_W(0) G1_M(1) G1_X(1) G1_W(1) G1_M(2) G1_W(2) G1_M(3) G1_W(3) G1_M(4) G1_M(5) G1_M(6) G1_M(7)
        }
        G1Step<J + 1>::run(acc, nxt, cur, xa, wa);
    }
};
template <> struct G1Step<40> { static DI void run(f32x16 (&)[8], G1Frag&, G1Frag&, const LAS char* const (&)[4], const LAS char* const (&)[4]) {} };

DI void phase1(const u16* __restrict__ x, const u16* __restrict__ wA, const u16* __restrict__ wB, char* __restrict__ ws, char* lds, int bid) {
    const int tid = threadIdx.x, lane = tid & 63, wave = rfl(tid >> 6);
    const unsigned lds0 = (unsigned)(uintptr_t)lds;
    const int xcd = bid & 7, j = (xcd >> 1) * 30 + (bid >> 3);
    const int ct = j >> 3, mt = (xcd & 1) * 8 + (j & 7);
    const int type = rfl(ct / 5), rot = rfl(G1_ROT);
    if (__builtin_expect(wave >= 4, 1)) {
        const int lw = wave - 4;
        unsigned xo[4], wo[2];
#pragma unroll
        for (int i = 0; i < 4; ++i) { const int row = (lw * 4 + i) * 8 + (lane >> 3); xo[i] = (unsigned)((mt * 128 + row) * 1280 + (((lane & 7) ^ ((row >> 1) & 7)) << 4)); }
#pragma unroll
        for (int i = 0; i < 2; ++i) { const int k = (lw * 2 + i) * 4 + (lane >> 4), s16 = lane & 15;
            wo[i] = (unsigned)(k * 3840 + ct * 256 + ((((s16 >> 2) ^ (k & 3)) << 6) | ((s16 & 3) << 4))); }
        auto ldx = [&](int u) {
            int kk = u + rot; kk -= kk >= 10 ? 10 : 0;
            const unsigned slot = lds0 + (u % 3) * 16384 + lw * 4096;
#pragma unroll
            for (int i = 0; i < 4; ++i) glds16((const char*)x + kk * 128, xo[i], slot + i * 1024);
        };
        auto ldw = [&](int t) {
            int kk = (t >> 1) + rot; kk -= kk >= 10 ? 10 : 0;
            const unsigned slot = lds0 + G1_WOFF + (t % 7) * 16384 + lw * 2048;
            const int go = (kk * 64 + (t & 1) * 32) * 3840;
            glds16((const char*)wB + go, wo[0], slot);        glds16((const char*)wB + go, wo[1], slot + 1024);
            glds16((const char*)wA + go, wo[0], slot + 8192); glds16((const char*)wA + go, wo[1], slot + 8192 + 1024);
        };
#pragma unroll
        for (int n = 0; n < G1P.cnt[0]; ++n) { const int i = G1P.seq[n]; if (i % 3 == 0) ldx(i / 3); else ldw(2 * (i / 3) + i % 3 - 1); }
        G1Load<0>::run(ldx, ldw LST_A);
        return;
    }
    const int wm = wave & 1, wn = wave >> 1;
    const int r = lane & 31, hh = lane >> 5, g = lane >> 4, q = (lane >> 2) & 3, p = lane & 3;
    const LAS char* xa[4]; const LAS char* wa[4];
#pragma unroll
    for (int ks = 0; ks < 4; ++ks) { xa[ks] = (const LAS char*)lds + (wm * 64 + r) * 128 + (((ks * 2 + hh) ^ ((r >> 1) & 7)) << 4); asm volatile("" : "+v"(xa[ks])); }
#pragma unroll
    for (int i = 0; i < 4; ++i) { wa[i] = (const LAS char*)lds + G1_WOFF + (i >> 1) * 65536 + (8 * (g >> 1) + q) * 256 + (((wn * 2 + (i & 1)) ^ q) << 6) + (g & 1) * 32 + p * 8;
        asm volatile("" : "+v"(wa[i])); }
    f32x16 acc[8] = {};
    G1Frag f0, f1;
    __builtin_amdgcn_s_setprio(2);
    asm volatile("s_barrier" ::: "memory");
    f0.x[0] = *(const LAS bf16x8*)(xa[0]); f0.x[1] = *(const LAS bf16x8*)(xa[0] + 4096);
#pragma unroll
    for (int i = 0; i < 4; ++i) { f0.w[i][0] = trd(wa[i & 1] + (i >> 1) * 8192); f0.w[i][1] = trd(wa[i & 1] + (i >> 1) * 8192 + 1024); }
    SB0();
    G1Step<0>::run(acc, f0, f1, xa, wa);
    PSTAMP(0, 2);
    constexpr float QS = 0.22360679774997896f * 1.4426950408889634f;
#pragma unroll
    for (int i = 0; i < 8; ++i) {
        const int mi = i & 1, jn = (i >> 1) & 1, pass = i >> 2;
        const size_t ib = type == 0 ? OFF_IQ + pass * 1280 : (pass ? OFF_IA : OFF_IB) + (type - 1) * 1280;
        const int c = (ct - type * 5) * 128 + wn * 64 + jn * 32 + r, h = c / 20, d = c - h * 20;
        f32x16 v = acc[i];
        if (pass == 0) { if (type == 0) v *= QS; }
        else if (type < 2) {
#pragma unroll
            for (int e = 0; e < 16; ++e) v[e] = phi(v[e]);
        }
        char* dst = ws + ib + (size_t)h * 163840 + (mt * 4 + wm * 2 + mi) * 2560 + hh * 320 + d * 16;
        const u32x4 s0 = u32x4{pk2(v[0], v[1]), pk2(v[2], v[3]), pk2(v[4], v[5]), pk2(v[6], v[7])};
        const u32x4 s1 = u32x4{pk2(v[8], v[9]), pk2(v[10], v[11]), pk2(v[12], v[13]), pk2(v[14], v[15])};
        asm volatile("global_store_dwordx4 %0, %1, off sc1\n\tglobal_store_dwordx4 %0, %2, off offset:640 sc1\n\ts_nop 1" :: "v"(dst), "v"(s0), "v"(s1) : "memory");
    }
}

__global__ __launch_bounds__(512, 2) void k1_qkv(const u16* __restrict__ x, const u16* __restrict__ wA, const u16* __restrict__ wB,
                                                 const u16* __restrict__ woA, const u16* __restrict__ woB, const u16* __restrict__ gate_w,
                                                 char* __restrict__ ws) {
    extern __shared__ __attribute__((aligned(16))) char lds[];
    PSTAMP(0, 0);
    if (__builtin_expect(blockIdx.x < 240, 1)) phase1(x, wA, wB, ws, lds, blockIdx.x);
    else p1_aux(x, woA, woB, gate_w, ws, lds, blockIdx.x - 240);
}

#define STR(x) #x
#define XS(x) STR(x)
#define A_E(i) "v_exp_f32 v" XS(i) ", v" XS(i) "\n"
#define A_C(d, a, b) "v_cvt_pk_bf16_f32 v" XS(d) ", v" XS(a) ", v" XS(b) "\n"
#define A_G(a, b, c, d, p0, p1) A_E(a) A_E(b) A_E(c) A_E(d) A_C(p0, a, b) A_C(p1, c, d)
#define A_M(d, a, b, c) "v_mfma_f32_32x32x16_bf16 " d ", " a ", " b ", " c "\n"
#define A_STEP(C0,C1,C2,C3,C4,C5,C6,C7,C8,C9,C10,C11,C12,C13,C14,C15, SN, KM, KT, V1, V0, NKMa, NKMb, NKT, NV1, NV0, OK0, OK1, OV1, OV0, PRIO) \
    "s_waitcnt lgkmcnt(0)\n" \
    A_M(SN, KM, "v[120:123]", "0") \
    "ds_read_b64_tr_b16 " NKMa ", v128 offset:" XS(OK0) "\n" \
    "ds_read_b64_tr_b16 " NKMb ", v128 offset:" XS(OK1) "\n" \
    "ds_read_b64_tr_b16 " NKT ", v129 offset:" XS(OK0) "\n" \
    A_G(C0, C1, C2, C3, 112, 113) \
    A_M("v[64:79]", V1, "v[116:119]", "v[64:79]") \
    "s_mov_b64 exec, s[72:73]\n" \
    "ds_read_b128 " NV1 ", v130 offset:" XS(OV1) "\n" \
    "ds_read_b128 " NV0 ", v130 offset:" XS(OV0) "\n" \
    "s_mov_b64 exec, -1\n" \
    A_G(C4, C5, C6, C7, 114, 115) \
    A_M(SN, KT, "v[124:127]", SN) \
    A_G(C8, C9, C10, C11, 116, 117) \
    A_M("v[64:79]", V0, "v[112:115]", "v[64:79]") \
    "s_setprio " XS(PRIO) "\n" \
    A_G(C12, C13, C14, C15, 118, 119)
#define A_EVEN(OK0, OK1, OV1, OV0, PRIO) A_STEP(32,33,34,35,36,37,38,39,40,41,42,43,44,45,46,47, "v[48:63]", "v[80:83]", "v[84:87]", "v[88:91]", "v[92:95]", \
    "v[96:97]", "v[98:99]", "v[100:101]", "v[104:107]", "v[108:111]", OK0, OK1, OV1, OV0, PRIO)
#define A_ODD(OK0, OK1, OV1, OV0, PRIO) A_STEP(48,49,50,51,52,53,54,55,56,57,58,59,60,61,62,63, "v[32:47]", "v[96:99]", "v[100:103]", "v[104:107]", "v[108:111]", \
    "v[80:81]", "v[82:83]", "v[84:85]", "v[88:91]", "v[92:95]", OK0, OK1, OV1, OV0, PRIO)

#define A_LD(g, off) "global_load_lds_dwordx4 v131, " g " offset:" XS(off) "\n"
#define A_BODY \
        "s_mov_b32 m0, s78\n s_nop 4\n" \
        A_EVEN(5120, 5184, 640, 2560, 2)     A_LD("s[74:75]", 0)    A_ODD(7680, 7744, 3200, 5120, 1) A_LD("s[74:75]", 1024) \
        A_EVEN(10240, 10304, 5760, 7680, 0)  A_LD("s[74:75]", 2048) A_ODD(12800, 12864, 8320, 10240, 3) \
        "s_cmp_eq_u32 s80, 0\n s_cbranch_scc1 .Lattn_skip\n s_mov_b32 m0, s79\n s_nop 0\n" \
        A_LD("s[76:77]", 0) A_LD("s[76:77]", 1024) A_LD("s[76:77]", 2048) ".Lattn_skip:\n" \
        A_EVEN(15360, 15424, 10880, 12800, 2) A_ODD(17920, 17984, 13440, 15360, 1) \
        A_EVEN(20480, 20544, 16000, 17920, 0) A_ODD(23040, 23104, 18560, 20480, 3) \
        "v_add_u32 v128, 0x5000, v128\n v_add_u32 v129, 0x5000, v129\n v_add_u32 v130, 0x5000, v130\n" \
        "s_waitcnt lgkmcnt(0)\n s_nop 2\n"
#define A_OPERANDS \
        : "+{v[32:63]}"(S), "+{v[64:79]}"(O), "+{v[80:111]}"(OPS), "+{v[112:119]}"(P), "+{v[128:131]}"(AD) \
        : "{v[120:127]}"(Q), "{s[72:73]}"(vmask), "{s[74:75]}"(ga), "{s[76:77]}"(gb), "{s78}"(ma), "{s79}"(mb), "{s80}"(enb) \
        : "memory", "scc", "s82", "s83"
template <bool FIRST>
DI void attn_chunk(f32x32& S, f32x16& O, u32x32& OPS, u32x8& P, u32x4& AD, const u32x8& Q, unsigned long long vmask,
                   unsigned ma, unsigned long long ga, unsigned mb, unsigned long long gb, unsigned enb) {
    if (FIRST) asm volatile("s_call_b64 s[82:83], .Lattn_body\n s_branch .Lattn_done\n .p2align 6\n .Lattn_body:\n" A_BODY "s_setpc_b64 s[82:83]\n .Lattn_done:\n" A_OPERANDS);
    else       asm volatile("s_call_b64 s[82:83], .Lattn_body\n" A_OPERANDS);
}

DI float xhalf_max(float m) { auto rr = __builtin_amdgcn_permlane32_swap(__float_as_uint(m), __float_as_uint(m), false, false);
    return fmaxf(__uint_as_float(rr[0]), __uint_as_float(rr[1])); }
DI float hi_half(float v) {
    auto rr = __builtin_amdgcn_permlane32_swap(__float_as_uint(v), __float_as_uint(v), false, false);
    return __uint_as_float(rr[1]); }
DI float sum16(float v) { auto rr = __builtin_amdgcn_permlane16_swap(__float_as_uint(v), __float_as_uint(v), false, false);
    return __uint_as_float(rr[0]) + __uint_as_float(rr[1]); }
DI float sum32(float v) { auto rr = __builtin_amdgcn_permlane32_swap(__float_as_uint(v), __float_as_uint(v), false, false);
    return __uint_as_float(rr[0]) + __uint_as_float(rr[1]); }
DI f32x4 sum_rows(f32x4 a, f32x4 b) {
    f32x4 s = a + b;
#pragma unroll
    for (int i = 0; i < 4; ++i) s[i] = sum32(sum16(s[i]));
    return s;
}
DI unsigned long long uni64(const void* p) { const unsigned long long a = (unsigned long long)(uintptr_t)p;
    return (unsigned long long)(unsigned)rfl((int)(unsigned)a) | ((unsigned long long)(unsigned)rfl((int)(unsigned)(a >> 32)) << 32); }
DI void store_o(rsrc_t rs, const f32x16& o, float f, int rt, int br, int h, int lane) {
    const int rb = (int)OFF_O + (rt * 80 + (h >> 2) * 10) * 1024;
    __builtin_amdgcn_raw_buffer_store_b128(u32x4{pk2(o[0] * f, o[1] * f), pk2(o[2] * f, o[3] * f), pk2(o[4] * f, o[5] * f), pk2(o[6] * f, o[7] * f)},
                                           rs, lane * 16, rb + (br * 4 + (h & 3)) * 1024, SC1);
    if (lane < 32) __builtin_amdgcn_raw_buffer_store_b64(u32x2{pk2(o[8] * f, o[9] * f), pk2(o[10] * f, o[11] * f)},
                                           rs, lane * 16, rb + (8 + br) * 1024 + ((h >> 1) & 1) * 512 + (h & 1) * 8, SC1);
}
DI void set_flag(rsrc_t rs, int off) { __builtin_amdgcn_raw_buffer_store_b32(1u, rs, 0, off, SC1); }
DI unsigned get_flag(rsrc_t rs, int voff, int off) { return __builtin_amdgcn_raw_buffer_load_b32(rs, voff, off, SC1); }
DI void wait_flag(rsrc_t rs, int voff, int off, unsigned v) {
#pragma clang loop unroll(disable)
    for (int spin = 0; spin < (1 << 22) && !__all(v == 1u); ++spin) { __builtin_amdgcn_s_sleep(1); v = get_flag(rs, voff, off); }
    __builtin_amdgcn_fence(__ATOMIC_ACQUIRE, "wavefront");
    asm volatile("" ::: "memory");
}

constexpr int L_SP = 137472, L_SQ = 153600, L_KVF = 10240, L_WO = 12288;

__global__ __launch_bounds__(512, 2) void k2_attn(char* __restrict__ ws, u16* __restrict__ out) {
    extern __shared__ __attribute__((aligned(16))) char lds[];
    PSTAMP(1, 0); WSTAMP(0);
    const int tid = threadIdx.x, lane = tid & 63, wave = rfl(tid >> 6), bid = blockIdx.x;
    const int h = (bid & 7) * 4 + ((bid >> 3) & 3), qb = bid >> 5;
    const int oq = bid & 7, rb = oq * 4 + ((bid >> 3) & 3), cb = bid >> 5;
    const int r = lane & 31, hh = lane >> 5;
    const unsigned lds0 = (unsigned)(uintptr_t)lds;
    const rsrc_t rs = __builtin_amdgcn_make_buffer_rsrc((void*)ws, 0, WS_USED, 0x00020000);
    const char* kvb = ws + OFF_IB + (size_t)h * 163840;
    const char* qim = ws + OFF_IQ + (size_t)h * 163840 + qb * 20480;
    const int rt = qb * 8 + wave;
    const int rc = r < 20 ? r : 19;
    const char* ap = ws + OFF_IA + (size_t)h * 163840 + rt * 2560 + hh * 320 + rc * 16;
    u32x4 a0 = *(const u32x4*)(ap), a1 = *(const u32x4*)(ap + 640), b0 = *(const u32x4*)(ap + 1280), b1 = *(const u32x4*)(ap + 1920);
    asm volatile("" ::: "memory");
    glds16(qim + wave * 2560, lane * 16, lds0 + L_SQ + wave * 1280);
    glds16(qim + wave * 2560 + 256, lane * 16, lds0 + L_SQ + wave * 1280 + 256);
#pragma unroll
    for (int i = 0; i < 3; ++i) glds16(kvb + (i * 8 + wave) * 1024, lane * 16, lds0 + (i * 8 + wave) * 1024);
    if (wave == 2) glds16(kvb + 24 * 1024, lane * 16, lds0 + 24 * 1024);
    const float gate = ((const float*)(ws + OFF_G))[rt * 32 + r];
    const u32x4 ONES = {0x3F803F80u, 0x3F803F80u, 0x3F803F80u, 0x3F803F80u};
    const f32x16 Z16 = {};
    {
        if (r >= 20) { a0 = u32x4{0u, 0u, 0u, 0u}; a1 = a0; b0 = ONES; b1 = ONES; }
        f32x16 kvacc = MFMA32(B8(a0), B8(b0), Z16); kvacc = MFMA32(B8(a1), B8(b1), kvacc);
        if (r <= 20) {
            LAS f32x4* dst = (LAS f32x4*)(lds + L_SP + ((wave * 2 + hh) * 21 + r) * 48);
            dst[0] = f32x4{kvacc[0], kvacc[1], kvacc[2], kvacc[3]};
            dst[1] = f32x4{kvacc[4], kvacc[5], kvacc[6], kvacc[7]};
            dst[2] = f32x4{kvacc[8], kvacc[9], kvacc[10], kvacc[11]};
        }
    }
    wait_vm_bar<0>();
    PSTAMP(1, 1); WSTAMP(1);
    const int g = lane >> 4, q = (lane >> 2) & 3, p = lane & 3;
    const int tro = (g & 1) * 640 + (p & 1) * 320 + q * 16 + (p >> 1) * 8;
    const LAS char* L = (const LAS char*)lds;
    const LAS char* kmp = L + tro + hh * 128;
    const LAS char* ktp = L + tro + 256;
    const LAS char* vp  = L + 1280 + hh * 320 + r * 16;
    const int ent0 = wave * 16 + (lane & 15), ent = ent0 < 126 ? ent0 : 125;
    {
        const f32x4 s = sum_rows(*(const LAS f32x4*)(L + L_SP + g * 2016 + ent * 16), *(const LAS f32x4*)(L + L_SP + (g + 4) * 2016 + ent * 16));
        if (lane < 16) __builtin_amdgcn_raw_buffer_store_b128(__builtin_bit_cast(u32x4, s), rs, ent0 * 16, (int)OFF_KVP + (h * 8 + qb) * 2048, SC1);
    }
    u32x8 Q;
    {
        const u32x2 a = trd(kmp + L_SQ + wave * 1280), b = trd(kmp + L_SQ + wave * 1280 + 64), c = trd(ktp + L_SQ + wave * 1280);
        Q[0] = a[0]; Q[1] = a[1]; Q[2] = b[0]; Q[3] = b[1]; Q[4] = hh ? 0u : c[0]; Q[5] = hh ? 0u : c[1]; Q[6] = 0u; Q[7] = 0u;
    }
    const bf16x8 qm = B8(((u32x4){Q[0], Q[1], Q[2], Q[3]}));
    f32x32 S; f32x16 O = {}; u32x32 OPS; u32x8 P = {}; u32x4 AD;
    {
        const u32x2 a = trd(kmp), b = trd(kmp + 64), c = trd(ktp);
        const bf16x8 k0 = B8(((u32x4){a[0], a[1], b[0], b[1]})), k1 = B8(((u32x4){c[0], c[1], 0x3F80u, 0u}));
        const f32x16 sm = MFMA32(k0, qm, Z16);
        f32x16 s0 = MFMA32(k1, B8(((u32x4){Q[4], Q[5], 0u, 0u})), sm);
        float m = s0[0];
#pragma unroll
        for (int i = 1; i < 16; ++i) m = fmaxf(m, s0[i]);
        m = xhalf_max(m);
        Q[6] = hh ? 0u : (pk2(-m, 0.f) & 0xffffu);
        s0 = MFMA32(k1, B8(((u32x4){Q[4], Q[5], Q[6], 0u})), sm);
#pragma unroll
        for (int i = 0; i < 16; ++i) { S[i] = s0[i]; S[16 + i] = 0.f; }
        const unsigned vc = r == 20 ? 0x3F803F80u : 0u;
#pragma unroll
        for (int i = 0; i < 32; ++i) OPS[i] = (i & 8) ? vc : 0u;
        const u32x2 a1 = trd(kmp + 2560), b1 = trd(kmp + 2560 + 64), c1 = trd(ktp + 2560);
        OPS[0] = a1[0]; OPS[1] = a1[1]; OPS[2] = b1[0]; OPS[3] = b1[1]; OPS[4] = c1[0]; OPS[5] = c1[1]; OPS[6] = 0x3F80u; OPS[22] = 0x3F80u;
        if (r < 20) { const u32x4 v0 = *(const LAS u32x4*)vp; OPS[12] = v0[0]; OPS[13] = v0[1]; OPS[14] = v0[2]; OPS[15] = v0[3]; }
        AD[0] = (unsigned)(uintptr_t)kmp; AD[1] = (unsigned)(uintptr_t)ktp; AD[2] = (unsigned)(uintptr_t)vp; AD[3] = lane * 16;
    }
    const unsigned long long vmask = 0x000FFFFF000FFFFFull;
    const char* wop = ws + OFF_WOT + (size_t)((cb * 8 + wave) * 25) * 1024;
    const int flA = (int)OFF_FL + (32 + oq * 4 + (wave >> 1)) * 128 + (wave & 1) * 64, flv = (lane & 3) * 16;
    unsigned fl = 0u;
    f32x4 kp0, kp1; u32x4 ka0; u32x2 ka1, pq0, pq1, pq2; bf16x8 wf[25];
#pragma unroll
    for (int c = 0; c < 8; ++c) {
        unsigned ma, mb; unsigned long long ga, gb;
        if (c < 3) {
            int oa = 25600 + (c * 16 + wave) * 3072, ob = oa + 24576;
            oa = oa < 160768 ? oa : 160768; ob = ob < 160768 ? ob : 160768;
            ma = lds0 + oa; mb = lds0 + ob; ga = uni64(kvb + oa); gb = uni64(kvb + ob);
        } else {
            ma = lds0 + L_WO + ((c - 3) * 8 + wave) * 3072; mb = ma; ga = uni64(wop + (10 + 3 * (c - 3)) * 1024); gb = ga;
        }
        if (c == 0) attn_chunk<true>(S, O, OPS, P, AD, Q, vmask, rfl(ma), ga, rfl(mb), gb, 1u);
        else attn_chunk<false>(S, O, OPS, P, AD, Q, vmask, rfl(ma), ga, rfl(mb), gb, c < 3 ? 1u : 0u);
        if (c == 0) {
            wait_vm_bar<0>();
            if (tid == 0) set_flag(rs, (int)OFF_FL + h * 128 + qb * 16);
            glds16(qim + wave * 2560 + 1280, lane * 16, lds0 + wave * 1280);
            glds16(qim + wave * 2560 + 1280 + 256, lane * 16, lds0 + wave * 1280 + 256);
        } else if (c == 1) {
            fl = get_flag(rs, (lane & 7) * 16, (int)OFF_FL + h * 128);
        } else if (c == 2) {
            wait_vm_bar<0>();
            wait_flag(rs, (lane & 7) * 16, (int)OFF_FL + h * 128, fl);
            kp0 = __builtin_bit_cast(f32x4, __builtin_amdgcn_raw_buffer_load_b128(rs, g * 2048 + ent * 16, (int)OFF_KVP + h * 16384, SC1));
            kp1 = __builtin_bit_cast(f32x4, __builtin_amdgcn_raw_buffer_load_b128(rs, g * 2048 + ent * 16, (int)OFF_KVP + h * 16384 + 8192, SC1));
        } else if (c == 3) {
            const f32x4 s = sum_rows(kp0, kp1);
            const int e3 = ent / 3;
            if (lane < 16) *(LAS u32x2*)(lds + L_KVF + e3 * 32 + (ent - e3 * 3) * 8) = u32x2{pk2(s[0], s[1]), pk2(s[2], s[3])};
            asm volatile("s_waitcnt lgkmcnt(0)\n\ts_barrier" ::: "memory");
            const int rr = r <= 20 ? r : 20;
            ka0 = *(const LAS u32x4*)(L + L_KVF + (hh * 21 + rr) * 32); ka1 = *(const LAS u32x2*)(L + L_KVF + (hh * 21 + rr) * 32 + 16);
            pq0 = trd(L + wave * 1280 + tro + hh * 64); pq1 = trd(L + wave * 1280 + tro + 128 + hh * 64); pq2 = trd(L + wave * 1280 + tro + 256);
        } else if (c == 4) {
            asm volatile("" : "+v"(ka0), "+v"(ka1), "+v"(pq0), "+v"(pq1), "+v"(pq2));
            if (hh) pq2 = u32x2{0u, 0u};
            f32x16 oa = MFMA32(B8(ka0), B8(((u32x4){pq0[0], pq0[1], pq1[0], pq1[1]})), Z16);
            oa = MFMA32(B8(((u32x4){ka1[0], ka1[1], 0u, 0u})), B8(((u32x4){pq2[0], pq2[1], 0u, 0u})), oa);
            const float den = hi_half(oa[8]) + 1e-6f;
            store_o(rs, oa, gate * __builtin_amdgcn_rcpf(den), rt, 0, h, lane);
            asm volatile("s_barrier" ::: "memory");
        } else if (c == 5) {
            wait_vm_bar<0>();
            if (tid == 0) set_flag(rs, (int)OFF_FL + (32 + qb * 4 + (h >> 3)) * 128 + (h & 7) * 16);
#pragma unroll
            for (int i = 0; i < 5; ++i) wf[i] = *(const bf16x8*)(wop + i * 1024 + lane * 16);
        } else if (c == 6) {
            asm volatile("s_barrier" ::: "memory");
            fl = get_flag(rs, flv, flA);
#pragma unroll
            for (int i = 0; i < 5; ++i) wf[5 + i] = *(const bf16x8*)(wop + 5120 + i * 1024 + lane * 16);
        }
    }
    __builtin_amdgcn_s_setprio(0);
    O = MFMA32(B8(((u32x4){OPS[8], OPS[9], OPS[10], OPS[11]})), B8(((u32x4){P[4], P[5], P[6], P[7]})), O);
    PSTAMP(1, 2); WSTAMP(2);
    const int ov = ((g >> 1) * 1024 + (g & 1) * 512 + (lane & 15) * 16), os = (int)OFF_O + (rb * 160 + wave * 10) * 1024;
    bf16x8 af[3][4];
    wait_flag(rs, flv, flA, fl);
#pragma unroll
    for (int k = 0; k < 2; ++k)
#pragma unroll
        for (int mi = 0; mi < 4; ++mi) af[k][mi] = B8(__builtin_amdgcn_raw_buffer_load_b128(rs, ov + k * 2048 + (mi & 1) * 256, os + (mi >> 1) * 81920, SC1));
    float l = hi_half(O[8]);
    {
        bool bad = !(l >= 1e-18f && l <= 1.0e30f);
#pragma unroll
        for (int i = 0; i < 12; ++i) bad = bad || !(fabsf(O[i]) <= 3.0e38f);
        if (__builtin_expect(__any(bad), 0)) {
            const bf16x8 qt0 = B8(((u32x4){Q[4], Q[5], 0u, 0u}));
            float m = -INFINITY; O = Z16;
            const int ko = (r >> 4) * 640 + ((r >> 2) & 1) * 320 + ((r >> 3) & 1) * 8 + (r & 3) * 2;
            for (int t = 0; t < 64; ++t) {
                const char* tb = kvb + t * 2560;
                unsigned kw[6];
#pragma unroll
                for (int i = 0; i < 6; ++i) {
                    const int d = i < 4 ? 8 * hh + 2 * i : 16 + 2 * (i - 4);
                    kw[i] = (unsigned)*(const u16*)(tb + ko + d * 16) | ((unsigned)*(const u16*)(tb + ko + d * 16 + 16) << 16);
                }
                u32x4 f0 = *(const u32x4*)(tb + 1280 + hh * 320 + rc * 16), f1 = *(const u32x4*)(tb + 1920 + hh * 320 + rc * 16);
                if (r >= 20) { f0 = ONES; f1 = ONES; }
                f32x16 s = MFMA32(B8(((u32x4){kw[0], kw[1], kw[2], kw[3]})), qm, Z16);
                s = MFMA32(B8(((u32x4){kw[4], kw[5], 0u, 0u})), qt0, s);
                float tm = s[0];
#pragma unroll
                for (int i = 1; i < 16; ++i) tm = fmaxf(tm, s[i]);
                tm = xhalf_max(tm);
                const float mn = fmaxf(m, tm);
                const float al = (m == -INFINITY) ? 0.f : __builtin_amdgcn_exp2f(m - mn);
                m = mn;
#pragma unroll
                for (int i = 0; i < 16; ++i) { O[i] *= al; s[i] = __builtin_amdgcn_exp2f(s[i] - mn); }
                u32x4 p0 = {pk2(s[0], s[1]), pk2(s[2], s[3]), pk2(s[4], s[5]), pk2(s[6], s[7])};
                u32x4 p1 = {pk2(s[8], s[9]), pk2(s[10], s[11]), pk2(s[12], s[13]), pk2(s[14], s[15])};
                O = MFMA32(B8(f0), B8(p0), O); O = MFMA32(B8(f1), B8(p1), O);
            }
            l = hi_half(O[8]);
        }
    }
    store_o(rs, O, (1.f - gate) * __builtin_amdgcn_rcpf(l), rt, 1, h, lane);
    asm volatile("s_waitcnt vmcnt(10)" ::: "memory");
#pragma unroll
    for (int i = 0; i < 15; ++i) wf[10 + i] = *(const LAS bf16x8*)(L + L_WO + ((i / 3) * 8 + wave) * 3072 + (i % 3) * 1024 + lane * 16);
    wait_vm_bar<0>();
    if (tid == 0) set_flag(rs, (int)OFF_FL + (64 + qb * 4 + (h >> 3)) * 128 + (h & 7) * 16);
    WSTAMP(3);
    f32x4 acc[4][5] = {};
#pragma unroll
    for (int k = 0; k < 2; ++k)
#pragma unroll
        for (int mi = 0; mi < 4; ++mi)
#pragma unroll
            for (int ni = 0; ni < 5; ++ni) acc[mi][ni] = MFMA16(wf[k * 5 + ni], af[k][mi], acc[mi][ni]);
    __builtin_amdgcn_sched_barrier(0);
    WSTAMP(4);
    wait_flag(rs, flv, flA + 4096, get_flag(rs, flv, flA + 4096));
    WSTAMP(5);
#pragma unroll
    for (int k = 0; k < 3; ++k)
#pragma unroll
        for (int mi = 0; mi < 4; ++mi) af[k][mi] = B8(__builtin_amdgcn_raw_buffer_load_b128(rs, ov + (k + 2) * 2048 + (mi & 1) * 256, os + (mi >> 1) * 81920, SC1));
    __builtin_amdgcn_sched_barrier(0);
#pragma unroll
    for (int k = 0; k < 3; ++k)
#pragma unroll
        for (int mi = 0; mi < 4; ++mi)
#pragma unroll
            for (int ni = 0; ni < 5; ++ni) acc[mi][ni] = MFMA16(wf[10 + k * 5 + ni], af[k][mi], acc[mi][ni]);
    LAS f32x4* red = (LAS f32x4*)lds;
#pragma unroll
    for (int mi = 0; mi < 4; ++mi)
#pragma unroll
        for (int ni = 0; ni < 5; ++ni) red[(wave * 20 + mi * 5 + ni) * 64 + lane] = acc[mi][ni];
    WSTAMP(6);
    __syncthreads();
#pragma unroll
    for (int it = 0; it < 2; ++it) {
        const int id = it * 512 + tid;
        if (id < 640) {
            const int row = id / 10, pc = id - row * 10;
            const int ix = ((row >> 4) * 5 + (pc >> 1)) * 64 + (pc & 1) * 32 + (row & 15);
            f32x4 v0 = {}, v1 = {};
#pragma unroll
            for (int w = 0; w < 8; ++w) { v0 += red[w * 1280 + ix]; v1 += red[w * 1280 + ix + 16]; }
            *(u32x4*)(out + (size_t)(rb * 64 + row) * D_ + cb * 80 + pc * 8) = u32x4{pk2(v0[0], v0[1]), pk2(v0[2], v0[3]), pk2(v1[0], v1[1]), pk2(v1[2], v1[3])};
        }
    }
}

extern "C" void kernel_launch(void* const* d_in, const int* in_sizes, int n_in, void* d_out, int out_size, void* d_ws, size_t ws_size,
                              hipStream_t stream) {
    const u16* x = (const u16*)d_in[0];
    const u16* wA = (const u16*)d_in[1];
    const u16* woA = (const u16*)d_in[2];
    const u16* wB = (const u16*)d_in[4];
    const u16* woB = (const u16*)d_in[5];
    const u16* gw = (const u16*)d_in[6];
    char* ws = (char*)d_ws;
    (void)hipFuncSetAttribute((const void*)k1_qkv, hipFuncAttributeMaxDynamicSharedMemorySize, LDS_BYTES);
    (void)hipFuncSetAttribute((const void*)k2_attn, hipFuncAttributeMaxDynamicSharedMemorySize, LDS_BYTES);
    k1_qkv<<<256, 512, LDS_BYTES, stream>>>(x, wA, wB, woA, woB, gw, ws);
    k2_attn<<<256, 512, LDS_BYTES, stream>>>(ws, (u16*)d_out);
}
```

```cpp
#include <hip/hip_runtime.h>
#include <hip/hip_bf16.h>
#include <stdint.h>

typedef short    bf16x8 __attribute__((ext_vector_type(8)));
typedef short    s16x4  __attribute__((ext_vector_type(4)));
typedef float    f32x2  __attribute__((ext_vector_type(2)));
typedef float    f32x4  __attribute__((ext_vector_type(4)));
typedef float    f32x16 __attribute__((ext_vector_type(16)));
typedef float    f32x32 __attribute__((ext_vector_type(32)));
typedef unsigned u32x2  __attribute__((ext_vector_type(2)));
typedef unsigned u32x4  __attribute__((ext_vector_type(4)));
typedef unsigned u32x8  __attribute__((ext_vector_type(8)));
typedef unsigned u32x32 __attribute__((ext_vector_type(32)));
typedef __bf16   bf2_t  __attribute__((ext_vector_type(2)));
typedef unsigned short u16;
#define LAS __attribute__((address_space(3)))
#define DI __device__ __forceinline__

constexpr int S_ = 2048, D_ = 640;
constexpr size_t IMG     = (size_t)32 * 163840;
constexpr size_t OFF_IQ  = 0;
constexpr size_t OFF_IB  = IMG;
constexpr size_t OFF_IA  = 2 * IMG;
constexpr size_t OFF_O   = 3 * IMG;
constexpr size_t OFF_WOT = 4 * IMG;
constexpr size_t OFF_G   = OFF_WOT + (size_t)1600 * 1024;
constexpr size_t OFF_KVP = OFF_G + 8192;
constexpr size_t OFF_FL  = OFF_KVP + (size_t)256 * 2048;
constexpr int    WS_USED = (int)(OFF_FL + 96 * 128);
typedef __amdgpu_buffer_rsrc_t rsrc_t;
constexpr int SC1 = 16;

#define CSTAMP(i)
#define PSTAMP(k, i)
#define WSTAMP(i)

DI unsigned pk2(float lo, float hi) { f32x2 v = {lo, hi}; return __builtin_bit_cast(unsigned, __builtin_convertvector(v, bf2_t)); }
DI float bflo(unsigned v) { return __uint_as_float(v << 16); }
DI float bfhi(unsigned v) { return __uint_as_float(v & 0xffff0000u); }
DI int rfl(int x) { return __builtin_amdgcn_readfirstlane(x); }
DI void glds16(const void* sbase, unsigned voff, unsigned lds_base) {
    const unsigned long long a = (unsigned long long)(uintptr_t)sbase;
    const unsigned long long u = (unsigned long long)(unsigned)rfl((int)(unsigned)a) | ((unsigned long long)(unsigned)rfl((int)(unsigned)(a >> 32)) << 32);
    asm volatile("s_mov_b32 m0, %0\n\ts_nop 0\n\tglobal_load_lds_dwordx4 %1, %2" :: "s"(rfl((int)lds_base)), "v"(voff), "s"(u) : "memory");
}
#define MFMA16(a, b, c) __builtin_amdgcn_mfma_f32_16x16x32_bf16((a), (b), (c), 0, 0, 0)
#define MFMA32(a, b, c) __builtin_amdgcn_mfma_f32_32x32x16_bf16((a), (b), (c), 0, 0, 0)
#define B8(x) __builtin_bit_cast(bf16x8, x)
DI u32x2 trd(const LAS char* p) { return __builtin_bit_cast(u32x2, __builtin_amdgcn_ds_read_tr16_b64_v4i16((LAS s16x4*)p)); }
DI float phi(float t) { return (t > 0.f ? t + 1.f : __expf(t)) + 1e-6f; }
template <int N> DI void wait_vm_bar() { asm volatile("s_waitcnt vmcnt(%0) lgkmcnt(0)\n\ts_barrier" :: "n"(N) : "memory"); }

constexpr int LDS_BYTES = 163840;

DI void p1_aux(const u16* __restrict__ x, const u16* __restrict__ woA, const u16* __restrict__ woB, const u16* __restrict__ gate_w,
               char* __restrict__ ws, char* lds, int a) {
    const int tid = threadIdx.x, lane = tid & 63, wave = rfl(tid >> 6);
    const unsigned lds0 = (unsigned)(uintptr_t)lds;
    const int br = a >> 3, u = a & 7;
    if (tid < 48) *(u32x4*)(ws + OFF_FL + (a * 48 + tid) * 16) = u32x4{0u, 0u, 0u, 0u};
    const char* wsrc = (const char*)(br ? woB : woA) + (size_t)u * 102400;
    for (int i = wave; i < 100; i += 8) glds16(wsrc + i * 1024, lane * 16, lds0 + i * 1024);
    const u32x4* xs = (const u32x4*)(x + (size_t)(a * 128 + wave * 16) * D_);
    u32x4 xv[20], gw[5];
#pragma unroll
    for (int i = 0; i < 20; ++i) xv[i] = xs[i * 64 + lane];
#pragma unroll
    for (int i = 0; i < 5; ++i) gw[i] = ((const u32x4*)gate_w)[(i * 64 + lane) % 80];
    LAS float* part = (LAS float*)(lds + 102400) + wave * 1280;
#pragma unroll
    for (int i = 0; i < 20; ++i) {
        float s = 0.f;
#pragma unroll
        for (int j = 0; j < 4; ++j) s += bflo(xv[i][j]) * bflo(gw[i % 5][j]) + bfhi(xv[i][j]) * bfhi(gw[i % 5][j]);
        part[i * 64 + lane] = s;
    }
    wait_vm_bar<0>();
    {
        float s = 0.f;
#pragma unroll
        for (int j = 0; j < 20; ++j) s += part[lane * 20 + j];
        s += __shfl_xor(s, 1); s += __shfl_xor(s, 2);
        if ((lane & 3) == 0) ((float*)(ws + OFF_G))[a * 128 + wave * 16 + (lane >> 2)] = 1.f / (1.f + __expf(-s));
    }
    const int g = lane >> 4, hh = g & 1, ns = g >> 1, q = (lane >> 2) & 3, p = lane & 3;
    for (int un = wave; un < 100; un += 8) {
        const int e = un / 20, np = un - e * 20, nt = np * 2 + ns;
        const int r0 = e < 4 ? e * 20 + 4 * hh : (2 * hh) * 20 + 16;
        const int r1 = e < 4 ? r0 + 8 : r0 + 20;
        const int ks16 = e < 4 ? br * 4 + e : 8 + br, cbk = nt / 5;
        const u32x2 lo = trd((const LAS char*)lds + (r0 + q) * 1280 + nt * 32 + p * 8);
        const u32x2 hi = trd((const LAS char*)lds + (r1 + q) * 1280 + nt * 32 + p * 8);
        *(u32x4*)(ws + OFF_WOT + (size_t)((cbk * 8 + u) * 25 + (ks16 >> 1) * 5 + nt - cbk * 5) * 1024 + ((ks16 & 1) * 2 + hh) * 256 + (lane & 15) * 16) = u32x4{lo[0], lo[1], hi[0], hi[1]};
    }
}

#define G1_P0 4
#define G1_P1 2
constexpr int g1_free(int i) { const int u = i / 3, m = i % 3, t = 2 * u + m - 1;
    return m == 0 ? (u < 3 ? -1 : 2 * (u - 2)) : (t < 7 ? -1 : t - 6); }
struct G1Plan { int seq[30]; int cnt[22]; int pos[30]; };
constexpr G1Plan g1_plan() {
    G1Plan p = {}; bool done[30] = {}; int n = 0;
    for (int t = 0; t <= 20; ++t) {
        int room = t == 0 ? G1_P0 : G1_P1;
        for (int i = 0; i < 30 && room > 0; ++i) if (!done[i] && g1_free(i) < t) { done[i] = true; p.pos[i] = n; p.seq[n++] = i; --room; }
        p.cnt[t] = n;
    }
    return p;
}
constexpr G1Plan G1P = g1_plan();
constexpr int g1_wait(int t) { const int a = G1P.pos[3 * (t >> 1)], b = G1P.pos[3 * (t >> 1) + 1 + (t & 1)]; return 4 * (G1P.cnt[t] - 1 - (a > b ? a : b)); }
constexpr bool g1_ok() { for (int t = 0; t < 20; ++t) if (g1_wait(t) < 0 || g1_wait(t) > 60) return false; return G1P.cnt[19] == 30; }
static_assert(g1_ok(), "loader plan");
#define G1_ROT xcd
#define G1_PF 0
#define G1_PFD 4
#define G1_PFD0 4
#define G1_ST 1
constexpr int G1_WOFF = 49152;

#define LST_P
#define LST_A
template <int T> struct G1Load {
    template <class FX, class FW> static DI void run(FX&& ldx, FW&& ldw LST_P) {
        asm volatile("s_waitcnt vmcnt(%0)\n\ts_barrier" :: "n"(g1_wait(T)) : "memory");
#pragma unroll
        for (int n = G1P.cnt[T]; n < G1P.cnt[T + 1]; ++n) { const int i = G1P.seq[n]; if (i % 3 == 0) ldx(i / 3); else ldw(2 * (i / 3) + i % 3 - 1); }
        G1Load<T + 1>::run(ldx, ldw LST_A);
    }
};
template <> struct G1Load<20> { template <class FX, class FW> static DI void run(FX&&, FW&& LST_P) {} };

struct G1Frag { bf16x8 x[2]; u32x2 w[4][2]; };
#define SB0() __builtin_amdgcn_sched_barrier(0)
template <int J> struct G1Step {
    static DI void run(f32x16 (&acc)[8], G1Frag& cur, G1Frag& nxt, const LAS char* const (&xa)[4], const LAS char* const (&wa)[4]) {
        constexpr int N = J + 1, t = N >> 1, xo = ((N >> 2) % 3) * 16384, sl = t % 7, wo = (sl & 3) * 16384 + (N & 1) * 4096, wi = (sl >> 2) * 2;
#define G1_M(i) acc[i] = MFMA32(cur.x[(i) & 1], B8(((u32x4){cur.w[(i) >> 1][0][0], cur.w[(i) >> 1][0][1], cur.w[(i) >> 1][1][0], cur.w[(i) >> 1][1][1]})), acc[i]); SB0();
#define G1_X(i) if (N < 40) nxt.x[i] = *(const LAS bf16x8*)(xa[N & 3] + xo + (i) * 4096);
#define G1_W(i) if (N < 40) { nxt.w[i][0] = trd(wa[wi + ((i) & 1)] + wo + ((i) >> 1) * 8192); nxt.w[i][1] = trd(wa[wi + ((i) & 1)] + wo + ((i) >> 1) * 8192 + 1024); } SB0();
        if (J & 1) {
            G1_M(0) G1_M(1)
            if (N < 40) asm volatile("s_waitcnt lgkmcnt(0)\n\ts_barrier" ::: "memory");
            SB0();
            G1_X(0) G1_W(0) G1_M(2) G1_X(1) G1_W(1) G1_M(3) G1_W(2) G1_M(4) G1_W(3) G1_M(5) G1_M(6) G1_M(7)
        } else {
            G1_M(0) G1_X(0) G1_W(0) G1_M(1) G1_X(1) G1_W(1) G1_M(2) G1_W(2) G1_M(3) G1_W(3) G1_M(4) G1_M(5) G1_M(6) G1_M(7)
        }
        G1Step<J + 1>::run(acc, nxt, cur, xa, wa);
    }
};
template <> struct G1Step<40> { static DI void run(f32x16 (&)[8], G1Frag&, G1Frag&, const LAS char* const (&)[4], const LAS char* const (&)[4]) {} };

DI void phase1(const u16* __restrict__ x, const u16* __restrict__ wA, const u16* __restrict__ wB, char* __restrict__ ws, char* lds, int bid) {
    const int tid = threadIdx.x, lane = tid & 63, wave = rfl(tid >> 6);
    const unsigned lds0 = (unsigned)(uintptr_t)lds;
    const int xcd = bid & 7, j = (xcd >> 1) * 30 + (bid >> 3);
    const int ct = j >> 3, mt = (xcd & 1) * 8 + (j & 7);
    const int type = rfl(ct / 5), rot = rfl(G1_ROT);
    if (__builtin_expect(wave >= 4, 1)) {
        const int lw = wave - 4;
        unsigned xo[4], wo[2];
#pragma unroll
        for (int i = 0; i < 4; ++i) { const int row = (lw * 4 + i) * 8 + (lane >> 3); xo[i] = (unsigned)((mt * 128 + row) * 1280 + (((lane & 7) ^ ((row >> 1) & 7)) << 4)); }
#pragma unroll
        for (int i = 0; i < 2; ++i) { const int k = (lw * 2 + i) * 4 + (lane >> 4), s16 = lane & 15;
            wo[i] = (unsigned)(k * 3840 + ct * 256 + ((((s16 >> 2) ^ (k & 3)) << 6) | ((s16 & 3) << 4))); }
        auto ldx = [&](int u) {
            int kk = u + rot; kk -= kk >= 10 ? 10 : 0;
            const unsigned slot = lds0 + (u % 3) * 16384 + lw * 4096;
#pragma unroll
            for (int i = 0; i < 4; ++i) glds16((const char*)x + kk * 128, xo[i], slot + i * 1024);
        };
        auto ldw = [&](int t) {
            int kk = (t >> 1) + rot; kk -= kk >= 10 ? 10 : 0;
            const unsigned slot = lds0 + G1_WOFF + (t % 7) * 16384 + lw * 2048;
            const int go = (kk * 64 + (t & 1) * 32) * 3840;
            glds16((const char*)wB + go, wo[0], slot);        glds16((const char*)wB + go, wo[1], slot + 1024);
            glds16((const char*)wA + go, wo[0], slot + 8192); glds16((const char*)wA + go, wo[1], slot + 8192 + 1024);
        };
#pragma unroll
        for (int n = 0; n < G1P.cnt[0]; ++n) { const int i = G1P.seq[n]; if (i % 3 == 0) ldx(i / 3); else ldw(2 * (i / 3) + i % 3 - 1); }
        G1Load<0>::run(ldx, ldw LST_A);
        return;
    }
    const int wm = wave & 1, wn = wave >> 1;
    const int r = lane & 31, hh = lane >> 5, g = lane >> 4, q = (lane >> 2) & 3, p = lane & 3;
    const LAS char* xa[4]; const LAS char* wa[4];
#pragma unroll
    for (int ks = 0; ks < 4; ++ks) { xa[ks] = (const LAS char*)lds + (wm * 64 + r) * 128 + (((ks * 2 + hh) ^ ((r >> 1) & 7)) << 4); asm volatile("" : "+v"(xa[ks])); }
#pragma unroll
    for (int i = 0; i < 4; ++i) { wa[i] = (const LAS char*)lds + G1_WOFF + (i >> 1) * 65536 + (8 * (g >> 1) + q) * 256 + (((wn * 2 + (i & 1)) ^ q) << 6) + (g & 1) * 32 + p * 8;
        asm volatile("" : "+v"(wa[i])); }
    f32x16 acc[8] = {};
    G1Frag f0, f1;
    __builtin_amdgcn_s_setprio(2);
    asm volatile("s_barrier" ::: "memory");
    f0.x[0] = *(const LAS bf16x8*)(xa[0]); f0.x[1] = *(const LAS bf16x8*)(xa[0] + 4096);
#pragma unroll
    for (int i = 0; i < 4; ++i) { f0.w[i][0] = trd(wa[i & 1] + (i >> 1) * 8192); f0.w[i][1] = trd(wa[i & 1] + (i >> 1) * 8192 + 1024); }
    SB0();
    G1Step<0>::run(acc, f0, f1, xa, wa);
    PSTAMP(0, 2);
    constexpr float QS = 0.22360679774997896f * 1.4426950408889634f;
#pragma unroll
    for (int i = 0; i < 8; ++i) {
        const int mi = i & 1, jn = (i >> 1) & 1, pass = i >> 2;
        const size_t ib = type == 0 ? OFF_IQ + pass * 1280 : (pass ? OFF_IA : OFF_IB) + (type - 1) * 1280;
        const int c = (ct - type * 5) * 128 + wn * 64 + jn * 32 + r, h = c / 20, d = c - h * 20;
        f32x16 v = acc[i];
        if (pass == 0) { if (type == 0) v *= QS; }
        else if (type < 2) {
#pragma unroll
            for (int e = 0; e < 16; ++e) v[e] = phi(v[e]);
        }
        char* dst = ws + ib + (size_t)h * 163840 + (mt * 4 + wm * 2 + mi) * 2560 + hh * 320 + d * 16;
        const u32x4 s0 = u32x4{pk2(v[0], v[1]), pk2(v[2], v[3]), pk2(v[4], v[5]), pk2(v[6], v[7])};
        const u32x4 s1 = u32x4{pk2(v[8], v[9]), pk2(v[10], v[11]), pk2(v[12], v[13]), pk2(v[14], v[15])};
        asm volatile("global_store_dwordx4 %0, %1, off sc1\n\tglobal_store_dwordx4 %0, %2, off offset:640 sc1\n\ts_nop 1" :: "v"(dst), "v"(s0), "v"(s1) : "memory");
    }
}

__global__ __launch_bounds__(512, 2) void k1_qkv(const u16* __restrict__ x, const u16* __restrict__ wA, const u16* __restrict__ wB,
                                                 const u16* __restrict__ woA, const u16* __restrict__ woB, const u16* __restrict__ gate_w,
                                                 char* __restrict__ ws) {
    extern __shared__ __attribute__((aligned(16))) char lds[];
    PSTAMP(0, 0);
    if (__builtin_expect(blockIdx.x < 240, 1)) phase1(x, wA, wB, ws, lds, blockIdx.x);
    else p1_aux(x, woA, woB, gate_w, ws, lds, blockIdx.x - 240);
}

#define STR(x) #x
#define XS(x) STR(x)
#define A_E(i) "v_exp_f32 v" XS(i) ", v" XS(i) "\n"
#define A_C(d, a, b) "v_cvt_pk_bf16_f32 v" XS(d) ", v" XS(a) ", v" XS(b) "\n"
#define A_G(a, b, c, d, p0, p1) A_E(a) A_E(b) A_E(c) A_E(d) A_C(p0, a, b) A_C(p1, c, d)
#define A_M(d, a, b, c) "v_mfma_f32_32x32x16_bf16 " d ", " a ", " b ", " c "\n"
#define A_STEP(C0,C1,C2,C3,C4,C5,C6,C7,C8,C9,C10,C11,C12,C13,C14,C15, SN, KM, KT, V1, V0, NKMa, NKMb, NKT, NV1, NV0, OK0, OK1, OV1, OV0, PRIO) \
    "s_waitcnt lgkmcnt(0)\n" \
    A_M(SN, KM, "v[120:123]", "0") \
    "ds_read_b64_tr_b16 " NKMa ", v128 offset:" XS(OK0) "\n" \
    "ds_read_b64_tr_b16 " NKMb ", v128 offset:" XS(OK1) "\n" \
    "ds_read_b64_tr_b16 " NKT ", v129 offset:" XS(OK0) "\n" \
    A_G(C0, C1, C2, C3, 112, 113) \
    A_M("v[64:79]", V1, "v[116:119]", "v[64:79]") \
    "s_mov_b64 exec, s[72:73]\n" \
    "ds_read_b128 " NV1 ", v130 offset:" XS(OV1) "\n" \
    "ds_read_b128 " NV0 ", v130 offset:" XS(OV0) "\n" \
    "s_mov_b64 exec, -1\n" \
    A_G(C4, C5, C6, C7, 114, 115) \
    A_M(SN, KT, "v[124:127]", SN) \
    A_G(C8, C9, C10, C11, 116, 117) \
    A_M("v[64:79]", V0, "v[112:115]", "v[64:79]") \
    "s_setprio " XS(PRIO) "\n" \
    A_G(C12, C13, C14, C15, 118, 119)
#define A_EVEN(OK0, OK1, OV1, OV0, PRIO) A_STEP(32,33,34,35,36,37,38,39,40,41,42,43,44,45,46,47, "v[48:63]", "v[80:83]", "v[84:87]", "v[88:91]", "v[92:95]", \
    "v[96:97]", "v[98:99]", "v[100:101]", "v[104:107]", "v[108:111]", OK0, OK1, OV1, OV0, PRIO)
#define A_ODD(OK0, OK1, OV1, OV0, PRIO) A_STEP(48,49,50,51,52,53,54,55,56,57,58,59,60,61,62,63, "v[32:47]", "v[96:99]", "v[100:103]", "v[104:107]", "v[108:111]", \
    "v[80:81]", "v[82:83]", "v[84:85]", "v[88:91]", "v[92:95]", OK0, OK1, OV1, OV0, PRIO)

#define A_LD(g, off) "global_load_lds_dwordx4 v131, " g " offset:" XS(off) "\n"
#define A_BODY \
        "s_mov_b32 m0, s78\n s_nop 4\n" \
        A_EVEN(5120, 5184, 640, 2560, 2)     A_ODD(7680, 7744, 3200, 5120, 1) \
        A_EVEN(10240, 10304, 5760, 7680, 0)  A_ODD(12800, 12864, 8320, 10240, 3) \
        "s_waitcnt vmcnt(0) lgkmcnt(0)\n s_mov_b64 exec, 1\n ds_add_u32 v127, v86\n s_mov_b64 exec, -1\n" \
        A_EVEN(15360, 15424, 10880, 12800, 2) A_LD("s[74:75]", 0) A_ODD(17920, 17984, 13440, 15360, 1) A_LD("s[74:75]", 1024) \
        A_EVEN(20480, 20544, 16000, 17920, 0) A_LD("s[74:75]", 2048) \
        "s_cmp_eq_u32 s80, 0\n s_cbranch_scc1 .Lattn_skip\n s_mov_b32 m0, s79\n s_nop 0\n" \
        A_LD("s[76:77]", 0) A_LD("s[76:77]", 1024) A_LD("s[76:77]", 2048) ".Lattn_skip:\n" \
        A_ODD(23040, 23104, 18560, 20480, 3) \
        "ds_read_b32 v132, v127\n" \
        "v_add_u32 v128, 0x5000, v128\n v_add_u32 v129, 0x5000, v129\n v_add_u32 v130, 0x5000, v130\n" \
        "s_waitcnt lgkmcnt(0)\n s_nop 0\n"
#define A_OPERANDS \
        : "+{v[32:63]}"(S), "+{v[64:79]}"(O), "+{v[80:111]}"(OPS), "+{v[112:119]}"(P), "+{v[128:131]}"(AD), "={v132}"(cnt) \
        : "{v[120:127]}"(Q), "{s[72:73]}"(vmask), "{s[74:75]}"(ga), "{s[76:77]}"(gb), "{s78}"(ma), "{s79}"(mb), "{s80}"(enb) \
        : "memory", "scc", "s82", "s83"
template <bool FIRST>
DI unsigned attn_chunk(f32x32& S, f32x16& O, u32x32& OPS, u32x8& P, u32x4& AD, const u32x8& Q, unsigned long long vmask,
                   unsigned ma, unsigned long long ga, unsigned mb, unsigned long long gb, unsigned enb) {
    unsigned cnt;
    if (FIRST) asm volatile("s_call_b64 s[82:83], .Lattn_body\n s_branch .Lattn_done\n .p2align 6\n .Lattn_body:\n" A_BODY "s_setpc_b64 s[82:83]\n .Lattn_done:\n" A_OPERANDS);
    else       asm volatile("s_call_b64 s[82:83], .Lattn_body\n" A_OPERANDS);
    return cnt;
}
DI void wait_cnt(char* lds, int off, unsigned cnt, int n) {
#pragma clang loop unroll(disable)
    while ((unsigned)rfl((int)cnt) < (unsigned)(n * 8 * 0x3F80)) { __builtin_amdgcn_s_sleep(1); cnt = *(volatile LAS unsigned*)(lds + off); }
    asm volatile("" ::: "memory");
}

DI float xhalf_max(float m) { auto rr = __builtin_amdgcn_permlane32_swap(__float_as_uint(m), __float_as_uint(m), false, false);
    return fmaxf(__uint_as_float(rr[0]), __uint_as_float(rr[1])); }
DI float hi_half(float v) {
    auto rr = __builtin_amdgcn_permlane32_swap(__float_as_uint(v), __float_as_uint(v), false, false);
    return __uint_as_float(rr[1]); }
DI float sum16(float v) { auto rr = __builtin_amdgcn_permlane16_swap(__float_as_uint(v), __float_as_uint(v), false, false);
    return __uint_as_float(rr[0]) + __uint_as_float(rr[1]); }
DI float sum32(float v) { auto rr = __builtin_amdgcn_permlane32_swap(__float_as_uint(v), __float_as_uint(v), false, false);
    return __uint_as_float(rr[0]) + __uint_as_float(rr[1]); }
DI f32x4 sum_rows(f32x4 a, f32x4 b) {
    f32x4 s = a + b;
#pragma unroll
    for (int i = 0; i < 4; ++i) s[i] = sum32(sum16(s[i]));
    return s;
}
DI unsigned long long uni64(const void* p) { const unsigned long long a = (unsigned long long)(uintptr_t)p;
    return (unsigned long long)(unsigned)rfl((int)(unsigned)a) | ((unsigned long long)(unsigned)rfl((int)(unsigned)(a >> 32)) << 32); }
DI void store_o(rsrc_t rs, const f32x16& o, float f, int rt, int br, int h, int lane) {
    const int rb = (int)OFF_O + (rt * 80 + (h >> 2) * 10) * 1024;
    __builtin_amdgcn_raw_buffer_store_b128(u32x4{pk2(o[0] * f, o[1] * f), pk2(o[2] * f, o[3] * f), pk2(o[4] * f, o[5] * f), pk2(o[6] * f, o[7] * f)},
                                           rs, lane * 16, rb + (br * 4 + (h & 3)) * 1024, SC1);
    if (lane < 32) __builtin_amdgcn_raw_buffer_store_b64(u32x2{pk2(o[8] * f, o[9] * f), pk2(o[10] * f, o[11] * f)},
                                           rs, lane * 16, rb + (8 + br) * 1024 + ((h >> 1) & 1) * 512 + (h & 1) * 8, SC1);
}
DI void set_flag(rsrc_t rs, int off) { __builtin_amdgcn_raw_buffer_store_b32(1u, rs, 0, off, SC1); }
DI unsigned get_flag(rsrc_t rs, int voff, int off) { return __builtin_amdgcn_raw_buffer_load_b32(rs, voff, off, SC1); }
DI unsigned a_ld1(unsigned long long sb, unsigned voff) { unsigned v; asm volatile("global_load_dword %0, %1, %2 sc1" : "=v"(v) : "v"(voff), "s"(sb) : "memory"); return v; }
DI u32x4 a_ld4s(unsigned long long sb, unsigned voff) { u32x4 v; asm volatile("global_load_dwordx4 %0, %1, %2 sc1" : "=v"(v) : "v"(voff), "s"(sb) : "memory"); return v; }
template <int OFF> DI u32x4 a_ld4(unsigned long long sb, unsigned voff) { u32x4 v;
    asm volatile("global_load_dwordx4 %0, %1, %2 offset:%3" : "=v"(v) : "v"(voff), "s"(sb), "n"(OFF) : "memory"); return v; }
template <class T> DI void landed(T& v) { asm volatile("" : "+v"(v)); }
DI void wait_flag(rsrc_t rs, int voff, int off, unsigned v) {
    if (__builtin_expect(!__all(v == 1u), 0)) {
#pragma clang loop unroll(disable)
        for (int spin = 0; spin < (1 << 22); ++spin) { if (__all(get_flag(rs, voff, off) == 1u)) break; __builtin_amdgcn_s_sleep(1); }
    }
    __builtin_amdgcn_fence(__ATOMIC_ACQUIRE, "wavefront");
    asm volatile("" ::: "memory");
}

constexpr int L_SP = 136192, L_K0 = 152320, L_SQ = 153600, L_QA = 1280, L_KVF = 11520, L_WO = 13312;

__global__ __launch_bounds__(512, 2) void k2_attn(char* __restrict__ ws, u16* __restrict__ out) {
    extern __shared__ __attribute__((aligned(16))) char lds[];
    PSTAMP(1, 0); WSTAMP(0);
    const int tid = threadIdx.x, lane = tid & 63, wave = rfl(tid >> 6), bid = blockIdx.x;
    const int h = (bid & 7) * 4 + ((bid >> 3) & 3), qb = bid >> 5;
    const int oq = bid & 7, rb = oq * 4 + ((bid >> 3) & 3), cb = bid >> 5;
    const int r = lane & 31, hh = lane >> 5;
    const unsigned lds0 = (unsigned)(uintptr_t)lds;
    const rsrc_t rs = __builtin_amdgcn_make_buffer_rsrc((void*)ws, 0, WS_USED, 0x00020000);
    const char* kvb = ws + OFF_IB + (size_t)h * 163840;
    const char* qim = ws + OFF_IQ + (size_t)h * 163840 + qb * 20480;
    const int rt = qb * 8 + wave;
    const int rc = r < 20 ? r : 19;
    glds16(qim + wave * 2560, lane * 16, lds0 + L_SQ + wave * 1280);
    glds16(qim + wave * 2560 + 256, lane * 16, lds0 + L_SQ + wave * 1280 + 256);
    glds16(kvb + (wave & 1) * 256, lane * 16, lds0 + L_K0 + (wave & 1) * 256);
#pragma unroll
    for (int i = 0; i < 3; ++i) glds16(kvb + (1 + i * 8 + wave) * 1024, lane * 16, lds0 + (1 + i * 8 + wave) * 1024);
    const char* ap = ws + OFF_IA + (size_t)h * 163840 + rt * 2560 + hh * 320 + rc * 16;
    u32x4 a0, a1, b0, b1; float gate;
    asm volatile("global_load_dwordx4 %0, %5, off\n\tglobal_load_dwordx4 %1, %5, off offset:640\n\tglobal_load_dwordx4 %2, %5, off offset:1280\n\t"
                 "global_load_dwordx4 %3, %5, off offset:1920\n\tglobal_load_dword %4, %6, off"
                 : "=&v"(a0), "=&v"(a1), "=&v"(b0), "=&v"(b1), "=&v"(gate) : "v"(ap), "v"((const float*)(ws + OFF_G) + rt * 32 + r) : "memory");
#pragma unroll
    for (int i = 0; i < 3; ++i) glds16(kvb + (25 + i * 8 + wave) * 1024, lane * 16, lds0 + (25 + i * 8 + wave) * 1024);
    if (tid < 2) *(LAS unsigned*)(lds + tid * 4) = 0u;
    const u32x4 ONES = {0x3F803F80u, 0x3F803F80u, 0x3F803F80u, 0x3F803F80u};
    const f32x16 Z16 = {};
    wait_vm_bar<8>();
    PSTAMP(1, 1); WSTAMP(1);
    const int g = lane >> 4, q = (lane >> 2) & 3, p = lane & 3;
    const int tro = (g & 1) * 640 + (p & 1) * 320 + q * 16 + (p >> 1) * 8;
    const LAS char* L = (const LAS char*)lds;
    const LAS char* kmp = L + tro + hh * 128;
    const LAS char* ktp = L + tro + 256;
    const LAS char* vp  = L + 1280 + hh * 320 + r * 16;
    const int ent0 = wave * 16 + (lane & 15), ent = ent0 < 126 ? ent0 : 125;
    u32x8 Q;
    {
        const u32x2 a = trd(kmp + L_SQ + wave * 1280), b = trd(kmp + L_SQ + wave * 1280 + 64), c = trd(ktp + L_SQ + wave * 1280);
        Q[0] = a[0]; Q[1] = a[1]; Q[2] = b[0]; Q[3] = b[1]; Q[4] = hh ? 0u : c[0]; Q[5] = hh ? 0u : c[1]; Q[6] = 0u; Q[7] = 0u;
    }
    const bf16x8 qm = B8(((u32x4){Q[0], Q[1], Q[2], Q[3]}));
    f32x32 S; f32x16 O = {}; u32x32 OPS; u32x8 P = {}; u32x4 AD;
    {
        const u32x2 a = trd(kmp + L_K0), b = trd(kmp + L_K0 + 64), c = trd(ktp + L_K0);
        const bf16x8 k0 = B8(((u32x4){a[0], a[1], b[0], b[1]})), k1 = B8(((u32x4){c[0], c[1], 0x3F80u, 0u}));
        const f32x16 sm = MFMA32(k0, qm, Z16);
        f32x16 s0 = MFMA32(k1, B8(((u32x4){Q[4], Q[5], 0u, 0u})), sm);
        float m = s0[0];
#pragma unroll
        for (int i = 1; i < 16; ++i) m = fmaxf(m, s0[i]);
        m = xhalf_max(m);
        Q[6] = hh ? 0u : (pk2(-m, 0.f) & 0xffffu);
        s0 = MFMA32(k1, B8(((u32x4){Q[4], Q[5], Q[6], 0u})), sm);
#pragma unroll
        for (int i = 0; i < 16; ++i) { S[i] = s0[i]; S[16 + i] = 0.f; }
        const unsigned vc = r == 20 ? 0x3F803F80u : 0u;
#pragma unroll
        for (int i = 0; i < 32; ++i) OPS[i] = (i & 8) ? vc : 0u;
        const u32x2 a1 = trd(kmp + 2560), b1 = trd(kmp + 2560 + 64), c1 = trd(ktp + 2560);
        OPS[0] = a1[0]; OPS[1] = a1[1]; OPS[2] = b1[0]; OPS[3] = b1[1]; OPS[4] = c1[0]; OPS[5] = c1[1]; OPS[6] = 0x3F80u; OPS[22] = 0x3F80u;
        if (r < 20) { const u32x4 v0 = *(const LAS u32x4*)vp; OPS[12] = v0[0]; OPS[13] = v0[1]; OPS[14] = v0[2]; OPS[15] = v0[3]; }
        AD[0] = (unsigned)(uintptr_t)kmp; AD[1] = (unsigned)(uintptr_t)ktp; AD[2] = (unsigned)(uintptr_t)vp; AD[3] = lane * 16;
    }
    {
        asm volatile("s_waitcnt vmcnt(3)" : "+v"(a0), "+v"(a1), "+v"(b0), "+v"(b1), "+v"(gate) :: "memory");
        if (r >= 20) { a0 = u32x4{0u, 0u, 0u, 0u}; a1 = a0; b0 = ONES; b1 = ONES; }
        f32x16 kvacc = MFMA32(B8(a0), B8(b0), Z16); kvacc = MFMA32(B8(a1), B8(b1), kvacc);
        if (r <= 20) {
            LAS f32x4* dst = (LAS f32x4*)(lds + L_SP + ((wave * 2 + hh) * 21 + r) * 48);
            dst[0] = f32x4{kvacc[0], kvacc[1], kvacc[2], kvacc[3]};
            dst[1] = f32x4{kvacc[4], kvacc[5], kvacc[6], kvacc[7]};
            dst[2] = f32x4{kvacc[8], kvacc[9], kvacc[10], kvacc[11]};
        }
    }
    const unsigned long long vmask = 0x000FFFFF000FFFFFull;
    const char* wop = ws + OFF_WOT + (size_t)((cb * 8 + wave) * 25) * 1024;
    const int flA = (int)OFF_FL + (32 + oq * 4 + (wave >> 1)) * 128 + (wave & 1) * 64, flv = (lane & 3) * 16;
    const int wo7 = wave < 3 ? L_QA + wave * 3072 : L_WO + (29 + wave) * 3072;
    unsigned fl = 0u;
    u32x4 kp0, kp1, wa[10]; bf16x8 wf[25];
    const unsigned long long wsu = uni64(ws), wou = uni64(wop + 4096);
#pragma unroll
    for (int c = 0; c < 8; ++c) {
        unsigned ma, mb; unsigned long long ga, gb;
        if (c < 3) {
            const int lim = (c == 1 ? L_SP : 163840) - 3072;
            const int oa = (c == 2 ? L_SP : 50176 + c * 49152) + wave * 3072, ob = oa + 24576 < lim ? oa + 24576 : lim;
            ma = lds0 + oa; mb = lds0 + ob; ga = uni64(kvb + oa); gb = uni64(kvb + ob);
        } else {
            ma = lds0 + (c == 7 ? wo7 : L_WO + ((c - 3) * 8 + wave) * 3072); mb = ma; ga = uni64(wop + (10 + 3 * (c - 3)) * 1024); gb = ga;
        }
        CSTAMP(2 * c);
        unsigned cnt;
        if (c == 0) cnt = attn_chunk<true>(S, O, OPS, P, AD, Q, vmask, rfl(ma), ga, rfl(mb), gb, 1u);
        else cnt = attn_chunk<false>(S, O, OPS, P, AD, Q, vmask, rfl(ma), ga, rfl(mb), gb, c < 3 ? 1u : 0u);
        CSTAMP(2 * c + 1);
        if (c < 7) wait_cnt(lds, 0, cnt, c + 1);
        if (c == 0) {
            const f32x4 s = sum_rows(*(const LAS f32x4*)(L + L_SP + g * 2016 + ent * 16), *(const LAS f32x4*)(L + L_SP + (g + 4) * 2016 + ent * 16));
            if (lane < 16) __builtin_amdgcn_raw_buffer_store_b128(__builtin_bit_cast(u32x4, s), rs, ent0 * 16, (int)OFF_KVP + (h * 8 + qb) * 2048, SC1);
            glds16(qim + wave * 2560 + 1280, lane * 16, lds0 + L_QA + wave * 1280);
            glds16(qim + wave * 2560 + 1280 + 256, lane * 16, lds0 + L_QA + wave * 1280 + 256);
        } else if (c == 1) {
            if (tid == 0) set_flag(rs, (int)OFF_FL + h * 128 + qb * 16);
        } else if (c == 2) {
            fl = a_ld1(wsu, (unsigned)OFF_FL + h * 128 + (lane & 7) * 16);
        } else if (c == 3) {
            landed(fl);
            wait_flag(rs, (lane & 7) * 16, (int)OFF_FL + h * 128, fl);
            kp0 = a_ld4s(wsu, (unsigned)OFF_KVP + h * 16384 + g * 2048 + ent * 16);
            kp1 = a_ld4s(wsu, (unsigned)OFF_KVP + h * 16384 + g * 2048 + ent * 16 + 8192);
        } else if (c == 4) {
            landed(kp0); landed(kp1);
            const f32x4 s = sum_rows(__builtin_bit_cast(f32x4, kp0), __builtin_bit_cast(f32x4, kp1));
            const int e3 = ent / 3;
            if (lane < 16) *(LAS u32x2*)(lds + L_KVF + e3 * 32 + (ent - e3 * 3) * 8) = u32x2{pk2(s[0], s[1]), pk2(s[2], s[3])};
            wa[0] = a_ld4<-4096>(wou, lane * 16); wa[1] = a_ld4<-3072>(wou, lane * 16); wa[2] = a_ld4<-2048>(wou, lane * 16);
            wa[3] = a_ld4<-1024>(wou, lane * 16); wa[4] = a_ld4<0>(wou, lane * 16);
        } else if (c == 5) {
            const int rr = r <= 20 ? r : 20;
            const u32x4 ka0 = *(const LAS u32x4*)(L + L_KVF + (hh * 21 + rr) * 32);
            const u32x2 ka1 = *(const LAS u32x2*)(L + L_KVF + (hh * 21 + rr) * 32 + 16);
            const LAS char* qa = L + L_QA + wave * 1280 + tro;
            const u32x2 pq0 = trd(qa + hh * 64), pq1 = trd(qa + 128 + hh * 64);
            u32x2 pq2 = trd(qa + 256);
            if (hh) pq2 = u32x2{0u, 0u};
            f32x16 oa = MFMA32(B8(ka0), B8(((u32x4){pq0[0], pq0[1], pq1[0], pq1[1]})), Z16);
            oa = MFMA32(B8(((u32x4){ka1[0], ka1[1], 0u, 0u})), B8(((u32x4){pq2[0], pq2[1], 0u, 0u})), oa);
            const float den = hi_half(oa[8]) + 1e-6f;
            store_o(rs, oa, gate * __builtin_amdgcn_rcpf(den), rt, 0, h, lane);
        } else if (c == 6) {
            if (tid == 0) set_flag(rs, (int)OFF_FL + (32 + qb * 4 + (h >> 3)) * 128 + (h & 7) * 16);
            wa[5] = a_ld4<1024>(wou, lane * 16); wa[6] = a_ld4<2048>(wou, lane * 16); wa[7] = a_ld4<3072>(wou, lane * 16);
            wa[8] = a_ld4<-4096>(wou + 8192, lane * 16); wa[9] = a_ld4<-3072>(wou + 8192, lane * 16);
        }
    }
#pragma unroll
    for (int i = 0; i < 10; ++i) { landed(wa[i]); wf[i] = B8(wa[i]); }
    fl = a_ld1(wsu, flA + flv);
    __builtin_amdgcn_s_setprio(0);
    O = MFMA32(B8(((u32x4){OPS[8], OPS[9], OPS[10], OPS[11]})), B8(((u32x4){P[4], P[5], P[6], P[7]})), O);
    PSTAMP(1, 2); WSTAMP(2);
    float l = hi_half(O[8]);
    {
        bool bad = !(l >= 1e-18f && l <= 1.0e30f);
#pragma unroll
        for (int i = 0; i < 12; ++i) bad = bad || !(fabsf(O[i]) <= 3.0e38f);
        if (__builtin_expect(__any(bad), 0)) {
            const bf16x8 qt0 = B8(((u32x4){Q[4], Q[5], 0u, 0u}));
            float m = -INFINITY; O = Z16;
            const int ko = (r >> 4) * 640 + ((r >> 2) & 1) * 320 + ((r >> 3) & 1) * 8 + (r & 3) * 2;
            for (int t = 0; t < 64; ++t) {
                const char* tb = kvb + t * 2560;
                unsigned kw[6];
#pragma unroll
                for (int i = 0; i < 6; ++i) {
                    const int d = i < 4 ? 8 * hh + 2 * i : 16 + 2 * (i - 4);
                    kw[i] = (unsigned)*(const u16*)(tb + ko + d * 16) | ((unsigned)*(const u16*)(tb + ko + d * 16 + 16) << 16);
                }
                u32x4 f0 = *(const u32x4*)(tb + 1280 + hh * 320 + rc * 16), f1 = *(const u32x4*)(tb + 1920 + hh * 320 + rc * 16);
                if (r >= 20) { f0 = ONES; f1 = ONES; }
                f32x16 s = MFMA32(B8(((u32x4){kw[0], kw[1], kw[2], kw[3]})), qm, Z16);
                s = MFMA32(B8(((u32x4){kw[4], kw[5], 0u, 0u})), qt0, s);
                float tm = s[0];
#pragma unroll
                for (int i = 1; i < 16; ++i) tm = fmaxf(tm, s[i]);
                tm = xhalf_max(tm);
                const float mn = fmaxf(m, tm);
                const float al = (m == -INFINITY) ? 0.f : __builtin_amdgcn_exp2f(m - mn);
                m = mn;
#pragma unroll
                for (int i = 0; i < 16; ++i) { O[i] *= al; s[i] = __builtin_amdgcn_exp2f(s[i] - mn); }
                u32x4 p0 = {pk2(s[0], s[1]), pk2(s[2], s[3]), pk2(s[4], s[5]), pk2(s[6], s[7])};
                u32x4 p1 = {pk2(s[8], s[9]), pk2(s[10], s[11]), pk2(s[12], s[13]), pk2(s[14], s[15])};
                O = MFMA32(B8(f0), B8(p0), O); O = MFMA32(B8(f1), B8(p1), O);
            }
            l = hi_half(O[8]);
        }
    }
    store_o(rs, O, (1.f - gate) * __builtin_amdgcn_rcpf(l), rt, 1, h, lane);
    const int ov = ((g >> 1) * 1024 + (g & 1) * 512 + (lane & 15) * 16), os = (int)OFF_O + (rb * 160 + wave * 10) * 1024;
    bf16x8 af[3][4];
    asm volatile("s_waitcnt vmcnt(2)" : "+v"(fl) :: "memory");
    wait_flag(rs, flv, flA, fl);
#pragma unroll
    for (int k = 0; k < 2; ++k)
#pragma unroll
        for (int mi = 0; mi < 4; ++mi) af[k][mi] = B8(__builtin_amdgcn_raw_buffer_load_b128(rs, ov + k * 2048 + (mi & 1) * 256, os + (mi >> 1) * 81920, SC1));
#pragma unroll
    for (int i = 0; i < 15; ++i) wf[10 + i] = *(const LAS bf16x8*)(L + (i < 12 ? L_WO + ((i / 3) * 8 + wave) * 3072 : wo7) + (i % 3) * 1024 + lane * 16);
    {
        unsigned old;
        asm volatile("s_waitcnt vmcnt(8) lgkmcnt(0)\n\ts_mov_b64 exec, 1\n\tds_add_rtn_u32 %0, %1, %2 offset:4\n\ts_mov_b64 exec, -1\n\ts_waitcnt lgkmcnt(0)"
                     : "=&v"(old) : "v"(lds0), "v"(0x3F80u) : "memory");
        if (rfl((int)old) == 7 * 0x3F80 && lane == 0) set_flag(rs, (int)OFF_FL + (64 + qb * 4 + (h >> 3)) * 128 + (h & 7) * 16);
    }
    WSTAMP(3);
    f32x4 acc[4][5] = {};
#pragma unroll
    for (int k = 0; k < 2; ++k)
#pragma unroll
        for (int mi = 0; mi < 4; ++mi)
#pragma unroll
            for (int ni = 0; ni < 5; ++ni) acc[mi][ni] = MFMA16(wf[k * 5 + ni], af[k][mi], acc[mi][ni]);
    __builtin_amdgcn_sched_barrier(0);
    WSTAMP(4);
    wait_flag(rs, flv, flA + 4096, get_flag(rs, flv, flA + 4096));
    WSTAMP(5);
#pragma unroll
    for (int k = 0; k < 3; ++k)
#pragma unroll
        for (int mi = 0; mi < 4; ++mi) af[k][mi] = B8(__builtin_amdgcn_raw_buffer_load_b128(rs, ov + (k + 2) * 2048 + (mi & 1) * 256, os + (mi >> 1) * 81920, SC1));
    wait_cnt(lds, 4, *(volatile LAS unsigned*)(lds + 4), 1);
    __builtin_amdgcn_sched_barrier(0);
#pragma unroll
    for (int k = 0; k < 3; ++k)
#pragma unroll
        for (int mi = 0; mi < 4; ++mi)
#pragma unroll
            for (int ni = 0; ni < 5; ++ni) acc[mi][ni] = MFMA16(wf[10 + k * 5 + ni], af[k][mi], acc[mi][ni]);
    LAS char* red = (LAS char*)lds;
    {
        LAS char* wp = red + wave * 20480 + (lane & 15) * 320 + ((g ^ ((lane >> 1) & 3)) << 4);
#pragma unroll
        for (int mi = 0; mi < 4; ++mi)
#pragma unroll
            for (int ni = 0; ni < 5; ++ni) if (mi + ni > 0 || tid != 0) *(LAS f32x4*)(wp + mi * 5120 + ni * 64) = acc[mi][ni];
    }
    WSTAMP(6);
    __syncthreads();
#pragma unroll
    for (int it = 0; it < 3; ++it) {
        const int id = it * 512 + tid;
        if (id < 1280) {
            const int row = id / 20, cs = id - row * 20, cc = cs ^ ((row >> 1) & 3);
            f32x4 v = *(const LAS f32x4*)(red + id * 16);
            if (id == 0) v = acc[0][0];
#pragma unroll
            for (int w = 1; w < 8; ++w) v += *(const LAS f32x4*)(red + w * 20480 + id * 16);
            *(u32x2*)(out + (size_t)(rb * 64 + row) * D_ + cb * 80 + cc * 4) = u32x2{pk2(v[0], v[1]), pk2(v[2], v[3])};
        }
    }
}

extern "C" void kernel_launch(void* const* d_in, const int* in_sizes, int n_in, void* d_out, int out_size, void* d_ws, size_t ws_size,
                              hipStream_t stream) {
    const u16* x = (const u16*)d_in[0];
    const u16* wA = (const u16*)d_in[1];
    const u16* woA = (const u16*)d_in[2];
    const u16* wB = (const u16*)d_in[4];
    const u16* woB = (const u16*)d_in[5];
    const u16* gw = (const u16*)d_in[6];
    char* ws = (char*)d_ws;
    (void)hipFuncSetAttribute((const void*)k1_qkv, hipFuncAttributeMaxDynamicSharedMemorySize, LDS_BYTES);
    (void)hipFuncSetAttribute((const void*)k2_attn, hipFuncAttributeMaxDynamicSharedMemorySize, LDS_BYTES);
    k1_qkv<<<256, 512, LDS_BYTES, stream>>>(x, wA, wB, woA, woB, gw, ws);
    k2_attn<<<256, 512, LDS_BYTES, stream>>>(ws, (u16*)d_out);
}
```

```cpp
#define PROBE 0
#include <hip/hip_runtime.h>
#include <hip/hip_bf16.h>
#include <stdint.h>

typedef short    bf16x8 __attribute__((ext_vector_type(8)));
typedef short    s16x4  __attribute__((ext_vector_type(4)));
typedef float    f32x2  __attribute__((ext_vector_type(2)));
typedef float    f32x4  __attribute__((ext_vector_type(4)));
typedef float    f32x16 __attribute__((ext_vector_type(16)));
typedef float    f32x32 __attribute__((ext_vector_type(32)));
typedef unsigned u32x2  __attribute__((ext_vector_type(2)));
typedef unsigned u32x4  __attribute__((ext_vector_type(4)));
typedef unsigned u32x8  __attribute__((ext_vector_type(8)));
typedef unsigned u32x32 __attribute__((ext_vector_type(32)));
typedef __bf16   bf2_t  __attribute__((ext_vector_type(2)));
typedef unsigned short u16;
#define LAS __attribute__((address_space(3)))
#define DI __device__ __forceinline__

constexpr int S_ = 2048, D_ = 640;
constexpr size_t IMG     = (size_t)32 * 163840;
constexpr size_t OFF_IQ  = 0;
constexpr size_t OFF_IB  = IMG;
constexpr size_t OFF_IA  = 2 * IMG;
constexpr size_t OFF_O   = 3 * IMG;
constexpr size_t OFF_WOT = 4 * IMG;
constexpr size_t OFF_G   = OFF_WOT + (size_t)1600 * 1024;
constexpr size_t OFF_KVP = OFF_G + 8192;
constexpr size_t OFF_FL  = OFF_KVP + (size_t)256 * 2048;
constexpr int    WS_USED = (int)(OFF_FL + 288 * 128);
constexpr unsigned EMPTY = 0xFFFFFFFFu;
typedef __amdgpu_buffer_rsrc_t rsrc_t;
constexpr int SC1 = 16;

#define CSTAMP(i)
#define PSTAMP(k, i)
#define WSTAMP(i)

DI unsigned pk2(float lo, float hi) { f32x2 v = {lo, hi}; return __builtin_bit_cast(unsigned, __builtin_convertvector(v, bf2_t)); }
DI float bflo(unsigned v) { return __uint_as_float(v << 16); }
DI float bfhi(unsigned v) { return __uint_as_float(v & 0xffff0000u); }
DI int rfl(int x) { return __builtin_amdgcn_readfirstlane(x); }
DI void glds16(const void* sbase, unsigned voff, unsigned lds_base) {
    const unsigned long long a = (unsigned long long)(uintptr_t)sbase;
    const unsigned long long u = (unsigned long long)(unsigned)rfl((int)(unsigned)a) | ((unsigned long long)(unsigned)rfl((int)(unsigned)(a >> 32)) << 32);
    asm volatile("s_mov_b32 m0, %0\n\ts_nop 0\n\tglobal_load_lds_dwordx4 %1, %2" :: "s"(rfl((int)lds_base)), "v"(voff), "s"(u) : "memory");
}
#define MFMA16(a, b, c) __builtin_amdgcn_mfma_f32_16x16x32_bf16((a), (b), (c), 0, 0, 0)
#define MFMA32(a, b, c) __builtin_amdgcn_mfma_f32_32x32x16_bf16((a), (b), (c), 0, 0, 0)
#define B8(x) __builtin_bit_cast(bf16x8, x)
DI u32x2 trd(const LAS char* p) { return __builtin_bit_cast(u32x2, __builtin_amdgcn_ds_read_tr16_b64_v4i16((LAS s16x4*)p)); }
DI float phi(float t) { return (t > 0.f ? t + 1.f : __expf(t)) + 1e-6f; }
template <int N> DI void wait_vm_bar() { asm volatile("s_waitcnt vmcnt(%0) lgkmcnt(0)\n\ts_barrier" :: "n"(N) : "memory"); }

constexpr int LDS_BYTES = 163840;

#define FILL_FIRST 0
#define FILL_AUX 0
#define FILL_EMPTY { const rsrc_t rs = __builtin_amdgcn_make_buffer_rsrc((void*)ws, 0, WS_USED, 0x00020000); \
    const u32x4 E4 = {EMPTY, EMPTY, EMPTY, EMPTY}; \
    _Pragma("unroll") for (int i = 0; i < 4; ++i) { \
        const int ub = (int)OFF_O + ((a * 4 + i) * 80 + wave * 5) * 1024; \
        _Pragma("unroll") for (int e = 0; e < 5; ++e) __builtin_amdgcn_raw_buffer_store_b128(E4, rs, lane * 16 + e * 1024, ub, FILL_AUX); \
        __builtin_amdgcn_raw_buffer_store_b128(E4, rs, tid * 16 + i * 8192, (int)OFF_KVP + a * 32768, FILL_AUX); } }
DI void p1_aux(const u16* __restrict__ x, const u16* __restrict__ woA, const u16* __restrict__ woB, const u16* __restrict__ gate_w,
               char* __restrict__ ws, char* lds, int a) {
    const int tid = threadIdx.x, lane = tid & 63, wave = rfl(tid >> 6);
    const unsigned lds0 = (unsigned)(uintptr_t)lds;
    const int br = a >> 3, u = a & 7;
    if (tid < 144) *(u32x4*)(ws + OFF_FL + (a * 144 + tid) * 16) = u32x4{0u, 0u, 0u, 0u};
    const char* wsrc = (const char*)(br ? woB : woA) + (size_t)u * 102400;
    for (int i = wave; i < 100; i += 8) glds16(wsrc + i * 1024, lane * 16, lds0 + i * 1024);
    const u32x4* xs = (const u32x4*)(x + (size_t)(a * 128 + wave * 16) * D_);
    u32x4 xv[20], gw[5];
#pragma unroll
    for (int i = 0; i < 20; ++i) xv[i] = xs[i * 64 + lane];
#pragma unroll
    for (int i = 0; i < 5; ++i) gw[i] = ((const u32x4*)gate_w)[(i * 64 + lane) % 80];
    LAS float* part = (LAS float*)(lds + 102400) + wave * 1280;
#pragma unroll
    for (int i = 0; i < 20; ++i) {
        float s = 0.f;
#pragma unroll
        for (int j = 0; j < 4; ++j) s += bflo(xv[i][j]) * bflo(gw[i % 5][j]) + bfhi(xv[i][j]) * bfhi(gw[i % 5][j]);
        part[i * 64 + lane] = s;
    }
    wait_vm_bar<0>();
    {
        float s = 0.f;
#pragma unroll
        for (int j = 0; j < 20; ++j) s += part[lane * 20 + j];
        s += __shfl_xor(s, 1); s += __shfl_xor(s, 2);
        if ((lane & 3) == 0) ((float*)(ws + OFF_G))[a * 128 + wave * 16 + (lane >> 2)] = 1.f / (1.f + __expf(-s));
    }
    const int g = lane >> 4, hh = g & 1, ns = g >> 1, q = (lane >> 2) & 3, p = lane & 3;
    for (int un = wave; un < 100; un += 8) {
        const int e = un / 20, np = un - e * 20, nt = np * 2 + ns;
        const int r0 = e < 4 ? e * 20 + 4 * hh : (2 * hh) * 20 + 16;
        const int r1 = e < 4 ? r0 + 8 : r0 + 20;
        const int ks16 = e < 4 ? (u & 1) * 4 + e : 8 + (u & 1), cbk = nt / 5;
        const u32x2 lo = trd((const LAS char*)lds + (r0 + q) * 1280 + nt * 32 + p * 8);
        const u32x2 hi = trd((const LAS char*)lds + (r1 + q) * 1280 + nt * 32 + p * 8);
        *(u32x4*)(ws + OFF_WOT + (size_t)((cbk * 8 + (u & 6) + br) * 25 + (ks16 >> 1) * 5 + nt - cbk * 5) * 1024 + ((ks16 & 1) * 2 + hh) * 256 + (lane & 15) * 16) = u32x4{lo[0], lo[1], hi[0], hi[1]};
    }
    FILL_EMPTY
}

#define G1_P0 4
#define G1_P1 2
constexpr int g1_free(int i) { const int u = i / 3, m = i % 3, t = 2 * u + m - 1;
    return m == 0 ? (u < 3 ? -1 : 2 * (u - 2)) : (t < 7 ? -1 : t - 6); }
struct G1Plan { int seq[30]; int cnt[22]; int pos[30]; };
constexpr G1Plan g1_plan() {
    G1Plan p = {}; bool done[30] = {}; int n = 0;
    for (int t = 0; t <= 20; ++t) {
        int room = t == 0 ? G1_P0 : G1_P1;
        for (int i = 0; i < 30 && room > 0; ++i) if (!done[i] && g1_free(i) < t) { done[i] = true; p.pos[i] = n; p.seq[n++] = i; --room; }
        p.cnt[t] = n;
    }
    return p;
}
constexpr G1Plan G1P = g1_plan();
constexpr int g1_wait(int t) { const int a = G1P.pos[3 * (t >> 1)], b = G1P.pos[3 * (t >> 1) + 1 + (t & 1)]; return 4 * (G1P.cnt[t] - 1 - (a > b ? a : b)); }
constexpr bool g1_ok() { for (int t = 0; t < 20; ++t) if (g1_wait(t) < 0 || g1_wait(t) > 60) return false; return G1P.cnt[19] == 30; }
static_assert(g1_ok(), "loader plan");
#define G1_ROT xcd
#define G1_PF 0
#define G1_PFD 4
#define G1_PFD0 4
#define G1_ST 1
constexpr int G1_WOFF = 49152;

#define LST_P
#define LST_A
template <int T> struct G1Load {
    template <class FX, class FW> static DI void run(FX&& ldx, FW&& ldw LST_P) {
        asm volatile("s_waitcnt vmcnt(%0)\n\ts_barrier" :: "n"(g1_wait(T)) : "memory");
#pragma unroll
        for (int n = G1P.cnt[T]; n < G1P.cnt[T + 1]; ++n) { const int i = G1P.seq[n]; if (i % 3 == 0) ldx(i / 3); else ldw(2 * (i / 3) + i % 3 - 1); }
        G1Load<T + 1>::run(ldx, ldw LST_A);
    }
};
template <> struct G1Load<20> { template <class FX, class FW> static DI void run(FX&&, FW&& LST_P) {} };

struct G1Frag { bf16x8 x[2]; u32x2 w[4][2]; };
#define SB0() __builtin_amdgcn_sched_barrier(0)
template <int J> struct G1Step {
    static DI void run(f32x16 (&acc)[8], G1Frag& cur, G1Frag& nxt, const LAS char* const (&xa)[4], const LAS char* const (&wa)[4]) {
        constexpr int N = J + 1, t = N >> 1, xo = ((N >> 2) % 3) * 16384, sl = t % 7, wo = (sl & 3) * 16384 + (N & 1) * 4096, wi = (sl >> 2) * 2;
#define G1_M(i) acc[i] = MFMA32(cur.x[(i) & 1], B8(((u32x4){cur.w[(i) >> 1][0][0], cur.w[(i) >> 1][0][1], cur.w[(i) >> 1][1][0], cur.w[(i) >> 1][1][1]})), acc[i]); SB0();
#define G1_X(i) if (N < 40) nxt.x[i] = *(const LAS bf16x8*)(xa[N & 3] + xo + (i) * 4096);
#define G1_W(i) if (N < 40) { nxt.w[i][0] = trd(wa[wi + ((i) & 1)] + wo + ((i) >> 1) * 8192); nxt.w[i][1] = trd(wa[wi + ((i) & 1)] + wo + ((i) >> 1) * 8192 + 1024); } SB0();
        if (J & 1) {
            G1_M(0) G1_M(1)
            if (N < 40) asm volatile("s_waitcnt lgkmcnt(0)\n\ts_barrier" ::: "memory");
            SB0();
            G1_X(0) G1_W(0) G1_M(2) G1_X(1) G1_W(1) G1_M(3) G1_W(2) G1_M(4) G1_W(3) G1_M(5) G1_M(6) G1_M(7)
        } else {
            G1_M(0) G1_X(0) G1_W(0) G1_M(1) G1_X(1) G1_W(1) G1_M(2) G1_W(2) G1_M(3) G1_W(3) G1_M(4) G1_M(5) G1_M(6) G1_M(7)
        }
        G1Step<J + 1>::run(acc, nxt, cur, xa, wa);
    }
};
template <> struct G1Step<40> { static DI void run(f32x16 (&)[8], G1Frag&, G1Frag&, const LAS char* const (&)[4], const LAS char* const (&)[4]) {} };

DI void phase1(const u16* __restrict__ x, const u16* __restrict__ wA, const u16* __restrict__ wB, char* __restrict__ ws, char* lds, int bid) {
    const int tid = threadIdx.x, lane = tid & 63, wave = rfl(tid >> 6);
    const unsigned lds0 = (unsigned)(uintptr_t)lds;
    const int xcd = bid & 7, j = (xcd >> 1) * 30 + (bid >> 3);
    const int ct = j >> 3, mt = (xcd & 1) * 8 + (j & 7);
    const int type = rfl(ct / 5), rot = rfl(G1_ROT);
    if (__builtin_expect(wave >= 4, 1)) {
        const int lw = wave - 4;
        unsigned xo[4], wo[2];
#pragma unroll
        for (int i = 0; i < 4; ++i) { const int row = (lw * 4 + i) * 8 + (lane >> 3); xo[i] = (unsigned)((mt * 128 + row) * 1280 + (((lane & 7) ^ ((row >> 1) & 7)) << 4)); }
#pragma unroll
        for (int i = 0; i < 2; ++i) { const int k = (lw * 2 + i) * 4 + (lane >> 4), s16 = lane & 15;
            wo[i] = (unsigned)(k * 3840 + ct * 256 + ((((s16 >> 2) ^ (k & 3)) << 6) | ((s16 & 3) << 4))); }
        auto ldx = [&](int u) {
            int kk = u + rot; kk -= kk >= 10 ? 10 : 0;
            const unsigned slot = lds0 + (u % 3) * 16384 + lw * 4096;
#pragma unroll
            for (int i = 0; i < 4; ++i) glds16((const char*)x + kk * 128, xo[i], slot + i * 1024);
        };
        auto ldw = [&](int t) {
            int kk = (t >> 1) + rot; kk -= kk >= 10 ? 10 : 0;
            const unsigned slot = lds0 + G1_WOFF + (t % 7) * 16384 + lw * 2048;
            const int go = (kk * 64 + (t & 1) * 32) * 3840;
            glds16((const char*)wB + go, wo[0], slot);        glds16((const char*)wB + go, wo[1], slot + 1024);
            glds16((const char*)wA + go, wo[0], slot + 8192); glds16((const char*)wA + go, wo[1], slot + 8192 + 1024);
        };
#pragma unroll
        for (int n = 0; n < G1P.cnt[0]; ++n) { const int i = G1P.seq[n]; if (i % 3 == 0) ldx(i / 3); else ldw(2 * (i / 3) + i % 3 - 1); }
        G1Load<0>::run(ldx, ldw LST_A);
        return;
    }
    const int wm = wave & 1, wn = wave >> 1;
    const int r = lane & 31, hh = lane >> 5, g = lane >> 4, q = (lane >> 2) & 3, p = lane & 3;
    const LAS char* xa[4]; const LAS char* wa[4];
#pragma unroll
    for (int ks = 0; ks < 4; ++ks) { xa[ks] = (const LAS char*)lds + (wm * 64 + r) * 128 + (((ks * 2 + hh) ^ ((r >> 1) & 7)) << 4); asm volatile("" : "+v"(xa[ks])); }
#pragma unroll
    for (int i = 0; i < 4; ++i) { wa[i] = (const LAS char*)lds + G1_WOFF + (i >> 1) * 65536 + (8 * (g >> 1) + q) * 256 + (((wn * 2 + (i & 1)) ^ q) << 6) + (g & 1) * 32 + p * 8;
        asm volatile("" : "+v"(wa[i])); }
    f32x16 acc[8] = {};
    G1Frag f0, f1;
    __builtin_amdgcn_s_setprio(2);
    asm volatile("s_barrier" ::: "memory");
    f0.x[0] = *(const LAS bf16x8*)(xa[0]); f0.x[1] = *(const LAS bf16x8*)(xa[0] + 4096);
#pragma unroll
    for (int i = 0; i < 4; ++i) { f0.w[i][0] = trd(wa[i & 1] + (i >> 1) * 8192); f0.w[i][1] = trd(wa[i & 1] + (i >> 1) * 8192 + 1024); }
    SB0();
    G1Step<0>::run(acc, f0, f1, xa, wa);
    PSTAMP(0, 2);
    constexpr float QS = 0.22360679774997896f * 1.4426950408889634f;
#pragma unroll
    for (int i = 0; i < 8; ++i) {
        const int mi = i & 1, jn = (i >> 1) & 1, pass = i >> 2;
        const size_t ib = type == 0 ? OFF_IQ + pass * 1280 : (pass ? OFF_IA : OFF_IB) + (type - 1) * 1280;
        const int c = (ct - type * 5) * 128 + wn * 64 + jn * 32 + r, h = c / 20, d = c - h * 20;
        f32x16 v = acc[i];
        if (pass == 0) { if (type == 0) v *= QS; }
        else if (type < 2) {
#pragma unroll
            for (int e = 0; e < 16; ++e) v[e] = phi(v[e]);
        }
        char* dst = ws + ib + (size_t)h * 163840 + (mt * 4 + wm * 2 + mi) * 2560 + hh * 320 + d * 16;
        const u32x4 s0 = u32x4{pk2(v[0], v[1]), pk2(v[2], v[3]), pk2(v[4], v[5]), pk2(v[6], v[7])};
        const u32x4 s1 = u32x4{pk2(v[8], v[9]), pk2(v[10], v[11]), pk2(v[12], v[13]), pk2(v[14], v[15])};
        asm volatile("global_store_dwordx4 %0, %1, off sc1\n\tglobal_store_dwordx4 %0, %2, off offset:640 sc1\n\ts_nop 1" :: "v"(dst), "v"(s0), "v"(s1) : "memory");
    }
}

__global__ __launch_bounds__(512, 2) void k1_qkv(const u16* __restrict__ x, const u16* __restrict__ wA, const u16* __restrict__ wB,
                                                 const u16* __restrict__ woA, const u16* __restrict__ woB, const u16* __restrict__ gate_w,
                                                 char* __restrict__ ws) {
    extern __shared__ __attribute__((aligned(16))) char lds[];
    int bid = blockIdx.x;
    asm volatile("" : "+s"(bid));
    PSTAMP(0, 0);
    const unsigned pt0 = (unsigned)__builtin_amdgcn_s_memrealtime();
    if (__builtin_expect(bid < 240, 1)) phase1(x, wA, wB, ws, lds, bid);
    else p1_aux(x, woA, woB, gate_w, ws, lds, bid - 240);
    if (threadIdx.x == 0) { asm volatile("s_waitcnt vmcnt(0)"); unsigned* pp = (unsigned*)(ws + (64 << 20)) + bid * 2; pp[0] = pt0; pp[1] = (unsigned)__builtin_amdgcn_s_memrealtime(); }
}

#define STR(x) #x
#define XS(x) STR(x)
#define A_E(i) "v_exp_f32 v" XS(i) ", v" XS(i) "\n"
#define A_C(d, a, b) "v_cvt_pk_bf16_f32 v" XS(d) ", v" XS(a) ", v" XS(b) "\n"
#define A_G(a, b, c, d, p0, p1) A_E(a) A_E(b) A_E(c) A_E(d) A_C(p0, a, b) A_C(p1, c, d)
#define A_M(d, a, b, c) "v_mfma_f32_32x32x16_bf16 " d ", " a ", " b ", " c "\n"
#define A_STEP(C0,C1,C2,C3,C4,C5,C6,C7,C8,C9,C10,C11,C12,C13,C14,C15, SN, KM, KT, V1, V0, NKMa, NKMb, NKT, NV1, NV0, OK0, OK1, OV1, OV0, PRIO) \
    "s_waitcnt lgkmcnt(0)\n" \
    A_M(SN, KM, "v[120:123]", "0") \
    "ds_read_b64_tr_b16 " NKMa ", v128 offset:" XS(OK0) "\n" \
    "ds_read_b64_tr_b16 " NKMb ", v128 offset:" XS(OK1) "\n" \
    "ds_read_b64_tr_b16 " NKT ", v129 offset:" XS(OK0) "\n" \
    A_G(C0, C1, C2, C3, 112, 113) \
    A_M("v[64:79]", V1, "v[116:119]", "v[64:79]") \
    "s_mov_b64 exec, s[72:73]\n" \
    "ds_read_b128 " NV1 ", v130 offset:" XS(OV1) "\n" \
    "ds_read_b128 " NV0 ", v130 offset:" XS(OV0) "\n" \
    "s_mov_b64 exec, -1\n" \
    A_G(C4, C5, C6, C7, 114, 115) \
    A_M(SN, KT, "v[124:127]", SN) \
    A_G(C8, C9, C10, C11, 116, 117) \
    A_M("v[64:79]", V0, "v[112:115]", "v[64:79]") \
    "s_setprio " XS(PRIO) "\n" \
    A_G(C12, C13, C14, C15, 118, 119)
#define A_EVEN(OK0, OK1, OV1, OV0, PRIO) A_STEP(32,33,34,35,36,37,38,39,40,41,42,43,44,45,46,47, "v[48:63]", "v[80:83]", "v[84:87]", "v[88:91]", "v[92:95]", \
    "v[96:97]", "v[98:99]", "v[100:101]", "v[104:107]", "v[108:111]", OK0, OK1, OV1, OV0, PRIO)
#define A_ODD(OK0, OK1, OV1, OV0, PRIO) A_STEP(48,49,50,51,52,53,54,55,56,57,58,59,60,61,62,63, "v[32:47]", "v[96:99]", "v[100:103]", "v[104:107]", "v[108:111]", \
    "v[80:81]", "v[82:83]", "v[84:85]", "v[88:91]", "v[92:95]", OK0, OK1, OV1, OV0, PRIO)

#define A_LD(g, off) "global_load_lds_dwordx4 v131, " g " offset:" XS(off) "\n"
#define A_LDA(off) "s_cbranch_scc1 .Lattn_s" XS(off) "\n" A_LD("s[74:75]", off) ".Lattn_s" XS(off) ":\n"
#define A_BODY \
        "s_mov_b32 m0, s78\n s_cmp_eq_u32 s80, 2\n s_nop 3\n" \
        A_EVEN(5120, 5184, 640, 2560, 2)     A_ODD(7680, 7744, 3200, 5120, 1) \
        A_EVEN(10240, 10304, 5760, 7680, 0)  A_ODD(12800, 12864, 8320, 10240, 3) \
        "s_waitcnt vmcnt(0) lgkmcnt(0)\n s_mov_b64 exec, 1\n ds_add_u32 v127, v86\n s_mov_b64 exec, -1\n" \
        A_EVEN(15360, 15424, 10880, 12800, 2) A_LDA(0) A_ODD(17920, 17984, 13440, 15360, 1) A_LDA(1024) \
        A_EVEN(20480, 20544, 16000, 17920, 0) A_LDA(2048) \
        "s_cmp_eq_u32 s80, 1\n s_cbranch_scc0 .Lattn_skip\n s_mov_b32 m0, s79\n s_nop 0\n" \
        A_LD("s[76:77]", 0) A_LD("s[76:77]", 1024) A_LD("s[76:77]", 2048) ".Lattn_skip:\n" \
        "ds_read_b32 v132, v127\n" \
        A_ODD(23040, 23104, 18560, 20480, 3) \
        "v_add_u32 v128, 0x5000, v128\n v_add_u32 v129, 0x5000, v129\n v_add_u32 v130, 0x5000, v130\n" \
        "s_waitcnt lgkmcnt(0)\n s_nop 0\n"
#define A_OPERANDS \
        : "+{v[32:63]}"(S), "+{v[64:79]}"(O), "+{v[80:111]}"(OPS), "+{v[112:119]}"(P), "+{v[128:131]}"(AD), "={v132}"(cnt) \
        : "{v[120:127]}"(Q), "{s[72:73]}"(vmask), "{s[74:75]}"(ga), "{s[76:77]}"(gb), "{s78}"(ma), "{s79}"(mb), "{s80}"(enb) \
        : "memory", "scc", "s82", "s83"
template <bool FIRST>
DI unsigned attn_chunk(f32x32& S, f32x16& O, u32x32& OPS, u32x8& P, u32x4& AD, const u32x8& Q, unsigned long long vmask,
                   unsigned ma, unsigned long long ga, unsigned mb, unsigned long long gb, unsigned enb) {
    unsigned cnt;
    if (FIRST) asm volatile("s_call_b64 s[82:83], .Lattn_body\n s_branch .Lattn_done\n .p2align 6\n .Lattn_body:\n" A_BODY "s_setpc_b64 s[82:83]\n .Lattn_done:\n" A_OPERANDS);
    else       asm volatile("s_call_b64 s[82:83], .Lattn_body\n" A_OPERANDS);
    return cnt;
}
DI void wait_cnt(char* lds, int off, unsigned cnt, int n) {
    if (__builtin_expect((unsigned)rfl((int)cnt) < (unsigned)(n * 0x3F80), 0)) {
#pragma clang loop unroll(disable)
        do { __builtin_amdgcn_s_sleep(1); cnt = *(volatile LAS unsigned*)(lds + off); } while ((unsigned)rfl((int)cnt) < (unsigned)(n * 0x3F80));
    }
    asm volatile("" ::: "memory");
}

DI void wait_cnt(char* lds, int off, int n) { wait_cnt(lds, off, *(volatile LAS unsigned*)(lds + off), n); }
template <int OFF> DI void add_cnt(unsigned lds0) {
    asm volatile("s_waitcnt lgkmcnt(0)\n\ts_mov_b64 exec, 1\n\tds_add_u32 %0, %1 offset:%2\n\ts_mov_b64 exec, -1" :: "v"(lds0), "v"(0x3F80u), "n"(OFF) : "memory"); }

DI float xhalf_max(float m) { auto rr = __builtin_amdgcn_permlane32_swap(__float_as_uint(m), __float_as_uint(m), false, false);
    return fmaxf(__uint_as_float(rr[0]), __uint_as_float(rr[1])); }
DI float hi_half(float v) {
    auto rr = __builtin_amdgcn_permlane32_swap(__float_as_uint(v), __float_as_uint(v), false, false);
    return __uint_as_float(rr[1]); }
DI float sum16(float v) { auto rr = __builtin_amdgcn_permlane16_swap(__float_as_uint(v), __float_as_uint(v), false, false);
    return __uint_as_float(rr[0]) + __uint_as_float(rr[1]); }
DI float sum32(float v) { auto rr = __builtin_amdgcn_permlane32_swap(__float_as_uint(v), __float_as_uint(v), false, false);
    return __uint_as_float(rr[0]) + __uint_as_float(rr[1]); }
DI f32x4 sum_rows(f32x4 a, f32x4 b) {
    f32x4 s = a + b;
#pragma unroll
    for (int i = 0; i < 4; ++i) s[i] = sum32(sum16(s[i]));
    return s;
}
DI unsigned long long uni64(const void* p) { const unsigned long long a = (unsigned long long)(uintptr_t)p;
    return (unsigned long long)(unsigned)rfl((int)(unsigned)a) | ((unsigned long long)(unsigned)rfl((int)(unsigned)(a >> 32)) << 32); }
DI unsigned full(unsigned v) { return v < EMPTY ? v : EMPTY - 1u; }
DI unsigned umax4(unsigned m, u32x4 v) { return max(max(max(m, v[0]), max(v[1], v[2])), v[3]); }
DI void store_o(rsrc_t rs, const f32x16& o, float f, int rt, int br, int h, int lane) {
    const int rb = (int)OFF_O + (rt * 80 + ((br ? 0 : 4) + (h >> 3)) * 10) * 1024;
    __builtin_amdgcn_raw_buffer_store_b128(u32x4{full(pk2(o[0] * f, o[1] * f)), full(pk2(o[2] * f, o[3] * f)), full(pk2(o[4] * f, o[5] * f)), full(pk2(o[6] * f, o[7] * f))},
                                           rs, lane * 16, rb + (h & 7) * 1024, SC1);
    if (lane < 32) __builtin_amdgcn_raw_buffer_store_b64(u32x2{full(pk2(o[8] * f, o[9] * f)), full(pk2(o[10] * f, o[11] * f))},
                                           rs, lane * 16, rb + (8 + ((h >> 2) & 1)) * 1024 + ((h >> 1) & 1) * 512 + (h & 1) * 8, SC1);
}
DI void set_flag(rsrc_t rs, int off) { __builtin_amdgcn_raw_buffer_store_b32(1u, rs, 0, off, SC1); }
DI unsigned get_flag(rsrc_t rs, int voff, int off) { return __builtin_amdgcn_raw_buffer_load_b32(rs, voff, off, SC1); }
DI unsigned a_ld1(unsigned long long sb, unsigned voff) { unsigned v; asm volatile("global_load_dword %0, %1, %2 sc1" : "=v"(v) : "v"(voff), "s"(sb) : "memory"); return v; }
DI u32x4 a_ld4s(unsigned long long sb, unsigned voff) { u32x4 v; asm volatile("global_load_dwordx4 %0, %1, %2 sc1" : "=v"(v) : "v"(voff), "s"(sb) : "memory"); return v; }
template <int OFF> DI u32x4 a_ld4so(unsigned long long sb, unsigned voff) { u32x4 v;
    asm volatile("global_load_dwordx4 %0, %1, %2 offset:%3 sc1" : "=v"(v) : "v"(voff), "s"(sb), "n"(OFF) : "memory"); return v; }
template <int OFF> DI u32x4 a_ld4(unsigned long long sb, unsigned voff) { u32x4 v;
    asm volatile("global_load_dwordx4 %0, %1, %2 offset:%3" : "=v"(v) : "v"(voff), "s"(sb), "n"(OFF) : "memory"); return v; }
template <class T> DI void landed(T& v) { asm volatile("" : "+v"(v)); }
DI void wait_flag(rsrc_t rs, int voff, int off, unsigned v) {
    if (__builtin_expect(!__all(v == 1u), 0)) {
#pragma clang loop unroll(disable)
        for (int spin = 0; spin < (1 << 22); ++spin) { if (__all(get_flag(rs, voff, off) == 1u)) break; __builtin_amdgcn_s_sleep(1); }
    }
    __builtin_amdgcn_fence(__ATOMIC_ACQUIRE, "wavefront");
    asm volatile("" ::: "memory");
}

constexpr int L_SP = 136192, L_K0 = 152320, L_SQ = 153600, L_QA = 1280, L_KVF = 11520, L_WO = 13312;

__global__ __launch_bounds__(512, 2) void k2_attn(char* __restrict__ ws, u16* __restrict__ out) {
    extern __shared__ __attribute__((aligned(16))) char lds[];
    int bid = blockIdx.x;
    asm volatile("" : "+s"(bid));
    PSTAMP(1, 0); WSTAMP(0);
    const unsigned pt0 = (unsigned)__builtin_amdgcn_s_memrealtime(), pc0 = (unsigned)__builtin_amdgcn_s_memtime();
    unsigned pt1 = 0, pt2 = 0;
    const int tid = threadIdx.x, lane = tid & 63, wave = rfl(tid >> 6);
    const int h = (bid & 7) * 4 + ((bid >> 3) & 3), qb = bid >> 5;
    const int oq = bid & 7, rb = oq * 4 + ((bid >> 3) & 3), cb = bid >> 5;
    const int r = lane & 31, hh = lane >> 5;
    const unsigned lds0 = (unsigned)(uintptr_t)lds;
    const rsrc_t rs = __builtin_amdgcn_make_buffer_rsrc((void*)ws, 0, WS_USED, 0x00020000);
    const char* kvb = ws + OFF_IB + (size_t)h * 163840;
    const char* qim = ws + OFF_IQ + (size_t)h * 163840 + qb * 20480;
    const int wr = (wave & 3) * 2 + (wave >> 2), rt = qb * 8 + wr;
    const int rc = r < 20 ? r : 19;
    glds16(qim + wr * 2560, lane * 16, lds0 + L_SQ + wave * 1280);
    glds16(qim + wr * 2560 + 256, lane * 16, lds0 + L_SQ + wave * 1280 + 256);
    glds16(kvb + (wave & 1) * 256, lane * 16, lds0 + L_K0 + (wave & 1) * 256);
#pragma unroll
    for (int i = 0; i < 3; ++i) glds16(kvb + (1 + i * 8 + wave) * 1024, lane * 16, lds0 + (1 + i * 8 + wave) * 1024);
    const char* ap = ws + OFF_IA + (size_t)h * 163840 + rt * 2560 + hh * 320 + rc * 16;
    u32x4 a0, a1, b0, b1; float gate;
    asm volatile("global_load_dwordx4 %0, %5, off\n\tglobal_load_dwordx4 %1, %5, off offset:640\n\tglobal_load_dwordx4 %2, %5, off offset:1280\n\t"
                 "global_load_dwordx4 %3, %5, off offset:1920\n\tglobal_load_dword %4, %6, off"
                 : "=&v"(a0), "=&v"(a1), "=&v"(b0), "=&v"(b1), "=&v"(gate) : "v"(ap), "v"((const float*)(ws + OFF_G) + rt * 32 + r) : "memory");
#pragma unroll
    for (int i = 0; i < 3; ++i) glds16(kvb + (25 + i * 8 + wave) * 1024, lane * 16, lds0 + (25 + i * 8 + wave) * 1024);
    if (tid < 4) *(LAS unsigned*)(lds + tid * 4) = 0u;
    const u32x4 ONES = {0x3F803F80u, 0x3F803F80u, 0x3F803F80u, 0x3F803F80u};
    const f32x16 Z16 = {};
    wait_vm_bar<8>();
    PSTAMP(1, 1); WSTAMP(1);
    pt1 = (unsigned)__builtin_amdgcn_s_memrealtime();
    const int g = lane >> 4, q = (lane >> 2) & 3, p = lane & 3;
    const int tro = (g & 1) * 640 + (p & 1) * 320 + q * 16 + (p >> 1) * 8;
    const LAS char* L = (const LAS char*)lds;
    const LAS char* kmp = L + tro + hh * 128;
    const LAS char* ktp = L + tro + 256;
    const LAS char* vp  = L + 1280 + hh * 320 + r * 16;
    const int ent = tid < 126 ? tid : 125;
    u32x8 Q;
    {
        const u32x2 a = trd(kmp + L_SQ + wave * 1280), b = trd(kmp + L_SQ + wave * 1280 + 64), c = trd(ktp + L_SQ + wave * 1280);
        Q[0] = a[0]; Q[1] = a[1]; Q[2] = b[0]; Q[3] = b[1]; Q[4] = hh ? 0u : c[0]; Q[5] = hh ? 0u : c[1]; Q[6] = 0u; Q[7] = 0u;
    }
    const bf16x8 qm = B8(((u32x4){Q[0], Q[1], Q[2], Q[3]}));
    f32x32 S; f32x16 O = {}; u32x32 OPS; u32x8 P = {}; u32x4 AD;
    {
        const u32x2 a = trd(kmp + L_K0), b = trd(kmp + L_K0 + 64), c = trd(ktp + L_K0);
        const bf16x8 k0 = B8(((u32x4){a[0], a[1], b[0], b[1]})), k1 = B8(((u32x4){c[0], c[1], 0x3F80u, 0u}));
        const f32x16 sm = MFMA32(k0, qm, Z16);
        f32x16 s0 = MFMA32(k1, B8(((u32x4){Q[4], Q[5], 0u, 0u})), sm);
        float m = s0[0];
#pragma unroll
        for (int i = 1; i < 16; ++i) m = fmaxf(m, s0[i]);
        m = xhalf_max(m);
        Q[6] = hh ? 0u : (pk2(-m, 0.f) & 0xffffu);
        s0 = MFMA32(k1, B8(((u32x4){Q[4], Q[5], Q[6], 0u})), sm);
#pragma unroll
        for (int i = 0; i < 16; ++i) { S[i] = s0[i]; S[16 + i] = 0.f; }
        const unsigned vc = r == 20 ? 0x3F803F80u : 0u;
#pragma unroll
        for (int i = 0; i < 32; ++i) OPS[i] = (i & 8) ? vc : 0u;
        const u32x2 a1 = trd(kmp + 2560), b1 = trd(kmp + 2560 + 64), c1 = trd(ktp + 2560);
        OPS[0] = a1[0]; OPS[1] = a1[1]; OPS[2] = b1[0]; OPS[3] = b1[1]; OPS[4] = c1[0]; OPS[5] = c1[1]; OPS[6] = 0x3F80u; OPS[22] = 0x3F80u;
        if (r < 20) { const u32x4 v0 = *(const LAS u32x4*)vp; OPS[12] = v0[0]; OPS[13] = v0[1]; OPS[14] = v0[2]; OPS[15] = v0[3]; }
        AD[0] = (unsigned)(uintptr_t)kmp; AD[1] = (unsigned)(uintptr_t)ktp; AD[2] = (unsigned)(uintptr_t)vp; AD[3] = lane * 16;
    }
    {
        asm volatile("s_waitcnt vmcnt(3)" : "+v"(a0), "+v"(a1), "+v"(b0), "+v"(b1), "+v"(gate) :: "memory");
        if (r >= 20) { a0 = u32x4{0u, 0u, 0u, 0u}; a1 = a0; b0 = ONES; b1 = ONES; }
        f32x16 kvacc = MFMA32(B8(a0), B8(b0), Z16); kvacc = MFMA32(B8(a1), B8(b1), kvacc);
        if (r <= 20) {
            LAS f32x4* dst = (LAS f32x4*)(lds + L_SP + ((wave * 2 + hh) * 21 + r) * 48);
            dst[0] = f32x4{kvacc[0], kvacc[1], kvacc[2], kvacc[3]};
            dst[1] = f32x4{kvacc[4], kvacc[5], kvacc[6], kvacc[7]};
            dst[2] = f32x4{kvacc[8], kvacc[9], kvacc[10], kvacc[11]};
        }
    }
    const unsigned long long vmask = 0x000FFFFF000FFFFFull;
    const int j = wave & 3, isE = (wave >> 2) ^ 1;
    const char* wop = ws + OFF_WOT + (size_t)((cb * 4 + j) * 50) * 1024;
    const int flA = (int)OFF_FL + (oq * 4 + j) * 128, flv = (lane & 7) * 16;
    const int ov = ((g >> 1) * 1024 + (g & 1) * 512 + (lane & 15) * 16), os = (int)OFF_O + (rb * 160 + j * 10) * 1024;
    unsigned fl = 0u;
    u32x4 kp[8], wa[20], aa[2][10];
    const unsigned long long wsu = uni64(ws);
    const unsigned long long kb = uni64(ws + OFF_KVP + 4096 + h * 16384), kb2 = uni64(ws + OFF_KVP + 12288 + h * 16384);
    const unsigned long long ob[2][2] = {{uni64(ws + os + 4096), uni64(ws + os + 8192)}, {uni64(ws + os + 81920 + 4096), uni64(ws + os + 81920 + 8192)}};
#define LD_H(A, vo, hf, en, C) asm volatile("s_cmp_lg_u32 %13, 0\n\ts_cselect_b64 exec, -1, 0\n\t" \
        "global_load_dwordx4 %0, %10, %11 offset:-4096 sc1\n\tglobal_load_dwordx4 %1, %10, %11 offset:-3840 sc1\n\t" \
        "global_load_dwordx4 %2, %10, %11 offset:-2048 sc1\n\tglobal_load_dwordx4 %3, %10, %11 offset:-1792 sc1\n\t" \
        "global_load_dwordx4 %4, %10, %11 sc1\n\tglobal_load_dwordx4 %5, %10, %11 offset:256 sc1\n\t" \
        "global_load_dwordx4 %6, %10, %11 offset:2048 sc1\n\tglobal_load_dwordx4 %7, %10, %11 offset:2304 sc1\n\t" \
        "global_load_dwordx4 %8, %10, %12 sc1\n\tglobal_load_dwordx4 %9, %10, %12 offset:256 sc1\n\ts_mov_b64 exec, -1" \
        : C(A[0]), C(A[1]), C(A[2]), C(A[3]), C(A[4]), C(A[5]), C(A[6]), C(A[7]), C(A[8]), C(A[9]) \
        : "v"(vo), "s"(ob[hf][0]), "s"(ob[hf][1]), "s"(en) : "memory", "scc");
#define LD_W4(A, f) asm volatile("s_cmp_lg_u32 %6, 0\n\ts_cselect_b64 exec, -1, 0\n\t" \
        "global_load_dwordx4 %0, %4, %5\n\tglobal_load_dwordx4 %1, %4, %5 offset:1024\n\t" \
        "global_load_dwordx4 %2, %4, %5 offset:2048\n\tglobal_load_dwordx4 %3, %4, %5 offset:3072\n\ts_mov_b64 exec, -1" \
        : "=&v"((A)[0]), "=&v"((A)[1]), "=&v"((A)[2]), "=&v"((A)[3]) : "v"(lane * 16), "s"(uni64(wop + (f) * 1024)), "s"(isE) : "memory", "scc");
#define LD_KP(TAIL) asm volatile("s_cmp_lt_u32 %11, 2\n\ts_cselect_b64 exec, -1, 0\n\t" \
        "global_load_dwordx4 %0, %8, %9 offset:-4096 sc1\n\tglobal_load_dwordx4 %1, %8, %9 offset:-2048 sc1\n\t" \
        "global_load_dwordx4 %2, %8, %9 sc1\n\tglobal_load_dwordx4 %3, %8, %9 offset:2048 sc1\n\t" \
        "global_load_dwordx4 %4, %8, %10 offset:-4096 sc1\n\tglobal_load_dwordx4 %5, %8, %10 offset:-2048 sc1\n\t" \
        "global_load_dwordx4 %6, %8, %10 sc1\n\tglobal_load_dwordx4 %7, %8, %10 offset:2048 sc1\n\t" \
        "s_mov_b64 exec, -1" TAIL \
        : "=&v"(kp[0]), "=&v"(kp[1]), "=&v"(kp[2]), "=&v"(kp[3]), "=&v"(kp[4]), "=&v"(kp[5]), "=&v"(kp[6]), "=&v"(kp[7]) \
        : "v"(ent * 16), "s"(kb), "s"(kb2), "s"(wave) : "memory", "scc")
#pragma unroll
    for (int c = 0; c < 8; ++c) {
        unsigned ma, mb; unsigned long long ga, gb;
        if (c < 3) {
            const int lim = (c == 1 ? L_SP : 163840) - 3072;
            const int oa = (c == 2 ? L_SP : 50176 + c * 49152) + wave * 3072, ob = oa + 24576 < lim ? oa + 24576 : lim;
            ma = lds0 + oa; mb = lds0 + ob; ga = uni64(kvb + oa); gb = uni64(kvb + ob);
        } else {
            ma = lds0 + (c == 7 ? 1024 + j * 3072 : L_WO + ((c - 3) * 8 + wr) * 3072); mb = ma;
            ga = uni64(wop + (c == 7 ? 44 : 20 + (wave >> 2) * 12 + 3 * (c - 3)) * 1024); gb = ga;
        }
        CSTAMP(2 * c);
        unsigned cnt;
        if (c == 0) cnt = attn_chunk<true>(S, O, OPS, P, AD, Q, vmask, rfl(ma), ga, rfl(mb), gb, 1u);
        else cnt = attn_chunk<false>(S, O, OPS, P, AD, Q, vmask, rfl(ma), ga, rfl(mb), gb, c < 3 ? 1u : c < 7 ? 0u : (unsigned)(2 - 2 * isE));
        CSTAMP(2 * c + 1);
        if (c < 7) wait_cnt(lds, 0, cnt, 8 * (c + 1));
        if (c == 0) {
            if (wave < 2) {
                f32x4 s = *(const LAS f32x4*)(L + L_SP + ent * 16);
#pragma unroll
                for (int w = 1; w < 8; ++w) s += *(const LAS f32x4*)(L + L_SP + w * 2016 + ent * 16);
                const u32x4 v = __builtin_bit_cast(u32x4, s);
                if (tid < 126) __builtin_amdgcn_raw_buffer_store_b128(u32x4{full(v[0]), full(v[1]), full(v[2]), full(v[3])}, rs, tid * 16, (int)OFF_KVP + (h * 8 + qb) * 2048, SC1);
            }
            glds16(qim + wr * 2560 + 1280, lane * 16, lds0 + L_QA + wave * 1280);
            glds16(qim + wr * 2560 + 1280 + 256, lane * 16, lds0 + L_QA + wave * 1280 + 256);
        } else if (c == 1) {
            LD_KP("");
        } else if (c == 2) {
#pragma unroll
            for (int j = 0; j < 8; ++j) landed(kp[j]);
            if (wave < 2) {
#pragma clang loop unroll(disable)
                for (int spin = 0; spin < (1 << 20); ++spin) {
                    unsigned m = 0u;
#pragma unroll
                    for (int j = 0; j < 8; ++j) m = umax4(m, kp[j]);
                    if (__builtin_expect(__all(m != EMPTY), 1)) break;
                    LD_KP("\n\ts_waitcnt vmcnt(0)");
                }
                f32x4 s = __builtin_bit_cast(f32x4, kp[0]);
#pragma unroll
                for (int j = 1; j < 8; ++j) s += __builtin_bit_cast(f32x4, kp[j]);
                const int e3 = ent / 3;
                *(LAS u32x2*)(lds + L_KVF + e3 * 32 + (ent - e3 * 3) * 8) = u32x2{pk2(s[0], s[1]), pk2(s[2], s[3])};
            }
        } else if (c == 3) {
            const int rr = r <= 20 ? r : 20;
            const u32x4 ka0 = *(const LAS u32x4*)(L + L_KVF + (hh * 21 + rr) * 32);
            const u32x2 ka1 = *(const LAS u32x2*)(L + L_KVF + (hh * 21 + rr) * 32 + 16);
            const LAS char* qa = L + L_QA + wave * 1280 + tro;
            const u32x2 pq0 = trd(qa + hh * 64), pq1 = trd(qa + 128 + hh * 64);
            u32x2 pq2 = trd(qa + 256);
            if (hh) pq2 = u32x2{0u, 0u};
            f32x16 oa = MFMA32(B8(ka0), B8(((u32x4){pq0[0], pq0[1], pq1[0], pq1[1]})), Z16);
            oa = MFMA32(B8(((u32x4){ka1[0], ka1[1], 0u, 0u})), B8(((u32x4){pq2[0], pq2[1], 0u, 0u})), oa);
            const float den = hi_half(oa[8]) + 1e-6f;
            store_o(rs, oa, gate * __builtin_amdgcn_rcpf(den), rt, 0, h, lane);
            LD_W4(wa, 0) LD_W4(wa + 4, 4)
        } else if (c == 4) {
            if (tid == 0) set_flag(rs, (int)OFF_FL + (qb * 4 + (h >> 3)) * 128 + (h & 7) * 16);
            LD_W4(wa + 8, 8) LD_W4(wa + 12, 12)
        } else if (c == 5) {
            fl = a_ld1(wsu, flA + flv);
            LD_W4(wa + 16, 16)
        } else if (c == 6) {
            landed(fl);
            if (wave < 4) wait_flag(rs, flv, flA, fl);
            LD_H(aa[0], ov + 40960, 0, isE, "=&v")
        }
    }
#pragma unroll
    for (int i = 0; i < 10; ++i) { landed(wa[i]); landed(wa[10 + i]); landed(aa[0][i]); }
    u32x4 wd[4];
    LD_H(aa[1], ov + 40960, 1, isE, "=&v") LD_W4(wd, 46)
    if (lane == 0) set_flag(rs, (int)OFF_FL + (32 + rt * 4 + (h >> 3)) * 128 + (h & 7) * 16);
    O = MFMA32(B8(((u32x4){OPS[8], OPS[9], OPS[10], OPS[11]})), B8(((u32x4){P[4], P[5], P[6], P[7]})), O);
    PSTAMP(1, 2); WSTAMP(2);
    pt2 = (unsigned)__builtin_amdgcn_s_memrealtime();
    float l = hi_half(O[8]);
    {
        bool bad = !(l >= 1e-18f && l <= 1.0e30f);
#pragma unroll
        for (int i = 0; i < 12; ++i) bad = bad || !(fabsf(O[i]) <= 3.0e38f);
        if (__builtin_expect(__any(bad), 0)) {
            const bf16x8 qt0 = B8(((u32x4){Q[4], Q[5], 0u, 0u}));
            float m = -INFINITY; O = Z16;
            int tl = tid;
            asm volatile("" : "+v"(tl));
            const int r = tl & 31, hh = (tl >> 5) & 1, rc = r < 20 ? r : 19;
            const int ko = (r >> 4) * 640 + ((r >> 2) & 1) * 320 + ((r >> 3) & 1) * 8 + (r & 3) * 2;
            for (int t = 0; t < 64; ++t) {
                const char* tb = kvb + t * 2560;
                unsigned kw[6];
#pragma unroll
                for (int i = 0; i < 6; ++i) {
                    const int d = i < 4 ? 8 * hh + 2 * i : 16 + 2 * (i - 4);
                    kw[i] = (unsigned)*(const u16*)(tb + ko + d * 16) | ((unsigned)*(const u16*)(tb + ko + d * 16 + 16) << 16);
                }
                u32x4 f0 = *(const u32x4*)(tb + 1280 + hh * 320 + rc * 16), f1 = *(const u32x4*)(tb + 1920 + hh * 320 + rc * 16);
                if (r >= 20) { f0 = ONES; f1 = ONES; }
                f32x16 s = MFMA32(B8(((u32x4){kw[0], kw[1], kw[2], kw[3]})), qm, Z16);
                s = MFMA32(B8(((u32x4){kw[4], kw[5], 0u, 0u})), qt0, s);
                float tm = s[0];
#pragma unroll
                for (int i = 1; i < 16; ++i) tm = fmaxf(tm, s[i]);
                tm = xhalf_max(tm);
                const float mn = fmaxf(m, tm);
                const float al = (m == -INFINITY) ? 0.f : __builtin_amdgcn_exp2f(m - mn);
                m = mn;
#pragma unroll
                for (int i = 0; i < 16; ++i) { O[i] *= al; s[i] = __builtin_amdgcn_exp2f(s[i] - mn); }
                u32x4 p0 = {pk2(s[0], s[1]), pk2(s[2], s[3]), pk2(s[4], s[5]), pk2(s[6], s[7])};
                u32x4 p1 = {pk2(s[8], s[9]), pk2(s[10], s[11]), pk2(s[12], s[13]), pk2(s[14], s[15])};
                O = MFMA32(B8(f0), B8(p0), O); O = MFMA32(B8(f1), B8(p1), O);
            }
            l = hi_half(O[8]);
        }
    }
    store_o(rs, O, (1.f - gate) * __builtin_amdgcn_rcpf(l), rt, 1, h, lane);
    __builtin_amdgcn_s_setprio(0);
    __builtin_amdgcn_sched_barrier(0);
    WSTAMP(3);
    constexpr int L_P0 = L_WO + 98304;
    if (wave < 4) {
        const LAS char* wl = L + j * 6144 + lane * 16;
        auto wfr = [&](int f) -> bf16x8 {
            if (f < 20) return B8(wa[f]);
            if (f < 44) return *(const LAS bf16x8*)(wl + L_WO + (((f - 20) % 12) / 3) * 24576 + ((f - 20) / 12) * 3072 + ((f - 20) % 3) * 1024);
            if (f < 46) return *(const LAS bf16x8*)(L + 1024 + j * 3072 + (f - 44) * 1024 + lane * 16);
            return B8(wd[f - 46]);
        };
        LAS char* wp = (LAS char*)lds + (lane & 15) * 320 + ((g ^ ((lane >> 1) & 3)) << 4);
        f32x4 acc[4][5] = {};
        {
            bf16x8 w4[5];
#pragma unroll
            for (int ni = 0; ni < 5; ++ni) w4[ni] = wfr(20 + ni);
#pragma unroll
            for (int hf = 0; hf < 2; ++hf) {
                if (hf) asm volatile("s_waitcnt vmcnt(3)" : "+v"(aa[1][0]), "+v"(aa[1][1]), "+v"(aa[1][2]), "+v"(aa[1][3]), "+v"(aa[1][4]), "+v"(aa[1][5]), "+v"(aa[1][6]),
                                     "+v"(aa[1][7]), "+v"(aa[1][8]), "+v"(aa[1][9]), "+v"(wd[0]), "+v"(wd[1]), "+v"(wd[2]), "+v"(wd[3]) :: "memory");
#pragma unroll
                for (int k = 0; k < 5; ++k)
#pragma unroll
                    for (int mi = 0; mi < 2; ++mi)
#pragma unroll
                        for (int ni = 0; ni < 5; ++ni) acc[hf * 2 + mi][ni] = MFMA16(k < 4 ? wfr(k * 5 + ni) : w4[ni], B8(aa[hf][2 * k + mi]), acc[hf * 2 + mi][ni]);
                __builtin_amdgcn_sched_barrier(0);
            }
        }
        const int flB = (int)OFF_FL + (32 + rb * 8 + j) * 128;
        u32x4 b0[10], b1[10];
        wait_flag(rs, flv, flB, get_flag(rs, flv, flB));
        WSTAMP(4);
        LD_H(b0, ov, 0, 1, "=&v")
        fl = a_ld1(wsu, flB + 512 + flv);
        wait_cnt(lds, 0, 64);
        asm volatile("s_waitcnt vmcnt(0)" : "+v"(fl) :: "memory");
        wait_flag(rs, flv, flB + 512, fl);
        WSTAMP(5);
        LD_H(b1, ov, 1, 1, "=&v")
#pragma clang loop unroll(disable)
        for (int spin = 0; spin < (1 << 20); ++spin) {
            unsigned m = 0u;
#pragma unroll
            for (int i = 0; i < 10; ++i) { landed(b0[i]); m = umax4(m, b0[i]); }
            if (__builtin_expect(__all(m != EMPTY), 1)) break;
            LD_H(b0, ov, 0, 1, "+v")
            asm volatile("s_waitcnt vmcnt(0)" ::: "memory");
        }
        bf16x8 wb[25];
#pragma unroll
        for (int k = 0; k < 5; ++k) {
#pragma unroll
            for (int ni = 0; ni < 5; ++ni) wb[k * 5 + ni] = wfr(25 + k * 5 + ni);
#pragma unroll
            for (int mi = 0; mi < 2; ++mi)
#pragma unroll
                for (int ni = 0; ni < 5; ++ni) acc[mi][ni] = MFMA16(wb[k * 5 + ni], B8(b0[2 * k + mi]), acc[mi][ni]);
        }
#pragma unroll
        for (int mi = 0; mi < 2; ++mi)
#pragma unroll
            for (int ni = 0; ni < 5; ++ni) *(LAS f32x4*)(wp + L_P0 + j * 10240 + mi * 5120 + ni * 64) = acc[mi][ni];
        add_cnt<12>(lds0);
        f32x4 ac1[2][5];
#pragma clang loop unroll(disable)
        for (int spin = 0; spin < (1 << 20); ++spin) {
            unsigned m = 0u;
#pragma unroll
            for (int k = 0; k < 5; ++k) {
                asm volatile("s_waitcnt vmcnt(%2)" : "+v"(b1[2 * k]), "+v"(b1[2 * k + 1]) : "n"(8 - 2 * k) : "memory");
#pragma unroll
                for (int mi = 0; mi < 2; ++mi)
#pragma unroll
                    for (int ni = 0; ni < 5; ++ni) ac1[mi][ni] = MFMA16(wb[k * 5 + ni], B8(b1[2 * k + mi]), k ? ac1[mi][ni] : acc[2 + mi][ni]);
                m = umax4(umax4(m, b1[2 * k]), b1[2 * k + 1]);
                __builtin_amdgcn_sched_barrier(0);
            }
            if (__builtin_expect(__all(m != EMPTY), 1)) break;
            LD_H(b1, ov, 1, 1, "+v")
        }
#pragma unroll
        for (int mi = 0; mi < 2; ++mi)
#pragma unroll
            for (int ni = 0; ni < 5; ++ni) *(LAS f32x4*)(wp + L_WO + j * 6144 + mi * 24576 + ni * 64) = ac1[mi][ni];
        WSTAMP(6);
        asm volatile("s_waitcnt lgkmcnt(0)\n\ts_barrier" ::: "memory");
    } else {
        const int t = tid - 256;
        const LAS char* rp[2][3]; unsigned oo[3];
#pragma unroll
        for (int i = 0; i < 3; ++i) {
            const int id = i < 2 || t < 128 ? i * 256 + t : 1, row = id / 20, cs = id - row * 20, cc = cs ^ ((row >> 1) & 3);
            rp[0][i] = L + L_P0 + id * 16; rp[1][i] = L + L_WO + id * 16 + (id >= 320 ? 24576 - 5120 : 0);
            oo[i] = (unsigned)(((rb * 64 + row) * D_ + cb * 80 + cc * 4) * 2);
            landed(rp[0][i]); landed(rp[1][i]); landed(oo[i]);
        }
        WSTAMP(4);
#pragma unroll
        for (int hf = 0; hf < 2; ++hf) {
            if (hf == 0) { wait_cnt(lds, 12, 4); WSTAMP(5); }
            else { WSTAMP(6); asm volatile("s_barrier" ::: "memory"); }
            f32x4 pv[3][4];
#pragma unroll
            for (int i = 0; i < 3; ++i)
#pragma unroll
                for (int w = 0; w < 4; ++w) pv[i][w] = *(const LAS f32x4*)(rp[hf][i] + w * (hf ? 6144 : 10240));
            __builtin_amdgcn_sched_barrier(0);
#pragma unroll
            for (int i = 0; i < 3; ++i) {
                const f32x4 v = (pv[i][0] + pv[i][1]) + (pv[i][2] + pv[i][3]);
                if (i < 2 || t < 128) *(u32x2*)((char*)out + hf * (32 * D_ * 2) + oo[i]) = u32x2{pk2(v[0], v[1]), pk2(v[2], v[3])};
            }
        }
    }
    if (bid == 0 && wave == 4) {
        const unsigned pt3 = (unsigned)__builtin_amdgcn_s_memrealtime(), pc3 = (unsigned)__builtin_amdgcn_s_memtime();
        const unsigned* pp = (const unsigned*)(ws + (64 << 20));
        int mn = 1 << 30, mx = -(1 << 30);
        for (int i = 0; i < 4; ++i) { const int b = i * 64 + (threadIdx.x & 63); mn = min(mn, (int)(pp[b * 2] - pt0)); mx = max(mx, (int)(pp[b * 2 + 1] - pt0)); }
        for (int o = 1; o < 64; o <<= 1) { mn = min(mn, __shfl_xor(mn, o)); mx = max(mx, __shfl_xor(mx, o)); }
        int X = PROBE == 0 ? 0 : PROBE == 1 ? mx - mn : PROBE == 2 ? -mx : PROBE == 3 ? (int)(pt3 - pt0) : PROBE == 4 ? (int)(pc3 - pc0) / 20 :
                PROBE == 5 ? (int)(pt1 - pt0) : PROBE == 6 ? (int)(pt2 - pt1) : (int)(pt3 - pt2);
        X = rfl(min(max(X, 0), 6000));
        while ((int)((unsigned)__builtin_amdgcn_s_memrealtime() - pt3) < 300 + 4 * X) __builtin_amdgcn_s_sleep(1);
    }
}

extern "C" void kernel_launch(void* const* d_in, const int* in_sizes, int n_in, void* d_out, int out_size, void* d_ws, size_t ws_size,
                              hipStream_t stream) {
    const u16* x = (const u16*)d_in[0];
    const u16* wA = (const u16*)d_in[1];
    const u16* woA = (const u16*)d_in[2];
    const u16* wB = (const u16*)d_in[4];
    const u16* woB = (const u16*)d_in[5];
    const u16* gw = (const u16*)d_in[6];
    char* ws = (char*)d_ws;
    (void)hipFuncSetAttribute((const void*)k1_qkv, hipFuncAttributeMaxDynamicSharedMemorySize, LDS_BYTES);
    (void)hipFuncSetAttribute((const void*)k2_attn, hipFuncAttributeMaxDynamicSharedMemorySize, LDS_BYTES);
    k1_qkv<<<256, 512, LDS_BYTES, stream>>>(x, wA, wB, woA, woB, gw, ws);
    k2_attn<<<256, 512, LDS_BYTES, stream>>>(ws, (u16*)d_out);
}
```

```cpp
#define FILL_AUX 16
#define FILL_FIRST 1
#include <hip/hip_runtime.h>
#include <hip/hip_bf16.h>
#include <stdint.h>

typedef short    bf16x8 __attribute__((ext_vector_type(8)));
typedef short    s16x4  __attribute__((ext_vector_type(4)));
typedef float    f32x2  __attribute__((ext_vector_type(2)));
typedef float    f32x4  __attribute__((ext_vector_type(4)));
typedef float    f32x16 __attribute__((ext_vector_type(16)));
typedef float    f32x32 __attribute__((ext_vector_type(32)));
typedef unsigned u32x2  __attribute__((ext_vector_type(2)));
typedef unsigned u32x4  __attribute__((ext_vector_type(4)));
typedef unsigned u32x8  __attribute__((ext_vector_type(8)));
typedef unsigned u32x32 __attribute__((ext_vector_type(32)));
typedef __bf16   bf2_t  __attribute__((ext_vector_type(2)));
typedef unsigned short u16;
#define LAS __attribute__((address_space(3)))
#define DI __device__ __forceinline__

constexpr int S_ = 2048, D_ = 640;
constexpr size_t IMG     = (size_t)32 * 163840;
constexpr size_t OFF_IQ  = 0;
constexpr size_t OFF_IB  = IMG;
constexpr size_t OFF_IA  = 2 * IMG;
constexpr size_t OFF_O   = 3 * IMG;
constexpr size_t OFF_WOT = 4 * IMG;
constexpr size_t OFF_G   = OFF_WOT + (size_t)1600 * 1024;
constexpr size_t OFF_KVP = OFF_G + 8192;
constexpr size_t OFF_FL  = OFF_KVP + (size_t)256 * 2048;
constexpr int    WS_USED = (int)(OFF_FL + 288 * 128);
constexpr unsigned EMPTY = 0xFFFFFFFFu;
typedef __amdgpu_buffer_rsrc_t rsrc_t;
constexpr int SC1 = 16;

#define CSTAMP(i)
#define PSTAMP(k, i)
#define WSTAMP(i)

DI unsigned pk2(float lo, float hi) { f32x2 v = {lo, hi}; return __builtin_bit_cast(unsigned, __builtin_convertvector(v, bf2_t)); }
DI float bflo(unsigned v) { return __uint_as_float(v << 16); }
DI float bfhi(unsigned v) { return __uint_as_float(v & 0xffff0000u); }
DI int rfl(int x) { return __builtin_amdgcn_readfirstlane(x); }
DI void glds16(const void* sbase, unsigned voff, unsigned lds_base) {
    const unsigned long long a = (unsigned long long)(uintptr_t)sbase;
    const unsigned long long u = (unsigned long long)(unsigned)rfl((int)(unsigned)a) | ((unsigned long long)(unsigned)rfl((int)(unsigned)(a >> 32)) << 32);
    asm volatile("s_mov_b32 m0, %0\n\ts_nop 0\n\tglobal_load_lds_dwordx4 %1, %2" :: "s"(rfl((int)lds_base)), "v"(voff), "s"(u) : "memory");
}
#define MFMA16(a, b, c) __builtin_amdgcn_mfma_f32_16x16x32_bf16((a), (b), (c), 0, 0, 0)
#define MFMA32(a, b, c) __builtin_amdgcn_mfma_f32_32x32x16_bf16((a), (b), (c), 0, 0, 0)
#define B8(x) __builtin_bit_cast(bf16x8, x)
DI u32x2 trd(const LAS char* p) { return __builtin_bit_cast(u32x2, __builtin_amdgcn_ds_read_tr16_b64_v4i16((LAS s16x4*)p)); }
DI float phi(float t) { return (t > 0.f ? t + 1.f : __expf(t)) + 1e-6f; }
template <int N> DI void wait_vm_bar() { asm volatile("s_waitcnt vmcnt(%0) lgkmcnt(0)\n\ts_barrier" :: "n"(N) : "memory"); }

constexpr int LDS_BYTES = 163840;

#define FILL_EMPTY { const rsrc_t rs = __builtin_amdgcn_make_buffer_rsrc((void*)ws, 0, WS_USED, 0x00020000); \
    const u32x4 E4 = {EMPTY, EMPTY, EMPTY, EMPTY}; \
    _Pragma("unroll") for (int i = 0; i < 4; ++i) { \
        const int ub = (int)OFF_O + ((a * 4 + i) * 80 + wave * 5) * 1024; \
        _Pragma("unroll") for (int e = 0; e < 5; ++e) __builtin_amdgcn_raw_buffer_store_b128(E4, rs, lane * 16 + e * 1024, ub, FILL_AUX); \
        __builtin_amdgcn_raw_buffer_store_b128(E4, rs, tid * 16 + i * 8192, (int)OFF_KVP + a * 32768, FILL_AUX); } }
DI void p1_aux(const u16* __restrict__ x, const u16* __restrict__ woA, const u16* __restrict__ woB, const u16* __restrict__ gate_w,
               char* __restrict__ ws, char* lds, int a) {
    const int tid = threadIdx.x, lane = tid & 63, wave = rfl(tid >> 6);
    const unsigned lds0 = (unsigned)(uintptr_t)lds;
    const int br = a >> 3, u = a & 7;
    if (tid < 144) *(u32x4*)(ws + OFF_FL + (a * 144 + tid) * 16) = u32x4{0u, 0u, 0u, 0u};
    const char* wsrc = (const char*)(br ? woB : woA) + (size_t)u * 102400;
    for (int i = wave; i < 100; i += 8) glds16(wsrc + i * 1024, lane * 16, lds0 + i * 1024);
    const u32x4* xs = (const u32x4*)(x + (size_t)(a * 128 + wave * 16) * D_);
    u32x4 xv[20], gw[5];
#pragma unroll
    for (int i = 0; i < 20; ++i) xv[i] = xs[i * 64 + lane];
#pragma unroll
    for (int i = 0; i < 5; ++i) gw[i] = ((const u32x4*)gate_w)[(i * 64 + lane) % 80];
    FILL_EMPTY
    LAS float* part = (LAS float*)(lds + 102400) + wave * 1280;
#pragma unroll
    for (int i = 0; i < 20; ++i) {
        float s = 0.f;
#pragma unroll
        for (int j = 0; j < 4; ++j) s += bflo(xv[i][j]) * bflo(gw[i % 5][j]) + bfhi(xv[i][j]) * bfhi(gw[i % 5][j]);
        part[i * 64 + lane] = s;
    }
    wait_vm_bar<0>();
    {
        float s = 0.f;
#pragma unroll
        for (int j = 0; j < 20; ++j) s += part[lane * 20 + j];
        s += __shfl_xor(s, 1); s += __shfl_xor(s, 2);
        if ((lane & 3) == 0) ((float*)(ws + OFF_G))[a * 128 + wave * 16 + (lane >> 2)] = 1.f / (1.f + __expf(-s));
    }
    const int g = lane >> 4, hh = g & 1, ns = g >> 1, q = (lane >> 2) & 3, p = lane & 3;
    for (int un = wave; un < 100; un += 8) {
        const int e = un / 20, np = un - e * 20, nt = np * 2 + ns;
        const int r0 = e < 4 ? e * 20 + 4 * hh : (2 * hh) * 20 + 16;
        const int r1 = e < 4 ? r0 + 8 : r0 + 20;
        const int ks16 = e < 4 ? (u & 1) * 4 + e : 8 + (u & 1), cbk = nt / 5;
        const u32x2 lo = trd((const LAS char*)lds + (r0 + q) * 1280 + nt * 32 + p * 8);
        const u32x2 hi = trd((const LAS char*)lds + (r1 + q) * 1280 + nt * 32 + p * 8);
        *(u32x4*)(ws + OFF_WOT + (size_t)((cbk * 8 + (u & 6) + br) * 25 + (ks16 >> 1) * 5 + nt - cbk * 5) * 1024 + ((ks16 & 1) * 2 + hh) * 256 + (lane & 15) * 16) = u32x4{lo[0], lo[1], hi[0], hi[1]};
    }
}

#define G1_P0 4
#define G1_P1 2
constexpr int g1_free(int i) { const int u = i / 3, m = i % 3, t = 2 * u + m - 1;
    return m == 0 ? (u < 3 ? -1 : 2 * (u - 2)) : (t < 7 ? -1 : t - 6); }
struct G1Plan { int seq[30]; int cnt[22]; int pos[30]; };
constexpr G1Plan g1_plan() {
    G1Plan p = {}; bool done[30] = {}; int n = 0;
    for (int t = 0; t <= 20; ++t) {
        int room = t == 0 ? G1_P0 : G1_P1;
        for (int i = 0; i < 30 && room > 0; ++i) if (!done[i] && g1_free(i) < t) { done[i] = true; p.pos[i] = n; p.seq[n++] = i; --room; }
        p.cnt[t] = n;
    }
    return p;
}
constexpr G1Plan G1P = g1_plan();
constexpr int g1_wait(int t) { const int a = G1P.pos[3 * (t >> 1)], b = G1P.pos[3 * (t >> 1) + 1 + (t & 1)]; return 4 * (G1P.cnt[t] - 1 - (a > b ? a : b)); }
constexpr bool g1_ok() { for (int t = 0; t < 20; ++t) if (g1_wait(t) < 0 || g1_wait(t) > 60) return false; return G1P.cnt[19] == 30; }
static_assert(g1_ok(), "loader plan");
#define G1_ROT xcd
#define G1_PF 0
#define G1_PFD 4
#define G1_PFD0 4
#define G1_ST 1
constexpr int G1_WOFF = 49152;

#define LST_P
#define LST_A
template <int T> struct G1Load {
    template <class FX, class FW> static DI void run(FX&& ldx, FW&& ldw LST_P) {
        asm volatile("s_waitcnt vmcnt(%0)\n\ts_barrier" :: "n"(g1_wait(T)) : "memory");
#pragma unroll
        for (int n = G1P.cnt[T]; n < G1P.cnt[T + 1]; ++n) { const int i = G1P.seq[n]; if (i % 3 == 0) ldx(i / 3); else ldw(2 * (i / 3) + i % 3 - 1); }
        G1Load<T + 1>::run(ldx, ldw LST_A);
    }
};
template <> struct G1Load<20> { template <class FX, class FW> static DI void run(FX&&, FW&& LST_P) {} };

struct G1Frag { bf16x8 x[2]; u32x2 w[4][2]; };
#define SB0() __builtin_amdgcn_sched_barrier(0)
template <int J> struct G1Step {
    static DI void run(f32x16 (&acc)[8], G1Frag& cur, G1Frag& nxt, const LAS char* const (&xa)[4], const LAS char* const (&wa)[4]) {
        constexpr int N = J + 1, t = N >> 1, xo = ((N >> 2) % 3) * 16384, sl = t % 7, wo = (sl & 3) * 16384 + (N & 1) * 4096, wi = (sl >> 2) * 2;
#define G1_M(i) acc[i] = MFMA32(cur.x[(i) & 1], B8(((u32x4){cur.w[(i) >> 1][0][0], cur.w[(i) >> 1][0][1], cur.w[(i) >> 1][1][0], cur.w[(i) >> 1][1][1]})), acc[i]); SB0();
#define G1_X(i) if (N < 40) nxt.x[i] = *(const LAS bf16x8*)(xa[N & 3] + xo + (i) * 4096);
#define G1_W(i) if (N < 40) { nxt.w[i][0] = trd(wa[wi + ((i) & 1)] + wo + ((i) >> 1) * 8192); nxt.w[i][1] = trd(wa[wi + ((i) & 1)] + wo + ((i) >> 1) * 8192 + 1024); } SB0();
        if (J & 1) {
            G1_M(0) G1_M(1)
            if (N < 40) asm volatile("s_waitcnt lgkmcnt(0)\n\ts_barrier" ::: "memory");
            SB0();
            G1_X(0) G1_W(0) G1_M(2) G1_X(1) G1_W(1) G1_M(3) G1_W(2) G1_M(4) G1_W(3) G1_M(5) G1_M(6) G1_M(7)
        } else {
            G1_M(0) G1_X(0) G1_W(0) G1_M(1) G1_X(1) G1_W(1) G1_M(2) G1_W(2) G1_M(3) G1_W(3) G1_M(4) G1_M(5) G1_M(6) G1_M(7)
        }
        G1Step<J + 1>::run(acc, nxt, cur, xa, wa);
    }
};
template <> struct G1Step<40> { static DI void run(f32x16 (&)[8], G1Frag&, G1Frag&, const LAS char* const (&)[4], const LAS char* const (&)[4]) {} };

DI void phase1(const u16* __restrict__ x, const u16* __restrict__ wA, const u16* __restrict__ wB, char* __restrict__ ws, char* lds, int bid) {
    const int tid = threadIdx.x, lane = tid & 63, wave = rfl(tid >> 6);
    const unsigned lds0 = (unsigned)(uintptr_t)lds;
    const int xcd = bid & 7, j = (xcd >> 1) * 30 + (bid >> 3);
    const int ct = j >> 3, mt = (xcd & 1) * 8 + (j & 7);
    const int type = rfl(ct / 5), rot = rfl(G1_ROT);
    if (__builtin_expect(wave >= 4, 1)) {
        const int lw = wave - 4;
        unsigned xo[4], wo[2];
#pragma unroll
        for (int i = 0; i < 4; ++i) { const int row = (lw * 4 + i) * 8 + (lane >> 3); xo[i] = (unsigned)((mt * 128 + row) * 1280 + (((lane & 7) ^ ((row >> 1) & 7)) << 4)); }
#pragma unroll
        for (int i = 0; i < 2; ++i) { const int k = (lw * 2 + i) * 4 + (lane >> 4), s16 = lane & 15;
            wo[i] = (unsigned)(k * 3840 + ct * 256 + ((((s16 >> 2) ^ (k & 3)) << 6) | ((s16 & 3) << 4))); }
        auto ldx = [&](int u) {
            int kk = u + rot; kk -= kk >= 10 ? 10 : 0;
            const unsigned slot = lds0 + (u % 3) * 16384 + lw * 4096;
#pragma unroll
            for (int i = 0; i < 4; ++i) glds16((const char*)x + kk * 128, xo[i], slot + i * 1024);
        };
        auto ldw = [&](int t) {
            int kk = (t >> 1) + rot; kk -= kk >= 10 ? 10 : 0;
            const unsigned slot = lds0 + G1_WOFF + (t % 7) * 16384 + lw * 2048;
            const int go = (kk * 64 + (t & 1) * 32) * 3840;
            glds16((const char*)wB + go, wo[0], slot);        glds16((const char*)wB + go, wo[1], slot + 1024);
            glds16((const char*)wA + go, wo[0], slot + 8192); glds16((const char*)wA + go, wo[1], slot + 8192 + 1024);
        };
#pragma unroll
        for (int n = 0; n < G1P.cnt[0]; ++n) { const int i = G1P.seq[n]; if (i % 3 == 0) ldx(i / 3); else ldw(2 * (i / 3) + i % 3 - 1); }
        G1Load<0>::run(ldx, ldw LST_A);
        return;
    }
    const int wm = wave & 1, wn = wave >> 1;
    const int r = lane & 31, hh = lane >> 5, g = lane >> 4, q = (lane >> 2) & 3, p = lane & 3;
    const LAS char* xa[4]; const LAS char* wa[4];
#pragma unroll
    for (int ks = 0; ks < 4; ++ks) { xa[ks] = (const LAS char*)lds + (wm * 64 + r) * 128 + (((ks * 2 + hh) ^ ((r >> 1) & 7)) << 4); asm volatile("" : "+v"(xa[ks])); }
#pragma unroll
    for (int i = 0; i < 4; ++i) { wa[i] = (const LAS char*)lds + G1_WOFF + (i >> 1) * 65536 + (8 * (g >> 1) + q) * 256 + (((wn * 2 + (i & 1)) ^ q) << 6) + (g & 1) * 32 + p * 8;
        asm volatile("" : "+v"(wa[i])); }
    f32x16 acc[8] = {};
    G1Frag f0, f1;
    __builtin_amdgcn_s_setprio(2);
    asm volatile("s_barrier" ::: "memory");
    f0.x[0] = *(const LAS bf16x8*)(xa[0]); f0.x[1] = *(const LAS bf16x8*)(xa[0] + 4096);
#pragma unroll
    for (int i = 0; i < 4; ++i) { f0.w[i][0] = trd(wa[i & 1] + (i >> 1) * 8192); f0.w[i][1] = trd(wa[i & 1] + (i >> 1) * 8192 + 1024); }
    SB0();
    G1Step<0>::run(acc, f0, f1, xa, wa);
    PSTAMP(0, 2);
    constexpr float QS = 0.22360679774997896f * 1.4426950408889634f;
#pragma unroll
    for (int i = 0; i < 8; ++i) {
        const int mi = i & 1, jn = (i >> 1) & 1, pass = i >> 2;
        const size_t ib = type == 0 ? OFF_IQ + pass * 1280 : (pass ? OFF_IA : OFF_IB) + (type - 1) * 1280;
        const int c = (ct - type * 5) * 128 + wn * 64 + jn * 32 + r, h = c / 20, d = c - h * 20;
        f32x16 v = acc[i];
        if (pass == 0) { if (type == 0) v *= QS; }
        else if (type < 2) {
#pragma unroll
            for (int e = 0; e < 16; ++e) v[e] = phi(v[e]);
        }
        char* dst = ws + ib + (size_t)h * 163840 + (mt * 4 + wm * 2 + mi) * 2560 + hh * 320 + d * 16;
        const u32x4 s0 = u32x4{pk2(v[0], v[1]), pk2(v[2], v[3]), pk2(v[4], v[5]), pk2(v[6], v[7])};
        const u32x4 s1 = u32x4{pk2(v[8], v[9]), pk2(v[10], v[11]), pk2(v[12], v[13]), pk2(v[14], v[15])};
        asm volatile("global_store_dwordx4 %0, %1, off sc1\n\tglobal_store_dwordx4 %0, %2, off offset:640 sc1\n\ts_nop 1" :: "v"(dst), "v"(s0), "v"(s1) : "memory");
    }
}

__global__ __launch_bounds__(512, 2) void k1_qkv(const u16* __restrict__ x, const u16* __restrict__ wA, const u16* __restrict__ wB,
                                                 const u16* __restrict__ woA, const u16* __restrict__ woB, const u16* __restrict__ gate_w,
                                                 char* __restrict__ ws) {
    extern __shared__ __attribute__((aligned(16))) char lds[];
    int bid = blockIdx.x;
    asm volatile("" : "+s"(bid));
    PSTAMP(0, 0);
    if (__builtin_expect(bid < 240, 1)) phase1(x, wA, wB, ws, lds, bid);
    else p1_aux(x, woA, woB, gate_w, ws, lds, bid - 240);
}

#define STR(x) #x
#define XS(x) STR(x)
#define A_E(i) "v_exp_f32 v" XS(i) ", v" XS(i) "\n"
#define A_C(d, a, b) "v_cvt_pk_bf16_f32 v" XS(d) ", v" XS(a) ", v" XS(b) "\n"
#define A_G(a, b, c, d, p0, p1) A_E(a) A_E(b) A_E(c) A_E(d) A_C(p0, a, b) A_C(p1, c, d)
#define A_M(d, a, b, c) "v_mfma_f32_32x32x16_bf16 " d ", " a ", " b ", " c "\n"
#define A_STEP(C0,C1,C2,C3,C4,C5,C6,C7,C8,C9,C10,C11,C12,C13,C14,C15, SN, KM, KT, V1, V0, NKMa, NKMb, NKT, NV1, NV0, OK0, OK1, OV1, OV0, PRIO) \
    "s_waitcnt lgkmcnt(0)\n" \
    A_M(SN, KM, "v[120:123]", "0") \
    "ds_read_b64_tr_b16 " NKMa ", v128 offset:" XS(OK0) "\n" \
    "ds_read_b64_tr_b16 " NKMb ", v128 offset:" XS(OK1) "\n" \
    "ds_read_b64_tr_b16 " NKT ", v129 offset:" XS(OK0) "\n" \
    A_G(C0, C1, C2, C3, 112, 113) \
    A_M("v[64:79]", V1, "v[116:119]", "v[64:79]") \
    "s_mov_b64 exec, s[72:73]\n" \
    "ds_read_b128 " NV1 ", v130 offset:" XS(OV1) "\n" \
    "ds_read_b128 " NV0 ", v130 offset:" XS(OV0) "\n" \
    "s_mov_b64 exec, -1\n" \
    A_G(C4, C5, C6, C7, 114, 115) \
    A_M(SN, KT, "v[124:127]", SN) \
    A_G(C8, C9, C10, C11, 116, 117) \
    A_M("v[64:79]", V0, "v[112:115]", "v[64:79]") \
    "s_setprio " XS(PRIO) "\n" \
    A_G(C12, C13, C14, C15, 118, 119)
#define A_EVEN(OK0, OK1, OV1, OV0, PRIO) A_STEP(32,33,34,35,36,37,38,39,40,41,42,43,44,45,46,47, "v[48:63]", "v[80:83]", "v[84:87]", "v[88:91]", "v[92:95]", \
    "v[96:97]", "v[98:99]", "v[100:101]", "v[104:107]", "v[108:111]", OK0, OK1, OV1, OV0, PRIO)
#define A_ODD(OK0, OK1, OV1, OV0, PRIO) A_STEP(48,49,50,51,52,53,54,55,56,57,58,59,60,61,62,63, "v[32:47]", "v[96:99]", "v[100:103]", "v[104:107]", "v[108:111]", \
    "v[80:81]", "v[82:83]", "v[84:85]", "v[88:91]", "v[92:95]", OK0, OK1, OV1, OV0, PRIO)

#define A_LD(g, off) "global_load_lds_dwordx4 v131, " g " offset:" XS(off) "\n"
#define A_LDA(off) "s_cbranch_scc1 .Lattn_s" XS(off) "\n" A_LD("s[74:75]", off) ".Lattn_s" XS(off) ":\n"
#define A_BODY \
        "s_mov_b32 m0, s78\n s_cmp_eq_u32 s80, 2\n s_nop 3\n" \
        A_EVEN(5120, 5184, 640, 2560, 2)     A_ODD(7680, 7744, 3200, 5120, 1) \
        A_EVEN(10240, 10304, 5760, 7680, 0)  A_ODD(12800, 12864, 8320, 10240, 3) \
        "s_waitcnt vmcnt(0) lgkmcnt(0)\n s_mov_b64 exec, 1\n ds_add_u32 v127, v86\n s_mov_b64 exec, -1\n" \
        A_EVEN(15360, 15424, 10880, 12800, 2) A_LDA(0) A_ODD(17920, 17984, 13440, 15360, 1) A_LDA(1024) \
        A_EVEN(20480, 20544, 16000, 17920, 0) A_LDA(2048) \
        "s_cmp_eq_u32 s80, 1\n s_cbranch_scc0 .Lattn_skip\n s_mov_b32 m0, s79\n s_nop 0\n" \
        A_LD("s[76:77]", 0) A_LD("s[76:77]", 1024) A_LD("s[76:77]", 2048) ".Lattn_skip:\n" \
        "ds_read_b32 v132, v127\n" \
        A_ODD(23040, 23104, 18560, 20480, 3) \
        "v_add_u32 v128, 0x5000, v128\n v_add_u32 v129, 0x5000, v129\n v_add_u32 v130, 0x5000, v130\n" \
        "s_waitcnt lgkmcnt(0)\n s_nop 0\n"
#define A_OPERANDS \
        : "+{v[32:63]}"(S), "+{v[64:79]}"(O), "+{v[80:111]}"(OPS), "+{v[112:119]}"(P), "+{v[128:131]}"(AD), "={v132}"(cnt) \
        : "{v[120:127]}"(Q), "{s[72:73]}"(vmask), "{s[74:75]}"(ga), "{s[76:77]}"(gb), "{s78}"(ma), "{s79}"(mb), "{s80}"(enb) \
        : "memory", "scc", "s82", "s83"
template <bool FIRST>
DI unsigned attn_chunk(f32x32& S, f32x16& O, u32x32& OPS, u32x8& P, u32x4& AD, const u32x8& Q, unsigned long long vmask,
                   unsigned ma, unsigned long long ga, unsigned mb, unsigned long long gb, unsigned enb) {
    unsigned cnt;
    if (FIRST) asm volatile("s_call_b64 s[82:83], .Lattn_body\n s_branch .Lattn_done\n .p2align 6\n .Lattn_body:\n" A_BODY "s_setpc_b64 s[82:83]\n .Lattn_done:\n" A_OPERANDS);
    else       asm volatile("s_call_b64 s[82:83], .Lattn_body\n" A_OPERANDS);
    return cnt;
}
DI void wait_cnt(char* lds, int off, unsigned cnt, int n) {
    if (__builtin_expect((unsigned)rfl((int)cnt) < (unsigned)(n * 0x3F80), 0)) {
#pragma clang loop unroll(disable)
        do { __builtin_amdgcn_s_sleep(1); cnt = *(volatile LAS unsigned*)(lds + off); } while ((unsigned)rfl((int)cnt) < (unsigned)(n * 0x3F80));
    }
    asm volatile("" ::: "memory");
}

DI void wait_cnt(char* lds, int off, int n) { wait_cnt(lds, off, *(volatile LAS unsigned*)(lds + off), n); }
template <int OFF> DI void add_cnt(unsigned lds0) {
    asm volatile("s_waitcnt lgkmcnt(0)\n\ts_mov_b64 exec, 1\n\tds_add_u32 %0, %1 offset:%2\n\ts_mov_b64 exec, -1" :: "v"(lds0), "v"(0x3F80u), "n"(OFF) : "memory"); }

DI float xhalf_max(float m) { auto rr = __builtin_amdgcn_permlane32_swap(__float_as_uint(m), __float_as_uint(m), false, false);
    return fmaxf(__uint_as_float(rr[0]), __uint_as_float(rr[1])); }
DI float hi_half(float v) {
    auto rr = __builtin_amdgcn_permlane32_swap(__float_as_uint(v), __float_as_uint(v), false, false);
    return __uint_as_float(rr[1]); }
DI float sum16(float v) { auto rr = __builtin_amdgcn_permlane16_swap(__float_as_uint(v), __float_as_uint(v), false, false);
    return __uint_as_float(rr[0]) + __uint_as_float(rr[1]); }
DI float sum32(float v) { auto rr = __builtin_amdgcn_permlane32_swap(__float_as_uint(v), __float_as_uint(v), false, false);
    return __uint_as_float(rr[0]) + __uint_as_float(rr[1]); }
DI f32x4 sum_rows(f32x4 a, f32x4 b) {
    f32x4 s = a + b;
#pragma unroll
    for (int i = 0; i < 4; ++i) s[i] = sum32(sum16(s[i]));
    return s;
}
DI unsigned long long uni64(const void* p) { const unsigned long long a = (unsigned long long)(uintptr_t)p;
    return (unsigned long long)(unsigned)rfl((int)(unsigned)a) | ((unsigned long long)(unsigned)rfl((int)(unsigned)(a >> 32)) << 32); }
DI unsigned full(unsigned v) { return v < EMPTY ? v : EMPTY - 1u; }
DI unsigned umax4(unsigned m, u32x4 v) { return max(max(max(m, v[0]), max(v[1], v[2])), v[3]); }
DI void store_o(rsrc_t rs, const f32x16& o, float f, int rt, int br, int h, int lane) {
    const int rb = (int)OFF_O + (rt * 80 + ((br ? 0 : 4) + (h >> 3)) * 10) * 1024;
    __builtin_amdgcn_raw_buffer_store_b128(u32x4{full(pk2(o[0] * f, o[1] * f)), full(pk2(o[2] * f, o[3] * f)), full(pk2(o[4] * f, o[5] * f)), full(pk2(o[6] * f, o[7] * f))},
                                           rs, lane * 16, rb + (h & 7) * 1024, SC1);
    if (lane < 32) __builtin_amdgcn_raw_buffer_store_b64(u32x2{full(pk2(o[8] * f, o[9] * f)), full(pk2(o[10] * f, o[11] * f))},
                                           rs, lane * 16, rb + (8 + ((h >> 2) & 1)) * 1024 + ((h >> 1) & 1) * 512 + (h & 1) * 8, SC1);
}
DI void set_flag(rsrc_t rs, int off) { __builtin_amdgcn_raw_buffer_store_b32(1u, rs, 0, off, SC1); }
DI unsigned get_flag(rsrc_t rs, int voff, int off) { return __builtin_amdgcn_raw_buffer_load_b32(rs, voff, off, SC1); }
DI unsigned a_ld1(unsigned long long sb, unsigned voff) { unsigned v; asm volatile("global_load_dword %0, %1, %2 sc1" : "=v"(v) : "v"(voff), "s"(sb) : "memory"); return v; }
DI u32x4 a_ld4s(unsigned long long sb, unsigned voff) { u32x4 v; asm volatile("global_load_dwordx4 %0, %1, %2 sc1" : "=v"(v) : "v"(voff), "s"(sb) : "memory"); return v; }
template <int OFF> DI u32x4 a_ld4so(unsigned long long sb, unsigned voff) { u32x4 v;
    asm volatile("global_load_dwordx4 %0, %1, %2 offset:%3 sc1" : "=v"(v) : "v"(voff), "s"(sb), "n"(OFF) : "memory"); return v; }
template <int OFF> DI u32x4 a_ld4(unsigned long long sb, unsigned voff) { u32x4 v;
    asm volatile("global_load_dwordx4 %0, %1, %2 offset:%3" : "=v"(v) : "v"(voff), "s"(sb), "n"(OFF) : "memory"); return v; }
template <class T> DI void landed(T& v) { asm volatile("" : "+v"(v)); }
DI void wait_flag(rsrc_t rs, int voff, int off, unsigned v) {
    if (__builtin_expect(!__all(v == 1u), 0)) {
#pragma clang loop unroll(disable)
        for (int spin = 0; spin < (1 << 22); ++spin) { if (__all(get_flag(rs, voff, off) == 1u)) break; __builtin_amdgcn_s_sleep(1); }
    }
    __builtin_amdgcn_fence(__ATOMIC_ACQUIRE, "wavefront");
    asm volatile("" ::: "memory");
}

constexpr int L_SP = 136192, L_K0 = 152320, L_SQ = 153600, L_QA = 1280, L_KVF = 11520, L_WO = 13312;

__global__ __launch_bounds__(512, 2) void k2_attn(char* __restrict__ ws, u16* __restrict__ out) {
    extern __shared__ __attribute__((aligned(16))) char lds[];
    int bid = blockIdx.x;
    asm volatile("" : "+s"(bid));
    PSTAMP(1, 0); WSTAMP(0);
    const int tid = threadIdx.x, lane = tid & 63, wave = rfl(tid >> 6);
    const int h = (bid & 7) * 4 + ((bid >> 3) & 3), qb = bid >> 5;
    const int oq = bid & 7, rb = oq * 4 + ((bid >> 3) & 3), cb = bid >> 5;
    const int r = lane & 31, hh = lane >> 5;
    const unsigned lds0 = (unsigned)(uintptr_t)lds;
    const rsrc_t rs = __builtin_amdgcn_make_buffer_rsrc((void*)ws, 0, WS_USED, 0x00020000);
    const char* kvb = ws + OFF_IB + (size_t)h * 163840;
    const char* qim = ws + OFF_IQ + (size_t)h * 163840 + qb * 20480;
    const int wr = (wave & 3) * 2 + (wave >> 2), rt = qb * 8 + wr;
    const int rc = r < 20 ? r : 19;
    glds16(qim + wr * 2560, lane * 16, lds0 + L_SQ + wave * 1280);
    glds16(qim + wr * 2560 + 256, lane * 16, lds0 + L_SQ + wave * 1280 + 256);
    glds16(kvb + (wave & 1) * 256, lane * 16, lds0 + L_K0 + (wave & 1) * 256);
#pragma unroll
    for (int i = 0; i < 3; ++i) glds16(kvb + (1 + i * 8 + wave) * 1024, lane * 16, lds0 + (1 + i * 8 + wave) * 1024);
    const char* ap = ws + OFF_IA + (size_t)h * 163840 + rt * 2560 + hh * 320 + rc * 16;
    u32x4 a0, a1, b0, b1; float gate;
    asm volatile("global_load_dwordx4 %0, %5, off\n\tglobal_load_dwordx4 %1, %5, off offset:640\n\tglobal_load_dwordx4 %2, %5, off offset:1280\n\t"
                 "global_load_dwordx4 %3, %5, off offset:1920\n\tglobal_load_dword %4, %6, off"
                 : "=&v"(a0), "=&v"(a1), "=&v"(b0), "=&v"(b1), "=&v"(gate) : "v"(ap), "v"((const float*)(ws + OFF_G) + rt * 32 + r) : "memory");
#pragma unroll
    for (int i = 0; i < 3; ++i) glds16(kvb + (25 + i * 8 + wave) * 1024, lane * 16, lds0 + (25 + i * 8 + wave) * 1024);
    if (tid < 4) *(LAS unsigned*)(lds + tid * 4) = 0u;
    const u32x4 ONES = {0x3F803F80u, 0x3F803F80u, 0x3F803F80u, 0x3F803F80u};
    const f32x16 Z16 = {};
    wait_vm_bar<8>();
    PSTAMP(1, 1); WSTAMP(1);
    const int g = lane >> 4, q = (lane >> 2) & 3, p = lane & 3;
    const int tro = (g & 1) * 640 + (p & 1) * 320 + q * 16 + (p >> 1) * 8;
    const LAS char* L = (const LAS char*)lds;
    const LAS char* kmp = L + tro + hh * 128;
    const LAS char* ktp = L + tro + 256;
    const LAS char* vp  = L + 1280 + hh * 320 + r * 16;
    const int ent = tid < 126 ? tid : 125;
    u32x8 Q;
    {
        const u32x2 a = trd(kmp + L_SQ + wave * 1280), b = trd(kmp + L_SQ + wave * 1280 + 64), c = trd(ktp + L_SQ + wave * 1280);
        Q[0] = a[0]; Q[1] = a[1]; Q[2] = b[0]; Q[3] = b[1]; Q[4] = hh ? 0u : c[0]; Q[5] = hh ? 0u : c[1]; Q[6] = 0u; Q[7] = 0u;
    }
    const bf16x8 qm = B8(((u32x4){Q[0], Q[1], Q[2], Q[3]}));
    f32x32 S; f32x16 O = {}; u32x32 OPS; u32x8 P = {}; u32x4 AD;
    {
        const u32x2 a = trd(kmp + L_K0), b = trd(kmp + L_K0 + 64), c = trd(ktp + L_K0);
        const bf16x8 k0 = B8(((u32x4){a[0], a[1], b[0], b[1]})), k1 = B8(((u32x4){c[0], c[1], 0x3F80u, 0u}));
        const f32x16 sm = MFMA32(k0, qm, Z16);
        f32x16 s0 = MFMA32(k1, B8(((u32x4){Q[4], Q[5], 0u, 0u})), sm);
        float m = s0[0];
#pragma unroll
        for (int i = 1; i < 16; ++i) m = fmaxf(m, s0[i]);
        m = xhalf_max(m);
        Q[6] = hh ? 0u : (pk2(-m, 0.f) & 0xffffu);
        s0 = MFMA32(k1, B8(((u32x4){Q[4], Q[5], Q[6], 0u})), sm);
#pragma unroll
        for (int i = 0; i < 16; ++i) { S[i] = s0[i]; S[16 + i] = 0.f; }
        const unsigned vc = r == 20 ? 0x3F803F80u : 0u;
#pragma unroll
        for (int i = 0; i < 32; ++i) OPS[i] = (i & 8) ? vc : 0u;
        const u32x2 a1 = trd(kmp + 2560), b1 = trd(kmp + 2560 + 64), c1 = trd(ktp + 2560);
        OPS[0] = a1[0]; OPS[1] = a1[1]; OPS[2] = b1[0]; OPS[3] = b1[1]; OPS[4] = c1[0]; OPS[5] = c1[1]; OPS[6] = 0x3F80u; OPS[22] = 0x3F80u;
        if (r < 20) { const u32x4 v0 = *(const LAS u32x4*)vp; OPS[12] = v0[0]; OPS[13] = v0[1]; OPS[14] = v0[2]; OPS[15] = v0[3]; }
        AD[0] = (unsigned)(uintptr_t)kmp; AD[1] = (unsigned)(uintptr_t)ktp; AD[2] = (unsigned)(uintptr_t)vp; AD[3] = lane * 16;
    }
    {
        asm volatile("s_waitcnt vmcnt(3)" : "+v"(a0), "+v"(a1), "+v"(b0), "+v"(b1), "+v"(gate) :: "memory");
        if (r >= 20) { a0 = u32x4{0u, 0u, 0u, 0u}; a1 = a0; b0 = ONES; b1 = ONES; }
        f32x16 kvacc = MFMA32(B8(a0), B8(b0), Z16); kvacc = MFMA32(B8(a1), B8(b1), kvacc);
        if (r <= 20) {
            LAS f32x4* dst = (LAS f32x4*)(lds + L_SP + ((wave * 2 + hh) * 21 + r) * 48);
            dst[0] = f32x4{kvacc[0], kvacc[1], kvacc[2], kvacc[3]};
            dst[1] = f32x4{kvacc[4], kvacc[5], kvacc[6], kvacc[7]};
            dst[2] = f32x4{kvacc[8], kvacc[9], kvacc[10], kvacc[11]};
        }
    }
    const unsigned long long vmask = 0x000FFFFF000FFFFFull;
    const int j = wave & 3, isE = (wave >> 2) ^ 1;
    const char* wop = ws + OFF_WOT + (size_t)((cb * 4 + j) * 50) * 1024;
    const int flA = (int)OFF_FL + (oq * 4 + j) * 128, flv = (lane & 7) * 16;
    const int ov = ((g >> 1) * 1024 + (g & 1) * 512 + (lane & 15) * 16), os = (int)OFF_O + (rb * 160 + j * 10) * 1024;
    unsigned fl = 0u;
    u32x4 kp[8], wa[20], aa[2][10];
    const unsigned long long wsu = uni64(ws);
    const unsigned long long kb = uni64(ws + OFF_KVP + 4096 + h * 16384), kb2 = uni64(ws + OFF_KVP + 12288 + h * 16384);
    const unsigned long long ob[2][2] = {{uni64(ws + os + 4096), uni64(ws + os + 8192)}, {uni64(ws + os + 81920 + 4096), uni64(ws + os + 81920 + 8192)}};
#define LD_H(A, vo, hf, en, C) asm volatile("s_cmp_lg_u32 %13, 0\n\ts_cselect_b64 exec, -1, 0\n\t" \
        "global_load_dwordx4 %0, %10, %11 offset:-4096 sc1\n\tglobal_load_dwordx4 %1, %10, %11 offset:-3840 sc1\n\t" \
        "global_load_dwordx4 %2, %10, %11 offset:-2048 sc1\n\tglobal_load_dwordx4 %3, %10, %11 offset:-1792 sc1\n\t" \
        "global_load_dwordx4 %4, %10, %11 sc1\n\tglobal_load_dwordx4 %5, %10, %11 offset:256 sc1\n\t" \
        "global_load_dwordx4 %6, %10, %11 offset:2048 sc1\n\tglobal_load_dwordx4 %7, %10, %11 offset:2304 sc1\n\t" \
        "global_load_dwordx4 %8, %10, %12 sc1\n\tglobal_load_dwordx4 %9, %10, %12 offset:256 sc1\n\ts_mov_b64 exec, -1" \
        : C(A[0]), C(A[1]), C(A[2]), C(A[3]), C(A[4]), C(A[5]), C(A[6]), C(A[7]), C(A[8]), C(A[9]) \
        : "v"(vo), "s"(ob[hf][0]), "s"(ob[hf][1]), "s"(en) : "memory", "scc");
#define LD_W4(A, f) asm volatile("s_cmp_lg_u32 %6, 0\n\ts_cselect_b64 exec, -1, 0\n\t" \
        "global_load_dwordx4 %0, %4, %5\n\tglobal_load_dwordx4 %1, %4, %5 offset:1024\n\t" \
        "global_load_dwordx4 %2, %4, %5 offset:2048\n\tglobal_load_dwordx4 %3, %4, %5 offset:3072\n\ts_mov_b64 exec, -1" \
        : "=&v"((A)[0]), "=&v"((A)[1]), "=&v"((A)[2]), "=&v"((A)[3]) : "v"(lane * 16), "s"(uni64(wop + (f) * 1024)), "s"(isE) : "memory", "scc");
#define LD_KP(TAIL) asm volatile("s_cmp_lt_u32 %11, 2\n\ts_cselect_b64 exec, -1, 0\n\t" \
        "global_load_dwordx4 %0, %8, %9 offset:-4096 sc1\n\tglobal_load_dwordx4 %1, %8, %9 offset:-2048 sc1\n\t" \
        "global_load_dwordx4 %2, %8, %9 sc1\n\tglobal_load_dwordx4 %3, %8, %9 offset:2048 sc1\n\t" \
        "global_load_dwordx4 %4, %8, %10 offset:-4096 sc1\n\tglobal_load_dwordx4 %5, %8, %10 offset:-2048 sc1\n\t" \
        "global_load_dwordx4 %6, %8, %10 sc1\n\tglobal_load_dwordx4 %7, %8, %10 offset:2048 sc1\n\t" \
        "s_mov_b64 exec, -1" TAIL \
        : "=&v"(kp[0]), "=&v"(kp[1]), "=&v"(kp[2]), "=&v"(kp[3]), "=&v"(kp[4]), "=&v"(kp[5]), "=&v"(kp[6]), "=&v"(kp[7]) \
        : "v"(ent * 16), "s"(kb), "s"(kb2), "s"(wave) : "memory", "scc")
#pragma unroll
    for (int c = 0; c < 8; ++c) {
        unsigned ma, mb; unsigned long long ga, gb;
        if (c < 3) {
            const int lim = (c == 1 ? L_SP : 163840) - 3072;
            const int oa = (c == 2 ? L_SP : 50176 + c * 49152) + wave * 3072, ob = oa + 24576 < lim ? oa + 24576 : lim;
            ma = lds0 + oa; mb = lds0 + ob; ga = uni64(kvb + oa); gb = uni64(kvb + ob);
        } else {
            ma = lds0 + (c == 7 ? 1024 + j * 3072 : L_WO + ((c - 3) * 8 + wr) * 3072); mb = ma;
            ga = uni64(wop + (c == 7 ? 44 : 20 + (wave >> 2) * 12 + 3 * (c - 3)) * 1024); gb = ga;
        }
        CSTAMP(2 * c);
        unsigned cnt;
        if (c == 0) cnt = attn_chunk<true>(S, O, OPS, P, AD, Q, vmask, rfl(ma), ga, rfl(mb), gb, 1u);
        else cnt = attn_chunk<false>(S, O, OPS, P, AD, Q, vmask, rfl(ma), ga, rfl(mb), gb, c < 3 ? 1u : c < 7 ? 0u : (unsigned)(2 - 2 * isE));
        CSTAMP(2 * c + 1);
        if (c < 7) wait_cnt(lds, 0, cnt, 8 * (c + 1));
        if (c == 0) {
            if (wave < 2) {
                f32x4 s = *(const LAS f32x4*)(L + L_SP + ent * 16);
#pragma unroll
                for (int w = 1; w < 8; ++w) s += *(const LAS f32x4*)(L + L_SP + w * 2016 + ent * 16);
                const u32x4 v = __builtin_bit_cast(u32x4, s);
                if (tid < 126) __builtin_amdgcn_raw_buffer_store_b128(u32x4{full(v[0]), full(v[1]), full(v[2]), full(v[3])}, rs, tid * 16, (int)OFF_KVP + (h * 8 + qb) * 2048, SC1);
            }
            glds16(qim + wr * 2560 + 1280, lane * 16, lds0 + L_QA + wave * 1280);
            glds16(qim + wr * 2560 + 1280 + 256, lane * 16, lds0 + L_QA + wave * 1280 + 256);
        } else if (c == 1) {
            LD_KP("");
        } else if (c == 2) {
#pragma unroll
            for (int j = 0; j < 8; ++j) landed(kp[j]);
            if (wave < 2) {
#pragma clang loop unroll(disable)
                for (int spin = 0; spin < (1 << 20); ++spin) {
                    unsigned m = 0u;
#pragma unroll
                    for (int j = 0; j < 8; ++j) m = umax4(m, kp[j]);
                    if (__builtin_expect(__all(m != EMPTY), 1)) break;
                    LD_KP("\n\ts_waitcnt vmcnt(0)");
                }
                f32x4 s = __builtin_bit_cast(f32x4, kp[0]);
#pragma unroll
                for (int j = 1; j < 8; ++j) s += __builtin_bit_cast(f32x4, kp[j]);
                const int e3 = ent / 3;
                *(LAS u32x2*)(lds + L_KVF + e3 * 32 + (ent - e3 * 3) * 8) = u32x2{pk2(s[0], s[1]), pk2(s[2], s[3])};
            }
        } else if (c == 3) {
            const int rr = r <= 20 ? r : 20;
            const u32x4 ka0 = *(const LAS u32x4*)(L + L_KVF + (hh * 21 + rr) * 32);
            const u32x2 ka1 = *(const LAS u32x2*)(L + L_KVF + (hh * 21 + rr) * 32 + 16);
            const LAS char* qa = L + L_QA + wave * 1280 + tro;
            const u32x2 pq0 = trd(qa + hh * 64), pq1 = trd(qa + 128 + hh * 64);
            u32x2 pq2 = trd(qa + 256);
            if (hh) pq2 = u32x2{0u, 0u};
            f32x16 oa = MFMA32(B8(ka0), B8(((u32x4){pq0[0], pq0[1], pq1[0], pq1[1]})), Z16);
            oa = MFMA32(B8(((u32x4){ka1[0], ka1[1], 0u, 0u})), B8(((u32x4){pq2[0], pq2[1], 0u, 0u})), oa);
            const float den = hi_half(oa[8]) + 1e-6f;
            store_o(rs, oa, gate * __builtin_amdgcn_rcpf(den), rt, 0, h, lane);
            LD_W4(wa, 0) LD_W4(wa + 4, 4)
        } else if (c == 4) {
            if (tid == 0) set_flag(rs, (int)OFF_FL + (qb * 4 + (h >> 3)) * 128 + (h & 7) * 16);
            LD_W4(wa + 8, 8) LD_W4(wa + 12, 12)
        } else if (c == 5) {
            fl = a_ld1(wsu, flA + flv);
            LD_W4(wa + 16, 16)
        } else if (c == 6) {
            landed(fl);
            if (wave < 4) wait_flag(rs, flv, flA, fl);
            LD_H(aa[0], ov + 40960, 0, isE, "=&v")
        }
    }
#pragma unroll
    for (int i = 0; i < 10; ++i) { landed(wa[i]); landed(wa[10 + i]); landed(aa[0][i]); }
    u32x4 wd[4];
    LD_H(aa[1], ov + 40960, 1, isE, "=&v") LD_W4(wd, 46)
    if (lane == 0) set_flag(rs, (int)OFF_FL + (32 + rt * 4 + (h >> 3)) * 128 + (h & 7) * 16);
    O = MFMA32(B8(((u32x4){OPS[8], OPS[9], OPS[10], OPS[11]})), B8(((u32x4){P[4], P[5], P[6], P[7]})), O);
    PSTAMP(1, 2); WSTAMP(2);
    float l = hi_half(O[8]);
    {
        bool bad = !(l >= 1e-18f && l <= 1.0e30f);
#pragma unroll
        for (int i = 0; i < 12; ++i) bad = bad || !(fabsf(O[i]) <= 3.0e38f);
        if (__builtin_expect(__any(bad), 0)) {
            const bf16x8 qt0 = B8(((u32x4){Q[4], Q[5], 0u, 0u}));
            float m = -INFINITY; O = Z16;
            int tl = tid;
            asm volatile("" : "+v"(tl));
            const int r = tl & 31, hh = (tl >> 5) & 1, rc = r < 20 ? r : 19;
            const int ko = (r >> 4) * 640 + ((r >> 2) & 1) * 320 + ((r >> 3) & 1) * 8 + (r & 3) * 2;
            for (int t = 0; t < 64; ++t) {
                const char* tb = kvb + t * 2560;
                unsigned kw[6];
#pragma unroll
                for (int i = 0; i < 6; ++i) {
                    const int d = i < 4 ? 8 * hh + 2 * i : 16 + 2 * (i - 4);
                    kw[i] = (unsigned)*(const u16*)(tb + ko + d * 16) | ((unsigned)*(const u16*)(tb + ko + d * 16 + 16) << 16);
                }
                u32x4 f0 = *(const u32x4*)(tb + 1280 + hh * 320 + rc * 16), f1 = *(const u32x4*)(tb + 1920 + hh * 320 + rc * 16);
                if (r >= 20) { f0 = ONES; f1 = ONES; }
                f32x16 s = MFMA32(B8(((u32x4){kw[0], kw[1], kw[2], kw[3]})), qm, Z16);
                s = MFMA32(B8(((u32x4){kw[4], kw[5], 0u, 0u})), qt0, s);
                float tm = s[0];
#pragma unroll
                for (int i = 1; i < 16; ++i) tm = fmaxf(tm, s[i]);
                tm = xhalf_max(tm);
                const float mn = fmaxf(m, tm);
                const float al = (m == -INFINITY) ? 0.f : __builtin_amdgcn_exp2f(m - mn);
                m = mn;
#pragma unroll
                for (int i = 0; i < 16; ++i) { O[i] *= al; s[i] = __builtin_amdgcn_exp2f(s[i] - mn); }
                u32x4 p0 = {pk2(s[0], s[1]), pk2(s[2], s[3]), pk2(s[4], s[5]), pk2(s[6], s[7])};
                u32x4 p1 = {pk2(s[8], s[9]), pk2(s[10], s[11]), pk2(s[12], s[13]), pk2(s[14], s[15])};
                O = MFMA32(B8(f0), B8(p0), O); O = MFMA32(B8(f1), B8(p1), O);
            }
            l = hi_half(O[8]);
        }
    }
    store_o(rs, O, (1.f - gate) * __builtin_amdgcn_rcpf(l), rt, 1, h, lane);
    __builtin_amdgcn_s_setprio(0);
    __builtin_amdgcn_sched_barrier(0);
    WSTAMP(3);
    constexpr int L_P0 = L_WO + 98304;
    if (wave < 4) {
        const LAS char* wl = L + j * 6144 + lane * 16;
        auto wfr = [&](int f) -> bf16x8 {
            if (f < 20) return B8(wa[f]);
            if (f < 44) return *(const LAS bf16x8*)(wl + L_WO + (((f - 20) % 12) / 3) * 24576 + ((f - 20) / 12) * 3072 + ((f - 20) % 3) * 1024);
            if (f < 46) return *(const LAS bf16x8*)(L + 1024 + j * 3072 + (f - 44) * 1024 + lane * 16);
            return B8(wd[f - 46]);
        };
        LAS char* wp = (LAS char*)lds + (lane & 15) * 320 + ((g ^ ((lane >> 1) & 3)) << 4);
        f32x4 acc[4][5] = {};
        {
            bf16x8 w4[5];
#pragma unroll
            for (int ni = 0; ni < 5; ++ni) w4[ni] = wfr(20 + ni);
#pragma unroll
            for (int hf = 0; hf < 2; ++hf) {
                if (hf) asm volatile("s_waitcnt vmcnt(3)" : "+v"(aa[1][0]), "+v"(aa[1][1]), "+v"(aa[1][2]), "+v"(aa[1][3]), "+v"(aa[1][4]), "+v"(aa[1][5]), "+v"(aa[1][6]),
                                     "+v"(aa[1][7]), "+v"(aa[1][8]), "+v"(aa[1][9]), "+v"(wd[0]), "+v"(wd[1]), "+v"(wd[2]), "+v"(wd[3]) :: "memory");
#pragma unroll
                for (int k = 0; k < 5; ++k)
#pragma unroll
                    for (int mi = 0; mi < 2; ++mi)
#pragma unroll
                        for (int ni = 0; ni < 5; ++ni) acc[hf * 2 + mi][ni] = MFMA16(k < 4 ? wfr(k * 5 + ni) : w4[ni], B8(aa[hf][2 * k + mi]), acc[hf * 2 + mi][ni]);
                __builtin_amdgcn_sched_barrier(0);
            }
        }
        const int flB = (int)OFF_FL + (32 + rb * 8 + j) * 128;
        u32x4 b0[10], b1[10];
        wait_flag(rs, flv, flB, get_flag(rs, flv, flB));
        WSTAMP(4);
        LD_H(b0, ov, 0, 1, "=&v")
        fl = a_ld1(wsu, flB + 512 + flv);
        wait_cnt(lds, 0, 64);
        asm volatile("s_waitcnt vmcnt(0)" : "+v"(fl) :: "memory");
        wait_flag(rs, flv, flB + 512, fl);
        WSTAMP(5);
        LD_H(b1, ov, 1, 1, "=&v")
#pragma clang loop unroll(disable)
        for (int spin = 0; spin < (1 << 20); ++spin) {
            unsigned m = 0u;
#pragma unroll
            for (int i = 0; i < 10; ++i) { landed(b0[i]); m = umax4(m, b0[i]); }
            if (__builtin_expect(__all(m != EMPTY), 1)) break;
            LD_H(b0, ov, 0, 1, "+v")
            asm volatile("s_waitcnt vmcnt(0)" ::: "memory");
        }
        bf16x8 wb[25];
#pragma unroll
        for (int k = 0; k < 5; ++k) {
#pragma unroll
            for (int ni = 0; ni < 5; ++ni) wb[k * 5 + ni] = wfr(25 + k * 5 + ni);
#pragma unroll
            for (int mi = 0; mi < 2; ++mi)
#pragma unroll
                for (int ni = 0; ni < 5; ++ni) acc[mi][ni] = MFMA16(wb[k * 5 + ni], B8(b0[2 * k + mi]), acc[mi][ni]);
        }
#pragma unroll
        for (int mi = 0; mi < 2; ++mi)
#pragma unroll
            for (int ni = 0; ni < 5; ++ni) *(LAS f32x4*)(wp + L_P0 + j * 10240 + mi * 5120 + ni * 64) = acc[mi][ni];
        add_cnt<12>(lds0);
        f32x4 ac1[2][5];
#pragma clang loop unroll(disable)
        for (int spin = 0; spin < (1 << 20); ++spin) {
            unsigned m = 0u;
#pragma unroll
            for (int k = 0; k < 5; ++k) {
                asm volatile("s_waitcnt vmcnt(%2)" : "+v"(b1[2 * k]), "+v"(b1[2 * k + 1]) : "n"(8 - 2 * k) : "memory");
#pragma unroll
                for (int mi = 0; mi < 2; ++mi)
#pragma unroll
                    for (int ni = 0; ni < 5; ++ni) ac1[mi][ni] = MFMA16(wb[k * 5 + ni], B8(b1[2 * k + mi]), k ? ac1[mi][ni] : acc[2 + mi][ni]);
                m = umax4(umax4(m, b1[2 * k]), b1[2 * k + 1]);
                __builtin_amdgcn_sched_barrier(0);
            }
            if (__builtin_expect(__all(m != EMPTY), 1)) break;
            LD_H(b1, ov, 1, 1, "+v")
        }
#pragma unroll
        for (int mi = 0; mi < 2; ++mi)
#pragma unroll
            for (int ni = 0; ni < 5; ++ni) *(LAS f32x4*)(wp + L_WO + j * 6144 + mi * 24576 + ni * 64) = ac1[mi][ni];
        WSTAMP(6);
        asm volatile("s_waitcnt lgkmcnt(0)\n\ts_barrier" ::: "memory");
    } else {
        const int t = tid - 256;
        const LAS char* rp[2][3]; unsigned oo[3];
#pragma unroll
        for (int i = 0; i < 3; ++i) {
            const int id = i < 2 || t < 128 ? i * 256 + t : 1, row = id / 20, cs = id - row * 20, cc = cs ^ ((row >> 1) & 3);
            rp[0][i] = L + L_P0 + id * 16; rp[1][i] = L + L_WO + id * 16 + (id >= 320 ? 24576 - 5120 : 0);
            oo[i] = (unsigned)(((rb * 64 + row) * D_ + cb * 80 + cc * 4) * 2);
            landed(rp[0][i]); landed(rp[1][i]); landed(oo[i]);
        }
        WSTAMP(4);
#pragma unroll
        for (int hf = 0; hf < 2; ++hf) {
            if (hf == 0) { wait_cnt(lds, 12, 4); WSTAMP(5); }
            else { WSTAMP(6); asm volatile("s_barrier" ::: "memory"); }
            f32x4 pv[3][4];
#pragma unroll
            for (int i = 0; i < 3; ++i)
#pragma unroll
                for (int w = 0; w < 4; ++w) pv[i][w] = *(const LAS f32x4*)(rp[hf][i] + w * (hf ? 6144 : 10240));
            __builtin_amdgcn_sched_barrier(0);
#pragma unroll
            for (int i = 0; i < 3; ++i) {
                const f32x4 v = (pv[i][0] + pv[i][1]) + (pv[i][2] + pv[i][3]);
                if (i < 2 || t < 128) *(u32x2*)((char*)out + hf * (32 * D_ * 2) + oo[i]) = u32x2{pk2(v[0], v[1]), pk2(v[2], v[3])};
            }
        }
    }
}

extern "C" void kernel_launch(void* const* d_in, const int* in_sizes, int n_in, void* d_out, int out_size, void* d_ws, size_t ws_size,
                              hipStream_t stream) {
    const u16* x = (const u16*)d_in[0];
    const u16* wA = (const u16*)d_in[1];
    const u16* woA = (const u16*)d_in[2];
    const u16* wB = (const u16*)d_in[4];
    const u16* woB = (const u16*)d_in[5];
    const u16* gw = (const u16*)d_in[6];
    char* ws = (char*)d_ws;
    (void)hipFuncSetAttribute((const void*)k1_qkv, hipFuncAttributeMaxDynamicSharedMemorySize, LDS_BYTES);
    (void)hipFuncSetAttribute((const void*)k2_attn, hipFuncAttributeMaxDynamicSharedMemorySize, LDS_BYTES);
    k1_qkv<<<256, 512, LDS_BYTES, stream>>>(x, wA, wB, woA, woB, gw, ws);
    k2_attn<<<256, 512, LDS_BYTES, stream>>>(ws, (u16*)d_out);
}
```

```cpp
#define FILL_C 2
#define OUT_AUX 16
#include <hip/hip_runtime.h>
#include <hip/hip_bf16.h>
#include <stdint.h>

typedef short    bf16x8 __attribute__((ext_vector_type(8)));
typedef short    s16x4  __attribute__((ext_vector_type(4)));
typedef float    f32x2  __attribute__((ext_vector_type(2)));
typedef float    f32x4  __attribute__((ext_vector_type(4)));
typedef float    f32x16 __attribute__((ext_vector_type(16)));
typedef float    f32x32 __attribute__((ext_vector_type(32)));
typedef unsigned u32x2  __attribute__((ext_vector_type(2)));
typedef unsigned u32x4  __attribute__((ext_vector_type(4)));
typedef unsigned u32x8  __attribute__((ext_vector_type(8)));
typedef unsigned u32x32 __attribute__((ext_vector_type(32)));
typedef __bf16   bf2_t  __attribute__((ext_vector_type(2)));
typedef unsigned short u16;
#define LAS __attribute__((address_space(3)))
#define DI __device__ __forceinline__

constexpr int S_ = 2048, D_ = 640;
constexpr size_t IMG     = (size_t)32 * 163840;
constexpr size_t OFF_IQ  = 0;
constexpr size_t OFF_IB  = IMG;
constexpr size_t OFF_IA  = 2 * IMG;
constexpr size_t OFF_O   = 3 * IMG;
constexpr size_t OFF_WOT = 4 * IMG;
constexpr size_t OFF_G   = OFF_WOT + (size_t)1600 * 1024;
constexpr size_t OFF_KVP = OFF_G + 8192;
constexpr size_t OFF_FL  = OFF_KVP + (size_t)256 * 2048;
constexpr int    WS_USED = (int)(OFF_FL + 288 * 128);
constexpr unsigned EMPTY = 0xFFFFFFFFu;
typedef __amdgpu_buffer_rsrc_t rsrc_t;
constexpr int SC1 = 16;

#define CSTAMP(i)
#define PSTAMP(k, i)
#define WSTAMP(i)

DI unsigned pk2(float lo, float hi) { f32x2 v = {lo, hi}; return __builtin_bit_cast(unsigned, __builtin_convertvector(v, bf2_t)); }
DI float bflo(unsigned v) { return __uint_as_float(v << 16); }
DI float bfhi(unsigned v) { return __uint_as_float(v & 0xffff0000u); }
DI int rfl(int x) { return __builtin_amdgcn_readfirstlane(x); }
DI void glds16(const void* sbase, unsigned voff, unsigned lds_base) {
    const unsigned long long a = (unsigned long long)(uintptr_t)sbase;
    const unsigned long long u = (unsigned long long)(unsigned)rfl((int)(unsigned)a) | ((unsigned long long)(unsigned)rfl((int)(unsigned)(a >> 32)) << 32);
    asm volatile("s_mov_b32 m0, %0\n\ts_nop 0\n\tglobal_load_lds_dwordx4 %1, %2" :: "s"(rfl((int)lds_base)), "v"(voff), "s"(u) : "memory");
}
#define MFMA16(a, b, c) __builtin_amdgcn_mfma_f32_16x16x32_bf16((a), (b), (c), 0, 0, 0)
#define MFMA32(a, b, c) __builtin_amdgcn_mfma_f32_32x32x16_bf16((a), (b), (c), 0, 0, 0)
#define B8(x) __builtin_bit_cast(bf16x8, x)
DI u32x2 trd(const LAS char* p) { return __builtin_bit_cast(u32x2, __builtin_amdgcn_ds_read_tr16_b64_v4i16((LAS s16x4*)p)); }
DI float pmax(float t, float c) { return __int_as_float(max(__float_as_int(t), __float_as_int(c))); }
template <int N> DI void wait_vm_bar() { asm volatile("s_waitcnt vmcnt(%0) lgkmcnt(0)\n\ts_barrier" :: "n"(N) : "memory"); }

constexpr int LDS_BYTES = 163840;
constexpr float LOG2E = 1.4426950408889634f;

DI void p1_aux(const u16* __restrict__ x, const u16* __restrict__ woA, const u16* __restrict__ woB, const u16* __restrict__ gate_w,
               char* __restrict__ ws, char* lds, int a) {
    const int tid = threadIdx.x, lane = tid & 63, wave = rfl(tid >> 6);
    const unsigned lds0 = (unsigned)(uintptr_t)lds;
    const int br = a >> 3, u = a & 7;
    if (tid < 144) *(u32x4*)(ws + OFF_FL + (a * 144 + tid) * 16) = u32x4{0u, 0u, 0u, 0u};
    const char* wsrc = (const char*)(br ? woB : woA) + (size_t)u * 102400;
    for (int i = wave; i < 100; i += 8) glds16(wsrc + i * 1024, lane * 16, lds0 + i * 1024);
    const u32x4* xs = (const u32x4*)(x + (size_t)(a * 128 + wave * 16) * D_);
    u32x4 xv[20], gw[5];
#pragma unroll
    for (int i = 0; i < 20; ++i) xv[i] = xs[i * 64 + lane];
#pragma unroll
    for (int i = 0; i < 5; ++i) gw[i] = ((const u32x4*)gate_w)[(i * 64 + lane) % 80];
    {
        const rsrc_t rs = __builtin_amdgcn_make_buffer_rsrc((void*)(ws + OFF_KVP + a * 32768), 0, 32768, 0x00020000);
#pragma unroll
        for (int i = 0; i < 4; ++i) __builtin_amdgcn_raw_buffer_store_b128(u32x4{EMPTY, EMPTY, EMPTY, EMPTY}, rs, tid * 16 + i * 8192, 0, SC1);
    }
    LAS float* part = (LAS float*)(lds + 102400) + wave * 1280;
#pragma unroll
    for (int i = 0; i < 20; ++i) {
        float s = 0.f;
#pragma unroll
        for (int j = 0; j < 4; ++j) s += bflo(xv[i][j]) * bflo(gw[i % 5][j]) + bfhi(xv[i][j]) * bfhi(gw[i % 5][j]);
        part[i * 64 + lane] = s;
    }
    wait_vm_bar<0>();
    {
        float s = 0.f;
#pragma unroll
        for (int j = 0; j < 20; ++j) s += part[lane * 20 + j];
        s += __shfl_xor(s, 1); s += __shfl_xor(s, 2);
        if ((lane & 3) == 0) ((float*)(ws + OFF_G))[a * 128 + wave * 16 + (lane >> 2)] = 1.f / (1.f + __expf(-s));
    }
    const int g = lane >> 4, hh = g & 1, ns = g >> 1, q = (lane >> 2) & 3, p = lane & 3;
    for (int un = wave; un < 100; un += 8) {
        const int e = un / 20, np = un - e * 20, nt = np * 2 + ns;
        const int r0 = e < 4 ? e * 20 + 4 * hh : (2 * hh) * 20 + 16;
        const int r1 = e < 4 ? r0 + 8 : r0 + 20;
        const int ks16 = e < 4 ? (u & 1) * 4 + e : 8 + (u & 1), cbk = nt / 5;
        const u32x2 lo = trd((const LAS char*)lds + (r0 + q) * 1280 + nt * 32 + p * 8);
        const u32x2 hi = trd((const LAS char*)lds + (r1 + q) * 1280 + nt * 32 + p * 8);
        *(u32x4*)(ws + OFF_WOT + (size_t)((cbk * 8 + (u & 6) + br) * 25 + (ks16 >> 1) * 5 + nt - cbk * 5) * 1024 + ((ks16 & 1) * 2 + hh) * 256 + (lane & 15) * 16) = u32x4{lo[0], lo[1], hi[0], hi[1]};
    }
}

#define G1_TL 3
#define G1_D 3
#define G1_D0 2
#define G1_DT 10
#define G1_DE 0
#define G1_NX 4
constexpr int G1_NW = 10 - G1_NX, G1_HB = 20 - 2 * G1_TL, G1_WOFF = G1_NX * 16384;
constexpr int g1_t(int h) { return h < 2 * G1_HB ? h >> 1 : h < 2 * G1_HB + 2 * G1_TL ? h - G1_HB : h - G1_HB - 2 * G1_TL; }
constexpr int g1_m(int h) { return h < 2 * G1_HB ? h & 1 : h < 2 * G1_HB + 2 * G1_TL ? 0 : 1; }
constexpr int g1_xi(int h) { return h < 2 * G1_HB ? (h >> 1) & 1 : h & 1; }
constexpr int g1_free(int i) { return i < 10 ? (i < G1_NX ? -1 : 2 * (i - G1_NX) + 2) : (i - 10 < G1_NW ? -1 : i - 10 - G1_NW + 1); }
struct G1Plan { int seq[30]; int cnt[22]; int pos[30]; };
constexpr G1Plan g1_plan() {
    G1Plan p = {}; bool done[30] = {}, seen[10] = {}; int ord[30] = {}, need[21] = {}, n = 0;
    for (int b = 0; b < 20; ++b) { const int u = g1_t(2 * b) >> 1; if (!seen[u]) { seen[u] = true; ord[n++] = u; } need[b] = n; ord[n++] = 10 + b; }
    need[20] = 30; n = 0;
    for (int b = 0; b <= 20; ++b) {
        const int ex = b > G1_DT ? b - G1_DT : 0, lim = 3 * (b >> 1) + 1 + (b & 1) + (b == 0 ? G1_D0 : G1_D) + (ex < G1_DE ? ex : G1_DE);
        for (int k = 0; k < 30 && k <= lim; ++k) { const int i = ord[k]; if (!done[i] && g1_free(i) < b) { done[i] = true; p.pos[i] = n; p.seq[n++] = i; } }
        p.cnt[b] = n;
    }
    return p;
}
constexpr G1Plan G1P = g1_plan();
constexpr int g1_wait(int b) { const int x = G1P.pos[g1_t(2 * b) >> 1], w = G1P.pos[10 + b]; return 4 * (G1P.cnt[b] - 1 - (x > w ? x : w)); }
constexpr bool g1_ok() { for (int b = 0; b < 20; ++b) if (g1_wait(b) < 0 || g1_wait(b) > 60) return false; return G1P.cnt[19] == 30 && G1_NX >= G1_TL && G1_NX <= 4; }
static_assert(g1_ok(), "loader plan");
#define G1_ROT ((0x27057250 >> (4 * xcd)) & 15)

#define LST_P
#define LST_A
template <int T> struct G1Load {
    template <class FX, class FW> static DI void run(FX&& ldx, FW&& ldw LST_P) {
        asm volatile("s_waitcnt vmcnt(%0)\n\ts_barrier" :: "n"(g1_wait(T)) : "memory");
#pragma unroll
        for (int n = G1P.cnt[T]; n < G1P.cnt[T + 1]; ++n) { const int i = G1P.seq[n]; if (i < 10) ldx(i); else ldw(i - 10); }
        G1Load<T + 1>::run(ldx, ldw LST_A);
    }
};
template <> struct G1Load<20> { template <class FX, class FW> static DI void run(FX&&, FW&& LST_P) {} };

struct G1Ctx {
    f32x4 acc[4][8];
    bf16x8 x[2][4]; u32x2 w[2][4][2];
    const LAS char* xa[2]; const LAS char* wa[8];
    int so[4];
    rsrc_t rB; float sc; f32x2 m[4]; u32x4 o;
};
#define SB0() __builtin_amdgcn_sched_barrier(0)
#define G1_SP 12
template <int J, int I> DI void g1_side(G1Ctx& c) {
    constexpr int K = (J - (40 - 2 * G1_TL)) * 16 + I;
    if (K > 0 && K <= 8 * G1_SP && (K - 1) % G1_SP == 8) asm volatile("" :: "v"(c.o));
    if (K >= 0 && K < 8 * G1_SP && K % G1_SP < 9) {
        constexpr int U = K < 0 ? 0 : K / G1_SP, e = K < 0 ? 0 : K % G1_SP, ni = (U >> 1) & 3, a = U & 1;
        constexpr int mi = e == 0 ? 0 : e == 1 ? 1 : e == 3 ? 2 : e == 5 ? 3 : -1, ci = e == 2 ? 0 : e == 4 ? 1 : e == 6 ? 2 : e == 7 ? 3 : -1;
        if (mi >= 0) { const f32x4 v = c.acc[2 * a + ((mi & 3) >> 1)][ni]; c.m[mi & 3] = f32x2{v[(mi & 1) * 2], v[(mi & 1) * 2 + 1]} * c.sc; }
        if (ci >= 0) c.o[ci & 3] = pk2(c.m[ci & 3][0], c.m[ci & 3][1]);
        if (e == 8) __builtin_amdgcn_raw_buffer_store_b128(c.o, c.rB, c.so[ni] + a * 2560, 0, SC1);
        SB0();
    }
}
template <int J> struct G1Step {
    static DI void run(G1Ctx& c) {
        constexpr int N = J + 1, t = g1_t(N), xo = ((t >> 1) % G1_NX) * 16384, sl = (N >> 1) % G1_NW, wo = (sl & 3) * 16384 + (N & 1) * 8192, wi = (sl >> 2) * 4;
        constexpr int ps = J & 1, ma = g1_m(J) * 4, xc = g1_xi(J), xn = g1_xi(N);
#define G1_M(i) c.acc[(i) & 3][ma + ((i) >> 2)] = MFMA16(c.x[xc][(i) & 3], B8(((u32x4){c.w[ps][(i) >> 2][0][0], c.w[ps][(i) >> 2][0][1], c.w[ps][(i) >> 2][1][0], c.w[ps][(i) >> 2][1][1]})), \
                c.acc[(i) & 3][ma + ((i) >> 2)]); SB0(); g1_side<J, i>(c);
#define G1_X(i) if (N < 40) c.x[xn][i] = *(const LAS bf16x8*)(c.xa[t & 1] + xo + ((i) >> 1) * 4096 + ((i) & 1) * 1024); SB0();
#define G1_W(i, e) if (N < 40) c.w[ps ^ 1][i][e] = trd(c.wa[wi + (i)] + wo + (e) * 1024); SB0();
        if (ps) {
            G1_M(0) G1_M(1) G1_M(2)
            if (N < 40) asm volatile("s_waitcnt lgkmcnt(0)\n\ts_barrier" ::: "memory");
            SB0();
            G1_X(0) G1_M(3) G1_W(0, 0) G1_M(4) G1_W(0, 1) G1_M(5) G1_X(1) G1_M(6) G1_X(2) G1_M(7) G1_X(3) G1_M(8) G1_W(1, 0) G1_M(9) G1_W(1, 1) G1_M(10)
            G1_W(2, 0) G1_M(11) G1_W(2, 1) G1_M(12) G1_W(3, 0) G1_M(13) G1_W(3, 1) G1_M(14) G1_M(15)
        } else if (xn != xc) {
            G1_M(0) G1_X(0) G1_M(1) G1_W(0, 0) G1_M(2) G1_W(0, 1) G1_M(3) G1_X(1) G1_M(4) G1_X(2) G1_M(5) G1_X(3) G1_M(6) G1_W(1, 0) G1_M(7) G1_W(1, 1)
            G1_M(8) G1_W(2, 0) G1_M(9) G1_W(2, 1) G1_M(10) G1_W(3, 0) G1_M(11) G1_W(3, 1) G1_M(12) G1_M(13) G1_M(14) G1_M(15)
        } else {
            G1_M(0) G1_W(0, 0) G1_M(1) G1_W(0, 1) G1_M(2) G1_W(1, 0) G1_M(3) G1_W(1, 1) G1_M(4) G1_W(2, 0) G1_M(5) G1_W(2, 1) G1_M(6) G1_W(3, 0) G1_M(7) G1_W(3, 1)
            G1_M(8) G1_M(9) G1_M(10) G1_M(11) G1_M(12) G1_M(13) G1_M(14) G1_M(15)
        }
        G1Step<J + 1>::run(c);
    }
};
template <> struct G1Step<40> { static DI void run(G1Ctx&) {} };
template <int K> struct G1Rest { static DI void run(G1Ctx& c) { g1_side<40 - 2 * G1_TL + K / 16, K % 16>(c); G1Rest<K + 1>::run(c); } };
template <> struct G1Rest<8 * G1_SP> { static DI void run(G1Ctx&) {} };

DI void phase1(const u16* __restrict__ x, const u16* __restrict__ wA, const u16* __restrict__ wB, char* __restrict__ ws, char* lds, int bid) {
    const int tid = threadIdx.x, lane = tid & 63, wave = rfl(tid >> 6);
    const unsigned lds0 = (unsigned)(uintptr_t)lds;
    const int xcd = bid & 7, j = (xcd >> 1) * 30 + (bid >> 3);
    const int ct = j >> 3, mt = (xcd & 1) * 8 + (j & 7);
    const int type = rfl(ct / 5), rot = rfl(G1_ROT);
    if (__builtin_expect(wave >= 4, 1)) {
        const int lw = wave - 4;
        unsigned xo[4], wo[2];
#pragma unroll
        for (int i = 0; i < 4; ++i) { const int row = (lw * 4 + i) * 8 + (lane >> 3); xo[i] = (unsigned)((mt * 128 + row) * 1280 + (((lane & 7) ^ ((row >> 1) & 3) ^ ((row >> 2) & 4)) << 4)); }
#pragma unroll
        for (int i = 0; i < 2; ++i) { const int k = (lw * 2 + i) * 4 + (lane >> 4), s16 = lane & 15;
            wo[i] = (unsigned)(k * 3840 + ct * 256 + ((s16 ^ ((k & 3) << 2) ^ ((k >> 2) & 2)) << 4)); }
        auto ldx = [&](int u) {
            int kk = u + rot; kk -= kk >= 10 ? 10 : 0;
            const unsigned slot = lds0 + (u % G1_NX) * 16384 + lw * 4096;
#pragma unroll
            for (int i = 0; i < 4; ++i) glds16((const char*)x + kk * 128, xo[i], slot + i * 1024);
        };
        auto ldw = [&](int s) {
            int kk = (g1_t(2 * s) >> 1) + rot; kk -= kk >= 10 ? 10 : 0;
            const unsigned slot = lds0 + G1_WOFF + (s % G1_NW) * 16384 + lw * 2048;
#pragma unroll
            for (int e = 0; e < 2; ++e) {
                const char* src = (const char*)(g1_m(2 * s + e) ? wA : wB) + (kk * 64 + (g1_t(2 * s + e) & 1) * 32) * 3840;
                glds16(src, wo[0], slot + e * 8192); glds16(src, wo[1], slot + e * 8192 + 1024);
            }
        };
#pragma unroll
        for (int n = 0; n < G1P.cnt[0]; ++n) { const int i = G1P.seq[n]; if (i < 10) ldx(i); else ldw(i - 10); }
        G1Load<0>::run(ldx, ldw LST_A);
        return;
    }
    const int wm = wave & 1, wn = wave >> 1;
    const int i16 = lane & 15, g = lane >> 4, q = (lane >> 2) & 3, p = lane & 3;
    G1Ctx c;
    const int xr = (i16 >> 3) * 16 + (i16 & 7);
#pragma unroll
    for (int ks = 0; ks < 2; ++ks) { c.xa[ks] = (const LAS char*)lds + (wm * 64 + xr) * 128 + (((ks * 4 + g) ^ ((xr >> 1) & 3) ^ ((xr >> 2) & 4)) << 4); asm volatile("" : "+v"(c.xa[ks])); }
#pragma unroll
    for (int i = 0; i < 8; ++i) { c.wa[i] = (const LAS char*)lds + G1_WOFF + (i >> 2) * 65536 + (8 * g + q) * 256 + (((wn * 4 + (i & 3)) ^ (q << 1) ^ (g & 1)) << 5) + p * 8;
        asm volatile("" : "+v"(c.wa[i])); }
#pragma unroll
    for (int i = 0; i < 4; ++i) { const int cc = (ct - type * 5) * 128 + wn * 64 + i * 16 + i16, h = cc / 20; c.so[i] = h * 163840 + g * 320 + (cc - h * 20) * 16; asm volatile("" : "+v"(c.so[i])); }
    constexpr float QS = 0.22360679774997896f * LOG2E;
    int sci = type == 0 ? __builtin_bit_cast(int, QS) : 0x3F800000;
    asm volatile("" : "+s"(sci));
    c.sc = __int_as_float(sci);
    const int tb = (mt * 4 + wm * 2) * 2560;
    const int sB = tb + (type == 0 ? (int)OFF_IQ : (int)OFF_IB + (type - 1) * 1280), sA = tb + (type == 0 ? (int)OFF_IQ + 1280 : (int)OFF_IA + (type - 1) * 1280);
    c.rB = __builtin_amdgcn_make_buffer_rsrc((void*)(ws + sB), 0, (int)IMG, 0x00020000);
    const rsrc_t rA = __builtin_amdgcn_make_buffer_rsrc((void*)(ws + sA), 0, (int)IMG, 0x00020000);
    {
        const float a0 = type < 2 ? 1e-6f : 0.f;
        f32x4 a4 = {a0, a0, a0, a0};
        asm volatile("" : "+v"(a4));
#pragma unroll
        for (int i = 0; i < 16; ++i) { c.acc[i & 3][i >> 2] = f32x4{0.f, 0.f, 0.f, 0.f}; c.acc[i & 3][4 + (i >> 2)] = a4; }
    }
    __builtin_amdgcn_s_setprio(2);
    asm volatile("s_barrier" ::: "memory");
#pragma unroll
    for (int i = 0; i < 4; ++i) {
        c.x[0][i] = *(const LAS bf16x8*)(c.xa[0] + (i >> 1) * 4096 + (i & 1) * 1024);
        if (i == 0) { c.w[0][0][0] = trd(c.wa[0]); c.w[0][0][1] = trd(c.wa[0] + 1024); }
    }
#pragma unroll
    for (int i = 1; i < 4; ++i) { c.w[0][i][0] = trd(c.wa[i]); c.w[0][i][1] = trd(c.wa[i] + 1024); }
    SB0();
    G1Step<0>::run(c);
    PSTAMP(0, 2);
    G1Rest<(32 * G1_TL < 8 * G1_SP ? 32 * G1_TL : 8 * G1_SP)>::run(c);
    if (type < 2) {
#pragma unroll
        for (int i = 0; i < 4; ++i)
#pragma unroll
            for (int a = 0; a < 2; ++a) {
                u32x4 o;
#pragma unroll
                for (int e = 0; e < 4; ++e) {
                    const f32x2 t = {c.acc[2 * a + (e >> 1)][4 + i][(e & 1) * 2], c.acc[2 * a + (e >> 1)][4 + i][(e & 1) * 2 + 1]};
                    const f32x2 m = __builtin_elementwise_fma(t, f32x2{LOG2E, LOG2E}, f32x2{-1e-6f * LOG2E, -1e-6f * LOG2E});
                    const f32x2 ex = {__builtin_amdgcn_fmed3f(__builtin_amdgcn_exp2f(m[0]), 0.f, 1.f), __builtin_amdgcn_fmed3f(__builtin_amdgcn_exp2f(m[1]), 0.f, 1.f)};
                    const f32x2 r = f32x2{pmax(t[0], 1e-6f), pmax(t[1], 1e-6f)} + ex;
                    o[e] = pk2(r[0], r[1]);
                }
                __builtin_amdgcn_raw_buffer_store_b128(o, rA, c.so[i] + a * 2560, 0, SC1);
            }
    } else {
#pragma unroll
        for (int i = 0; i < 4; ++i)
#pragma unroll
            for (int a = 0; a < 2; ++a) {
                const f32x4 u = c.acc[2 * a][4 + i], v = c.acc[2 * a + 1][4 + i];
                __builtin_amdgcn_raw_buffer_store_b128(u32x4{pk2(u[0], u[1]), pk2(u[2], u[3]), pk2(v[0], v[1]), pk2(v[2], v[3])}, rA, c.so[i] + a * 2560, 0, SC1);
            }
    }
}

__global__ __launch_bounds__(512, 2) void k1_qkv(const u16* __restrict__ x, const u16* __restrict__ wA, const u16* __restrict__ wB,
                                                 const u16* __restrict__ woA, const u16* __restrict__ woB, const u16* __restrict__ gate_w,
                                                 char* __restrict__ ws) {
    extern __shared__ __attribute__((aligned(16))) char lds[];
    int bid = blockIdx.x;
    asm volatile("" : "+s"(bid));
    PSTAMP(0, 0);
    if (__builtin_expect(bid < 240, 1)) phase1(x, wA, wB, ws, lds, bid);
    else p1_aux(x, woA, woB, gate_w, ws, lds, bid - 240);
}

#define STR(x) #x
#define XS(x) STR(x)
#define A_E(i) "v_exp_f32 v" XS(i) ", v" XS(i) "\n"
#define A_C(d, a, b) "v_cvt_pk_bf16_f32 v" XS(d) ", v" XS(a) ", v" XS(b) "\n"
#define A_G(a, b, c, d, p0, p1) A_E(a) A_E(b) A_E(c) A_E(d) A_C(p0, a, b) A_C(p1, c, d)
#define A_M(d, a, b, c) "v_mfma_f32_32x32x16_bf16 " d ", " a ", " b ", " c "\n"
#define A_STEP(C0,C1,C2,C3,C4,C5,C6,C7,C8,C9,C10,C11,C12,C13,C14,C15, SN, KM, KT, V1, V0, NKMa, NKMb, NKT, NV1, NV0, OK0, OK1, OV1, OV0, PRIO) \
    "s_waitcnt lgkmcnt(0)\n" \
    A_M(SN, KM, "v[120:123]", "0") \
    "ds_read_b64_tr_b16 " NKMa ", v128 offset:" XS(OK0) "\n" \
    "ds_read_b64_tr_b16 " NKMb ", v128 offset:" XS(OK1) "\n" \
    "ds_read_b64_tr_b16 " NKT ", v129 offset:" XS(OK0) "\n" \
    A_G(C0, C1, C2, C3, 112, 113) \
    A_M("v[64:79]", V1, "v[116:119]", "v[64:79]") \
    "s_mov_b64 exec, s[72:73]\n" \
    "ds_read_b128 " NV1 ", v130 offset:" XS(OV1) "\n" \
    "ds_read_b128 " NV0 ", v130 offset:" XS(OV0) "\n" \
    "s_mov_b64 exec, -1\n" \
    A_G(C4, C5, C6, C7, 114, 115) \
    A_M(SN, KT, "v[124:127]", SN) \
    A_G(C8, C9, C10, C11, 116, 117) \
    A_M("v[64:79]", V0, "v[112:115]", "v[64:79]") \
    "s_setprio " XS(PRIO) "\n" \
    A_G(C12, C13, C14, C15, 118, 119)
#define A_EVEN(OK0, OK1, OV1, OV0, PRIO) A_STEP(32,33,34,35,36,37,38,39,40,41,42,43,44,45,46,47, "v[48:63]", "v[80:83]", "v[84:87]", "v[88:91]", "v[92:95]", \
    "v[96:97]", "v[98:99]", "v[100:101]", "v[104:107]", "v[108:111]", OK0, OK1, OV1, OV0, PRIO)
#define A_ODD(OK0, OK1, OV1, OV0, PRIO) A_STEP(48,49,50,51,52,53,54,55,56,57,58,59,60,61,62,63, "v[32:47]", "v[96:99]", "v[100:103]", "v[104:107]", "v[108:111]", \
    "v[80:81]", "v[82:83]", "v[84:85]", "v[88:91]", "v[92:95]", OK0, OK1, OV1, OV0, PRIO)

#define A_LD(g, off) "global_load_lds_dwordx4 v131, " g " offset:" XS(off) "\n"
#define A_LDA(off) "s_cbranch_scc1 .Lattn_s" XS(off) "\n" A_LD("s[74:75]", off) ".Lattn_s" XS(off) ":\n"
#define A_BODY \
        "s_mov_b32 m0, s78\n s_cmp_eq_u32 s80, 2\n s_nop 3\n" \
        A_EVEN(5120, 5184, 640, 2560, 2)     A_ODD(7680, 7744, 3200, 5120, 1) \
        A_EVEN(10240, 10304, 5760, 7680, 0)  A_ODD(12800, 12864, 8320, 10240, 3) \
        "s_waitcnt vmcnt(0) lgkmcnt(0)\n s_mov_b64 exec, 1\n ds_add_u32 v127, v86\n s_mov_b64 exec, -1\n" \
        A_EVEN(15360, 15424, 10880, 12800, 2) A_LDA(0) A_ODD(17920, 17984, 13440, 15360, 1) A_LDA(1024) \
        A_EVEN(20480, 20544, 16000, 17920, 0) A_LDA(2048) \
        "s_cmp_eq_u32 s80, 1\n s_cbranch_scc0 .Lattn_skip\n s_mov_b32 m0, s79\n s_nop 0\n" \
        A_LD("s[76:77]", 0) A_LD("s[76:77]", 1024) A_LD("s[76:77]", 2048) ".Lattn_skip:\n" \
        "ds_read_b32 v132, v127\n" \
        A_ODD(23040, 23104, 18560, 20480, 3) \
        "v_add_u32 v128, 0x5000, v128\n v_add_u32 v129, 0x5000, v129\n v_add_u32 v130, 0x5000, v130\n" \
        "s_waitcnt lgkmcnt(0)\n s_nop 0\n"
#define A_OPERANDS \
        : "+{v[32:63]}"(S), "+{v[64:79]}"(O), "+{v[80:111]}"(OPS), "+{v[112:119]}"(P), "+{v[128:131]}"(AD), "={v132}"(cnt) \
        : "{v[120:127]}"(Q), "{s[72:73]}"(vmask), "{s[74:75]}"(ga), "{s[76:77]}"(gb), "{s78}"(ma), "{s79}"(mb), "{s80}"(enb) \
        : "memory", "scc", "s82", "s83"
template <bool FIRST>
DI unsigned attn_chunk(f32x32& S, f32x16& O, u32x32& OPS, u32x8& P, u32x4& AD, const u32x8& Q, unsigned long long vmask,
                   unsigned ma, unsigned long long ga, unsigned mb, unsigned long long gb, unsigned enb) {
    unsigned cnt;
    if (FIRST) asm volatile("s_call_b64 s[82:83], .Lattn_body\n s_branch .Lattn_done\n .p2align 6\n .Lattn_body:\n" A_BODY "s_setpc_b64 s[82:83]\n .Lattn_done:\n" A_OPERANDS);
    else       asm volatile("s_call_b64 s[82:83], .Lattn_body\n" A_OPERANDS);
    return cnt;
}
DI void wait_cnt(char* lds, int off, unsigned cnt, int n) {
    if (__builtin_expect((unsigned)rfl((int)cnt) < (unsigned)(n * 0x3F80), 0)) {
#pragma clang loop unroll(disable)
        do { __builtin_amdgcn_s_sleep(1); cnt = *(volatile LAS unsigned*)(lds + off); } while ((unsigned)rfl((int)cnt) < (unsigned)(n * 0x3F80));
    }
    asm volatile("" ::: "memory");
}

DI void wait_cnt(char* lds, int off, int n) { wait_cnt(lds, off, *(volatile LAS unsigned*)(lds + off), n); }
template <int OFF> DI void add_cnt(unsigned lds0) {
    asm volatile("s_waitcnt lgkmcnt(0)\n\ts_mov_b64 exec, 1\n\tds_add_u32 %0, %1 offset:%2\n\ts_mov_b64 exec, -1" :: "v"(lds0), "v"(0x3F80u), "n"(OFF) : "memory"); }

DI float xhalf_max(float m) { auto rr = __builtin_amdgcn_permlane32_swap(__float_as_uint(m), __float_as_uint(m), false, false);
    return fmaxf(__uint_as_float(rr[0]), __uint_as_float(rr[1])); }
DI float hi_half(float v) {
    auto rr = __builtin_amdgcn_permlane32_swap(__float_as_uint(v), __float_as_uint(v), false, false);
    return __uint_as_float(rr[1]); }
DI float sum16(float v) { auto rr = __builtin_amdgcn_permlane16_swap(__float_as_uint(v), __float_as_uint(v), false, false);
    return __uint_as_float(rr[0]) + __uint_as_float(rr[1]); }
DI float sum32(float v) { auto rr = __builtin_amdgcn_permlane32_swap(__float_as_uint(v), __float_as_uint(v), false, false);
    return __uint_as_float(rr[0]) + __uint_as_float(rr[1]); }
DI f32x4 sum_rows(f32x4 a, f32x4 b) {
    f32x4 s = a + b;
#pragma unroll
    for (int i = 0; i < 4; ++i) s[i] = sum32(sum16(s[i]));
    return s;
}
DI unsigned long long uni64(const void* p) { const unsigned long long a = (unsigned long long)(uintptr_t)p;
    return (unsigned long long)(unsigned)rfl((int)(unsigned)a) | ((unsigned long long)(unsigned)rfl((int)(unsigned)(a >> 32)) << 32); }
DI unsigned full(unsigned v) { return v < EMPTY ? v : EMPTY - 1u; }
DI unsigned umax4(unsigned m, u32x4 v) { return max(max(max(m, v[0]), max(v[1], v[2])), v[3]); }
DI void store_o(rsrc_t rs, const f32x16& o, float f, int rt, int br, int h, int lane) {
    const int rb = (int)OFF_O + (rt * 80 + ((br ? 0 : 4) + (h >> 3)) * 10) * 1024;
    __builtin_amdgcn_raw_buffer_store_b128(u32x4{full(pk2(o[0] * f, o[1] * f)), full(pk2(o[2] * f, o[3] * f)), full(pk2(o[4] * f, o[5] * f)), full(pk2(o[6] * f, o[7] * f))},
                                           rs, lane * 16, rb + (h & 7) * 1024, SC1);
    if (lane < 32) __builtin_amdgcn_raw_buffer_store_b64(u32x2{full(pk2(o[8] * f, o[9] * f)), full(pk2(o[10] * f, o[11] * f))},
                                           rs, lane * 16, rb + (8 + ((h >> 2) & 1)) * 1024 + ((h >> 1) & 1) * 512 + (h & 1) * 8, SC1);
}
DI void empty_o(rsrc_t rs, int rt, int h, int lane) {
    const int rb = (int)OFF_O + (rt * 80 + (h >> 3) * 10) * 1024;
    u32x4 e = {EMPTY, EMPTY, EMPTY, EMPTY};
    __builtin_amdgcn_raw_buffer_store_b128(e, rs, lane * 16, rb + (h & 7) * 1024, SC1);
    if (lane < 32) __builtin_amdgcn_raw_buffer_store_b64(u32x2{e[0], e[1]}, rs, lane * 16, rb + (8 + ((h >> 2) & 1)) * 1024 + ((h >> 1) & 1) * 512 + (h & 1) * 8, SC1);
    asm volatile("" :: "v"(e));
}
DI void set_flag(rsrc_t rs, int off) { __builtin_amdgcn_raw_buffer_store_b32(1u, rs, 0, off, SC1); }
DI unsigned get_flag(rsrc_t rs, int voff, int off) { return __builtin_amdgcn_raw_buffer_load_b32(rs, voff, off, SC1); }
DI unsigned a_ld1(unsigned long long sb, unsigned voff) { unsigned v; asm volatile("global_load_dword %0, %1, %2 sc1" : "=v"(v) : "v"(voff), "s"(sb) : "memory"); return v; }
DI u32x4 a_ld4s(unsigned long long sb, unsigned voff) { u32x4 v; asm volatile("global_load_dwordx4 %0, %1, %2 sc1" : "=v"(v) : "v"(voff), "s"(sb) : "memory"); return v; }
template <int OFF> DI u32x4 a_ld4so(unsigned long long sb, unsigned voff) { u32x4 v;
    asm volatile("global_load_dwordx4 %0, %1, %2 offset:%3 sc1" : "=v"(v) : "v"(voff), "s"(sb), "n"(OFF) : "memory"); return v; }
template <int OFF> DI u32x4 a_ld4(unsigned long long sb, unsigned voff) { u32x4 v;
    asm volatile("global_load_dwordx4 %0, %1, %2 offset:%3" : "=v"(v) : "v"(voff), "s"(sb), "n"(OFF) : "memory"); return v; }
template <class T> DI void landed(T& v) { asm volatile("" : "+v"(v)); }
DI void wait_flag(rsrc_t rs, int voff, int off, unsigned v) {
    if (__builtin_expect(!__all(v == 1u), 0)) {
#pragma clang loop unroll(disable)
        for (int spin = 0; spin < (1 << 22); ++spin) { if (__all(get_flag(rs, voff, off) == 1u)) break; __builtin_amdgcn_s_sleep(1); }
    }
    __builtin_amdgcn_fence(__ATOMIC_ACQUIRE, "wavefront");
    asm volatile("" ::: "memory");
}

constexpr int L_SP = 136192, L_K0 = 152320, L_SQ = 153600, L_QA = 1280, L_KVF = 11520, L_WO = 13312;

__global__ __launch_bounds__(512, 2) void k2_attn(char* __restrict__ ws, u16* __restrict__ out) {
    extern __shared__ __attribute__((aligned(16))) char lds[];
    int bid = blockIdx.x;
    asm volatile("" : "+s"(bid));
    PSTAMP(1, 0); WSTAMP(0);
    const int tid = threadIdx.x, lane = tid & 63, wave = rfl(tid >> 6);
    const int h = (bid & 7) * 4 + ((bid >> 3) & 3), qb = bid >> 5;
    const int oq = bid & 7, rb = oq * 4 + ((bid >> 3) & 3), cb = bid >> 5;
    const int r = lane & 31, hh = lane >> 5;
    const unsigned lds0 = (unsigned)(uintptr_t)lds;
    const rsrc_t rs = __builtin_amdgcn_make_buffer_rsrc((void*)ws, 0, WS_USED, 0x00020000);
    const char* kvb = ws + OFF_IB + (size_t)h * 163840;
    const char* qim = ws + OFF_IQ + (size_t)h * 163840 + qb * 20480;
    const int wr = (wave & 3) * 2 + (wave >> 2), rt = qb * 8 + wr;
    const int rc = r < 20 ? r : 19;
    glds16(qim + wr * 2560, lane * 16, lds0 + L_SQ + wave * 1280);
    glds16(qim + wr * 2560 + 256, lane * 16, lds0 + L_SQ + wave * 1280 + 256);
    glds16(kvb + (wave & 1) * 256, lane * 16, lds0 + L_K0 + (wave & 1) * 256);
#pragma unroll
    for (int i = 0; i < 3; ++i) glds16(kvb + (1 + i * 8 + wave) * 1024, lane * 16, lds0 + (1 + i * 8 + wave) * 1024);
    const char* ap = ws + OFF_IA + (size_t)h * 163840 + rt * 2560 + hh * 320 + rc * 16;
    u32x4 a0, a1, b0, b1; float gate;
    asm volatile("global_load_dwordx4 %0, %5, off\n\tglobal_load_dwordx4 %1, %5, off offset:640\n\tglobal_load_dwordx4 %2, %5, off offset:1280\n\t"
                 "global_load_dwordx4 %3, %5, off offset:1920\n\tglobal_load_dword %4, %6, off"
                 : "=&v"(a0), "=&v"(a1), "=&v"(b0), "=&v"(b1), "=&v"(gate) : "v"(ap), "v"((const float*)(ws + OFF_G) + rt * 32 + r) : "memory");
#pragma unroll
    for (int i = 0; i < 3; ++i) glds16(kvb + (25 + i * 8 + wave) * 1024, lane * 16, lds0 + (25 + i * 8 + wave) * 1024);
    if (tid < 4) *(LAS unsigned*)(lds + tid * 4) = 0u;
    const u32x4 ONES = {0x3F803F80u, 0x3F803F80u, 0x3F803F80u, 0x3F803F80u};
    const f32x16 Z16 = {};
    wait_vm_bar<8>();
    PSTAMP(1, 1); WSTAMP(1);
    const int g = lane >> 4, q = (lane >> 2) & 3, p = lane & 3;
    const int tro = (g & 1) * 640 + (p & 1) * 320 + q * 16 + (p >> 1) * 8;
    const LAS char* L = (const LAS char*)lds;
    const LAS char* kmp = L + tro + hh * 128;
    const LAS char* ktp = L + tro + 256;
    const LAS char* vp  = L + 1280 + hh * 320 + r * 16;
    const int ent = tid < 126 ? tid : 125;
    u32x8 Q;
    {
        const u32x2 a = trd(kmp + L_SQ + wave * 1280), b = trd(kmp + L_SQ + wave * 1280 + 64), c = trd(ktp + L_SQ + wave * 1280);
        Q[0] = a[0]; Q[1] = a[1]; Q[2] = b[0]; Q[3] = b[1]; Q[4] = hh ? 0u : c[0]; Q[5] = hh ? 0u : c[1]; Q[6] = 0u; Q[7] = 0u;
    }
    const bf16x8 qm = B8(((u32x4){Q[0], Q[1], Q[2], Q[3]}));
    f32x32 S; f32x16 O = {}; u32x32 OPS; u32x8 P = {}; u32x4 AD;
    {
        const u32x2 a = trd(kmp + L_K0), b = trd(kmp + L_K0 + 64), c = trd(ktp + L_K0);
        const bf16x8 k0 = B8(((u32x4){a[0], a[1], b[0], b[1]})), k1 = B8(((u32x4){c[0], c[1], 0x3F80u, 0u}));
        const f32x16 sm = MFMA32(k0, qm, Z16);
        f32x16 s0 = MFMA32(k1, B8(((u32x4){Q[4], Q[5], 0u, 0u})), sm);
        float m = s0[0];
#pragma unroll
        for (int i = 1; i < 16; ++i) m = fmaxf(m, s0[i]);
        m = xhalf_max(m);
        Q[6] = hh ? 0u : (pk2(-m, 0.f) & 0xffffu);
        s0 = MFMA32(k1, B8(((u32x4){Q[4], Q[5], Q[6], 0u})), sm);
#pragma unroll
        for (int i = 0; i < 16; ++i) { S[i] = s0[i]; S[16 + i] = 0.f; }
        const unsigned vc = r == 20 ? 0x3F803F80u : 0u;
#pragma unroll
        for (int i = 0; i < 32; ++i) OPS[i] = (i & 8) ? vc : 0u;
        const u32x2 a1 = trd(kmp + 2560), b1 = trd(kmp + 2560 + 64), c1 = trd(ktp + 2560);
        OPS[0] = a1[0]; OPS[1] = a1[1]; OPS[2] = b1[0]; OPS[3] = b1[1]; OPS[4] = c1[0]; OPS[5] = c1[1]; OPS[6] = 0x3F80u; OPS[22] = 0x3F80u;
        if (r < 20) { const u32x4 v0 = *(const LAS u32x4*)vp; OPS[12] = v0[0]; OPS[13] = v0[1]; OPS[14] = v0[2]; OPS[15] = v0[3]; }
        AD[0] = (unsigned)(uintptr_t)kmp; AD[1] = (unsigned)(uintptr_t)ktp; AD[2] = (unsigned)(uintptr_t)vp; AD[3] = lane * 16;
    }
    {
        asm volatile("s_waitcnt vmcnt(3)" : "+v"(a0), "+v"(a1), "+v"(b0), "+v"(b1), "+v"(gate) :: "memory");
        if (r >= 20) { a0 = u32x4{0u, 0u, 0u, 0u}; a1 = a0; b0 = ONES; b1 = ONES; }
        f32x16 kvacc = MFMA32(B8(a0), B8(b0), Z16); kvacc = MFMA32(B8(a1), B8(b1), kvacc);
        if (r <= 20) {
            LAS f32x4* dst = (LAS f32x4*)(lds + L_SP + ((wave * 2 + hh) * 21 + r) * 48);
            dst[0] = f32x4{kvacc[0], kvacc[1], kvacc[2], kvacc[3]};
            dst[1] = f32x4{kvacc[4], kvacc[5], kvacc[6], kvacc[7]};
            dst[2] = f32x4{kvacc[8], kvacc[9], kvacc[10], kvacc[11]};
        }
    }
    const unsigned long long vmask = 0x000FFFFF000FFFFFull;
    const int j = wave & 3, isE = (wave >> 2) ^ 1;
    const char* wop = ws + OFF_WOT + (size_t)((cb * 4 + j) * 50) * 1024;
    const int flA = (int)OFF_FL + (oq * 4 + j) * 128, flv = (lane & 7) * 16;
    const int ov = ((g >> 1) * 1024 + (g & 1) * 512 + (lane & 15) * 16), os = (int)OFF_O + (rb * 160 + j * 10) * 1024;
    unsigned fl = 0u;
    u32x4 kp[8], wa[20], aa[2][10];
    const unsigned long long wsu = uni64(ws);
    const unsigned long long kb = uni64(ws + OFF_KVP + 4096 + h * 16384), kb2 = uni64(ws + OFF_KVP + 12288 + h * 16384);
    const unsigned long long ob[2][2] = {{uni64(ws + os + 4096), uni64(ws + os + 8192)}, {uni64(ws + os + 81920 + 4096), uni64(ws + os + 81920 + 8192)}};
#define LD_H(A, vo, hf, en, C) asm volatile("s_cmp_lg_u32 %13, 0\n\ts_cselect_b64 exec, -1, 0\n\t" \
        "global_load_dwordx4 %0, %10, %11 offset:-4096 sc1\n\tglobal_load_dwordx4 %1, %10, %11 offset:-3840 sc1\n\t" \
        "global_load_dwordx4 %2, %10, %11 offset:-2048 sc1\n\tglobal_load_dwordx4 %3, %10, %11 offset:-1792 sc1\n\t" \
        "global_load_dwordx4 %4, %10, %11 sc1\n\tglobal_load_dwordx4 %5, %10, %11 offset:256 sc1\n\t" \
        "global_load_dwordx4 %6, %10, %11 offset:2048 sc1\n\tglobal_load_dwordx4 %7, %10, %11 offset:2304 sc1\n\t" \
        "global_load_dwordx4 %8, %10, %12 sc1\n\tglobal_load_dwordx4 %9, %10, %12 offset:256 sc1\n\ts_mov_b64 exec, -1" \
        : C(A[0]), C(A[1]), C(A[2]), C(A[3]), C(A[4]), C(A[5]), C(A[6]), C(A[7]), C(A[8]), C(A[9]) \
        : "v"(vo), "s"(ob[hf][0]), "s"(ob[hf][1]), "s"(en) : "memory", "scc");
#define LD_W4(A, f) asm volatile("s_cmp_lg_u32 %6, 0\n\ts_cselect_b64 exec, -1, 0\n\t" \
        "global_load_dwordx4 %0, %4, %5\n\tglobal_load_dwordx4 %1, %4, %5 offset:1024\n\t" \
        "global_load_dwordx4 %2, %4, %5 offset:2048\n\tglobal_load_dwordx4 %3, %4, %5 offset:3072\n\ts_mov_b64 exec, -1" \
        : "=&v"((A)[0]), "=&v"((A)[1]), "=&v"((A)[2]), "=&v"((A)[3]) : "v"(lane * 16), "s"(uni64(wop + (f) * 1024)), "s"(isE) : "memory", "scc");
#define LD_KP(TAIL) asm volatile("s_cmp_lt_u32 %11, 2\n\ts_cselect_b64 exec, -1, 0\n\t" \
        "global_load_dwordx4 %0, %8, %9 offset:-4096 sc1\n\tglobal_load_dwordx4 %1, %8, %9 offset:-2048 sc1\n\t" \
        "global_load_dwordx4 %2, %8, %9 sc1\n\tglobal_load_dwordx4 %3, %8, %9 offset:2048 sc1\n\t" \
        "global_load_dwordx4 %4, %8, %10 offset:-4096 sc1\n\tglobal_load_dwordx4 %5, %8, %10 offset:-2048 sc1\n\t" \
        "global_load_dwordx4 %6, %8, %10 sc1\n\tglobal_load_dwordx4 %7, %8, %10 offset:2048 sc1\n\t" \
        "s_mov_b64 exec, -1" TAIL \
        : "=&v"(kp[0]), "=&v"(kp[1]), "=&v"(kp[2]), "=&v"(kp[3]), "=&v"(kp[4]), "=&v"(kp[5]), "=&v"(kp[6]), "=&v"(kp[7]) \
        : "v"(ent * 16), "s"(kb), "s"(kb2), "s"(wave) : "memory", "scc")
#pragma unroll
    for (int c = 0; c < 8; ++c) {
        unsigned ma, mb; unsigned long long ga, gb;
        if (c < 3) {
            const int lim = (c == 1 ? L_SP : 163840) - 3072;
            const int oa = (c == 2 ? L_SP : 50176 + c * 49152) + wave * 3072, ob = oa + 24576 < lim ? oa + 24576 : lim;
            ma = lds0 + oa; mb = lds0 + ob; ga = uni64(kvb + oa); gb = uni64(kvb + ob);
        } else {
            ma = lds0 + (c == 7 ? 1024 + j * 3072 : L_WO + ((c - 3) * 8 + wr) * 3072); mb = ma;
            ga = uni64(wop + (c == 7 ? 44 : 20 + (wave >> 2) * 12 + 3 * (c - 3)) * 1024); gb = ga;
        }
        CSTAMP(2 * c);
        unsigned cnt;
        if (c == 0) cnt = attn_chunk<true>(S, O, OPS, P, AD, Q, vmask, rfl(ma), ga, rfl(mb), gb, 1u);
        else cnt = attn_chunk<false>(S, O, OPS, P, AD, Q, vmask, rfl(ma), ga, rfl(mb), gb, c < 3 ? 1u : c < 7 ? 0u : (unsigned)(2 - 2 * isE));
        CSTAMP(2 * c + 1);
        if (c < 7) wait_cnt(lds, 0, cnt, 8 * (c + 1));
        if (c == FILL_C && c != 1) empty_o(rs, rt, h, lane);
        if (c == 0) {
            if (wave < 2) {
                f32x4 s = *(const LAS f32x4*)(L + L_SP + ent * 16);
#pragma unroll
                for (int w = 1; w < 8; ++w) s += *(const LAS f32x4*)(L + L_SP + w * 2016 + ent * 16);
                const u32x4 v = __builtin_bit_cast(u32x4, s);
                if (tid < 126) __builtin_amdgcn_raw_buffer_store_b128(u32x4{full(v[0]), full(v[1]), full(v[2]), full(v[3])}, rs, tid * 16, (int)OFF_KVP + (h * 8 + qb) * 2048, SC1);
            }
            glds16(qim + wr * 2560 + 1280, lane * 16, lds0 + L_QA + wave * 1280);
            glds16(qim + wr * 2560 + 1280 + 256, lane * 16, lds0 + L_QA + wave * 1280 + 256);
        } else if (c == 1) {
            LD_KP("");
            if (FILL_C == 1) empty_o(rs, rt, h, lane);
        } else if (c == 2) {
#pragma unroll
            for (int j = 0; j < 8; ++j) landed(kp[j]);
            if (wave < 2) {
#pragma clang loop unroll(disable)
                for (int spin = 0; spin < (1 << 20); ++spin) {
                    unsigned m = 0u;
#pragma unroll
                    for (int j = 0; j < 8; ++j) m = umax4(m, kp[j]);
                    if (__builtin_expect(__all(m != EMPTY), 1)) break;
                    LD_KP("\n\ts_waitcnt vmcnt(0)");
                }
                f32x4 s = __builtin_bit_cast(f32x4, kp[0]);
#pragma unroll
                for (int j = 1; j < 8; ++j) s += __builtin_bit_cast(f32x4, kp[j]);
                const int e3 = ent / 3;
                *(LAS u32x2*)(lds + L_KVF + e3 * 32 + (ent - e3 * 3) * 8) = u32x2{pk2(s[0], s[1]), pk2(s[2], s[3])};
            }
        } else if (c == 3) {
            const int rr = r <= 20 ? r : 20;
            const u32x4 ka0 = *(const LAS u32x4*)(L + L_KVF + (hh * 21 + rr) * 32);
            const u32x2 ka1 = *(const LAS u32x2*)(L + L_KVF + (hh * 21 + rr) * 32 + 16);
            const LAS char* qa = L + L_QA + wave * 1280 + tro;
            const u32x2 pq0 = trd(qa + hh * 64), pq1 = trd(qa + 128 + hh * 64);
            u32x2 pq2 = trd(qa + 256);
            if (hh) pq2 = u32x2{0u, 0u};
            f32x16 oa = MFMA32(B8(ka0), B8(((u32x4){pq0[0], pq0[1], pq1[0], pq1[1]})), Z16);
            oa = MFMA32(B8(((u32x4){ka1[0], ka1[1], 0u, 0u})), B8(((u32x4){pq2[0], pq2[1], 0u, 0u})), oa);
            const float den = hi_half(oa[8]) + 1e-6f;
            store_o(rs, oa, gate * __builtin_amdgcn_rcpf(den), rt, 0, h, lane);
            LD_W4(wa, 0) LD_W4(wa + 4, 4)
        } else if (c == 4) {
            if (tid == 0) set_flag(rs, (int)OFF_FL + (qb * 4 + (h >> 3)) * 128 + (h & 7) * 16);
            LD_W4(wa + 8, 8) LD_W4(wa + 12, 12)
        } else if (c == 5) {
            fl = a_ld1(wsu, flA + flv);
            LD_W4(wa + 16, 16)
        } else if (c == 6) {
            landed(fl);
            if (wave < 4) wait_flag(rs, flv, flA, fl);
            LD_H(aa[0], ov + 40960, 0, isE, "=&v")
        }
    }
#pragma unroll
    for (int i = 0; i < 10; ++i) { landed(wa[i]); landed(wa[10 + i]); landed(aa[0][i]); }
    u32x4 wd[4];
    LD_H(aa[1], ov + 40960, 1, isE, "=&v") LD_W4(wd, 46)
    if (lane == 0) set_flag(rs, (int)OFF_FL + (32 + rt * 4 + (h >> 3)) * 128 + (h & 7) * 16);
    O = MFMA32(B8(((u32x4){OPS[8], OPS[9], OPS[10], OPS[11]})), B8(((u32x4){P[4], P[5], P[6], P[7]})), O);
    PSTAMP(1, 2); WSTAMP(2);
    float l = hi_half(O[8]);
    {
        bool bad = !(l >= 1e-18f && l <= 1.0e30f);
#pragma unroll
        for (int i = 0; i < 12; ++i) bad = bad || !(fabsf(O[i]) <= 3.0e38f);
        if (__builtin_expect(__any(bad), 0)) {
            const bf16x8 qt0 = B8(((u32x4){Q[4], Q[5], 0u, 0u}));
            float m = -INFINITY; O = Z16;
            int tl = tid;
            asm volatile("" : "+v"(tl));
            const int r = tl & 31, hh = (tl >> 5) & 1, rc = r < 20 ? r : 19;
            const int ko = (r >> 4) * 640 + ((r >> 2) & 1) * 320 + ((r >> 3) & 1) * 8 + (r & 3) * 2;
            for (int t = 0; t < 64; ++t) {
                const char* tb = kvb + t * 2560;
                unsigned kw[6];
#pragma unroll
                for (int i = 0; i < 6; ++i) {
                    const int d = i < 4 ? 8 * hh + 2 * i : 16 + 2 * (i - 4);
                    kw[i] = (unsigned)*(const u16*)(tb + ko + d * 16) | ((unsigned)*(const u16*)(tb + ko + d * 16 + 16) << 16);
                }
                u32x4 f0 = *(const u32x4*)(tb + 1280 + hh * 320 + rc * 16), f1 = *(const u32x4*)(tb + 1920 + hh * 320 + rc * 16);
                if (r >= 20) { f0 = ONES; f1 = ONES; }
                f32x16 s = MFMA32(B8(((u32x4){kw[0], kw[1], kw[2], kw[3]})), qm, Z16);
                s = MFMA32(B8(((u32x4){kw[4], kw[5], 0u, 0u})), qt0, s);
                float tm = s[0];
#pragma unroll
                for (int i = 1; i < 16; ++i) tm = fmaxf(tm, s[i]);
                tm = xhalf_max(tm);
                const float mn = fmaxf(m, tm);
                const float al = (m == -INFINITY) ? 0.f : __builtin_amdgcn_exp2f(m - mn);
                m = mn;
#pragma unroll
                for (int i = 0; i < 16; ++i) { O[i] *= al; s[i] = __builtin_amdgcn_exp2f(s[i] - mn); }
                u32x4 p0 = {pk2(s[0], s[1]), pk2(s[2], s[3]), pk2(s[4], s[5]), pk2(s[6], s[7])};
                u32x4 p1 = {pk2(s[8], s[9]), pk2(s[10], s[11]), pk2(s[12], s[13]), pk2(s[14], s[15])};
                O = MFMA32(B8(f0), B8(p0), O); O = MFMA32(B8(f1), B8(p1), O);
            }
            l = hi_half(O[8]);
        }
    }
    store_o(rs, O, (1.f - gate) * __builtin_amdgcn_rcpf(l), rt, 1, h, lane);
    __builtin_amdgcn_s_setprio(0);
    __builtin_amdgcn_sched_barrier(0);
    WSTAMP(3);
    constexpr int L_P0 = L_WO + 98304;
    if (wave < 4) {
        const LAS char* wl = L + j * 6144 + lane * 16;
        auto wfr = [&](int f) -> bf16x8 {
            if (f < 20) return B8(wa[f]);
            if (f < 44) return *(const LAS bf16x8*)(wl + L_WO + (((f - 20) % 12) / 3) * 24576 + ((f - 20) / 12) * 3072 + ((f - 20) % 3) * 1024);
            if (f < 46) return *(const LAS bf16x8*)(L + 1024 + j * 3072 + (f - 44) * 1024 + lane * 16);
            return B8(wd[f - 46]);
        };
        LAS char* wp = (LAS char*)lds + (lane & 15) * 320 + ((g ^ ((lane >> 1) & 3)) << 4);
        f32x4 acc[4][5] = {};
        {
            bf16x8 w4[5];
#pragma unroll
            for (int ni = 0; ni < 5; ++ni) w4[ni] = wfr(20 + ni);
#pragma unroll
            for (int hf = 0; hf < 2; ++hf) {
                if (hf) asm volatile("s_waitcnt vmcnt(3)" : "+v"(aa[1][0]), "+v"(aa[1][1]), "+v"(aa[1][2]), "+v"(aa[1][3]), "+v"(aa[1][4]), "+v"(aa[1][5]), "+v"(aa[1][6]),
                                     "+v"(aa[1][7]), "+v"(aa[1][8]), "+v"(aa[1][9]), "+v"(wd[0]), "+v"(wd[1]), "+v"(wd[2]), "+v"(wd[3]) :: "memory");
#pragma unroll
                for (int k = 0; k < 5; ++k)
#pragma unroll
                    for (int mi = 0; mi < 2; ++mi)
#pragma unroll
                        for (int ni = 0; ni < 5; ++ni) acc[hf * 2 + mi][ni] = MFMA16(k < 4 ? wfr(k * 5 + ni) : w4[ni], B8(aa[hf][2 * k + mi]), acc[hf * 2 + mi][ni]);
                __builtin_amdgcn_sched_barrier(0);
            }
        }
        const int flB = (int)OFF_FL + (32 + rb * 8 + j) * 128;
        u32x4 b0[10], b1[10];
        wait_flag(rs, flv, flB, get_flag(rs, flv, flB));
        WSTAMP(4);
        LD_H(b0, ov, 0, 1, "=&v")
        fl = a_ld1(wsu, flB + 512 + flv);
        wait_cnt(lds, 0, 64);
        asm volatile("s_waitcnt vmcnt(0)" : "+v"(fl) :: "memory");
        wait_flag(rs, flv, flB + 512, fl);
        WSTAMP(5);
        LD_H(b1, ov, 1, 1, "=&v")
#pragma clang loop unroll(disable)
        for (int spin = 0; spin < (1 << 20); ++spin) {
            unsigned m = 0u;
#pragma unroll
            for (int i = 0; i < 10; ++i) { landed(b0[i]); m = umax4(m, b0[i]); }
            if (__builtin_expect(__all(m != EMPTY), 1)) break;
            LD_H(b0, ov, 0, 1, "+v")
            asm volatile("s_waitcnt vmcnt(0)" ::: "memory");
        }
        bf16x8 wb[25];
#pragma unroll
        for (int k = 0; k < 5; ++k) {
#pragma unroll
            for (int ni = 0; ni < 5; ++ni) wb[k * 5 + ni] = wfr(25 + k * 5 + ni);
#pragma unroll
            for (int mi = 0; mi < 2; ++mi)
#pragma unroll
                for (int ni = 0; ni < 5; ++ni) acc[mi][ni] = MFMA16(wb[k * 5 + ni], B8(b0[2 * k + mi]), acc[mi][ni]);
        }
#pragma unroll
        for (int mi = 0; mi < 2; ++mi)
#pragma unroll
            for (int ni = 0; ni < 5; ++ni) *(LAS f32x4*)(wp + L_P0 + j * 10240 + mi * 5120 + ni * 64) = acc[mi][ni];
        add_cnt<12>(lds0);
        f32x4 ac1[2][5];
#pragma clang loop unroll(disable)
        for (int spin = 0; spin < (1 << 20); ++spin) {
            unsigned m = 0u;
#pragma unroll
            for (int k = 0; k < 5; ++k) {
                asm volatile("s_waitcnt vmcnt(%2)" : "+v"(b1[2 * k]), "+v"(b1[2 * k + 1]) : "n"(8 - 2 * k) : "memory");
#pragma unroll
                for (int mi = 0; mi < 2; ++mi)
#pragma unroll
                    for (int ni = 0; ni < 5; ++ni) ac1[mi][ni] = MFMA16(wb[k * 5 + ni], B8(b1[2 * k + mi]), k ? ac1[mi][ni] : acc[2 + mi][ni]);
                m = umax4(umax4(m, b1[2 * k]), b1[2 * k + 1]);
                __builtin_amdgcn_sched_barrier(0);
            }
            if (__builtin_expect(__all(m != EMPTY), 1)) break;
            LD_H(b1, ov, 1, 1, "+v")
        }
#pragma unroll
        for (int mi = 0; mi < 2; ++mi)
#pragma unroll
            for (int ni = 0; ni < 5; ++ni) *(LAS f32x4*)(wp + L_WO + j * 6144 + mi * 24576 + ni * 64) = ac1[mi][ni];
        WSTAMP(6);
        asm volatile("s_waitcnt lgkmcnt(0)\n\ts_barrier" ::: "memory");
    } else {
        const int t = tid - 256;
        const LAS char* rp[2][3]; unsigned oo[3];
#pragma unroll
        for (int i = 0; i < 3; ++i) {
            const int id = i < 2 || t < 128 ? i * 256 + t : 1, row = id / 20, cs = id - row * 20, cc = cs ^ ((row >> 1) & 3);
            rp[0][i] = L + L_P0 + id * 16; rp[1][i] = L + L_WO + id * 16 + (id >= 320 ? 24576 - 5120 : 0);
            oo[i] = (unsigned)(((rb * 64 + row) * D_ + cb * 80 + cc * 4) * 2);
            landed(rp[0][i]); landed(rp[1][i]); landed(oo[i]);
        }
        WSTAMP(4);
#pragma unroll
        for (int hf = 0; hf < 2; ++hf) {
            if (hf == 0) { wait_cnt(lds, 12, 4); WSTAMP(5); }
            else { WSTAMP(6); asm volatile("s_barrier" ::: "memory"); }
            f32x4 pv[3][4];
#pragma unroll
            for (int i = 0; i < 3; ++i)
#pragma unroll
                for (int w = 0; w < 4; ++w) pv[i][w] = *(const LAS f32x4*)(rp[hf][i] + w * (hf ? 6144 : 10240));
            __builtin_amdgcn_sched_barrier(0);
#pragma unroll
            for (int i = 0; i < 3; ++i) {
                const f32x4 v = (pv[i][0] + pv[i][1]) + (pv[i][2] + pv[i][3]);
                if (i < 2 || t < 128) __builtin_amdgcn_raw_buffer_store_b64(u32x2{pk2(v[0], v[1]), pk2(v[2], v[3])},
                    __builtin_amdgcn_make_buffer_rsrc((void*)out, 0, S_ * D_ * 2, 0x00020000), oo[i], hf * (32 * D_ * 2), OUT_AUX);
            }
        }
    }
}

extern "C" void kernel_launch(void* const* d_in, const int* in_sizes, int n_in, void* d_out, int out_size, void* d_ws, size_t ws_size,
                              hipStream_t stream) {
    const u16* x = (const u16*)d_in[0];
    const u16* wA = (const u16*)d_in[1];
    const u16* woA = (const u16*)d_in[2];
    const u16* wB = (const u16*)d_in[4];
    const u16* woB = (const u16*)d_in[5];
    const u16* gw = (const u16*)d_in[6];
    char* ws = (char*)d_ws;
    (void)hipFuncSetAttribute((const void*)k1_qkv, hipFuncAttributeMaxDynamicSharedMemorySize, LDS_BYTES);
    (void)hipFuncSetAttribute((const void*)k2_attn, hipFuncAttributeMaxDynamicSharedMemorySize, LDS_BYTES);
    k1_qkv<<<256, 512, LDS_BYTES, stream>>>(x, wA, wB, woA, woB, gw, ws);
    k2_attn<<<256, 512, LDS_BYTES, stream>>>(ws, (u16*)d_out);
}
```

```cpp
#define PROBE 0
#define OUT_AUX 16
#include <hip/hip_runtime.h>
#include <hip/hip_bf16.h>
#include <stdint.h>

typedef short    bf16x8 __attribute__((ext_vector_type(8)));
typedef short    s16x4  __attribute__((ext_vector_type(4)));
typedef float    f32x2  __attribute__((ext_vector_type(2)));
typedef float    f32x4  __attribute__((ext_vector_type(4)));
typedef float    f32x16 __attribute__((ext_vector_type(16)));
typedef float    f32x32 __attribute__((ext_vector_type(32)));
typedef unsigned u32x2  __attribute__((ext_vector_type(2)));
typedef unsigned u32x4  __attribute__((ext_vector_type(4)));
typedef unsigned u32x8  __attribute__((ext_vector_type(8)));
typedef unsigned u32x32 __attribute__((ext_vector_type(32)));
typedef __bf16   bf2_t  __attribute__((ext_vector_type(2)));
typedef unsigned short u16;
#define LAS __attribute__((address_space(3)))
#define DI __device__ __forceinline__

constexpr int S_ = 2048, D_ = 640;
constexpr size_t IMG     = (size_t)32 * 163840;
constexpr size_t OFF_IQ  = 0;
constexpr size_t OFF_IB  = IMG;
constexpr size_t OFF_IA  = 2 * IMG;
constexpr size_t OFF_O   = 3 * IMG;
constexpr size_t OFF_WOT = 4 * IMG;
constexpr size_t OFF_G   = OFF_WOT + (size_t)1600 * 1024;
constexpr size_t OFF_KVP = OFF_G + 8192;
constexpr size_t OFF_FL  = OFF_KVP + (size_t)256 * 2048;
constexpr int    WS_USED = (int)(OFF_FL + 288 * 128);
constexpr unsigned EMPTY = 0xFFFFFFFFu;
typedef __amdgpu_buffer_rsrc_t rsrc_t;
constexpr int SC1 = 16;

#define CSTAMP(i)
#define PSTAMP(k, i)
#define WSTAMP(i)

DI unsigned pk2(float lo, float hi) { f32x2 v = {lo, hi}; return __builtin_bit_cast(unsigned, __builtin_convertvector(v, bf2_t)); }
DI float bflo(unsigned v) { return __uint_as_float(v << 16); }
DI float bfhi(unsigned v) { return __uint_as_float(v & 0xffff0000u); }
DI int rfl(int x) { return __builtin_amdgcn_readfirstlane(x); }
DI void glds16(const void* sbase, unsigned voff, unsigned lds_base) {
    const unsigned long long a = (unsigned long long)(uintptr_t)sbase;
    const unsigned long long u = (unsigned long long)(unsigned)rfl((int)(unsigned)a) | ((unsigned long long)(unsigned)rfl((int)(unsigned)(a >> 32)) << 32);
    asm volatile("s_mov_b32 m0, %0\n\ts_nop 0\n\tglobal_load_lds_dwordx4 %1, %2" :: "s"(rfl((int)lds_base)), "v"(voff), "s"(u) : "memory");
}
#define MFMA16(a, b, c) __builtin_amdgcn_mfma_f32_16x16x32_bf16((a), (b), (c), 0, 0, 0)
#define MFMA32(a, b, c) __builtin_amdgcn_mfma_f32_32x32x16_bf16((a), (b), (c), 0, 0, 0)
#define B8(x) __builtin_bit_cast(bf16x8, x)
DI u32x2 trd(const LAS char* p) { return __builtin_bit_cast(u32x2, __builtin_amdgcn_ds_read_tr16_b64_v4i16((LAS s16x4*)p)); }
DI float pmax(float t, float c) { return __int_as_float(max(__float_as_int(t), __float_as_int(c))); }
template <int N> DI void wait_vm_bar() { asm volatile("s_waitcnt vmcnt(%0) lgkmcnt(0)\n\ts_barrier" :: "n"(N) : "memory"); }

constexpr int LDS_BYTES = 163840;
constexpr float LOG2E = 1.4426950408889634f;

DI void p1_aux(const u16* __restrict__ x, const u16* __restrict__ woA, const u16* __restrict__ woB, const u16* __restrict__ gate_w,
               char* __restrict__ ws, char* lds, int a) {
    const int tid = threadIdx.x, lane = tid & 63, wave = rfl(tid >> 6);
    const unsigned lds0 = (unsigned)(uintptr_t)lds;
    const int br = a >> 3, u = a & 7;
    if (tid < 144) *(u32x4*)(ws + OFF_FL + (a * 144 + tid) * 16) = u32x4{0u, 0u, 0u, 0u};
    const char* wsrc = (const char*)(br ? woB : woA) + (size_t)u * 102400;
    for (int i = wave; i < 100; i += 8) glds16(wsrc + i * 1024, lane * 16, lds0 + i * 1024);
    const u32x4* xs = (const u32x4*)(x + (size_t)(a * 128 + wave * 16) * D_);
    u32x4 xv[20], gw[5];
#pragma unroll
    for (int i = 0; i < 20; ++i) xv[i] = xs[i * 64 + lane];
#pragma unroll
    for (int i = 0; i < 5; ++i) gw[i] = ((const u32x4*)gate_w)[(i * 64 + lane) % 80];
    {
        const rsrc_t rs = __builtin_amdgcn_make_buffer_rsrc((void*)(ws + OFF_KVP + a * 32768), 0, 32768, 0x00020000);
#pragma unroll
        for (int i = 0; i < 4; ++i) __builtin_amdgcn_raw_buffer_store_b128(u32x4{EMPTY, EMPTY, EMPTY, EMPTY}, rs, tid * 16 + i * 8192, 0, SC1);
    }
    LAS float* part = (LAS float*)(lds + 102400) + wave * 1280;
#pragma unroll
    for (int i = 0; i < 20; ++i) {
        float s = 0.f;
#pragma unroll
        for (int j = 0; j < 4; ++j) s += bflo(xv[i][j]) * bflo(gw[i % 5][j]) + bfhi(xv[i][j]) * bfhi(gw[i % 5][j]);
        part[i * 64 + lane] = s;
    }
    wait_vm_bar<0>();
    {
        float s = 0.f;
#pragma unroll
        for (int j = 0; j < 20; ++j) s += part[lane * 20 + j];
        s += __shfl_xor(s, 1); s += __shfl_xor(s, 2);
        if ((lane & 3) == 0) ((float*)(ws + OFF_G))[a * 128 + wave * 16 + (lane >> 2)] = 1.f / (1.f + __expf(-s));
    }
    const int g = lane >> 4, hh = g & 1, ns = g >> 1, q = (lane >> 2) & 3, p = lane & 3;
    for (int un = wave; un < 100; un += 8) {
        const int e = un / 20, np = un - e * 20, nt = np * 2 + ns;
        const int r0 = e < 4 ? e * 20 + 4 * hh : (2 * hh) * 20 + 16;
        const int r1 = e < 4 ? r0 + 8 : r0 + 20;
        const int ks16 = e < 4 ? (u & 1) * 4 + e : 8 + (u & 1), cbk = nt / 5;
        const u32x2 lo = trd((const LAS char*)lds + (r0 + q) * 1280 + nt * 32 + p * 8);
        const u32x2 hi = trd((const LAS char*)lds + (r1 + q) * 1280 + nt * 32 + p * 8);
        *(u32x4*)(ws + OFF_WOT + (size_t)((cbk * 8 + (u & 6) + br) * 25 + (ks16 >> 1) * 5 + nt - cbk * 5) * 1024 + ((ks16 & 1) * 2 + hh) * 256 + (lane & 15) * 16) = u32x4{lo[0], lo[1], hi[0], hi[1]};
    }
}

#define G1_TL 3
#define G1_D 3
#define G1_D0 2
#define G1_DT 10
#define G1_DE 0
#define G1_NX 4
constexpr int G1_NW = 10 - G1_NX, G1_HB = 20 - 2 * G1_TL, G1_WOFF = G1_NX * 16384;
constexpr int g1_t(int h) { return h < 2 * G1_HB ? h >> 1 : h < 2 * G1_HB + 2 * G1_TL ? h - G1_HB : h - G1_HB - 2 * G1_TL; }
constexpr int g1_m(int h) { return h < 2 * G1_HB ? h & 1 : h < 2 * G1_HB + 2 * G1_TL ? 0 : 1; }
constexpr int g1_xi(int h) { return h < 2 * G1_HB ? (h >> 1) & 1 : h & 1; }
constexpr int g1_free(int i) { return i < 10 ? (i < G1_NX ? -1 : 2 * (i - G1_NX) + 2) : (i - 10 < G1_NW ? -1 : i - 10 - G1_NW + 1); }
struct G1Plan { int seq[30]; int cnt[22]; int pos[30]; };
constexpr G1Plan g1_plan() {
    G1Plan p = {}; bool done[30] = {}, seen[10] = {}; int ord[30] = {}, need[21] = {}, n = 0;
    for (int b = 0; b < 20; ++b) { const int u = g1_t(2 * b) >> 1; if (!seen[u]) { seen[u] = true; ord[n++] = u; } need[b] = n; ord[n++] = 10 + b; }
    need[20] = 30; n = 0;
    for (int b = 0; b <= 20; ++b) {
        const int ex = b > G1_DT ? b - G1_DT : 0, lim = 3 * (b >> 1) + 1 + (b & 1) + (b == 0 ? G1_D0 : G1_D) + (ex < G1_DE ? ex : G1_DE);
        for (int k = 0; k < 30 && k <= lim; ++k) { const int i = ord[k]; if (!done[i] && g1_free(i) < b) { done[i] = true; p.pos[i] = n; p.seq[n++] = i; } }
        p.cnt[b] = n;
    }
    return p;
}
constexpr G1Plan G1P = g1_plan();
constexpr int g1_wait(int b) { const int x = G1P.pos[g1_t(2 * b) >> 1], w = G1P.pos[10 + b]; return 4 * (G1P.cnt[b] - 1 - (x > w ? x : w)); }
constexpr bool g1_ok() { for (int b = 0; b < 20; ++b) if (g1_wait(b) < 0 || g1_wait(b) > 60) return false; return G1P.cnt[19] == 30 && G1_NX >= G1_TL && G1_NX <= 4; }
static_assert(g1_ok(), "loader plan");
#define G1_ROT ((0x27057250 >> (4 * xcd)) & 15)

#define LST_P
#define LST_A
template <int T> struct G1Load {
    template <class FX, class FW> static DI void run(FX&& ldx, FW&& ldw LST_P) {
        asm volatile("s_waitcnt vmcnt(%0)\n\ts_barrier" :: "n"(g1_wait(T)) : "memory");
#pragma unroll
        for (int n = G1P.cnt[T]; n < G1P.cnt[T + 1]; ++n) { const int i = G1P.seq[n]; if (i < 10) ldx(i); else ldw(i - 10); }
        G1Load<T + 1>::run(ldx, ldw LST_A);
    }
};
template <> struct G1Load<20> { template <class FX, class FW> static DI void run(FX&&, FW&& LST_P) {} };

struct G1Ctx {
    f32x4 acc[4][8];
    bf16x8 x[2][4]; u32x2 w[2][4][2];
    const LAS char* xa[2]; const LAS char* wa[8];
    int so[4];
    rsrc_t rB; float sc; f32x2 m[4]; u32x4 o;
};
#define SB0() __builtin_amdgcn_sched_barrier(0)
#define G1_SP 12
template <int J, int I> DI void g1_side(G1Ctx& c) {
    constexpr int K = (J - (40 - 2 * G1_TL)) * 16 + I;
    if (K > 0 && K <= 8 * G1_SP && (K - 1) % G1_SP == 8) asm volatile("" :: "v"(c.o));
    if (K >= 0 && K < 8 * G1_SP && K % G1_SP < 9) {
        constexpr int U = K < 0 ? 0 : K / G1_SP, e = K < 0 ? 0 : K % G1_SP, ni = (U >> 1) & 3, a = U & 1;
        constexpr int mi = e == 0 ? 0 : e == 1 ? 1 : e == 3 ? 2 : e == 5 ? 3 : -1, ci = e == 2 ? 0 : e == 4 ? 1 : e == 6 ? 2 : e == 7 ? 3 : -1;
        if (mi >= 0) { const f32x4 v = c.acc[2 * a + ((mi & 3) >> 1)][ni]; c.m[mi & 3] = f32x2{v[(mi & 1) * 2], v[(mi & 1) * 2 + 1]} * c.sc; }
        if (ci >= 0) c.o[ci & 3] = pk2(c.m[ci & 3][0], c.m[ci & 3][1]);
        if (e == 8) __builtin_amdgcn_raw_buffer_store_b128(c.o, c.rB, c.so[ni] + a * 2560, 0, SC1);
        SB0();
    }
}
template <int J> struct G1Step {
    static DI void run(G1Ctx& c) {
        constexpr int N = J + 1, t = g1_t(N), xo = ((t >> 1) % G1_NX) * 16384, sl = (N >> 1) % G1_NW, wo = (sl & 3) * 16384 + (N & 1) * 8192, wi = (sl >> 2) * 4;
        constexpr int ps = J & 1, ma = g1_m(J) * 4, xc = g1_xi(J), xn = g1_xi(N);
#define G1_M(i) c.acc[(i) & 3][ma + ((i) >> 2)] = MFMA16(c.x[xc][(i) & 3], B8(((u32x4){c.w[ps][(i) >> 2][0][0], c.w[ps][(i) >> 2][0][1], c.w[ps][(i) >> 2][1][0], c.w[ps][(i) >> 2][1][1]})), \
                c.acc[(i) & 3][ma + ((i) >> 2)]); SB0(); g1_side<J, i>(c);
#define G1_X(i) if (N < 40) c.x[xn][i] = *(const LAS bf16x8*)(c.xa[t & 1] + xo + ((i) >> 1) * 4096 + ((i) & 1) * 1024); SB0();
#define G1_W(i, e) if (N < 40) c.w[ps ^ 1][i][e] = trd(c.wa[wi + (i)] + wo + (e) * 1024); SB0();
        if (ps) {
            G1_M(0) G1_M(1) G1_M(2)
            if (N < 40) asm volatile("s_waitcnt lgkmcnt(0)\n\ts_barrier" ::: "memory");
            SB0();
            G1_X(0) G1_M(3) G1_W(0, 0) G1_M(4) G1_W(0, 1) G1_M(5) G1_X(1) G1_M(6) G1_X(2) G1_M(7) G1_X(3) G1_M(8) G1_W(1, 0) G1_M(9) G1_W(1, 1) G1_M(10)
            G1_W(2, 0) G1_M(11) G1_W(2, 1) G1_M(12) G1_W(3, 0) G1_M(13) G1_W(3, 1) G1_M(14) G1_M(15)
        } else if (xn != xc) {
            G1_M(0) G1_X(0) G1_M(1) G1_W(0, 0) G1_M(2) G1_W(0, 1) G1_M(3) G1_X(1) G1_M(4) G1_X(2) G1_M(5) G1_X(3) G1_M(6) G1_W(1, 0) G1_M(7) G1_W(1, 1)
            G1_M(8) G1_W(2, 0) G1_M(9) G1_W(2, 1) G1_M(10) G1_W(3, 0) G1_M(11) G1_W(3, 1) G1_M(12) G1_M(13) G1_M(14) G1_M(15)
        } else {
            G1_M(0) G1_W(0, 0) G1_M(1) G1_W(0, 1) G1_M(2) G1_W(1, 0) G1_M(3) G1_W(1, 1) G1_M(4) G1_W(2, 0) G1_M(5) G1_W(2, 1) G1_M(6) G1_W(3, 0) G1_M(7) G1_W(3, 1)
            G1_M(8) G1_M(9) G1_M(10) G1_M(11) G1_M(12) G1_M(13) G1_M(14) G1_M(15)
        }
        G1Step<J + 1>::run(c);
    }
};
template <> struct G1Step<40> { static DI void run(G1Ctx&) {} };
template <int K> struct G1Rest { static DI void run(G1Ctx& c) { g1_side<40 - 2 * G1_TL + K / 16, K % 16>(c); G1Rest<K + 1>::run(c); } };
template <> struct G1Rest<8 * G1_SP> { static DI void run(G1Ctx&) {} };

DI void phase1(const u16* __restrict__ x, const u16* __restrict__ wA, const u16* __restrict__ wB, char* __restrict__ ws, char* lds, int bid) {
    const int tid = threadIdx.x, lane = tid & 63, wave = rfl(tid >> 6);
    const unsigned lds0 = (unsigned)(uintptr_t)lds;
    const int xcd = bid & 7, j = (xcd >> 1) * 30 + (bid >> 3);
    const int ct = j >> 3, mt = (xcd & 1) * 8 + (j & 7);
    const int type = rfl(ct / 5), rot = rfl(G1_ROT);
    if (__builtin_expect(wave >= 4, 1)) {
        const int lw = wave - 4;
        unsigned xo[4], wo[2];
#pragma unroll
        for (int i = 0; i < 4; ++i) { const int row = (lw * 4 + i) * 8 + (lane >> 3); xo[i] = (unsigned)((mt * 128 + row) * 1280 + (((lane & 7) ^ ((row >> 1) & 3) ^ ((row >> 2) & 4)) << 4)); }
#pragma unroll
        for (int i = 0; i < 2; ++i) { const int k = (lw * 2 + i) * 4 + (lane >> 4), s16 = lane & 15;
            wo[i] = (unsigned)(k * 3840 + ct * 256 + ((s16 ^ ((k & 3) << 2) ^ ((k >> 2) & 2)) << 4)); }
        auto ldx = [&](int u) {
            int kk = u + rot; kk -= kk >= 10 ? 10 : 0;
            const unsigned slot = lds0 + (u % G1_NX) * 16384 + lw * 4096;
#pragma unroll
            for (int i = 0; i < 4; ++i) glds16((const char*)x + kk * 128, xo[i], slot + i * 1024);
        };
        auto ldw = [&](int s) {
            int kk = (g1_t(2 * s) >> 1) + rot; kk -= kk >= 10 ? 10 : 0;
            const unsigned slot = lds0 + G1_WOFF + (s % G1_NW) * 16384 + lw * 2048;
#pragma unroll
            for (int e = 0; e < 2; ++e) {
                const char* src = (const char*)(g1_m(2 * s + e) ? wA : wB) + (kk * 64 + (g1_t(2 * s + e) & 1) * 32) * 3840;
                glds16(src, wo[0], slot + e * 8192); glds16(src, wo[1], slot + e * 8192 + 1024);
            }
        };
#pragma unroll
        for (int n = 0; n < G1P.cnt[0]; ++n) { const int i = G1P.seq[n]; if (i < 10) ldx(i); else ldw(i - 10); }
        G1Load<0>::run(ldx, ldw LST_A);
        return;
    }
    const int wm = wave & 1, wn = wave >> 1;
    const int i16 = lane & 15, g = lane >> 4, q = (lane >> 2) & 3, p = lane & 3;
    G1Ctx c;
    const int xr = (i16 >> 3) * 16 + (i16 & 7);
#pragma unroll
    for (int ks = 0; ks < 2; ++ks) { c.xa[ks] = (const LAS char*)lds + (wm * 64 + xr) * 128 + (((ks * 4 + g) ^ ((xr >> 1) & 3) ^ ((xr >> 2) & 4)) << 4); asm volatile("" : "+v"(c.xa[ks])); }
#pragma unroll
    for (int i = 0; i < 8; ++i) { c.wa[i] = (const LAS char*)lds + G1_WOFF + (i >> 2) * 65536 + (8 * g + q) * 256 + (((wn * 4 + (i & 3)) ^ (q << 1) ^ (g & 1)) << 5) + p * 8;
        asm volatile("" : "+v"(c.wa[i])); }
#pragma unroll
    for (int i = 0; i < 4; ++i) { const int cc = (ct - type * 5) * 128 + wn * 64 + i * 16 + i16, h = cc / 20; c.so[i] = h * 163840 + g * 320 + (cc - h * 20) * 16; asm volatile("" : "+v"(c.so[i])); }
    constexpr float QS = 0.22360679774997896f * LOG2E;
    int sci = type == 0 ? __builtin_bit_cast(int, QS) : 0x3F800000;
    asm volatile("" : "+s"(sci));
    c.sc = __int_as_float(sci);
    const int tb = (mt * 4 + wm * 2) * 2560;
    const int sB = tb + (type == 0 ? (int)OFF_IQ : (int)OFF_IB + (type - 1) * 1280), sA = tb + (type == 0 ? (int)OFF_IQ + 1280 : (int)OFF_IA + (type - 1) * 1280);
    c.rB = __builtin_amdgcn_make_buffer_rsrc((void*)(ws + sB), 0, (int)IMG, 0x00020000);
    const rsrc_t rA = __builtin_amdgcn_make_buffer_rsrc((void*)(ws + sA), 0, (int)IMG, 0x00020000);
    {
        const float a0 = type < 2 ? 1e-6f : 0.f;
        f32x4 a4 = {a0, a0, a0, a0};
        asm volatile("" : "+v"(a4));
#pragma unroll
        for (int i = 0; i < 16; ++i) { c.acc[i & 3][i >> 2] = f32x4{0.f, 0.f, 0.f, 0.f}; c.acc[i & 3][4 + (i >> 2)] = a4; }
    }
    __builtin_amdgcn_s_setprio(2);
    asm volatile("s_barrier" ::: "memory");
#pragma unroll
    for (int i = 0; i < 4; ++i) {
        c.x[0][i] = *(const LAS bf16x8*)(c.xa[0] + (i >> 1) * 4096 + (i & 1) * 1024);
        if (i == 0) { c.w[0][0][0] = trd(c.wa[0]); c.w[0][0][1] = trd(c.wa[0] + 1024); }
    }
#pragma unroll
    for (int i = 1; i < 4; ++i) { c.w[0][i][0] = trd(c.wa[i]); c.w[0][i][1] = trd(c.wa[i] + 1024); }
    SB0();
    G1Step<0>::run(c);
    PSTAMP(0, 2);
    G1Rest<(32 * G1_TL < 8 * G1_SP ? 32 * G1_TL : 8 * G1_SP)>::run(c);
    if (type < 2) {
#pragma unroll
        for (int i = 0; i < 4; ++i)
#pragma unroll
            for (int a = 0; a < 2; ++a) {
                u32x4 o;
#pragma unroll
                for (int e = 0; e < 4; ++e) {
                    const f32x2 t = {c.acc[2 * a + (e >> 1)][4 + i][(e & 1) * 2], c.acc[2 * a + (e >> 1)][4 + i][(e & 1) * 2 + 1]};
                    const f32x2 m = __builtin_elementwise_fma(t, f32x2{LOG2E, LOG2E}, f32x2{-1e-6f * LOG2E, -1e-6f * LOG2E});
                    const f32x2 ex = {__builtin_amdgcn_fmed3f(__builtin_amdgcn_exp2f(m[0]), 0.f, 1.f), __builtin_amdgcn_fmed3f(__builtin_amdgcn_exp2f(m[1]), 0.f, 1.f)};
                    const f32x2 r = f32x2{pmax(t[0], 1e-6f), pmax(t[1], 1e-6f)} + ex;
                    o[e] = pk2(r[0], r[1]);
                }
                __builtin_amdgcn_raw_buffer_store_b128(o, rA, c.so[i] + a * 2560, 0, SC1);
            }
    } else {
#pragma unroll
        for (int i = 0; i < 4; ++i)
#pragma unroll
            for (int a = 0; a < 2; ++a) {
                const f32x4 u = c.acc[2 * a][4 + i], v = c.acc[2 * a + 1][4 + i];
                __builtin_amdgcn_raw_buffer_store_b128(u32x4{pk2(u[0], u[1]), pk2(u[2], u[3]), pk2(v[0], v[1]), pk2(v[2], v[3])}, rA, c.so[i] + a * 2560, 0, SC1);
            }
    }
}

__global__ __launch_bounds__(512, 2) void k1_qkv(const u16* __restrict__ x, const u16* __restrict__ wA, const u16* __restrict__ wB,
                                                 const u16* __restrict__ woA, const u16* __restrict__ woB, const u16* __restrict__ gate_w,
                                                 char* __restrict__ ws) {
    extern __shared__ __attribute__((aligned(16))) char lds[];
    int bid = blockIdx.x;
    asm volatile("" : "+s"(bid));
    PSTAMP(0, 0);
    const unsigned pt0 = (unsigned)__builtin_amdgcn_s_memrealtime();
    if (__builtin_expect(bid < 240, 1)) phase1(x, wA, wB, ws, lds, bid);
    else p1_aux(x, woA, woB, gate_w, ws, lds, bid - 240);
    if (threadIdx.x == 0) { asm volatile("s_waitcnt vmcnt(0)"); unsigned* pp = (unsigned*)(ws + (64 << 20)) + bid * 2; pp[0] = pt0; pp[1] = (unsigned)__builtin_amdgcn_s_memrealtime(); }
}

#define STR(x) #x
#define XS(x) STR(x)
#define A_E(i) "v_exp_f32 v" XS(i) ", v" XS(i) "\n"
#define A_C(d, a, b) "v_cvt_pk_bf16_f32 v" XS(d) ", v" XS(a) ", v" XS(b) "\n"
#define A_G(a, b, c, d, p0, p1) A_E(a) A_E(b) A_E(c) A_E(d) A_C(p0, a, b) A_C(p1, c, d)
#define A_M(d, a, b, c) "v_mfma_f32_32x32x16_bf16 " d ", " a ", " b ", " c "\n"
#define A_STEP(C0,C1,C2,C3,C4,C5,C6,C7,C8,C9,C10,C11,C12,C13,C14,C15, SN, KM, KT, V1, V0, NKMa, NKMb, NKT, NV1, NV0, OK0, OK1, OV1, OV0, PRIO) \
    "s_waitcnt lgkmcnt(0)\n" \
    A_M(SN, KM, "v[120:123]", "0") \
    "ds_read_b64_tr_b16 " NKMa ", v128 offset:" XS(OK0) "\n" \
    "ds_read_b64_tr_b16 " NKMb ", v128 offset:" XS(OK1) "\n" \
    "ds_read_b64_tr_b16 " NKT ", v129 offset:" XS(OK0) "\n" \
    A_G(C0, C1, C2, C3, 112, 113) \
    A_M("v[64:79]", V1, "v[116:119]", "v[64:79]") \
    "s_mov_b64 exec, s[72:73]\n" \
    "ds_read_b128 " NV1 ", v130 offset:" XS(OV1) "\n" \
    "ds_read_b128 " NV0 ", v130 offset:" XS(OV0) "\n" \
    "s_mov_b64 exec, -1\n" \
    A_G(C4, C5, C6, C7, 114, 115) \
    A_M(SN, KT, "v[124:127]", SN) \
    A_G(C8, C9, C10, C11, 116, 117) \
    A_M("v[64:79]", V0, "v[112:115]", "v[64:79]") \
    "s_setprio " XS(PRIO) "\n" \
    A_G(C12, C13, C14, C15, 118, 119)
#define A_EVEN(OK0, OK1, OV1, OV0, PRIO) A_STEP(32,33,34,35,36,37,38,39,40,41,42,43,44,45,46,47, "v[48:63]", "v[80:83]", "v[84:87]", "v[88:91]", "v[92:95]", \
    "v[96:97]", "v[98:99]", "v[100:101]", "v[104:107]", "v[108:111]", OK0, OK1, OV1, OV0, PRIO)
#define A_ODD(OK0, OK1, OV1, OV0, PRIO) A_STEP(48,49,50,51,52,53,54,55,56,57,58,59,60,61,62,63, "v[32:47]", "v[96:99]", "v[100:103]", "v[104:107]", "v[108:111]", \
    "v[80:81]", "v[82:83]", "v[84:85]", "v[88:91]", "v[92:95]", OK0, OK1, OV1, OV0, PRIO)

#define A_LD(g, off) "global_load_lds_dwordx4 v131, " g " offset:" XS(off) "\n"
#define A_LDA(off) "s_cbranch_scc1 .Lattn_s" XS(off) "\n" A_LD("s[74:75]", off) ".Lattn_s" XS(off) ":\n"
#define A_BODY \
        "s_mov_b32 m0, s78\n s_cmp_eq_u32 s80, 2\n s_nop 3\n" \
        A_EVEN(5120, 5184, 640, 2560, 2)     A_ODD(7680, 7744, 3200, 5120, 1) \
        A_EVEN(10240, 10304, 5760, 7680, 0)  A_ODD(12800, 12864, 8320, 10240, 3) \
        "s_waitcnt vmcnt(0) lgkmcnt(0)\n s_mov_b64 exec, 1\n ds_add_u32 v127, v86\n s_mov_b64 exec, -1\n" \
        A_EVEN(15360, 15424, 10880, 12800, 2) A_LDA(0) A_ODD(17920, 17984, 13440, 15360, 1) A_LDA(1024) \
        A_EVEN(20480, 20544, 16000, 17920, 0) A_LDA(2048) \
        "s_cmp_eq_u32 s80, 1\n s_cbranch_scc0 .Lattn_skip\n s_mov_b32 m0, s79\n s_nop 0\n" \
        A_LD("s[76:77]", 0) A_LD("s[76:77]", 1024) A_LD("s[76:77]", 2048) ".Lattn_skip:\n" \
        "ds_read_b32 v132, v127\n" \
        A_ODD(23040, 23104, 18560, 20480, 3) \
        "v_add_u32 v128, 0x5000, v128\n v_add_u32 v129, 0x5000, v129\n v_add_u32 v130, 0x5000, v130\n" \
        "s_waitcnt lgkmcnt(0)\n s_nop 0\n"
#define A_OPERANDS \
        : "+{v[32:63]}"(S), "+{v[64:79]}"(O), "+{v[80:111]}"(OPS), "+{v[112:119]}"(P), "+{v[128:131]}"(AD), "={v132}"(cnt) \
        : "{v[120:127]}"(Q), "{s[72:73]}"(vmask), "{s[74:75]}"(ga), "{s[76:77]}"(gb), "{s78}"(ma), "{s79}"(mb), "{s80}"(enb) \
        : "memory", "scc", "s82", "s83"
template <bool FIRST>
DI unsigned attn_chunk(f32x32& S, f32x16& O, u32x32& OPS, u32x8& P, u32x4& AD, const u32x8& Q, unsigned long long vmask,
                   unsigned ma, unsigned long long ga, unsigned mb, unsigned long long gb, unsigned enb) {
    unsigned cnt;
    if (FIRST) asm volatile("s_call_b64 s[82:83], .Lattn_body\n s_branch .Lattn_done\n .p2align 6\n .Lattn_body:\n" A_BODY "s_setpc_b64 s[82:83]\n .Lattn_done:\n" A_OPERANDS);
    else       asm volatile("s_call_b64 s[82:83], .Lattn_body\n" A_OPERANDS);
    return cnt;
}
DI void wait_cnt(char* lds, int off, unsigned cnt, int n) {
    if (__builtin_expect((unsigned)rfl((int)cnt) < (unsigned)(n * 0x3F80), 0)) {
#pragma clang loop unroll(disable)
        do { __builtin_amdgcn_s_sleep(1); cnt = *(volatile LAS unsigned*)(lds + off); } while ((unsigned)rfl((int)cnt) < (unsigned)(n * 0x3F80));
    }
    asm volatile("" ::: "memory");
}

DI void wait_cnt(char* lds, int off, int n) { wait_cnt(lds, off, *(volatile LAS unsigned*)(lds + off), n); }
template <int OFF> DI void add_cnt(unsigned lds0) {
    asm volatile("s_waitcnt lgkmcnt(0)\n\ts_mov_b64 exec, 1\n\tds_add_u32 %0, %1 offset:%2\n\ts_mov_b64 exec, -1" :: "v"(lds0), "v"(0x3F80u), "n"(OFF) : "memory"); }

DI float xhalf_max(float m) { auto rr = __builtin_amdgcn_permlane32_swap(__float_as_uint(m), __float_as_uint(m), false, false);
    return fmaxf(__uint_as_float(rr[0]), __uint_as_float(rr[1])); }
DI float hi_half(float v) {
    auto rr = __builtin_amdgcn_permlane32_swap(__float_as_uint(v), __float_as_uint(v), false, false);
    return __uint_as_float(rr[1]); }
DI float sum16(float v) { auto rr = __builtin_amdgcn_permlane16_swap(__float_as_uint(v), __float_as_uint(v), false, false);
    return __uint_as_float(rr[0]) + __uint_as_float(rr[1]); }
DI float sum32(float v) { auto rr = __builtin_amdgcn_permlane32_swap(__float_as_uint(v), __float_as_uint(v), false, false);
    return __uint_as_float(rr[0]) + __uint_as_float(rr[1]); }
DI f32x4 sum_rows(f32x4 a, f32x4 b) {
    f32x4 s = a + b;
#pragma unroll
    for (int i = 0; i < 4; ++i) s[i] = sum32(sum16(s[i]));
    return s;
}
DI unsigned long long uni64(const void* p) { const unsigned long long a = (unsigned long long)(uintptr_t)p;
    return (unsigned long long)(unsigned)rfl((int)(unsigned)a) | ((unsigned long long)(unsigned)rfl((int)(unsigned)(a >> 32)) << 32); }
DI unsigned full(unsigned v) { return v < EMPTY ? v : EMPTY - 1u; }
DI unsigned umax4(unsigned m, u32x4 v) { return max(max(max(m, v[0]), max(v[1], v[2])), v[3]); }
DI void store_o(rsrc_t rs, const f32x16& o, float f, int rt, int br, int h, int lane) {
    const int rb = (int)OFF_O + (rt * 80 + ((br ? 0 : 4) + (h >> 3)) * 10) * 1024;
    __builtin_amdgcn_raw_buffer_store_b128(u32x4{full(pk2(o[0] * f, o[1] * f)), full(pk2(o[2] * f, o[3] * f)), full(pk2(o[4] * f, o[5] * f)), full(pk2(o[6] * f, o[7] * f))},
                                           rs, lane * 16, rb + (h & 7) * 1024, SC1);
    if (lane < 32) __builtin_amdgcn_raw_buffer_store_b64(u32x2{full(pk2(o[8] * f, o[9] * f)), full(pk2(o[10] * f, o[11] * f))},
                                           rs, lane * 16, rb + (8 + ((h >> 2) & 1)) * 1024 + ((h >> 1) & 1) * 512 + (h & 1) * 8, SC1);
}
DI void empty_o(rsrc_t rs, int rt, int h, int lane) {
    const int rb = (int)OFF_O + (rt * 80 + (h >> 3) * 10) * 1024;
    u32x4 e = {EMPTY, EMPTY, EMPTY, EMPTY};
    __builtin_amdgcn_raw_buffer_store_b128(e, rs, lane * 16, rb + (h & 7) * 1024, SC1);
    if (lane < 32) __builtin_amdgcn_raw_buffer_store_b64(u32x2{e[0], e[1]}, rs, lane * 16, rb + (8 + ((h >> 2) & 1)) * 1024 + ((h >> 1) & 1) * 512 + (h & 1) * 8, SC1);
    asm volatile("" :: "v"(e));
}
DI void set_flag(rsrc_t rs, int off) { __builtin_amdgcn_raw_buffer_store_b32(1u, rs, 0, off, SC1); }
DI unsigned get_flag(rsrc_t rs, int voff, int off) { return __builtin_amdgcn_raw_buffer_load_b32(rs, voff, off, SC1); }
DI unsigned a_ld1(unsigned long long sb, unsigned voff) { unsigned v; asm volatile("global_load_dword %0, %1, %2 sc1" : "=v"(v) : "v"(voff), "s"(sb) : "memory"); return v; }
DI u32x4 a_ld4s(unsigned long long sb, unsigned voff) { u32x4 v; asm volatile("global_load_dwordx4 %0, %1, %2 sc1" : "=v"(v) : "v"(voff), "s"(sb) : "memory"); return v; }
template <int OFF> DI u32x4 a_ld4so(unsigned long long sb, unsigned voff) { u32x4 v;
    asm volatile("global_load_dwordx4 %0, %1, %2 offset:%3 sc1" : "=v"(v) : "v"(voff), "s"(sb), "n"(OFF) : "memory"); return v; }
template <int OFF> DI u32x4 a_ld4(unsigned long long sb, unsigned voff) { u32x4 v;
    asm volatile("global_load_dwordx4 %0, %1, %2 offset:%3" : "=v"(v) : "v"(voff), "s"(sb), "n"(OFF) : "memory"); return v; }
template <class T> DI void landed(T& v) { asm volatile("" : "+v"(v)); }
DI void wait_flag(rsrc_t rs, int voff, int off, unsigned v) {
    if (__builtin_expect(!__all(v == 1u), 0)) {
#pragma clang loop unroll(disable)
        for (int spin = 0; spin < (1 << 22); ++spin) { if (__all(get_flag(rs, voff, off) == 1u)) break; __builtin_amdgcn_s_sleep(1); }
    }
    __builtin_amdgcn_fence(__ATOMIC_ACQUIRE, "wavefront");
    asm volatile("" ::: "memory");
}

#define FILL_C 1
constexpr int L_SP = 136192, L_K0 = 152320, L_SQ = 153600, L_QA = 1280, L_KVF = 11520, L_WO = 13312;

__global__ __launch_bounds__(512, 2) void k2_attn(char* __restrict__ ws, u16* __restrict__ out) {
    extern __shared__ __attribute__((aligned(16))) char lds[];
    int bid = blockIdx.x;
    asm volatile("" : "+s"(bid));
    PSTAMP(1, 0); WSTAMP(0);
    const unsigned pt0 = (unsigned)__builtin_amdgcn_s_memrealtime(), pc0 = (unsigned)__builtin_amdgcn_s_memtime();
    unsigned pt1 = 0, pt2 = 0;
    const int tid = threadIdx.x, lane = tid & 63, wave = rfl(tid >> 6);
    const int h = (bid & 7) * 4 + ((bid >> 3) & 3), qb = bid >> 5;
    const int oq = bid & 7, rb = oq * 4 + ((bid >> 3) & 3), cb = bid >> 5;
    const int r = lane & 31, hh = lane >> 5;
    const unsigned lds0 = (unsigned)(uintptr_t)lds;
    const rsrc_t rs = __builtin_amdgcn_make_buffer_rsrc((void*)ws, 0, WS_USED, 0x00020000);
    const char* kvb = ws + OFF_IB + (size_t)h * 163840;
    const char* qim = ws + OFF_IQ + (size_t)h * 163840 + qb * 20480;
    const int wr = (wave & 3) * 2 + (wave >> 2), rt = qb * 8 + wr;
    const int rc = r < 20 ? r : 19;
    glds16(qim + wr * 2560, lane * 16, lds0 + L_SQ + wave * 1280);
    glds16(qim + wr * 2560 + 256, lane * 16, lds0 + L_SQ + wave * 1280 + 256);
    glds16(kvb + (wave & 1) * 256, lane * 16, lds0 + L_K0 + (wave & 1) * 256);
#pragma unroll
    for (int i = 0; i < 3; ++i) glds16(kvb + (1 + i * 8 + wave) * 1024, lane * 16, lds0 + (1 + i * 8 + wave) * 1024);
    const char* ap = ws + OFF_IA + (size_t)h * 163840 + rt * 2560 + hh * 320 + rc * 16;
    u32x4 a0, a1, b0, b1; float gate;
    asm volatile("global_load_dwordx4 %0, %5, off\n\tglobal_load_dwordx4 %1, %5, off offset:640\n\tglobal_load_dwordx4 %2, %5, off offset:1280\n\t"
                 "global_load_dwordx4 %3, %5, off offset:1920\n\tglobal_load_dword %4, %6, off"
                 : "=&v"(a0), "=&v"(a1), "=&v"(b0), "=&v"(b1), "=&v"(gate) : "v"(ap), "v"((const float*)(ws + OFF_G) + rt * 32 + r) : "memory");
#pragma unroll
    for (int i = 0; i < 3; ++i) glds16(kvb + (25 + i * 8 + wave) * 1024, lane * 16, lds0 + (25 + i * 8 + wave) * 1024);
    if (tid < 4) *(LAS unsigned*)(lds + tid * 4) = 0u;
    const u32x4 ONES = {0x3F803F80u, 0x3F803F80u, 0x3F803F80u, 0x3F803F80u};
    const f32x16 Z16 = {};
    wait_vm_bar<8>();
    PSTAMP(1, 1); WSTAMP(1);
    pt1 = (unsigned)__builtin_amdgcn_s_memrealtime();
    const int g = lane >> 4, q = (lane >> 2) & 3, p = lane & 3;
    const int tro = (g & 1) * 640 + (p & 1) * 320 + q * 16 + (p >> 1) * 8;
    const LAS char* L = (const LAS char*)lds;
    const LAS char* kmp = L + tro + hh * 128;
    const LAS char* ktp = L + tro + 256;
    const LAS char* vp  = L + 1280 + hh * 320 + r * 16;
    const int ent = tid < 126 ? tid : 125;
    u32x8 Q;
    {
        const u32x2 a = trd(kmp + L_SQ + wave * 1280), b = trd(kmp + L_SQ + wave * 1280 + 64), c = trd(ktp + L_SQ + wave * 1280);
        Q[0] = a[0]; Q[1] = a[1]; Q[2] = b[0]; Q[3] = b[1]; Q[4] = hh ? 0u : c[0]; Q[5] = hh ? 0u : c[1]; Q[6] = 0u; Q[7] = 0u;
    }
    const bf16x8 qm = B8(((u32x4){Q[0], Q[1], Q[2], Q[3]}));
    f32x32 S; f32x16 O = {}; u32x32 OPS; u32x8 P = {}; u32x4 AD;
    {
        const u32x2 a = trd(kmp + L_K0), b = trd(kmp + L_K0 + 64), c = trd(ktp + L_K0);
        const bf16x8 k0 = B8(((u32x4){a[0], a[1], b[0], b[1]})), k1 = B8(((u32x4){c[0], c[1], 0x3F80u, 0u}));
        const f32x16 sm = MFMA32(k0, qm, Z16);
        f32x16 s0 = MFMA32(k1, B8(((u32x4){Q[4], Q[5], 0u, 0u})), sm);
        float m = s0[0];
#pragma unroll
        for (int i = 1; i < 16; ++i) m = fmaxf(m, s0[i]);
        m = xhalf_max(m);
        Q[6] = hh ? 0u : (pk2(-m, 0.f) & 0xffffu);
        s0 = MFMA32(k1, B8(((u32x4){Q[4], Q[5], Q[6], 0u})), sm);
#pragma unroll
        for (int i = 0; i < 16; ++i) { S[i] = s0[i]; S[16 + i] = 0.f; }
        const unsigned vc = r == 20 ? 0x3F803F80u : 0u;
#pragma unroll
        for (int i = 0; i < 32; ++i) OPS[i] = (i & 8) ? vc : 0u;
        const u32x2 a1 = trd(kmp + 2560), b1 = trd(kmp + 2560 + 64), c1 = trd(ktp + 2560);
        OPS[0] = a1[0]; OPS[1] = a1[1]; OPS[2] = b1[0]; OPS[3] = b1[1]; OPS[4] = c1[0]; OPS[5] = c1[1]; OPS[6] = 0x3F80u; OPS[22] = 0x3F80u;
        if (r < 20) { const u32x4 v0 = *(const LAS u32x4*)vp; OPS[12] = v0[0]; OPS[13] = v0[1]; OPS[14] = v0[2]; OPS[15] = v0[3]; }
        AD[0] = (unsigned)(uintptr_t)kmp; AD[1] = (unsigned)(uintptr_t)ktp; AD[2] = (unsigned)(uintptr_t)vp; AD[3] = lane * 16;
    }
    {
        asm volatile("s_waitcnt vmcnt(3)" : "+v"(a0), "+v"(a1), "+v"(b0), "+v"(b1), "+v"(gate) :: "memory");
        if (r >= 20) { a0 = u32x4{0u, 0u, 0u, 0u}; a1 = a0; b0 = ONES; b1 = ONES; }
        f32x16 kvacc = MFMA32(B8(a0), B8(b0), Z16); kvacc = MFMA32(B8(a1), B8(b1), kvacc);
        if (r <= 20) {
            LAS f32x4* dst = (LAS f32x4*)(lds + L_SP + ((wave * 2 + hh) * 21 + r) * 48);
            dst[0] = f32x4{kvacc[0], kvacc[1], kvacc[2], kvacc[3]};
            dst[1] = f32x4{kvacc[4], kvacc[5], kvacc[6], kvacc[7]};
            dst[2] = f32x4{kvacc[8], kvacc[9], kvacc[10], kvacc[11]};
        }
    }
    const unsigned long long vmask = 0x000FFFFF000FFFFFull;
    const int j = wave & 3, isE = (wave >> 2) ^ 1;
    const char* wop = ws + OFF_WOT + (size_t)((cb * 4 + j) * 50) * 1024;
    const int flA = (int)OFF_FL + (oq * 4 + j) * 128, flv = (lane & 7) * 16;
    const int ov = ((g >> 1) * 1024 + (g & 1) * 512 + (lane & 15) * 16), os = (int)OFF_O + (rb * 160 + j * 10) * 1024;
    unsigned fl = 0u;
    u32x4 kp[8], wa[20], aa[2][10];
    const unsigned long long wsu = uni64(ws);
    const unsigned long long kb = uni64(ws + OFF_KVP + 4096 + h * 16384), kb2 = uni64(ws + OFF_KVP + 12288 + h * 16384);
    const unsigned long long ob[2][2] = {{uni64(ws + os + 4096), uni64(ws + os + 8192)}, {uni64(ws + os + 81920 + 4096), uni64(ws + os + 81920 + 8192)}};
#define LD_H(A, vo, hf, en, C) asm volatile("s_cmp_lg_u32 %13, 0\n\ts_cselect_b64 exec, -1, 0\n\t" \
        "global_load_dwordx4 %0, %10, %11 offset:-4096 sc1\n\tglobal_load_dwordx4 %1, %10, %11 offset:-3840 sc1\n\t" \
        "global_load_dwordx4 %2, %10, %11 offset:-2048 sc1\n\tglobal_load_dwordx4 %3, %10, %11 offset:-1792 sc1\n\t" \
        "global_load_dwordx4 %4, %10, %11 sc1\n\tglobal_load_dwordx4 %5, %10, %11 offset:256 sc1\n\t" \
        "global_load_dwordx4 %6, %10, %11 offset:2048 sc1\n\tglobal_load_dwordx4 %7, %10, %11 offset:2304 sc1\n\t" \
        "global_load_dwordx4 %8, %10, %12 sc1\n\tglobal_load_dwordx4 %9, %10, %12 offset:256 sc1\n\ts_mov_b64 exec, -1" \
        : C(A[0]), C(A[1]), C(A[2]), C(A[3]), C(A[4]), C(A[5]), C(A[6]), C(A[7]), C(A[8]), C(A[9]) \
        : "v"(vo), "s"(ob[hf][0]), "s"(ob[hf][1]), "s"(en) : "memory", "scc");
#define LD_W4(A, f) asm volatile("s_cmp_lg_u32 %6, 0\n\ts_cselect_b64 exec, -1, 0\n\t" \
        "global_load_dwordx4 %0, %4, %5\n\tglobal_load_dwordx4 %1, %4, %5 offset:1024\n\t" \
        "global_load_dwordx4 %2, %4, %5 offset:2048\n\tglobal_load_dwordx4 %3, %4, %5 offset:3072\n\ts_mov_b64 exec, -1" \
        : "=&v"((A)[0]), "=&v"((A)[1]), "=&v"((A)[2]), "=&v"((A)[3]) : "v"(lane * 16), "s"(uni64(wop + (f) * 1024)), "s"(isE) : "memory", "scc");
#define LD_KP(TAIL) asm volatile("s_cmp_lt_u32 %11, 2\n\ts_cselect_b64 exec, -1, 0\n\t" \
        "global_load_dwordx4 %0, %8, %9 offset:-4096 sc1\n\tglobal_load_dwordx4 %1, %8, %9 offset:-2048 sc1\n\t" \
        "global_load_dwordx4 %2, %8, %9 sc1\n\tglobal_load_dwordx4 %3, %8, %9 offset:2048 sc1\n\t" \
        "global_load_dwordx4 %4, %8, %10 offset:-4096 sc1\n\tglobal_load_dwordx4 %5, %8, %10 offset:-2048 sc1\n\t" \
        "global_load_dwordx4 %6, %8, %10 sc1\n\tglobal_load_dwordx4 %7, %8, %10 offset:2048 sc1\n\t" \
        "s_mov_b64 exec, -1" TAIL \
        : "=&v"(kp[0]), "=&v"(kp[1]), "=&v"(kp[2]), "=&v"(kp[3]), "=&v"(kp[4]), "=&v"(kp[5]), "=&v"(kp[6]), "=&v"(kp[7]) \
        : "v"(ent * 16), "s"(kb), "s"(kb2), "s"(wave) : "memory", "scc")
#pragma unroll
    for (int c = 0; c < 8; ++c) {
        unsigned ma, mb; unsigned long long ga, gb;
        if (c < 3) {
            const int lim = (c == 1 ? L_SP : 163840) - 3072;
            const int oa = (c == 2 ? L_SP : 50176 + c * 49152) + wave * 3072, ob = oa + 24576 < lim ? oa + 24576 : lim;
            ma = lds0 + oa; mb = lds0 + ob; ga = uni64(kvb + oa); gb = uni64(kvb + ob);
        } else {
            ma = lds0 + (c == 7 ? 1024 + j * 3072 : L_WO + ((c - 3) * 8 + wr) * 3072); mb = ma;
            ga = uni64(wop + (c == 7 ? 44 : 20 + (wave >> 2) * 12 + 3 * (c - 3)) * 1024); gb = ga;
        }
        CSTAMP(2 * c);
        unsigned cnt;
        if (c == 0) cnt = attn_chunk<true>(S, O, OPS, P, AD, Q, vmask, rfl(ma), ga, rfl(mb), gb, 1u);
        else cnt = attn_chunk<false>(S, O, OPS, P, AD, Q, vmask, rfl(ma), ga, rfl(mb), gb, c < 3 ? 1u : c < 7 ? 0u : (unsigned)(2 - 2 * isE));
        CSTAMP(2 * c + 1);
        if (c < 7) wait_cnt(lds, 0, cnt, 8 * (c + 1));
        if (c == FILL_C && c != 1) empty_o(rs, rt, h, lane);
        if (c == 0) {
            if (wave < 2) {
                f32x4 s = *(const LAS f32x4*)(L + L_SP + ent * 16);
#pragma unroll
                for (int w = 1; w < 8; ++w) s += *(const LAS f32x4*)(L + L_SP + w * 2016 + ent * 16);
                const u32x4 v = __builtin_bit_cast(u32x4, s);
                if (tid < 126) __builtin_amdgcn_raw_buffer_store_b128(u32x4{full(v[0]), full(v[1]), full(v[2]), full(v[3])}, rs, tid * 16, (int)OFF_KVP + (h * 8 + qb) * 2048, SC1);
            }
            glds16(qim + wr * 2560 + 1280, lane * 16, lds0 + L_QA + wave * 1280);
            glds16(qim + wr * 2560 + 1280 + 256, lane * 16, lds0 + L_QA + wave * 1280 + 256);
        } else if (c == 1) {
            LD_KP("");
            if (FILL_C == 1) empty_o(rs, rt, h, lane);
        } else if (c == 2) {
#pragma unroll
            for (int j = 0; j < 8; ++j) landed(kp[j]);
            if (wave < 2) {
#pragma clang loop unroll(disable)
                for (int spin = 0; spin < (1 << 20); ++spin) {
                    unsigned m = 0u;
#pragma unroll
                    for (int j = 0; j < 8; ++j) m = umax4(m, kp[j]);
                    if (__builtin_expect(__all(m != EMPTY), 1)) break;
                    LD_KP("\n\ts_waitcnt vmcnt(0)");
                }
                f32x4 s = __builtin_bit_cast(f32x4, kp[0]);
#pragma unroll
                for (int j = 1; j < 8; ++j) s += __builtin_bit_cast(f32x4, kp[j]);
                const int e3 = ent / 3;
                *(LAS u32x2*)(lds + L_KVF + e3 * 32 + (ent - e3 * 3) * 8) = u32x2{pk2(s[0], s[1]), pk2(s[2], s[3])};
            }
        } else if (c == 3) {
            const int rr = r <= 20 ? r : 20;
            const u32x4 ka0 = *(const LAS u32x4*)(L + L_KVF + (hh * 21 + rr) * 32);
            const u32x2 ka1 = *(const LAS u32x2*)(L + L_KVF + (hh * 21 + rr) * 32 + 16);
            const LAS char* qa = L + L_QA + wave * 1280 + tro;
            const u32x2 pq0 = trd(qa + hh * 64), pq1 = trd(qa + 128 + hh * 64);
            u32x2 pq2 = trd(qa + 256);
            if (hh) pq2 = u32x2{0u, 0u};
            f32x16 oa = MFMA32(B8(ka0), B8(((u32x4){pq0[0], pq0[1], pq1[0], pq1[1]})), Z16);
            oa = MFMA32(B8(((u32x4){ka1[0], ka1[1], 0u, 0u})), B8(((u32x4){pq2[0], pq2[1], 0u, 0u})), oa);
            const float den = hi_half(oa[8]) + 1e-6f;
            store_o(rs, oa, gate * __builtin_amdgcn_rcpf(den), rt, 0, h, lane);
            LD_W4(wa, 0) LD_W4(wa + 4, 4)
        } else if (c == 4) {
            if (tid == 0) set_flag(rs, (int)OFF_FL + (qb * 4 + (h >> 3)) * 128 + (h & 7) * 16);
            LD_W4(wa + 8, 8) LD_W4(wa + 12, 12)
        } else if (c == 5) {
            fl = a_ld1(wsu, flA + flv);
            LD_W4(wa + 16, 16)
        } else if (c == 6) {
            landed(fl);
            if (wave < 4) wait_flag(rs, flv, flA, fl);
            LD_H(aa[0], ov + 40960, 0, isE, "=&v")
        }
    }
#pragma unroll
    for (int i = 0; i < 10; ++i) { landed(wa[i]); landed(wa[10 + i]); landed(aa[0][i]); }
    u32x4 wd[4];
    LD_H(aa[1], ov + 40960, 1, isE, "=&v") LD_W4(wd, 46)
    if (lane == 0) set_flag(rs, (int)OFF_FL + (32 + rt * 4 + (h >> 3)) * 128 + (h & 7) * 16);
    O = MFMA32(B8(((u32x4){OPS[8], OPS[9], OPS[10], OPS[11]})), B8(((u32x4){P[4], P[5], P[6], P[7]})), O);
    PSTAMP(1, 2); WSTAMP(2);
    pt2 = (unsigned)__builtin_amdgcn_s_memrealtime();
    float l = hi_half(O[8]);
    {
        bool bad = !(l >= 1e-18f && l <= 1.0e30f);
#pragma unroll
        for (int i = 0; i < 12; ++i) bad = bad || !(fabsf(O[i]) <= 3.0e38f);
        if (__builtin_expect(__any(bad), 0)) {
            const bf16x8 qt0 = B8(((u32x4){Q[4], Q[5], 0u, 0u}));
            float m = -INFINITY; O = Z16;
            int tl = tid;
            asm volatile("" : "+v"(tl));
            const int r = tl & 31, hh = (tl >> 5) & 1, rc = r < 20 ? r : 19;
            const int ko = (r >> 4) * 640 + ((r >> 2) & 1) * 320 + ((r >> 3) & 1) * 8 + (r & 3) * 2;
            for (int t = 0; t < 64; ++t) {
                const char* tb = kvb + t * 2560;
                unsigned kw[6];
#pragma unroll
                for (int i = 0; i < 6; ++i) {
                    const int d = i < 4 ? 8 * hh + 2 * i : 16 + 2 * (i - 4);
                    kw[i] = (unsigned)*(const u16*)(tb + ko + d * 16) | ((unsigned)*(const u16*)(tb + ko + d * 16 + 16) << 16);
                }
                u32x4 f0 = *(const u32x4*)(tb + 1280 + hh * 320 + rc * 16), f1 = *(const u32x4*)(tb + 1920 + hh * 320 + rc * 16);
                if (r >= 20) { f0 = ONES; f1 = ONES; }
                f32x16 s = MFMA32(B8(((u32x4){kw[0], kw[1], kw[2], kw[3]})), qm, Z16);
                s = MFMA32(B8(((u32x4){kw[4], kw[5], 0u, 0u})), qt0, s);
                float tm = s[0];
#pragma unroll
                for (int i = 1; i < 16; ++i) tm = fmaxf(tm, s[i]);
                tm = xhalf_max(tm);
                const float mn = fmaxf(m, tm);
                const float al = (m == -INFINITY) ? 0.f : __builtin_amdgcn_exp2f(m - mn);
                m = mn;
#pragma unroll
                for (int i = 0; i < 16; ++i) { O[i] *= al; s[i] = __builtin_amdgcn_exp2f(s[i] - mn); }
                u32x4 p0 = {pk2(s[0], s[1]), pk2(s[2], s[3]), pk2(s[4], s[5]), pk2(s[6], s[7])};
                u32x4 p1 = {pk2(s[8], s[9]), pk2(s[10], s[11]), pk2(s[12], s[13]), pk2(s[14], s[15])};
                O = MFMA32(B8(f0), B8(p0), O); O = MFMA32(B8(f1), B8(p1), O);
            }
            l = hi_half(O[8]);
        }
    }
    store_o(rs, O, (1.f - gate) * __builtin_amdgcn_rcpf(l), rt, 1, h, lane);
    __builtin_amdgcn_s_setprio(0);
    __builtin_amdgcn_sched_barrier(0);
    WSTAMP(3);
    constexpr int L_P0 = L_WO + 98304;
    if (wave < 4) {
        const LAS char* wl = L + j * 6144 + lane * 16;
        auto wfr = [&](int f) -> bf16x8 {
            if (f < 20) return B8(wa[f]);
            if (f < 44) return *(const LAS bf16x8*)(wl + L_WO + (((f - 20) % 12) / 3) * 24576 + ((f - 20) / 12) * 3072 + ((f - 20) % 3) * 1024);
            if (f < 46) return *(const LAS bf16x8*)(L + 1024 + j * 3072 + (f - 44) * 1024 + lane * 16);
            return B8(wd[f - 46]);
        };
        LAS char* wp = (LAS char*)lds + (lane & 15) * 320 + ((g ^ ((lane >> 1) & 3)) << 4);
        f32x4 acc[4][5] = {};
        {
            bf16x8 w4[5];
#pragma unroll
            for (int ni = 0; ni < 5; ++ni) w4[ni] = wfr(20 + ni);
#pragma unroll
            for (int hf = 0; hf < 2; ++hf) {
                if (hf) asm volatile("s_waitcnt vmcnt(3)" : "+v"(aa[1][0]), "+v"(aa[1][1]), "+v"(aa[1][2]), "+v"(aa[1][3]), "+v"(aa[1][4]), "+v"(aa[1][5]), "+v"(aa[1][6]),
                                     "+v"(aa[1][7]), "+v"(aa[1][8]), "+v"(aa[1][9]), "+v"(wd[0]), "+v"(wd[1]), "+v"(wd[2]), "+v"(wd[3]) :: "memory");
#pragma unroll
                for (int k = 0; k < 5; ++k)
#pragma unroll
                    for (int mi = 0; mi < 2; ++mi)
#pragma unroll
                        for (int ni = 0; ni < 5; ++ni) acc[hf * 2 + mi][ni] = MFMA16(k < 4 ? wfr(k * 5 + ni) : w4[ni], B8(aa[hf][2 * k + mi]), acc[hf * 2 + mi][ni]);
                __builtin_amdgcn_sched_barrier(0);
            }
        }
        const int flB = (int)OFF_FL + (32 + rb * 8 + j) * 128;
        u32x4 b0[10], b1[10];
        wait_flag(rs, flv, flB, get_flag(rs, flv, flB));
        WSTAMP(4);
        LD_H(b0, ov, 0, 1, "=&v")
        fl = a_ld1(wsu, flB + 512 + flv);
        wait_cnt(lds, 0, 64);
        asm volatile("s_waitcnt vmcnt(0)" : "+v"(fl) :: "memory");
        wait_flag(rs, flv, flB + 512, fl);
        WSTAMP(5);
        LD_H(b1, ov, 1, 1, "=&v")
#pragma clang loop unroll(disable)
        for (int spin = 0; spin < (1 << 20); ++spin) {
            unsigned m = 0u;
#pragma unroll
            for (int i = 0; i < 10; ++i) { landed(b0[i]); m = umax4(m, b0[i]); }
            if (__builtin_expect(__all(m != EMPTY), 1)) break;
            LD_H(b0, ov, 0, 1, "+v")
            asm volatile("s_waitcnt vmcnt(0)" ::: "memory");
        }
        bf16x8 wb[25];
#pragma unroll
        for (int k = 0; k < 5; ++k) {
#pragma unroll
            for (int ni = 0; ni < 5; ++ni) wb[k * 5 + ni] = wfr(25 + k * 5 + ni);
#pragma unroll
            for (int mi = 0; mi < 2; ++mi)
#pragma unroll
                for (int ni = 0; ni < 5; ++ni) acc[mi][ni] = MFMA16(wb[k * 5 + ni], B8(b0[2 * k + mi]), acc[mi][ni]);
        }
#pragma unroll
        for (int mi = 0; mi < 2; ++mi)
#pragma unroll
            for (int ni = 0; ni < 5; ++ni) *(LAS f32x4*)(wp + L_P0 + j * 10240 + mi * 5120 + ni * 64) = acc[mi][ni];
        add_cnt<12>(lds0);
        f32x4 ac1[2][5];
#pragma clang loop unroll(disable)
        for (int spin = 0; spin < (1 << 20); ++spin) {
            unsigned m = 0u;
#pragma unroll
            for (int k = 0; k < 5; ++k) {
                asm volatile("s_waitcnt vmcnt(%2)" : "+v"(b1[2 * k]), "+v"(b1[2 * k + 1]) : "n"(8 - 2 * k) : "memory");
#pragma unroll
                for (int mi = 0; mi < 2; ++mi)
#pragma unroll
                    for (int ni = 0; ni < 5; ++ni) ac1[mi][ni] = MFMA16(wb[k * 5 + ni], B8(b1[2 * k + mi]), k ? ac1[mi][ni] : acc[2 + mi][ni]);
                m = umax4(umax4(m, b1[2 * k]), b1[2 * k + 1]);
                __builtin_amdgcn_sched_barrier(0);
            }
            if (__builtin_expect(__all(m != EMPTY), 1)) break;
            LD_H(b1, ov, 1, 1, "+v")
        }
#pragma unroll
        for (int mi = 0; mi < 2; ++mi)
#pragma unroll
            for (int ni = 0; ni < 5; ++ni) *(LAS f32x4*)(wp + L_WO + j * 6144 + mi * 24576 + ni * 64) = ac1[mi][ni];
        WSTAMP(6);
        asm volatile("s_waitcnt lgkmcnt(0)\n\ts_barrier" ::: "memory");
    } else {
        const int t = tid - 256;
        const LAS char* rp[2][3]; unsigned oo[3];
#pragma unroll
        for (int i = 0; i < 3; ++i) {
            const int id = i < 2 || t < 128 ? i * 256 + t : 1, row = id / 20, cs = id - row * 20, cc = cs ^ ((row >> 1) & 3);
            rp[0][i] = L + L_P0 + id * 16; rp[1][i] = L + L_WO + id * 16 + (id >= 320 ? 24576 - 5120 : 0);
            oo[i] = (unsigned)(((rb * 64 + row) * D_ + cb * 80 + cc * 4) * 2);
            landed(rp[0][i]); landed(rp[1][i]); landed(oo[i]);
        }
        WSTAMP(4);
#pragma unroll
        for (int hf = 0; hf < 2; ++hf) {
            if (hf == 0) { wait_cnt(lds, 12, 4); WSTAMP(5); }
            else { WSTAMP(6); asm volatile("s_barrier" ::: "memory"); }
            f32x4 pv[3][4];
#pragma unroll
            for (int i = 0; i < 3; ++i)
#pragma unroll
                for (int w = 0; w < 4; ++w) pv[i][w] = *(const LAS f32x4*)(rp[hf][i] + w * (hf ? 6144 : 10240));
            __builtin_amdgcn_sched_barrier(0);
#pragma unroll
            for (int i = 0; i < 3; ++i) {
                const f32x4 v = (pv[i][0] + pv[i][1]) + (pv[i][2] + pv[i][3]);
                if (i < 2 || t < 128) __builtin_amdgcn_raw_buffer_store_b64(u32x2{pk2(v[0], v[1]), pk2(v[2], v[3])},
                    __builtin_amdgcn_make_buffer_rsrc((void*)out, 0, S_ * D_ * 2, 0x00020000), oo[i], hf * (32 * D_ * 2), OUT_AUX);
            }
        }
    }
    if (bid == 0 && wave == 4) {
        const unsigned pt3 = (unsigned)__builtin_amdgcn_s_memrealtime(), pc3 = (unsigned)__builtin_amdgcn_s_memtime();
        const unsigned* pp = (const unsigned*)(ws + (64 << 20));
        int mn = 1 << 30, mx = -(1 << 30);
        for (int i = 0; i < 4; ++i) { const int b = i * 64 + (threadIdx.x & 63); mn = min(mn, (int)(pp[b * 2] - pt0)); mx = max(mx, (int)(pp[b * 2 + 1] - pt0)); }
        for (int o = 1; o < 64; o <<= 1) { mn = min(mn, __shfl_xor(mn, o)); mx = max(mx, __shfl_xor(mx, o)); }
        int X = PROBE == 0 ? 0 : PROBE == 1 ? mx - mn : PROBE == 2 ? -mx : PROBE == 3 ? (int)(pt3 - pt0) : PROBE == 4 ? (int)(pc3 - pc0) / 20 :
                PROBE == 5 ? (int)(pt1 - pt0) : PROBE == 6 ? (int)(pt2 - pt1) : PROBE == 7 ? (int)(pt3 - pt2) : (int)(pt3 - pt0) - mn;
        X = rfl(min(max(X, 0), 6000));
        while ((int)((unsigned)__builtin_amdgcn_s_memrealtime() - pt3) < 300 + 4 * X) __builtin_amdgcn_s_sleep(1);
    }
}

extern "C" void kernel_launch(void* const* d_in, const int* in_sizes, int n_in, void* d_out, int out_size, void* d_ws, size_t ws_size,
                              hipStream_t stream) {
    const u16* x = (const u16*)d_in[0];
    const u16* wA = (const u16*)d_in[1];
    const u16* woA = (const u16*)d_in[2];
    const u16* wB = (const u16*)d_in[4];
    const u16* woB = (const u16*)d_in[5];
    const u16* gw = (const u16*)d_in[6];
    char* ws = (char*)d_ws;
    (void)hipFuncSetAttribute((const void*)k1_qkv, hipFuncAttributeMaxDynamicSharedMemorySize, LDS_BYTES);
    (void)hipFuncSetAttribute((const void*)k2_attn, hipFuncAttributeMaxDynamicSharedMemorySize, LDS_BYTES);
    k1_qkv<<<256, 512, LDS_BYTES, stream>>>(x, wA, wB, woA, woB, gw, ws);
    k2_attn<<<256, 512, LDS_BYTES, stream>>>(ws, (u16*)d_out);
}
```

```cpp
#define PROBE 121
#define OUT_AUX 16
#include <hip/hip_runtime.h>
#include <hip/hip_bf16.h>
#include <stdint.h>

typedef short    bf16x8 __attribute__((ext_vector_type(8)));
typedef short    s16x4  __attribute__((ext_vector_type(4)));
typedef float    f32x2  __attribute__((ext_vector_type(2)));
typedef float    f32x4  __attribute__((ext_vector_type(4)));
typedef float    f32x16 __attribute__((ext_vector_type(16)));
typedef float    f32x32 __attribute__((ext_vector_type(32)));
typedef unsigned u32x2  __attribute__((ext_vector_type(2)));
typedef unsigned u32x4  __attribute__((ext_vector_type(4)));
typedef unsigned u32x8  __attribute__((ext_vector_type(8)));
typedef unsigned u32x32 __attribute__((ext_vector_type(32)));
typedef __bf16   bf2_t  __attribute__((ext_vector_type(2)));
typedef unsigned short u16;
#define LAS __attribute__((address_space(3)))
#define DI __device__ __forceinline__

constexpr int S_ = 2048, D_ = 640;
constexpr size_t IMG     = (size_t)32 * 163840;
constexpr size_t OFF_IQ  = 0;
constexpr size_t OFF_IB  = IMG;
constexpr size_t OFF_IA  = 2 * IMG;
constexpr size_t OFF_O   = 3 * IMG;
constexpr size_t OFF_WOT = 4 * IMG;
constexpr size_t OFF_G   = OFF_WOT + (size_t)1600 * 1024;
constexpr size_t OFF_KVP = OFF_G + 8192;
constexpr size_t OFF_FL  = OFF_KVP + (size_t)256 * 2048;
constexpr int    WS_USED = (int)(OFF_FL + 288 * 128);
constexpr unsigned EMPTY = 0xFFFFFFFFu;
typedef __amdgpu_buffer_rsrc_t rsrc_t;
constexpr int SC1 = 16;

#define CSTAMP(i)
#define PSTAMP(k, i)
#define WSTAMP(i)

DI unsigned pk2(float lo, float hi) { f32x2 v = {lo, hi}; return __builtin_bit_cast(unsigned, __builtin_convertvector(v, bf2_t)); }
DI float bflo(unsigned v) { return __uint_as_float(v << 16); }
DI float bfhi(unsigned v) { return __uint_as_float(v & 0xffff0000u); }
DI int rfl(int x) { return __builtin_amdgcn_readfirstlane(x); }
DI void glds16(const void* sbase, unsigned voff, unsigned lds_base) {
    const unsigned long long a = (unsigned long long)(uintptr_t)sbase;
    const unsigned long long u = (unsigned long long)(unsigned)rfl((int)(unsigned)a) | ((unsigned long long)(unsigned)rfl((int)(unsigned)(a >> 32)) << 32);
    asm volatile("s_mov_b32 m0, %0\n\ts_nop 0\n\tglobal_load_lds_dwordx4 %1, %2" :: "s"(rfl((int)lds_base)), "v"(voff), "s"(u) : "memory");
}
#define MFMA16(a, b, c) __builtin_amdgcn_mfma_f32_16x16x32_bf16((a), (b), (c), 0, 0, 0)
#define MFMA32(a, b, c) __builtin_amdgcn_mfma_f32_32x32x16_bf16((a), (b), (c), 0, 0, 0)
#define B8(x) __builtin_bit_cast(bf16x8, x)
DI u32x2 trd(const LAS char* p) { return __builtin_bit_cast(u32x2, __builtin_amdgcn_ds_read_tr16_b64_v4i16((LAS s16x4*)p)); }
DI float pmax(float t, float c) { return __int_as_float(max(__float_as_int(t), __float_as_int(c))); }
template <int N> DI void wait_vm_bar() { asm volatile("s_waitcnt vmcnt(%0) lgkmcnt(0)\n\ts_barrier" :: "n"(N) : "memory"); }

constexpr int LDS_BYTES = 163840;
constexpr float LOG2E = 1.4426950408889634f;

DI void p1_aux(const u16* __restrict__ x, const u16* __restrict__ woA, const u16* __restrict__ woB, const u16* __restrict__ gate_w,
               char* __restrict__ ws, char* lds, int a) {
    const int tid = threadIdx.x, lane = tid & 63, wave = rfl(tid >> 6);
    const unsigned lds0 = (unsigned)(uintptr_t)lds;
    const int br = a >> 3, u = a & 7;
    if (tid < 144) *(u32x4*)(ws + OFF_FL + (a * 144 + tid) * 16) = u32x4{0u, 0u, 0u, 0u};
    const char* wsrc = (const char*)(br ? woB : woA) + (size_t)u * 102400;
    for (int i = wave; i < 100; i += 8) glds16(wsrc + i * 1024, lane * 16, lds0 + i * 1024);
    const u32x4* xs = (const u32x4*)(x + (size_t)(a * 128 + wave * 16) * D_);
    u32x4 xv[20], gw[5];
#pragma unroll
    for (int i = 0; i < 20; ++i) xv[i] = xs[i * 64 + lane];
#pragma unroll
    for (int i = 0; i < 5; ++i) gw[i] = ((const u32x4*)gate_w)[(i * 64 + lane) % 80];
    {
        const rsrc_t rs = __builtin_amdgcn_make_buffer_rsrc((void*)(ws + OFF_KVP + a * 32768), 0, 32768, 0x00020000);
#pragma unroll
        for (int i = 0; i < 4; ++i) __builtin_amdgcn_raw_buffer_store_b128(u32x4{EMPTY, EMPTY, EMPTY, EMPTY}, rs, tid * 16 + i * 8192, 0, SC1);
    }
    LAS float* part = (LAS float*)(lds + 102400) + wave * 1280;
#pragma unroll
    for (int i = 0; i < 20; ++i) {
        float s = 0.f;
#pragma unroll
        for (int j = 0; j < 4; ++j) s += bflo(xv[i][j]) * bflo(gw[i % 5][j]) + bfhi(xv[i][j]) * bfhi(gw[i % 5][j]);
        part[i * 64 + lane] = s;
    }
    wait_vm_bar<0>();
    {
        float s = 0.f;
#pragma unroll
        for (int j = 0; j < 20; ++j) s += part[lane * 20 + j];
        s += __shfl_xor(s, 1); s += __shfl_xor(s, 2);
        if ((lane & 3) == 0) ((float*)(ws + OFF_G))[a * 128 + wave * 16 + (lane >> 2)] = 1.f / (1.f + __expf(-s));
    }
    const int g = lane >> 4, hh = g & 1, ns = g >> 1, q = (lane >> 2) & 3, p = lane & 3;
    for (int un = wave; un < 100; un += 8) {
        const int e = un / 20, np = un - e * 20, nt = np * 2 + ns;
        const int r0 = e < 4 ? e * 20 + 4 * hh : (2 * hh) * 20 + 16;
        const int r1 = e < 4 ? r0 + 8 : r0 + 20;
        const int ks16 = e < 4 ? (u & 1) * 4 + e : 8 + (u & 1), cbk = nt / 5;
        const u32x2 lo = trd((const LAS char*)lds + (r0 + q) * 1280 + nt * 32 + p * 8);
        const u32x2 hi = trd((const LAS char*)lds + (r1 + q) * 1280 + nt * 32 + p * 8);
        *(u32x4*)(ws + OFF_WOT + (size_t)((cbk * 8 + (u & 6) + br) * 25 + (ks16 >> 1) * 5 + nt - cbk * 5) * 1024 + ((ks16 & 1) * 2 + hh) * 256 + (lane & 15) * 16) = u32x4{lo[0], lo[1], hi[0], hi[1]};
    }
}

#define G1_TL 3
#define G1_D 3
#define G1_D0 2
#define G1_DT 10
#define G1_DE 0
#define G1_NX 4
constexpr int G1_NW = 10 - G1_NX, G1_HB = 20 - 2 * G1_TL, G1_WOFF = G1_NX * 16384;
constexpr int g1_t(int h) { return h < 2 * G1_HB ? h >> 1 : h < 2 * G1_HB + 2 * G1_TL ? h - G1_HB : h - G1_HB - 2 * G1_TL; }
constexpr int g1_m(int h) { return h < 2 * G1_HB ? h & 1 : h < 2 * G1_HB + 2 * G1_TL ? 0 : 1; }
constexpr int g1_xi(int h) { return h < 2 * G1_HB ? (h >> 1) & 1 : h & 1; }
constexpr int g1_free(int i) { return i < 10 ? (i < G1_NX ? -1 : 2 * (i - G1_NX) + 2) : (i - 10 < G1_NW ? -1 : i - 10 - G1_NW + 1); }
struct G1Plan { int seq[30]; int cnt[22]; int pos[30]; };
constexpr G1Plan g1_plan() {
    G1Plan p = {}; bool done[30] = {}, seen[10] = {}; int ord[30] = {}, need[21] = {}, n = 0;
    for (int b = 0; b < 20; ++b) { const int u = g1_t(2 * b) >> 1; if (!seen[u]) { seen[u] = true; ord[n++] = u; } need[b] = n; ord[n++] = 10 + b; }
    need[20] = 30; n = 0;
    for (int b = 0; b <= 20; ++b) {
        const int ex = b > G1_DT ? b - G1_DT : 0, lim = 3 * (b >> 1) + 1 + (b & 1) + (b == 0 ? G1_D0 : G1_D) + (ex < G1_DE ? ex : G1_DE);
        for (int k = 0; k < 30 && k <= lim; ++k) { const int i = ord[k]; if (!done[i] && g1_free(i) < b) { done[i] = true; p.pos[i] = n; p.seq[n++] = i; } }
        p.cnt[b] = n;
    }
    return p;
}
constexpr G1Plan G1P = g1_plan();
constexpr int g1_wait(int b) { const int x = G1P.pos[g1_t(2 * b) >> 1], w = G1P.pos[10 + b]; return 4 * (G1P.cnt[b] - 1 - (x > w ? x : w)); }
constexpr bool g1_ok() { for (int b = 0; b < 20; ++b) if (g1_wait(b) < 0 || g1_wait(b) > 60) return false; return G1P.cnt[19] == 30 && G1_NX >= G1_TL && G1_NX <= 4; }
static_assert(g1_ok(), "loader plan");
#define G1_ROT ((0x27057250 >> (4 * xcd)) & 15)

#define LST_P
#define LST_A
template <int T> struct G1Load {
    template <class FX, class FW> static DI void run(FX&& ldx, FW&& ldw LST_P) {
        asm volatile("s_waitcnt vmcnt(%0)\n\ts_barrier" :: "n"(g1_wait(T)) : "memory");
        unsigned long long ptm = 0;
        if (T == PROBE - 100) asm volatile("s_memrealtime %0\n\ts_waitcnt lgkmcnt(0)" : "=s"(ptm));
#pragma unroll
        for (int n = G1P.cnt[T]; n < G1P.cnt[T + 1]; ++n) { const int i = G1P.seq[n]; if (i < 10) ldx(i); else ldw(i - 10); }
        G1Load<T + 1>::run(ldx, ldw LST_A);
        if (T == PROBE - 100) ldx(-1, (unsigned)ptm);
    }
};
template <> struct G1Load<20> { template <class FX, class FW> static DI void run(FX&&, FW&& LST_P) {} };

struct G1Ctx {
    f32x4 acc[4][8];
    bf16x8 x[2][4]; u32x2 w[2][4][2];
    const LAS char* xa[2]; const LAS char* wa[8];
    int so[4];
    rsrc_t rB; float sc; f32x2 m[4]; u32x4 o;
};
#define SB0() __builtin_amdgcn_sched_barrier(0)
#define G1_SP 12
template <int J, int I> DI void g1_side(G1Ctx& c) {
    constexpr int K = (J - (40 - 2 * G1_TL)) * 16 + I;
    if (K > 0 && K <= 8 * G1_SP && (K - 1) % G1_SP == 8) asm volatile("" :: "v"(c.o));
    if (K >= 0 && K < 8 * G1_SP && K % G1_SP < 9) {
        constexpr int U = K < 0 ? 0 : K / G1_SP, e = K < 0 ? 0 : K % G1_SP, ni = (U >> 1) & 3, a = U & 1;
        constexpr int mi = e == 0 ? 0 : e == 1 ? 1 : e == 3 ? 2 : e == 5 ? 3 : -1, ci = e == 2 ? 0 : e == 4 ? 1 : e == 6 ? 2 : e == 7 ? 3 : -1;
        if (mi >= 0) { const f32x4 v = c.acc[2 * a + ((mi & 3) >> 1)][ni]; c.m[mi & 3] = f32x2{v[(mi & 1) * 2], v[(mi & 1) * 2 + 1]} * c.sc; }
        if (ci >= 0) c.o[ci & 3] = pk2(c.m[ci & 3][0], c.m[ci & 3][1]);
        if (e == 8) __builtin_amdgcn_raw_buffer_store_b128(c.o, c.rB, c.so[ni] + a * 2560, 0, SC1);
        SB0();
    }
}
template <int J> struct G1Step {
    static DI void run(G1Ctx& c) {
        constexpr int N = J + 1, t = g1_t(N), xo = ((t >> 1) % G1_NX) * 16384, sl = (N >> 1) % G1_NW, wo = (sl & 3) * 16384 + (N & 1) * 8192, wi = (sl >> 2) * 4;
        constexpr int ps = J & 1, ma = g1_m(J) * 4, xc = g1_xi(J), xn = g1_xi(N);
#define G1_M(i) c.acc[(i) & 3][ma + ((i) >> 2)] = MFMA16(c.x[xc][(i) & 3], B8(((u32x4){c.w[ps][(i) >> 2][0][0], c.w[ps][(i) >> 2][0][1], c.w[ps][(i) >> 2][1][0], c.w[ps][(i) >> 2][1][1]})), \
                c.acc[(i) & 3][ma + ((i) >> 2)]); SB0(); g1_side<J, i>(c);
#define G1_X(i) if (N < 40) c.x[xn][i] = *(const LAS bf16x8*)(c.xa[t & 1] + xo + ((i) >> 1) * 4096 + ((i) & 1) * 1024); SB0();
#define G1_W(i, e) if (N < 40) c.w[ps ^ 1][i][e] = trd(c.wa[wi + (i)] + wo + (e) * 1024); SB0();
        if (ps) {
            G1_M(0) G1_M(1) G1_M(2)
            if (N < 40) asm volatile("s_waitcnt lgkmcnt(0)\n\ts_barrier" ::: "memory");
            SB0();
            G1_X(0) G1_M(3) G1_W(0, 0) G1_M(4) G1_W(0, 1) G1_M(5) G1_X(1) G1_M(6) G1_X(2) G1_M(7) G1_X(3) G1_M(8) G1_W(1, 0) G1_M(9) G1_W(1, 1) G1_M(10)
            G1_W(2, 0) G1_M(11) G1_W(2, 1) G1_M(12) G1_W(3, 0) G1_M(13) G1_W(3, 1) G1_M(14) G1_M(15)
        } else if (xn != xc) {
            G1_M(0) G1_X(0) G1_M(1) G1_W(0, 0) G1_M(2) G1_W(0, 1) G1_M(3) G1_X(1) G1_M(4) G1_X(2) G1_M(5) G1_X(3) G1_M(6) G1_W(1, 0) G1_M(7) G1_W(1, 1)
            G1_M(8) G1_W(2, 0) G1_M(9) G1_W(2, 1) G1_M(10) G1_W(3, 0) G1_M(11) G1_W(3, 1) G1_M(12) G1_M(13) G1_M(14) G1_M(15)
        } else {
            G1_M(0) G1_W(0, 0) G1_M(1) G1_W(0, 1) G1_M(2) G1_W(1, 0) G1_M(3) G1_W(1, 1) G1_M(4) G1_W(2, 0) G1_M(5) G1_W(2, 1) G1_M(6) G1_W(3, 0) G1_M(7) G1_W(3, 1)
            G1_M(8) G1_M(9) G1_M(10) G1_M(11) G1_M(12) G1_M(13) G1_M(14) G1_M(15)
        }
        G1Step<J + 1>::run(c);
    }
};
template <> struct G1Step<40> { static DI void run(G1Ctx&) {} };
template <int K> struct G1Rest { static DI void run(G1Ctx& c) { g1_side<40 - 2 * G1_TL + K / 16, K % 16>(c); G1Rest<K + 1>::run(c); } };
template <> struct G1Rest<8 * G1_SP> { static DI void run(G1Ctx&) {} };

DI void phase1(const u16* __restrict__ x, const u16* __restrict__ wA, const u16* __restrict__ wB, char* __restrict__ ws, char* lds, int bid) {
    const int tid = threadIdx.x, lane = tid & 63, wave = rfl(tid >> 6);
    const unsigned lds0 = (unsigned)(uintptr_t)lds;
    const int xcd = bid & 7, j = (xcd >> 1) * 30 + (bid >> 3);
    const int ct = j >> 3, mt = (xcd & 1) * 8 + (j & 7);
    const int type = rfl(ct / 5), rot = rfl(G1_ROT);
    if (__builtin_expect(wave >= 4, 1)) {
        const int lw = wave - 4;
        unsigned xo[4], wo[2];
#pragma unroll
        for (int i = 0; i < 4; ++i) { const int row = (lw * 4 + i) * 8 + (lane >> 3); xo[i] = (unsigned)((mt * 128 + row) * 1280 + (((lane & 7) ^ ((row >> 1) & 3) ^ ((row >> 2) & 4)) << 4)); }
#pragma unroll
        for (int i = 0; i < 2; ++i) { const int k = (lw * 2 + i) * 4 + (lane >> 4), s16 = lane & 15;
            wo[i] = (unsigned)(k * 3840 + ct * 256 + ((s16 ^ ((k & 3) << 2) ^ ((k >> 2) & 2)) << 4)); }
        auto ldx = [&](int u, unsigned st = 0u) {
            if (u < 0) { if (tid == 256) ((unsigned*)(ws + (64 << 20)))[512 + bid] = st; return; }
            int kk = u + rot; kk -= kk >= 10 ? 10 : 0;
            const unsigned slot = lds0 + (u % G1_NX) * 16384 + lw * 4096;
#pragma unroll
            for (int i = 0; i < 4; ++i) glds16((const char*)x + kk * 128, xo[i], slot + i * 1024);
        };
        auto ldw = [&](int s) {
            int kk = (g1_t(2 * s) >> 1) + rot; kk -= kk >= 10 ? 10 : 0;
            const unsigned slot = lds0 + G1_WOFF + (s % G1_NW) * 16384 + lw * 2048;
#pragma unroll
            for (int e = 0; e < 2; ++e) {
                const char* src = (const char*)(g1_m(2 * s + e) ? wA : wB) + (kk * 64 + (g1_t(2 * s + e) & 1) * 32) * 3840;
                glds16(src, wo[0], slot + e * 8192); glds16(src, wo[1], slot + e * 8192 + 1024);
            }
        };
#pragma unroll
        for (int n = 0; n < G1P.cnt[0]; ++n) { const int i = G1P.seq[n]; if (i < 10) ldx(i); else ldw(i - 10); }
        G1Load<0>::run(ldx, ldw LST_A);
        return;
    }
    const int wm = wave & 1, wn = wave >> 1;
    const int i16 = lane & 15, g = lane >> 4, q = (lane >> 2) & 3, p = lane & 3;
    G1Ctx c;
    const int xr = (i16 >> 3) * 16 + (i16 & 7);
#pragma unroll
    for (int ks = 0; ks < 2; ++ks) { c.xa[ks] = (const LAS char*)lds + (wm * 64 + xr) * 128 + (((ks * 4 + g) ^ ((xr >> 1) & 3) ^ ((xr >> 2) & 4)) << 4); asm volatile("" : "+v"(c.xa[ks])); }
#pragma unroll
    for (int i = 0; i < 8; ++i) { c.wa[i] = (const LAS char*)lds + G1_WOFF + (i >> 2) * 65536 + (8 * g + q) * 256 + (((wn * 4 + (i & 3)) ^ (q << 1) ^ (g & 1)) << 5) + p * 8;
        asm volatile("" : "+v"(c.wa[i])); }
#pragma unroll
    for (int i = 0; i < 4; ++i) { const int cc = (ct - type * 5) * 128 + wn * 64 + i * 16 + i16, h = cc / 20; c.so[i] = h * 163840 + g * 320 + (cc - h * 20) * 16; asm volatile("" : "+v"(c.so[i])); }
    constexpr float QS = 0.22360679774997896f * LOG2E;
    int sci = type == 0 ? __builtin_bit_cast(int, QS) : 0x3F800000;
    asm volatile("" : "+s"(sci));
    c.sc = __int_as_float(sci);
    const int tb = (mt * 4 + wm * 2) * 2560;
    const int sB = tb + (type == 0 ? (int)OFF_IQ : (int)OFF_IB + (type - 1) * 1280), sA = tb + (type == 0 ? (int)OFF_IQ + 1280 : (int)OFF_IA + (type - 1) * 1280);
    c.rB = __builtin_amdgcn_make_buffer_rsrc((void*)(ws + sB), 0, (int)IMG, 0x00020000);
    const rsrc_t rA = __builtin_amdgcn_make_buffer_rsrc((void*)(ws + sA), 0, (int)IMG, 0x00020000);
    {
        const float a0 = type < 2 ? 1e-6f : 0.f;
        f32x4 a4 = {a0, a0, a0, a0};
        asm volatile("" : "+v"(a4));
#pragma unroll
        for (int i = 0; i < 16; ++i) { c.acc[i & 3][i >> 2] = f32x4{0.f, 0.f, 0.f, 0.f}; c.acc[i & 3][4 + (i >> 2)] = a4; }
    }
    __builtin_amdgcn_s_setprio(2);
    asm volatile("s_barrier" ::: "memory");
#pragma unroll
    for (int i = 0; i < 4; ++i) {
        c.x[0][i] = *(const LAS bf16x8*)(c.xa[0] + (i >> 1) * 4096 + (i & 1) * 1024);
        if (i == 0) { c.w[0][0][0] = trd(c.wa[0]); c.w[0][0][1] = trd(c.wa[0] + 1024); }
    }
#pragma unroll
    for (int i = 1; i < 4; ++i) { c.w[0][i][0] = trd(c.wa[i]); c.w[0][i][1] = trd(c.wa[i] + 1024); }
    SB0();
    G1Step<0>::run(c);
    PSTAMP(0, 2);
    G1Rest<(32 * G1_TL < 8 * G1_SP ? 32 * G1_TL : 8 * G1_SP)>::run(c);
    if (type < 2) {
#pragma unroll
        for (int i = 0; i < 4; ++i)
#pragma unroll
            for (int a = 0; a < 2; ++a) {
                u32x4 o;
#pragma unroll
                for (int e = 0; e < 4; ++e) {
                    const f32x2 t = {c.acc[2 * a + (e >> 1)][4 + i][(e & 1) * 2], c.acc[2 * a + (e >> 1)][4 + i][(e & 1) * 2 + 1]};
                    const f32x2 m = __builtin_elementwise_fma(t, f32x2{LOG2E, LOG2E}, f32x2{-1e-6f * LOG2E, -1e-6f * LOG2E});
                    const f32x2 ex = {__builtin_amdgcn_fmed3f(__builtin_amdgcn_exp2f(m[0]), 0.f, 1.f), __builtin_amdgcn_fmed3f(__builtin_amdgcn_exp2f(m[1]), 0.f, 1.f)};
                    const f32x2 r = f32x2{pmax(t[0], 1e-6f), pmax(t[1], 1e-6f)} + ex;
                    o[e] = pk2(r[0], r[1]);
                }
                __builtin_amdgcn_raw_buffer_store_b128(o, rA, c.so[i] + a * 2560, 0, SC1);
            }
    } else {
#pragma unroll
        for (int i = 0; i < 4; ++i)
#pragma unroll
            for (int a = 0; a < 2; ++a) {
                const f32x4 u = c.acc[2 * a][4 + i], v = c.acc[2 * a + 1][4 + i];
                __builtin_amdgcn_raw_buffer_store_b128(u32x4{pk2(u[0], u[1]), pk2(u[2], u[3]), pk2(v[0], v[1]), pk2(v[2], v[3])}, rA, c.so[i] + a * 2560, 0, SC1);
            }
    }
}

__global__ __launch_bounds__(512, 2) void k1_qkv(const u16* __restrict__ x, const u16* __restrict__ wA, const u16* __restrict__ wB,
                                                 const u16* __restrict__ woA, const u16* __restrict__ woB, const u16* __restrict__ gate_w,
                                                 char* __restrict__ ws) {
    extern __shared__ __attribute__((aligned(16))) char lds[];
    int bid = blockIdx.x;
    asm volatile("" : "+s"(bid));
    PSTAMP(0, 0);
    const unsigned pt0 = (unsigned)__builtin_amdgcn_s_memrealtime();
    if (__builtin_expect(bid < 240, 1)) phase1(x, wA, wB, ws, lds, bid);
    else p1_aux(x, woA, woB, gate_w, ws, lds, bid - 240);
    if (threadIdx.x == 0) { asm volatile("s_waitcnt vmcnt(0)"); unsigned* pp = (unsigned*)(ws + (64 << 20)) + bid * 2; pp[0] = pt0; pp[1] = (unsigned)__builtin_amdgcn_s_memrealtime(); }
}

#define STR(x) #x
#define XS(x) STR(x)
#define A_E(i) "v_exp_f32 v" XS(i) ", v" XS(i) "\n"
#define A_C(d, a, b) "v_cvt_pk_bf16_f32 v" XS(d) ", v" XS(a) ", v" XS(b) "\n"
#define A_G(a, b, c, d, p0, p1) A_E(a) A_E(b) A_E(c) A_E(d) A_C(p0, a, b) A_C(p1, c, d)
#define A_M(d, a, b, c) "v_mfma_f32_32x32x16_bf16 " d ", " a ", " b ", " c "\n"
#define A_STEP(C0,C1,C2,C3,C4,C5,C6,C7,C8,C9,C10,C11,C12,C13,C14,C15, SN, KM, KT, V1, V0, NKMa, NKMb, NKT, NV1, NV0, OK0, OK1, OV1, OV0, PRIO) \
    "s_waitcnt lgkmcnt(0)\n" \
    A_M(SN, KM, "v[120:123]", "0") \
    "ds_read_b64_tr_b16 " NKMa ", v128 offset:" XS(OK0) "\n" \
    "ds_read_b64_tr_b16 " NKMb ", v128 offset:" XS(OK1) "\n" \
    "ds_read_b64_tr_b16 " NKT ", v129 offset:" XS(OK0) "\n" \
    A_G(C0, C1, C2, C3, 112, 113) \
    A_M("v[64:79]", V1, "v[116:119]", "v[64:79]") \
    "s_mov_b64 exec, s[72:73]\n" \
    "ds_read_b128 " NV1 ", v130 offset:" XS(OV1) "\n" \
    "ds_read_b128 " NV0 ", v130 offset:" XS(OV0) "\n" \
    "s_mov_b64 exec, -1\n" \
    A_G(C4, C5, C6, C7, 114, 115) \
    A_M(SN, KT, "v[124:127]", SN) \
    A_G(C8, C9, C10, C11, 116, 117) \
    A_M("v[64:79]", V0, "v[112:115]", "v[64:79]") \
    "s_setprio " XS(PRIO) "\n" \
    A_G(C12, C13, C14, C15, 118, 119)
#define A_EVEN(OK0, OK1, OV1, OV0, PRIO) A_STEP(32,33,34,35,36,37,38,39,40,41,42,43,44,45,46,47, "v[48:63]", "v[80:83]", "v[84:87]", "v[88:91]", "v[92:95]", \
    "v[96:97]", "v[98:99]", "v[100:101]", "v[104:107]", "v[108:111]", OK0, OK1, OV1, OV0, PRIO)
#define A_ODD(OK0, OK1, OV1, OV0, PRIO) A_STEP(48,49,50,51,52,53,54,55,56,57,58,59,60,61,62,63, "v[32:47]", "v[96:99]", "v[100:103]", "v[104:107]", "v[108:111]", \
    "v[80:81]", "v[82:83]", "v[84:85]", "v[88:91]", "v[92:95]", OK0, OK1, OV1, OV0, PRIO)

#define A_LD(g, off) "global_load_lds_dwordx4 v131, " g " offset:" XS(off) "\n"
#define A_LDA(off) "s_cbranch_scc1 .Lattn_s" XS(off) "\n" A_LD("s[74:75]", off) ".Lattn_s" XS(off) ":\n"
#define A_BODY \
        "s_mov_b32 m0, s78\n s_cmp_eq_u32 s80, 2\n s_nop 3\n" \
        A_EVEN(5120, 5184, 640, 2560, 2)     A_ODD(7680, 7744, 3200, 5120, 1) \
        A_EVEN(10240, 10304, 5760, 7680, 0)  A_ODD(12800, 12864, 8320, 10240, 3) \
        "s_waitcnt vmcnt(0) lgkmcnt(0)\n s_mov_b64 exec, 1\n ds_add_u32 v127, v86\n s_mov_b64 exec, -1\n" \
        A_EVEN(15360, 15424, 10880, 12800, 2) A_LDA(0) A_ODD(17920, 17984, 13440, 15360, 1) A_LDA(1024) \
        A_EVEN(20480, 20544, 16000, 17920, 0) A_LDA(2048) \
        "s_cmp_eq_u32 s80, 1\n s_cbranch_scc0 .Lattn_skip\n s_mov_b32 m0, s79\n s_nop 0\n" \
        A_LD("s[76:77]", 0) A_LD("s[76:77]", 1024) A_LD("s[76:77]", 2048) ".Lattn_skip:\n" \
        "ds_read_b32 v132, v127\n" \
        A_ODD(23040, 23104, 18560, 20480, 3) \
        "v_add_u32 v128, 0x5000, v128\n v_add_u32 v129, 0x5000, v129\n v_add_u32 v130, 0x5000, v130\n" \
        "s_waitcnt lgkmcnt(0)\n s_nop 0\n"
#define A_OPERANDS \
        : "+{v[32:63]}"(S), "+{v[64:79]}"(O), "+{v[80:111]}"(OPS), "+{v[112:119]}"(P), "+{v[128:131]}"(AD), "={v132}"(cnt) \
        : "{v[120:127]}"(Q), "{s[72:73]}"(vmask), "{s[74:75]}"(ga), "{s[76:77]}"(gb), "{s78}"(ma), "{s79}"(mb), "{s80}"(enb) \
        : "memory", "scc", "s82", "s83"
template <bool FIRST>
DI unsigned attn_chunk(f32x32& S, f32x16& O, u32x32& OPS, u32x8& P, u32x4& AD, const u32x8& Q, unsigned long long vmask,
                   unsigned ma, unsigned long long ga, unsigned mb, unsigned long long gb, unsigned enb) {
    unsigned cnt;
    if (FIRST) asm volatile("s_call_b64 s[82:83], .Lattn_body\n s_branch .Lattn_done\n .p2align 6\n .Lattn_body:\n" A_BODY "s_setpc_b64 s[82:83]\n .Lattn_done:\n" A_OPERANDS);
    else       asm volatile("s_call_b64 s[82:83], .Lattn_body\n" A_OPERANDS);
    return cnt;
}
DI void wait_cnt(char* lds, int off, unsigned cnt, int n) {
    if (__builtin_expect((unsigned)rfl((int)cnt) < (unsigned)(n * 0x3F80), 0)) {
#pragma clang loop unroll(disable)
        do { __builtin_amdgcn_s_sleep(1); cnt = *(volatile LAS unsigned*)(lds + off); } while ((unsigned)rfl((int)cnt) < (unsigned)(n * 0x3F80));
    }
    asm volatile("" ::: "memory");
}

DI void wait_cnt(char* lds, int off, int n) { wait_cnt(lds, off, *(volatile LAS unsigned*)(lds + off), n); }
template <int OFF> DI void add_cnt(unsigned lds0) {
    asm volatile("s_waitcnt lgkmcnt(0)\n\ts_mov_b64 exec, 1\n\tds_add_u32 %0, %1 offset:%2\n\ts_mov_b64 exec, -1" :: "v"(lds0), "v"(0x3F80u), "n"(OFF) : "memory"); }

DI float xhalf_max(float m) { auto rr = __builtin_amdgcn_permlane32_swap(__float_as_uint(m), __float_as_uint(m), false, false);
    return fmaxf(__uint_as_float(rr[0]), __uint_as_float(rr[1])); }
DI float hi_half(float v) {
    auto rr = __builtin_amdgcn_permlane32_swap(__float_as_uint(v), __float_as_uint(v), false, false);
    return __uint_as_float(rr[1]); }
DI float sum16(float v) { auto rr = __builtin_amdgcn_permlane16_swap(__float_as_uint(v), __float_as_uint(v), false, false);
    return __uint_as_float(rr[0]) + __uint_as_float(rr[1]); }
DI float sum32(float v) { auto rr = __builtin_amdgcn_permlane32_swap(__float_as_uint(v), __float_as_uint(v), false, false);
    return __uint_as_float(rr[0]) + __uint_as_float(rr[1]); }
DI f32x4 sum_rows(f32x4 a, f32x4 b) {
    f32x4 s = a + b;
#pragma unroll
    for (int i = 0; i < 4; ++i) s[i] = sum32(sum16(s[i]));
    return s;
}
DI unsigned long long uni64(const void* p) { const unsigned long long a = (unsigned long long)(uintptr_t)p;
    return (unsigned long long)(unsigned)rfl((int)(unsigned)a) | ((unsigned long long)(unsigned)rfl((int)(unsigned)(a >> 32)) << 32); }
DI unsigned full(unsigned v) { return v < EMPTY ? v : EMPTY - 1u; }
DI unsigned umax4(unsigned m, u32x4 v) { return max(max(max(m, v[0]), max(v[1], v[2])), v[3]); }
DI void store_o(rsrc_t rs, const f32x16& o, float f, int rt, int br, int h, int lane) {
    const int rb = (int)OFF_O + (rt * 80 + ((br ? 0 : 4) + (h >> 3)) * 10) * 1024;
    __builtin_amdgcn_raw_buffer_store_b128(u32x4{full(pk2(o[0] * f, o[1] * f)), full(pk2(o[2] * f, o[3] * f)), full(pk2(o[4] * f, o[5] * f)), full(pk2(o[6] * f, o[7] * f))},
                                           rs, lane * 16, rb + (h & 7) * 1024, SC1);
    if (lane < 32) __builtin_amdgcn_raw_buffer_store_b64(u32x2{full(pk2(o[8] * f, o[9] * f)), full(pk2(o[10] * f, o[11] * f))},
                                           rs, lane * 16, rb + (8 + ((h >> 2) & 1)) * 1024 + ((h >> 1) & 1) * 512 + (h & 1) * 8, SC1);
}
DI void empty_o(rsrc_t rs, int rt, int h, int lane) {
    const int rb = (int)OFF_O + (rt * 80 + (h >> 3) * 10) * 1024;
    u32x4 e = {EMPTY, EMPTY, EMPTY, EMPTY};
    __builtin_amdgcn_raw_buffer_store_b128(e, rs, lane * 16, rb + (h & 7) * 1024, SC1);
    if (lane < 32) __builtin_amdgcn_raw_buffer_store_b64(u32x2{e[0], e[1]}, rs, lane * 16, rb + (8 + ((h >> 2) & 1)) * 1024 + ((h >> 1) & 1) * 512 + (h & 1) * 8, SC1);
    asm volatile("" :: "v"(e));
}
DI void set_flag(rsrc_t rs, int off) { __builtin_amdgcn_raw_buffer_store_b32(1u, rs, 0, off, SC1); }
DI unsigned get_flag(rsrc_t rs, int voff, int off) { return __builtin_amdgcn_raw_buffer_load_b32(rs, voff, off, SC1); }
DI unsigned a_ld1(unsigned long long sb, unsigned voff) { unsigned v; asm volatile("global_load_dword %0, %1, %2 sc1" : "=v"(v) : "v"(voff), "s"(sb) : "memory"); return v; }
DI u32x4 a_ld4s(unsigned long long sb, unsigned voff) { u32x4 v; asm volatile("global_load_dwordx4 %0, %1, %2 sc1" : "=v"(v) : "v"(voff), "s"(sb) : "memory"); return v; }
template <int OFF> DI u32x4 a_ld4so(unsigned long long sb, unsigned voff) { u32x4 v;
    asm volatile("global_load_dwordx4 %0, %1, %2 offset:%3 sc1" : "=v"(v) : "v"(voff), "s"(sb), "n"(OFF) : "memory"); return v; }
template <int OFF> DI u32x4 a_ld4(unsigned long long sb, unsigned voff) { u32x4 v;
    asm volatile("global_load_dwordx4 %0, %1, %2 offset:%3" : "=v"(v) : "v"(voff), "s"(sb), "n"(OFF) : "memory"); return v; }
template <class T> DI void landed(T& v) { asm volatile("" : "+v"(v)); }
DI void wait_flag(rsrc_t rs, int voff, int off, unsigned v) {
    if (__builtin_expect(!__all(v == 1u), 0)) {
#pragma clang loop unroll(disable)
        for (int spin = 0; spin < (1 << 22); ++spin) { if (__all(get_flag(rs, voff, off) == 1u)) break; __builtin_amdgcn_s_sleep(1); }
    }
    __builtin_amdgcn_fence(__ATOMIC_ACQUIRE, "wavefront");
    asm volatile("" ::: "memory");
}

#define FILL_C 1
constexpr int L_SP = 136192, L_K0 = 152320, L_SQ = 153600, L_QA = 1280, L_KVF = 11520, L_WO = 13312;

__global__ __launch_bounds__(512, 2) void k2_attn(char* __restrict__ ws, u16* __restrict__ out) {
    extern __shared__ __attribute__((aligned(16))) char lds[];
    int bid = blockIdx.x;
    asm volatile("" : "+s"(bid));
    PSTAMP(1, 0); WSTAMP(0);
    const unsigned pt0 = (unsigned)__builtin_amdgcn_s_memrealtime(), pc0 = (unsigned)__builtin_amdgcn_s_memtime();
    unsigned pt1 = 0, pt2 = 0;
    const int tid = threadIdx.x, lane = tid & 63, wave = rfl(tid >> 6);
    const int h = (bid & 7) * 4 + ((bid >> 3) & 3), qb = bid >> 5;
    const int oq = bid & 7, rb = oq * 4 + ((bid >> 3) & 3), cb = bid >> 5;
    const int r = lane & 31, hh = lane >> 5;
    const unsigned lds0 = (unsigned)(uintptr_t)lds;
    const rsrc_t rs = __builtin_amdgcn_make_buffer_rsrc((void*)ws, 0, WS_USED, 0x00020000);
    const char* kvb = ws + OFF_IB + (size_t)h * 163840;
    const char* qim = ws + OFF_IQ + (size_t)h * 163840 + qb * 20480;
    const int wr = (wave & 3) * 2 + (wave >> 2), rt = qb * 8 + wr;
    const int rc = r < 20 ? r : 19;
    glds16(qim + wr * 2560, lane * 16, lds0 + L_SQ + wave * 1280);
    glds16(qim + wr * 2560 + 256, lane * 16, lds0 + L_SQ + wave * 1280 + 256);
    glds16(kvb + (wave & 1) * 256, lane * 16, lds0 + L_K0 + (wave & 1) * 256);
#pragma unroll
    for (int i = 0; i < 3; ++i) glds16(kvb + (1 + i * 8 + wave) * 1024, lane * 16, lds0 + (1 + i * 8 + wave) * 1024);
    const char* ap = ws + OFF_IA + (size_t)h * 163840 + rt * 2560 + hh * 320 + rc * 16;
    u32x4 a0, a1, b0, b1; float gate;
    asm volatile("global_load_dwordx4 %0, %5, off\n\tglobal_load_dwordx4 %1, %5, off offset:640\n\tglobal_load_dwordx4 %2, %5, off offset:1280\n\t"
                 "global_load_dwordx4 %3, %5, off offset:1920\n\tglobal_load_dword %4, %6, off"
                 : "=&v"(a0), "=&v"(a1), "=&v"(b0), "=&v"(b1), "=&v"(gate) : "v"(ap), "v"((const float*)(ws + OFF_G) + rt * 32 + r) : "memory");
#pragma unroll
    for (int i = 0; i < 3; ++i) glds16(kvb + (25 + i * 8 + wave) * 1024, lane * 16, lds0 + (25 + i * 8 + wave) * 1024);
    if (tid < 4) *(LAS unsigned*)(lds + tid * 4) = 0u;
    const u32x4 ONES = {0x3F803F80u, 0x3F803F80u, 0x3F803F80u, 0x3F803F80u};
    const f32x16 Z16 = {};
    wait_vm_bar<8>();
    PSTAMP(1, 1); WSTAMP(1);
    pt1 = (unsigned)__builtin_amdgcn_s_memrealtime();
    const int g = lane >> 4, q = (lane >> 2) & 3, p = lane & 3;
    const int tro = (g & 1) * 640 + (p & 1) * 320 + q * 16 + (p >> 1) * 8;
    const LAS char* L = (const LAS char*)lds;
    const LAS char* kmp = L + tro + hh * 128;
    const LAS char* ktp = L + tro + 256;
    const LAS char* vp  = L + 1280 + hh * 320 + r * 16;
    const int ent = tid < 126 ? tid : 125;
    u32x8 Q;
    {
        const u32x2 a = trd(kmp + L_SQ + wave * 1280), b = trd(kmp + L_SQ + wave * 1280 + 64), c = trd(ktp + L_SQ + wave * 1280);
        Q[0] = a[0]; Q[1] = a[1]; Q[2] = b[0]; Q[3] = b[1]; Q[4] = hh ? 0u : c[0]; Q[5] = hh ? 0u : c[1]; Q[6] = 0u; Q[7] = 0u;
    }
    const bf16x8 qm = B8(((u32x4){Q[0], Q[1], Q[2], Q[3]}));
    f32x32 S; f32x16 O = {}; u32x32 OPS; u32x8 P = {}; u32x4 AD;
    {
        const u32x2 a = trd(kmp + L_K0), b = trd(kmp + L_K0 + 64), c = trd(ktp + L_K0);
        const bf16x8 k0 = B8(((u32x4){a[0], a[1], b[0], b[1]})), k1 = B8(((u32x4){c[0], c[1], 0x3F80u, 0u}));
        const f32x16 sm = MFMA32(k0, qm, Z16);
        f32x16 s0 = MFMA32(k1, B8(((u32x4){Q[4], Q[5], 0u, 0u})), sm);
        float m = s0[0];
#pragma unroll
        for (int i = 1; i < 16; ++i) m = fmaxf(m, s0[i]);
        m = xhalf_max(m);
        Q[6] = hh ? 0u : (pk2(-m, 0.f) & 0xffffu);
        s0 = MFMA32(k1, B8(((u32x4){Q[4], Q[5], Q[6], 0u})), sm);
#pragma unroll
        for (int i = 0; i < 16; ++i) { S[i] = s0[i]; S[16 + i] = 0.f; }
        const unsigned vc = r == 20 ? 0x3F803F80u : 0u;
#pragma unroll
        for (int i = 0; i < 32; ++i) OPS[i] = (i & 8) ? vc : 0u;
        const u32x2 a1 = trd(kmp + 2560), b1 = trd(kmp + 2560 + 64), c1 = trd(ktp + 2560);
        OPS[0] = a1[0]; OPS[1] = a1[1]; OPS[2] = b1[0]; OPS[3] = b1[1]; OPS[4] = c1[0]; OPS[5] = c1[1]; OPS[6] = 0x3F80u; OPS[22] = 0x3F80u;
        if (r < 20) { const u32x4 v0 = *(const LAS u32x4*)vp; OPS[12] = v0[0]; OPS[13] = v0[1]; OPS[14] = v0[2]; OPS[15] = v0[3]; }
        AD[0] = (unsigned)(uintptr_t)kmp; AD[1] = (unsigned)(uintptr_t)ktp; AD[2] = (unsigned)(uintptr_t)vp; AD[3] = lane * 16;
    }
    {
        asm volatile("s_waitcnt vmcnt(3)" : "+v"(a0), "+v"(a1), "+v"(b0), "+v"(b1), "+v"(gate) :: "memory");
        if (r >= 20) { a0 = u32x4{0u, 0u, 0u, 0u}; a1 = a0; b0 = ONES; b1 = ONES; }
        f32x16 kvacc = MFMA32(B8(a0), B8(b0), Z16); kvacc = MFMA32(B8(a1), B8(b1), kvacc);
        if (r <= 20) {
            LAS f32x4* dst = (LAS f32x4*)(lds + L_SP + ((wave * 2 + hh) * 21 + r) * 48);
            dst[0] = f32x4{kvacc[0], kvacc[1], kvacc[2], kvacc[3]};
            dst[1] = f32x4{kvacc[4], kvacc[5], kvacc[6], kvacc[7]};
            dst[2] = f32x4{kvacc[8], kvacc[9], kvacc[10], kvacc[11]};
        }
    }
    const unsigned long long vmask = 0x000FFFFF000FFFFFull;
    const int j = wave & 3, isE = (wave >> 2) ^ 1;
    const char* wop = ws + OFF_WOT + (size_t)((cb * 4 + j) * 50) * 1024;
    const int flA = (int)OFF_FL + (oq * 4 + j) * 128, flv = (lane & 7) * 16;
    const int ov = ((g >> 1) * 1024 + (g & 1) * 512 + (lane & 15) * 16), os = (int)OFF_O + (rb * 160 + j * 10) * 1024;
    unsigned fl = 0u;
    u32x4 kp[8], wa[20], aa[2][10];
    const unsigned long long wsu = uni64(ws);
    const unsigned long long kb = uni64(ws + OFF_KVP + 4096 + h * 16384), kb2 = uni64(ws + OFF_KVP + 12288 + h * 16384);
    const unsigned long long ob[2][2] = {{uni64(ws + os + 4096), uni64(ws + os + 8192)}, {uni64(ws + os + 81920 + 4096), uni64(ws + os + 81920 + 8192)}};
#define LD_H(A, vo, hf, en, C) asm volatile("s_cmp_lg_u32 %13, 0\n\ts_cselect_b64 exec, -1, 0\n\t" \
        "global_load_dwordx4 %0, %10, %11 offset:-4096 sc1\n\tglobal_load_dwordx4 %1, %10, %11 offset:-3840 sc1\n\t" \
        "global_load_dwordx4 %2, %10, %11 offset:-2048 sc1\n\tglobal_load_dwordx4 %3, %10, %11 offset:-1792 sc1\n\t" \
        "global_load_dwordx4 %4, %10, %11 sc1\n\tglobal_load_dwordx4 %5, %10, %11 offset:256 sc1\n\t" \
        "global_load_dwordx4 %6, %10, %11 offset:2048 sc1\n\tglobal_load_dwordx4 %7, %10, %11 offset:2304 sc1\n\t" \
        "global_load_dwordx4 %8, %10, %12 sc1\n\tglobal_load_dwordx4 %9, %10, %12 offset:256 sc1\n\ts_mov_b64 exec, -1" \
        : C(A[0]), C(A[1]), C(A[2]), C(A[3]), C(A[4]), C(A[5]), C(A[6]), C(A[7]), C(A[8]), C(A[9]) \
        : "v"(vo), "s"(ob[hf][0]), "s"(ob[hf][1]), "s"(en) : "memory", "scc");
#define LD_W4(A, f) asm volatile("s_cmp_lg_u32 %6, 0\n\ts_cselect_b64 exec, -1, 0\n\t" \
        "global_load_dwordx4 %0, %4, %5\n\tglobal_load_dwordx4 %1, %4, %5 offset:1024\n\t" \
        "global_load_dwordx4 %2, %4, %5 offset:2048\n\tglobal_load_dwordx4 %3, %4, %5 offset:3072\n\ts_mov_b64 exec, -1" \
        : "=&v"((A)[0]), "=&v"((A)[1]), "=&v"((A)[2]), "=&v"((A)[3]) : "v"(lane * 16), "s"(uni64(wop + (f) * 1024)), "s"(isE) : "memory", "scc");
#define LD_KP(TAIL) asm volatile("s_cmp_lt_u32 %11, 2\n\ts_cselect_b64 exec, -1, 0\n\t" \
        "global_load_dwordx4 %0, %8, %9 offset:-4096 sc1\n\tglobal_load_dwordx4 %1, %8, %9 offset:-2048 sc1\n\t" \
        "global_load_dwordx4 %2, %8, %9 sc1\n\tglobal_load_dwordx4 %3, %8, %9 offset:2048 sc1\n\t" \
        "global_load_dwordx4 %4, %8, %10 offset:-4096 sc1\n\tglobal_load_dwordx4 %5, %8, %10 offset:-2048 sc1\n\t" \
        "global_load_dwordx4 %6, %8, %10 sc1\n\tglobal_load_dwordx4 %7, %8, %10 offset:2048 sc1\n\t" \
        "s_mov_b64 exec, -1" TAIL \
        : "=&v"(kp[0]), "=&v"(kp[1]), "=&v"(kp[2]), "=&v"(kp[3]), "=&v"(kp[4]), "=&v"(kp[5]), "=&v"(kp[6]), "=&v"(kp[7]) \
        : "v"(ent * 16), "s"(kb), "s"(kb2), "s"(wave) : "memory", "scc")
#pragma unroll
    for (int c = 0; c < 8; ++c) {
        unsigned ma, mb; unsigned long long ga, gb;
        if (c < 3) {
            const int lim = (c == 1 ? L_SP : 163840) - 3072;
            const int oa = (c == 2 ? L_SP : 50176 + c * 49152) + wave * 3072, ob = oa + 24576 < lim ? oa + 24576 : lim;
            ma = lds0 + oa; mb = lds0 + ob; ga = uni64(kvb + oa); gb = uni64(kvb + ob);
        } else {
            ma = lds0 + (c == 7 ? 1024 + j * 3072 : L_WO + ((c - 3) * 8 + wr) * 3072); mb = ma;
            ga = uni64(wop + (c == 7 ? 44 : 20 + (wave >> 2) * 12 + 3 * (c - 3)) * 1024); gb = ga;
        }
        CSTAMP(2 * c);
        unsigned cnt;
        if (c == 0) cnt = attn_chunk<true>(S, O, OPS, P, AD, Q, vmask, rfl(ma), ga, rfl(mb), gb, 1u);
        else cnt = attn_chunk<false>(S, O, OPS, P, AD, Q, vmask, rfl(ma), ga, rfl(mb), gb, c < 3 ? 1u : c < 7 ? 0u : (unsigned)(2 - 2 * isE));
        CSTAMP(2 * c + 1);
        if (c < 7) wait_cnt(lds, 0, cnt, 8 * (c + 1));
        if (c == FILL_C && c != 1) empty_o(rs, rt, h, lane);
        if (c == 0) {
            if (wave < 2) {
                f32x4 s = *(const LAS f32x4*)(L + L_SP + ent * 16);
#pragma unroll
                for (int w = 1; w < 8; ++w) s += *(const LAS f32x4*)(L + L_SP + w * 2016 + ent * 16);
                const u32x4 v = __builtin_bit_cast(u32x4, s);
                if (tid < 126) __builtin_amdgcn_raw_buffer_store_b128(u32x4{full(v[0]), full(v[1]), full(v[2]), full(v[3])}, rs, tid * 16, (int)OFF_KVP + (h * 8 + qb) * 2048, SC1);
            }
            glds16(qim + wr * 2560 + 1280, lane * 16, lds0 + L_QA + wave * 1280);
            glds16(qim + wr * 2560 + 1280 + 256, lane * 16, lds0 + L_QA + wave * 1280 + 256);
        } else if (c == 1) {
            LD_KP("");
            if (FILL_C == 1) empty_o(rs, rt, h, lane);
        } else if (c == 2) {
#pragma unroll
            for (int j = 0; j < 8; ++j) landed(kp[j]);
            if (wave < 2) {
#pragma clang loop unroll(disable)
                for (int spin = 0; spin < (1 << 20); ++spin) {
                    unsigned m = 0u;
#pragma unroll
                    for (int j = 0; j < 8; ++j) m = umax4(m, kp[j]);
                    if (__builtin_expect(__all(m != EMPTY), 1)) break;
                    LD_KP("\n\ts_waitcnt vmcnt(0)");
                }
                f32x4 s = __builtin_bit_cast(f32x4, kp[0]);
#pragma unroll
                for (int j = 1; j < 8; ++j) s += __builtin_bit_cast(f32x4, kp[j]);
                const int e3 = ent / 3;
                *(LAS u32x2*)(lds + L_KVF + e3 * 32 + (ent - e3 * 3) * 8) = u32x2{pk2(s[0], s[1]), pk2(s[2], s[3])};
            }
        } else if (c == 3) {
            const int rr = r <= 20 ? r : 20;
            const u32x4 ka0 = *(const LAS u32x4*)(L + L_KVF + (hh * 21 + rr) * 32);
            const u32x2 ka1 = *(const LAS u32x2*)(L + L_KVF + (hh * 21 + rr) * 32 + 16);
            const LAS char* qa = L + L_QA + wave * 1280 + tro;
            const u32x2 pq0 = trd(qa + hh * 64), pq1 = trd(qa + 128 + hh * 64);
            u32x2 pq2 = trd(qa + 256);
            if (hh) pq2 = u32x2{0u, 0u};
            f32x16 oa = MFMA32(B8(ka0), B8(((u32x4){pq0[0], pq0[1], pq1[0], pq1[1]})), Z16);
            oa = MFMA32(B8(((u32x4){ka1[0], ka1[1], 0u, 0u})), B8(((u32x4){pq2[0], pq2[1], 0u, 0u})), oa);
            const float den = hi_half(oa[8]) + 1e-6f;
            store_o(rs, oa, gate * __builtin_amdgcn_rcpf(den), rt, 0, h, lane);
            LD_W4(wa, 0) LD_W4(wa + 4, 4)
        } else if (c == 4) {
            if (tid == 0) set_flag(rs, (int)OFF_FL + (qb * 4 + (h >> 3)) * 128 + (h & 7) * 16);
            LD_W4(wa + 8, 8) LD_W4(wa + 12, 12)
        } else if (c == 5) {
            fl = a_ld1(wsu, flA + flv);
            LD_W4(wa + 16, 16)
        } else if (c == 6) {
            landed(fl);
            if (wave < 4) wait_flag(rs, flv, flA, fl);
            LD_H(aa[0], ov + 40960, 0, isE, "=&v")
        }
    }
#pragma unroll
    for (int i = 0; i < 10; ++i) { landed(wa[i]); landed(wa[10 + i]); landed(aa[0][i]); }
    u32x4 wd[4];
    LD_H(aa[1], ov + 40960, 1, isE, "=&v") LD_W4(wd, 46)
    if (lane == 0) set_flag(rs, (int)OFF_FL + (32 + rt * 4 + (h >> 3)) * 128 + (h & 7) * 16);
    O = MFMA32(B8(((u32x4){OPS[8], OPS[9], OPS[10], OPS[11]})), B8(((u32x4){P[4], P[5], P[6], P[7]})), O);
    PSTAMP(1, 2); WSTAMP(2);
    pt2 = (unsigned)__builtin_amdgcn_s_memrealtime();
    float l = hi_half(O[8]);
    {
        bool bad = !(l >= 1e-18f && l <= 1.0e30f);
#pragma unroll
        for (int i = 0; i < 12; ++i) bad = bad || !(fabsf(O[i]) <= 3.0e38f);
        if (__builtin_expect(__any(bad), 0)) {
            const bf16x8 qt0 = B8(((u32x4){Q[4], Q[5], 0u, 0u}));
            float m = -INFINITY; O = Z16;
            int tl = tid;
            asm volatile("" : "+v"(tl));
            const int r = tl & 31, hh = (tl >> 5) & 1, rc = r < 20 ? r : 19;
            const int ko = (r >> 4) * 640 + ((r >> 2) & 1) * 320 + ((r >> 3) & 1) * 8 + (r & 3) * 2;
            for (int t = 0; t < 64; ++t) {
                const char* tb = kvb + t * 2560;
                unsigned kw[6];
#pragma unroll
                for (int i = 0; i < 6; ++i) {
                    const int d = i < 4 ? 8 * hh + 2 * i : 16 + 2 * (i - 4);
                    kw[i] = (unsigned)*(const u16*)(tb + ko + d * 16) | ((unsigned)*(const u16*)(tb + ko + d * 16 + 16) << 16);
                }
                u32x4 f0 = *(const u32x4*)(tb + 1280 + hh * 320 + rc * 16), f1 = *(const u32x4*)(tb + 1920 + hh * 320 + rc * 16);
                if (r >= 20) { f0 = ONES; f1 = ONES; }
                f32x16 s = MFMA32(B8(((u32x4){kw[0], kw[1], kw[2], kw[3]})), qm, Z16);
                s = MFMA32(B8(((u32x4){kw[4], kw[5], 0u, 0u})), qt0, s);
                float tm = s[0];
#pragma unroll
                for (int i = 1; i < 16; ++i) tm = fmaxf(tm, s[i]);
                tm = xhalf_max(tm);
                const float mn = fmaxf(m, tm);
                const float al = (m == -INFINITY) ? 0.f : __builtin_amdgcn_exp2f(m - mn);
                m = mn;
#pragma unroll
                for (int i = 0; i < 16; ++i) { O[i] *= al; s[i] = __builtin_amdgcn_exp2f(s[i] - mn); }
                u32x4 p0 = {pk2(s[0], s[1]), pk2(s[2], s[3]), pk2(s[4], s[5]), pk2(s[6], s[7])};
                u32x4 p1 = {pk2(s[8], s[9]), pk2(s[10], s[11]), pk2(s[12], s[13]), pk2(s[14], s[15])};
                O = MFMA32(B8(f0), B8(p0), O); O = MFMA32(B8(f1), B8(p1), O);
            }
            l = hi_half(O[8]);
        }
    }
    store_o(rs, O, (1.f - gate) * __builtin_amdgcn_rcpf(l), rt, 1, h, lane);
    __builtin_amdgcn_s_setprio(0);
    __builtin_amdgcn_sched_barrier(0);
    WSTAMP(3);
    constexpr int L_P0 = L_WO + 98304;
    if (wave < 4) {
        const LAS char* wl = L + j * 6144 + lane * 16;
        auto wfr = [&](int f) -> bf16x8 {
            if (f < 20) return B8(wa[f]);
            if (f < 44) return *(const LAS bf16x8*)(wl + L_WO + (((f - 20) % 12) / 3) * 24576 + ((f - 20) / 12) * 3072 + ((f - 20) % 3) * 1024);
            if (f < 46) return *(const LAS bf16x8*)(L + 1024 + j * 3072 + (f - 44) * 1024 + lane * 16);
            return B8(wd[f - 46]);
        };
        LAS char* wp = (LAS char*)lds + (lane & 15) * 320 + ((g ^ ((lane >> 1) & 3)) << 4);
        f32x4 acc[4][5] = {};
        {
            bf16x8 w4[5];
#pragma unroll
            for (int ni = 0; ni < 5; ++ni) w4[ni] = wfr(20 + ni);
#pragma unroll
            for (int hf = 0; hf < 2; ++hf) {
                if (hf) asm volatile("s_waitcnt vmcnt(3)" : "+v"(aa[1][0]), "+v"(aa[1][1]), "+v"(aa[1][2]), "+v"(aa[1][3]), "+v"(aa[1][4]), "+v"(aa[1][5]), "+v"(aa[1][6]),
                                     "+v"(aa[1][7]), "+v"(aa[1][8]), "+v"(aa[1][9]), "+v"(wd[0]), "+v"(wd[1]), "+v"(wd[2]), "+v"(wd[3]) :: "memory");
#pragma unroll
                for (int k = 0; k < 5; ++k)
#pragma unroll
                    for (int mi = 0; mi < 2; ++mi)
#pragma unroll
                        for (int ni = 0; ni < 5; ++ni) acc[hf * 2 + mi][ni] = MFMA16(k < 4 ? wfr(k * 5 + ni) : w4[ni], B8(aa[hf][2 * k + mi]), acc[hf * 2 + mi][ni]);
                __builtin_amdgcn_sched_barrier(0);
            }
        }
        const int flB = (int)OFF_FL + (32 + rb * 8 + j) * 128;
        u32x4 b0[10], b1[10];
        wait_flag(rs, flv, flB, get_flag(rs, flv, flB));
        WSTAMP(4);
        LD_H(b0, ov, 0, 1, "=&v")
        fl = a_ld1(wsu, flB + 512 + flv);
        wait_cnt(lds, 0, 64);
        asm volatile("s_waitcnt vmcnt(0)" : "+v"(fl) :: "memory");
        wait_flag(rs, flv, flB + 512, fl);
        WSTAMP(5);
        LD_H(b1, ov, 1, 1, "=&v")
#pragma clang loop unroll(disable)
        for (int spin = 0; spin < (1 << 20); ++spin) {
            unsigned m = 0u;
#pragma unroll
            for (int i = 0; i < 10; ++i) { landed(b0[i]); m = umax4(m, b0[i]); }
            if (__builtin_expect(__all(m != EMPTY), 1)) break;
            LD_H(b0, ov, 0, 1, "+v")
            asm volatile("s_waitcnt vmcnt(0)" ::: "memory");
        }
        bf16x8 wb[25];
#pragma unroll
        for (int k = 0; k < 5; ++k) {
#pragma unroll
            for (int ni = 0; ni < 5; ++ni) wb[k * 5 + ni] = wfr(25 + k * 5 + ni);
#pragma unroll
            for (int mi = 0; mi < 2; ++mi)
#pragma unroll
                for (int ni = 0; ni < 5; ++ni) acc[mi][ni] = MFMA16(wb[k * 5 + ni], B8(b0[2 * k + mi]), acc[mi][ni]);
        }
#pragma unroll
        for (int mi = 0; mi < 2; ++mi)
#pragma unroll
            for (int ni = 0; ni < 5; ++ni) *(LAS f32x4*)(wp + L_P0 + j * 10240 + mi * 5120 + ni * 64) = acc[mi][ni];
        add_cnt<12>(lds0);
        f32x4 ac1[2][5];
#pragma clang loop unroll(disable)
        for (int spin = 0; spin < (1 << 20); ++spin) {
            unsigned m = 0u;
#pragma unroll
            for (int k = 0; k < 5; ++k) {
                asm volatile("s_waitcnt vmcnt(%2)" : "+v"(b1[2 * k]), "+v"(b1[2 * k + 1]) : "n"(8 - 2 * k) : "memory");
#pragma unroll
                for (int mi = 0; mi < 2; ++mi)
#pragma unroll
                    for (int ni = 0; ni < 5; ++ni) ac1[mi][ni] = MFMA16(wb[k * 5 + ni], B8(b1[2 * k + mi]), k ? ac1[mi][ni] : acc[2 + mi][ni]);
                m = umax4(umax4(m, b1[2 * k]), b1[2 * k + 1]);
                __builtin_amdgcn_sched_barrier(0);
            }
            if (__builtin_expect(__all(m != EMPTY), 1)) break;
            LD_H(b1, ov, 1, 1, "+v")
        }
#pragma unroll
        for (int mi = 0; mi < 2; ++mi)
#pragma unroll
            for (int ni = 0; ni < 5; ++ni) *(LAS f32x4*)(wp + L_WO + j * 6144 + mi * 24576 + ni * 64) = ac1[mi][ni];
        WSTAMP(6);
        asm volatile("s_waitcnt lgkmcnt(0)\n\ts_barrier" ::: "memory");
    } else {
        const int t = tid - 256;
        const LAS char* rp[2][3]; unsigned oo[3];
#pragma unroll
        for (int i = 0; i < 3; ++i) {
            const int id = i < 2 || t < 128 ? i * 256 + t : 1, row = id / 20, cs = id - row * 20, cc = cs ^ ((row >> 1) & 3);
            rp[0][i] = L + L_P0 + id * 16; rp[1][i] = L + L_WO + id * 16 + (id >= 320 ? 24576 - 5120 : 0);
            oo[i] = (unsigned)(((rb * 64 + row) * D_ + cb * 80 + cc * 4) * 2);
            landed(rp[0][i]); landed(rp[1][i]); landed(oo[i]);
        }
        WSTAMP(4);
#pragma unroll
        for (int hf = 0; hf < 2; ++hf) {
            if (hf == 0) { wait_cnt(lds, 12, 4); WSTAMP(5); }
            else { WSTAMP(6); asm volatile("s_barrier" ::: "memory"); }
            f32x4 pv[3][4];
#pragma unroll
            for (int i = 0; i < 3; ++i)
#pragma unroll
                for (int w = 0; w < 4; ++w) pv[i][w] = *(const LAS f32x4*)(rp[hf][i] + w * (hf ? 6144 : 10240));
            __builtin_amdgcn_sched_barrier(0);
#pragma unroll
            for (int i = 0; i < 3; ++i) {
                const f32x4 v = (pv[i][0] + pv[i][1]) + (pv[i][2] + pv[i][3]);
                if (i < 2 || t < 128) __builtin_amdgcn_raw_buffer_store_b64(u32x2{pk2(v[0], v[1]), pk2(v[2], v[3])},
                    __builtin_amdgcn_make_buffer_rsrc((void*)out, 0, S_ * D_ * 2, 0x00020000), oo[i], hf * (32 * D_ * 2), OUT_AUX);
            }
        }
    }
    if (bid == 0 && wave == 4) {
        const unsigned pt3 = (unsigned)__builtin_amdgcn_s_memrealtime(), pc3 = (unsigned)__builtin_amdgcn_s_memtime();
        const unsigned* pp = (const unsigned*)(ws + (64 << 20));
        int mn = 1 << 30, mx = -(1 << 30);
        for (int i = 0; i < 4; ++i) { const int b = i * 64 + (threadIdx.x & 63); mn = min(mn, (int)(pp[b * 2] - pt0)); mx = max(mx, (int)(pp[b * 2 + 1] - pt0)); }
        for (int o = 1; o < 64; o <<= 1) { mn = min(mn, __shfl_xor(mn, o)); mx = max(mx, __shfl_xor(mx, o)); }
        int X = PROBE == 0 ? 0 : PROBE == 1 ? mx - mn : PROBE == 2 ? -mx : PROBE == 3 ? (int)(pt3 - pt0) : PROBE == 4 ? (int)(pc3 - pc0) / 20 :
                PROBE == 5 ? (int)(pt1 - pt0) : PROBE == 6 ? (int)(pt2 - pt1) : PROBE == 7 ? (int)(pt3 - pt2) : (int)(pt3 - pt0) - mn;
        if (PROBE >= 100) {
            int sm = 0, mz = -(1 << 30);
            for (int i = 0; i < 4; ++i) { const int b = i * 64 + (threadIdx.x & 63);
                const int v = (int)((PROBE == 121 ? pp[b * 2] : PROBE >= 122 ? pp[b * 2 + 1] : pp[512 + b]) - pt0) - mn;
                if (b < 240) sm += v; else mz = max(mz, v); }
            for (int o = 1; o < 64; o <<= 1) { sm += __shfl_xor(sm, o); mz = max(mz, __shfl_xor(mz, o)); }
            X = 4 * (PROBE == 130 ? mz : sm / 240);
        }
        X = rfl(min(max(X, 0), 12000));
        while ((int)((unsigned)__builtin_amdgcn_s_memrealtime() - pt3) < 300 + 4 * X) __builtin_amdgcn_s_sleep(1);
    }
}

extern "C" void kernel_launch(void* const* d_in, const int* in_sizes, int n_in, void* d_out, int out_size, void* d_ws, size_t ws_size,
                              hipStream_t stream) {
    const u16* x = (const u16*)d_in[0];
    const u16* wA = (const u16*)d_in[1];
    const u16* woA = (const u16*)d_in[2];
    const u16* wB = (const u16*)d_in[4];
    const u16* woB = (const u16*)d_in[5];
    const u16* gw = (const u16*)d_in[6];
    char* ws = (char*)d_ws;
    (void)hipFuncSetAttribute((const void*)k1_qkv, hipFuncAttributeMaxDynamicSharedMemorySize, LDS_BYTES);
    (void)hipFuncSetAttribute((const void*)k2_attn, hipFuncAttributeMaxDynamicSharedMemorySize, LDS_BYTES);
    k1_qkv<<<256, 512, LDS_BYTES, stream>>>(x, wA, wB, woA, woB, gw, ws);
    k2_attn<<<256, 512, LDS_BYTES, stream>>>(ws, (u16*)d_out);
}
```

```cpp
#define PROBE 205
#define G1_TL 0
#define OUT_AUX 16
#include <hip/hip_runtime.h>
#include <hip/hip_bf16.h>
#include <stdint.h>

typedef short    bf16x8 __attribute__((ext_vector_type(8)));
typedef short    s16x4  __attribute__((ext_vector_type(4)));
typedef float    f32x2  __attribute__((ext_vector_type(2)));
typedef float    f32x4  __attribute__((ext_vector_type(4)));
typedef float    f32x16 __attribute__((ext_vector_type(16)));
typedef float    f32x32 __attribute__((ext_vector_type(32)));
typedef unsigned u32x2  __attribute__((ext_vector_type(2)));
typedef unsigned u32x4  __attribute__((ext_vector_type(4)));
typedef unsigned u32x8  __attribute__((ext_vector_type(8)));
typedef unsigned u32x32 __attribute__((ext_vector_type(32)));
typedef __bf16   bf2_t  __attribute__((ext_vector_type(2)));
typedef unsigned short u16;
#define LAS __attribute__((address_space(3)))
#define DI __device__ __forceinline__

constexpr int S_ = 2048, D_ = 640;
constexpr size_t IMG     = (size_t)32 * 163840;
constexpr size_t OFF_IQ  = 0;
constexpr size_t OFF_IB  = IMG;
constexpr size_t OFF_IA  = 2 * IMG;
constexpr size_t OFF_O   = 3 * IMG;
constexpr size_t OFF_WOT = 4 * IMG;
constexpr size_t OFF_G   = OFF_WOT + (size_t)1600 * 1024;
constexpr size_t OFF_KVP = OFF_G + 8192;
constexpr size_t OFF_FL  = OFF_KVP + (size_t)256 * 2048;
constexpr int    WS_USED = (int)(OFF_FL + 288 * 128);
constexpr unsigned EMPTY = 0xFFFFFFFFu;
typedef __amdgpu_buffer_rsrc_t rsrc_t;
constexpr int SC1 = 16;

#define CSTAMP(i)
#define PSTAMP(k, i)
#define WSTAMP(i)

DI unsigned pk2(float lo, float hi) { f32x2 v = {lo, hi}; return __builtin_bit_cast(unsigned, __builtin_convertvector(v, bf2_t)); }
DI float bflo(unsigned v) { return __uint_as_float(v << 16); }
DI float bfhi(unsigned v) { return __uint_as_float(v & 0xffff0000u); }
DI int rfl(int x) { return __builtin_amdgcn_readfirstlane(x); }
DI void glds16(const void* sbase, unsigned voff, unsigned lds_base) {
    const unsigned long long a = (unsigned long long)(uintptr_t)sbase;
    const unsigned long long u = (unsigned long long)(unsigned)rfl((int)(unsigned)a) | ((unsigned long long)(unsigned)rfl((int)(unsigned)(a >> 32)) << 32);
    asm volatile("s_mov_b32 m0, %0\n\ts_nop 0\n\tglobal_load_lds_dwordx4 %1, %2" :: "s"(rfl((int)lds_base)), "v"(voff), "s"(u) : "memory");
}
#define MFMA16(a, b, c) __builtin_amdgcn_mfma_f32_16x16x32_bf16((a), (b), (c), 0, 0, 0)
#define MFMA32(a, b, c) __builtin_amdgcn_mfma_f32_32x32x16_bf16((a), (b), (c), 0, 0, 0)
#define B8(x) __builtin_bit_cast(bf16x8, x)
DI u32x2 trd(const LAS char* p) { return __builtin_bit_cast(u32x2, __builtin_amdgcn_ds_read_tr16_b64_v4i16((LAS s16x4*)p)); }
DI float pmax(float t, float c) { return __int_as_float(max(__float_as_int(t), __float_as_int(c))); }
template <int N> DI void wait_vm_bar() { asm volatile("s_waitcnt vmcnt(%0) lgkmcnt(0)\n\ts_barrier" :: "n"(N) : "memory"); }

constexpr int LDS_BYTES = 163840;
constexpr float LOG2E = 1.4426950408889634f;

DI void p1_aux(const u16* __restrict__ x, const u16* __restrict__ woA, const u16* __restrict__ woB, const u16* __restrict__ gate_w,
               char* __restrict__ ws, char* lds, int a) {
    const int tid = threadIdx.x, lane = tid & 63, wave = rfl(tid >> 6);
    const unsigned lds0 = (unsigned)(uintptr_t)lds;
    const int br = a >> 3, u = a & 7;
    if (tid < 144) *(u32x4*)(ws + OFF_FL + (a * 144 + tid) * 16) = u32x4{0u, 0u, 0u, 0u};
    const char* wsrc = (const char*)(br ? woB : woA) + (size_t)u * 102400;
    for (int i = wave; i < 100; i += 8) glds16(wsrc + i * 1024, lane * 16, lds0 + i * 1024);
    const u32x4* xs = (const u32x4*)(x + (size_t)(a * 128 + wave * 16) * D_);
    u32x4 xv[20], gw[5];
#pragma unroll
    for (int i = 0; i < 20; ++i) xv[i] = xs[i * 64 + lane];
#pragma unroll
    for (int i = 0; i < 5; ++i) gw[i] = ((const u32x4*)gate_w)[(i * 64 + lane) % 80];
    {
        const rsrc_t rs = __builtin_amdgcn_make_buffer_rsrc((void*)(ws + OFF_KVP + a * 32768), 0, 32768, 0x00020000);
#pragma unroll
        for (int i = 0; i < 4; ++i) __builtin_amdgcn_raw_buffer_store_b128(u32x4{EMPTY, EMPTY, EMPTY, EMPTY}, rs, tid * 16 + i * 8192, 0, SC1);
    }
    LAS float* part = (LAS float*)(lds + 102400) + wave * 1280;
#pragma unroll
    for (int i = 0; i < 20; ++i) {
        float s = 0.f;
#pragma unroll
        for (int j = 0; j < 4; ++j) s += bflo(xv[i][j]) * bflo(gw[i % 5][j]) + bfhi(xv[i][j]) * bfhi(gw[i % 5][j]);
        part[i * 64 + lane] = s;
    }
    wait_vm_bar<0>();
    {
        float s = 0.f;
#pragma unroll
        for (int j = 0; j < 20; ++j) s += part[lane * 20 + j];
        s += __shfl_xor(s, 1); s += __shfl_xor(s, 2);
        if ((lane & 3) == 0) ((float*)(ws + OFF_G))[a * 128 + wave * 16 + (lane >> 2)] = 1.f / (1.f + __expf(-s));
    }
    const int g = lane >> 4, hh = g & 1, ns = g >> 1, q = (lane >> 2) & 3, p = lane & 3;
    for (int un = wave; un < 100; un += 8) {
        const int e = un / 20, np = un - e * 20, nt = np * 2 + ns;
        const int r0 = e < 4 ? e * 20 + 4 * hh : (2 * hh) * 20 + 16;
        const int r1 = e < 4 ? r0 + 8 : r0 + 20;
        const int ks16 = e < 4 ? (u & 1) * 4 + e : 8 + (u & 1), cbk = nt / 5;
        const u32x2 lo = trd((const LAS char*)lds + (r0 + q) * 1280 + nt * 32 + p * 8);
        const u32x2 hi = trd((const LAS char*)lds + (r1 + q) * 1280 + nt * 32 + p * 8);
        *(u32x4*)(ws + OFF_WOT + (size_t)((cbk * 8 + (u & 6) + br) * 25 + (ks16 >> 1) * 5 + nt - cbk * 5) * 1024 + ((ks16 & 1) * 2 + hh) * 256 + (lane & 15) * 16) = u32x4{lo[0], lo[1], hi[0], hi[1]};
    }
}

#define G1_D 3
#define G1_D0 2
#define G1_DT 10
#define G1_DE 0
#define G1_NX 4
constexpr int G1_NW = 10 - G1_NX, G1_HB = 20 - 2 * G1_TL, G1_WOFF = G1_NX * 16384;
constexpr int g1_t(int h) { return h < 2 * G1_HB ? h >> 1 : h < 2 * G1_HB + 2 * G1_TL ? h - G1_HB : h - G1_HB - 2 * G1_TL; }
constexpr int g1_m(int h) { return h < 2 * G1_HB ? h & 1 : h < 2 * G1_HB + 2 * G1_TL ? 0 : 1; }
constexpr int g1_xi(int h) { return h < 2 * G1_HB ? (h >> 1) & 1 : h & 1; }
constexpr int g1_free(int i) { return i < 10 ? (i < G1_NX ? -1 : 2 * (i - G1_NX) + 2) : (i - 10 < G1_NW ? -1 : i - 10 - G1_NW + 1); }
struct G1Plan { int seq[30]; int cnt[22]; int pos[30]; };
constexpr G1Plan g1_plan() {
    G1Plan p = {}; bool done[30] = {}, seen[10] = {}; int ord[30] = {}, need[21] = {}, n = 0;
    for (int b = 0; b < 20; ++b) { const int u = g1_t(2 * b) >> 1; if (!seen[u]) { seen[u] = true; ord[n++] = u; } need[b] = n; ord[n++] = 10 + b; }
    need[20] = 30; n = 0;
    for (int b = 0; b <= 20; ++b) {
        const int ex = b > G1_DT ? b - G1_DT : 0, lim = 3 * (b >> 1) + 1 + (b & 1) + (b == 0 ? G1_D0 : G1_D) + (ex < G1_DE ? ex : G1_DE);
        for (int k = 0; k < 30 && k <= lim; ++k) { const int i = ord[k]; if (!done[i] && g1_free(i) < b) { done[i] = true; p.pos[i] = n; p.seq[n++] = i; } }
        p.cnt[b] = n;
    }
    return p;
}
constexpr G1Plan G1P = g1_plan();
constexpr int g1_wait(int b) { const int x = G1P.pos[g1_t(2 * b) >> 1], w = G1P.pos[10 + b]; return 4 * (G1P.cnt[b] - 1 - (x > w ? x : w)); }
constexpr bool g1_ok() { for (int b = 0; b < 20; ++b) if (g1_wait(b) < 0 || g1_wait(b) > 60) return false; return G1P.cnt[19] == 30 && G1_NX >= G1_TL && G1_NX <= 4; }
static_assert(g1_ok(), "loader plan");
#define G1_ROT ((0x27057250 >> (4 * xcd)) & 15)

#define LST_P
#define LST_A
template <int T> struct G1Load {
    template <class FX, class FW> static DI void run(FX&& ldx, FW&& ldw LST_P) {
        asm volatile("s_waitcnt vmcnt(%0)\n\ts_barrier" :: "n"(g1_wait(T)) : "memory");
        unsigned long long ptm = 0;
        if (T == PROBE - 100) asm volatile("s_memrealtime %0\n\ts_waitcnt lgkmcnt(0)" : "=s"(ptm));
        if (T == PROBE - 200) asm volatile("s_memtime %0\n\ts_waitcnt lgkmcnt(0)" : "=s"(ptm));
#pragma unroll
        for (int n = G1P.cnt[T]; n < G1P.cnt[T + 1]; ++n) { const int i = G1P.seq[n]; if (i < 10) ldx(i); else ldw(i - 10); }
        G1Load<T + 1>::run(ldx, ldw LST_A);
        if (T == PROBE - 100 || T == PROBE - 200) ldx(-1, (unsigned)ptm);
    }
};
template <> struct G1Load<20> { template <class FX, class FW> static DI void run(FX&&, FW&& LST_P) {} };

struct G1Ctx {
    f32x4 acc[4][8];
    bf16x8 x[2][4]; u32x2 w[2][4][2];
    const LAS char* xa[2]; const LAS char* wa[8];
    int so[4];
    rsrc_t rB; float sc; f32x2 m[4]; u32x4 o;
};
#define SB0() __builtin_amdgcn_sched_barrier(0)
#define G1_SP 12
template <int J, int I> DI void g1_side(G1Ctx& c) {
    constexpr int K = (J - (40 - 2 * G1_TL)) * 16 + I;
    if (K > 0 && K <= 8 * G1_SP && (K - 1) % G1_SP == 8) asm volatile("" :: "v"(c.o));
    if (K >= 0 && K < 8 * G1_SP && K % G1_SP < 9) {
        constexpr int U = K < 0 ? 0 : K / G1_SP, e = K < 0 ? 0 : K % G1_SP, ni = (U >> 1) & 3, a = U & 1;
        constexpr int mi = e == 0 ? 0 : e == 1 ? 1 : e == 3 ? 2 : e == 5 ? 3 : -1, ci = e == 2 ? 0 : e == 4 ? 1 : e == 6 ? 2 : e == 7 ? 3 : -1;
        if (mi >= 0) { const f32x4 v = c.acc[2 * a + ((mi & 3) >> 1)][ni]; c.m[mi & 3] = f32x2{v[(mi & 1) * 2], v[(mi & 1) * 2 + 1]} * c.sc; }
        if (ci >= 0) c.o[ci & 3] = pk2(c.m[ci & 3][0], c.m[ci & 3][1]);
        if (e == 8) __builtin_amdgcn_raw_buffer_store_b128(c.o, c.rB, c.so[ni] + a * 2560, 0, SC1);
        SB0();
    }
}
template <int J> struct G1Step {
    static DI void run(G1Ctx& c) {
        constexpr int N = J + 1, t = g1_t(N), xo = ((t >> 1) % G1_NX) * 16384, sl = (N >> 1) % G1_NW, wo = (sl & 3) * 16384 + (N & 1) * 8192, wi = (sl >> 2) * 4;
        constexpr int ps = J & 1, ma = g1_m(J) * 4, xc = g1_xi(J), xn = g1_xi(N);
#define G1_M(i) c.acc[(i) & 3][ma + ((i) >> 2)] = MFMA16(c.x[xc][(i) & 3], B8(((u32x4){c.w[ps][(i) >> 2][0][0], c.w[ps][(i) >> 2][0][1], c.w[ps][(i) >> 2][1][0], c.w[ps][(i) >> 2][1][1]})), \
                c.acc[(i) & 3][ma + ((i) >> 2)]); SB0(); g1_side<J, i>(c);
#define G1_X(i) if (N < 40) c.x[xn][i] = *(const LAS bf16x8*)(c.xa[t & 1] + xo + ((i) >> 1) * 4096 + ((i) & 1) * 1024); SB0();
#define G1_W(i, e) if (N < 40) c.w[ps ^ 1][i][e] = trd(c.wa[wi + (i)] + wo + (e) * 1024); SB0();
        if (ps) {
            G1_M(0) G1_M(1) G1_M(2)
            if (N < 40) asm volatile("s_waitcnt lgkmcnt(0)\n\ts_barrier" ::: "memory");
            SB0();
            G1_X(0) G1_M(3) G1_W(0, 0) G1_M(4) G1_W(0, 1) G1_M(5) G1_X(1) G1_M(6) G1_X(2) G1_M(7) G1_X(3) G1_M(8) G1_W(1, 0) G1_M(9) G1_W(1, 1) G1_M(10)
            G1_W(2, 0) G1_M(11) G1_W(2, 1) G1_M(12) G1_W(3, 0) G1_M(13) G1_W(3, 1) G1_M(14) G1_M(15)
        } else if (xn != xc) {
            G1_M(0) G1_X(0) G1_M(1) G1_W(0, 0) G1_M(2) G1_W(0, 1) G1_M(3) G1_X(1) G1_M(4) G1_X(2) G1_M(5) G1_X(3) G1_M(6) G1_W(1, 0) G1_M(7) G1_W(1, 1)
            G1_M(8) G1_W(2, 0) G1_M(9) G1_W(2, 1) G1_M(10) G1_W(3, 0) G1_M(11) G1_W(3, 1) G1_M(12) G1_M(13) G1_M(14) G1_M(15)
        } else {
            G1_M(0) G1_W(0, 0) G1_M(1) G1_W(0, 1) G1_M(2) G1_W(1, 0) G1_M(3) G1_W(1, 1) G1_M(4) G1_W(2, 0) G1_M(5) G1_W(2, 1) G1_M(6) G1_W(3, 0) G1_M(7) G1_W(3, 1)
            G1_M(8) G1_M(9) G1_M(10) G1_M(11) G1_M(12) G1_M(13) G1_M(14) G1_M(15)
        }
        G1Step<J + 1>::run(c);
    }
};
template <> struct G1Step<40> { static DI void run(G1Ctx&) {} };
template <int K> struct G1Rest { static DI void run(G1Ctx& c) { g1_side<40 - 2 * G1_TL + K / 16, K % 16>(c); G1Rest<K + 1>::run(c); } };
template <> struct G1Rest<8 * G1_SP> { static DI void run(G1Ctx&) {} };

DI void phase1(const u16* __restrict__ x, const u16* __restrict__ wA, const u16* __restrict__ wB, char* __restrict__ ws, char* lds, int bid) {
    const int tid = threadIdx.x, lane = tid & 63, wave = rfl(tid >> 6);
    const unsigned lds0 = (unsigned)(uintptr_t)lds;
    const int xcd = bid & 7, j = (xcd >> 1) * 30 + (bid >> 3);
    const int ct = j >> 3, mt = (xcd & 1) * 8 + (j & 7);
    const int type = rfl(ct / 5), rot = rfl(G1_ROT);
    const unsigned pcs = (unsigned)__builtin_amdgcn_s_memtime();
    if (__builtin_expect(wave >= 4, 1)) {
        const int lw = wave - 4;
        unsigned xo[4], wo[2];
#pragma unroll
        for (int i = 0; i < 4; ++i) { const int row = (lw * 4 + i) * 8 + (lane >> 3); xo[i] = (unsigned)((mt * 128 + row) * 1280 + (((lane & 7) ^ ((row >> 1) & 3) ^ ((row >> 2) & 4)) << 4)); }
#pragma unroll
        for (int i = 0; i < 2; ++i) { const int k = (lw * 2 + i) * 4 + (lane >> 4), s16 = lane & 15;
            wo[i] = (unsigned)(k * 3840 + ct * 256 + ((s16 ^ ((k & 3) << 2) ^ ((k >> 2) & 2)) << 4)); }
        auto ldx = [&](int u, unsigned st = 0u) {
            if (u < 0) { if (tid == 256) ((unsigned*)(ws + (64 << 20)))[512 + bid] = PROBE >= 200 ? st - pcs : st; return; }
            int kk = u + rot; kk -= kk >= 10 ? 10 : 0;
            const unsigned slot = lds0 + (u % G1_NX) * 16384 + lw * 4096;
#pragma unroll
            for (int i = 0; i < 4; ++i) glds16((const char*)x + kk * 128, xo[i], slot + i * 1024);
        };
        auto ldw = [&](int s) {
            int kk = (g1_t(2 * s) >> 1) + rot; kk -= kk >= 10 ? 10 : 0;
            const unsigned slot = lds0 + G1_WOFF + (s % G1_NW) * 16384 + lw * 2048;
#pragma unroll
            for (int e = 0; e < 2; ++e) {
                const char* src = (const char*)(g1_m(2 * s + e) ? wA : wB) + (kk * 64 + (g1_t(2 * s + e) & 1) * 32) * 3840;
                glds16(src, wo[0], slot + e * 8192); glds16(src, wo[1], slot + e * 8192 + 1024);
            }
        };
#pragma unroll
        for (int n = 0; n < G1P.cnt[0]; ++n) { const int i = G1P.seq[n]; if (i < 10) ldx(i); else ldw(i - 10); }
        G1Load<0>::run(ldx, ldw LST_A);
        return;
    }
    const int wm = wave & 1, wn = wave >> 1;
    const int i16 = lane & 15, g = lane >> 4, q = (lane >> 2) & 3, p = lane & 3;
    G1Ctx c;
    const int xr = (i16 >> 3) * 16 + (i16 & 7);
#pragma unroll
    for (int ks = 0; ks < 2; ++ks) { c.xa[ks] = (const LAS char*)lds + (wm * 64 + xr) * 128 + (((ks * 4 + g) ^ ((xr >> 1) & 3) ^ ((xr >> 2) & 4)) << 4); asm volatile("" : "+v"(c.xa[ks])); }
#pragma unroll
    for (int i = 0; i < 8; ++i) { c.wa[i] = (const LAS char*)lds + G1_WOFF + (i >> 2) * 65536 + (8 * g + q) * 256 + (((wn * 4 + (i & 3)) ^ (q << 1) ^ (g & 1)) << 5) + p * 8;
        asm volatile("" : "+v"(c.wa[i])); }
#pragma unroll
    for (int i = 0; i < 4; ++i) { const int cc = (ct - type * 5) * 128 + wn * 64 + i * 16 + i16, h = cc / 20; c.so[i] = h * 163840 + g * 320 + (cc - h * 20) * 16; asm volatile("" : "+v"(c.so[i])); }
    constexpr float QS = 0.22360679774997896f * LOG2E;
    int sci = type == 0 ? __builtin_bit_cast(int, QS) : 0x3F800000;
    asm volatile("" : "+s"(sci));
    c.sc = __int_as_float(sci);
    const int tb = (mt * 4 + wm * 2) * 2560;
    const int sB = tb + (type == 0 ? (int)OFF_IQ : (int)OFF_IB + (type - 1) * 1280), sA = tb + (type == 0 ? (int)OFF_IQ + 1280 : (int)OFF_IA + (type - 1) * 1280);
    c.rB = __builtin_amdgcn_make_buffer_rsrc((void*)(ws + sB), 0, (int)IMG, 0x00020000);
    const rsrc_t rA = __builtin_amdgcn_make_buffer_rsrc((void*)(ws + sA), 0, (int)IMG, 0x00020000);
    {
        const float a0 = type < 2 ? 1e-6f : 0.f;
        f32x4 a4 = {a0, a0, a0, a0};
        asm volatile("" : "+v"(a4));
#pragma unroll
        for (int i = 0; i < 16; ++i) { c.acc[i & 3][i >> 2] = f32x4{0.f, 0.f, 0.f, 0.f}; c.acc[i & 3][4 + (i >> 2)] = a4; }
    }
    __builtin_amdgcn_s_setprio(2);
    asm volatile("s_barrier" ::: "memory");
#pragma unroll
    for (int i = 0; i < 4; ++i) {
        c.x[0][i] = *(const LAS bf16x8*)(c.xa[0] + (i >> 1) * 4096 + (i & 1) * 1024);
        if (i == 0) { c.w[0][0][0] = trd(c.wa[0]); c.w[0][0][1] = trd(c.wa[0] + 1024); }
    }
#pragma unroll
    for (int i = 1; i < 4; ++i) { c.w[0][i][0] = trd(c.wa[i]); c.w[0][i][1] = trd(c.wa[i] + 1024); }
    SB0();
    G1Step<0>::run(c);
    PSTAMP(0, 2);
    G1Rest<(32 * G1_TL < 8 * G1_SP ? 32 * G1_TL : 8 * G1_SP)>::run(c);
    if (type < 2) {
#pragma unroll
        for (int i = 0; i < 4; ++i)
#pragma unroll
            for (int a = 0; a < 2; ++a) {
                u32x4 o;
#pragma unroll
                for (int e = 0; e < 4; ++e) {
                    const f32x2 t = {c.acc[2 * a + (e >> 1)][4 + i][(e & 1) * 2], c.acc[2 * a + (e >> 1)][4 + i][(e & 1) * 2 + 1]};
                    const f32x2 m = __builtin_elementwise_fma(t, f32x2{LOG2E, LOG2E}, f32x2{-1e-6f * LOG2E, -1e-6f * LOG2E});
                    const f32x2 ex = {__builtin_amdgcn_fmed3f(__builtin_amdgcn_exp2f(m[0]), 0.f, 1.f), __builtin_amdgcn_fmed3f(__builtin_amdgcn_exp2f(m[1]), 0.f, 1.f)};
                    const f32x2 r = f32x2{pmax(t[0], 1e-6f), pmax(t[1], 1e-6f)} + ex;
                    o[e] = pk2(r[0], r[1]);
                }
                __builtin_amdgcn_raw_buffer_store_b128(o, rA, c.so[i] + a * 2560, 0, SC1);
            }
    } else {
#pragma unroll
        for (int i = 0; i < 4; ++i)
#pragma unroll
            for (int a = 0; a < 2; ++a) {
                const f32x4 u = c.acc[2 * a][4 + i], v = c.acc[2 * a + 1][4 + i];
                __builtin_amdgcn_raw_buffer_store_b128(u32x4{pk2(u[0], u[1]), pk2(u[2], u[3]), pk2(v[0], v[1]), pk2(v[2], v[3])}, rA, c.so[i] + a * 2560, 0, SC1);
            }
    }
}

__global__ __launch_bounds__(512, 2) void k1_qkv(const u16* __restrict__ x, const u16* __restrict__ wA, const u16* __restrict__ wB,
                                                 const u16* __restrict__ woA, const u16* __restrict__ woB, const u16* __restrict__ gate_w,
                                                 char* __restrict__ ws) {
    extern __shared__ __attribute__((aligned(16))) char lds[];
    int bid = blockIdx.x;
    asm volatile("" : "+s"(bid));
    PSTAMP(0, 0);
    const unsigned pt0 = (unsigned)__builtin_amdgcn_s_memrealtime();
    if (__builtin_expect(bid < 240, 1)) phase1(x, wA, wB, ws, lds, bid);
    else p1_aux(x, woA, woB, gate_w, ws, lds, bid - 240);
    if (threadIdx.x == 0) { asm volatile("s_waitcnt vmcnt(0)"); unsigned* pp = (unsigned*)(ws + (64 << 20)) + bid * 2; pp[0] = pt0; pp[1] = (unsigned)__builtin_amdgcn_s_memrealtime(); }
}

#define STR(x) #x
#define XS(x) STR(x)
#define A_E(i) "v_exp_f32 v" XS(i) ", v" XS(i) "\n"
#define A_C(d, a, b) "v_cvt_pk_bf16_f32 v" XS(d) ", v" XS(a) ", v" XS(b) "\n"
#define A_G(a, b, c, d, p0, p1) A_E(a) A_E(b) A_E(c) A_E(d) A_C(p0, a, b) A_C(p1, c, d)
#define A_M(d, a, b, c) "v_mfma_f32_32x32x16_bf16 " d ", " a ", " b ", " c "\n"
#define A_STEP(C0,C1,C2,C3,C4,C5,C6,C7,C8,C9,C10,C11,C12,C13,C14,C15, SN, KM, KT, V1, V0, NKMa, NKMb, NKT, NV1, NV0, OK0, OK1, OV1, OV0, PRIO) \
    "s_waitcnt lgkmcnt(0)\n" \
    A_M(SN, KM, "v[120:123]", "0") \
    "ds_read_b64_tr_b16 " NKMa ", v128 offset:" XS(OK0) "\n" \
    "ds_read_b64_tr_b16 " NKMb ", v128 offset:" XS(OK1) "\n" \
    "ds_read_b64_tr_b16 " NKT ", v129 offset:" XS(OK0) "\n" \
    A_G(C0, C1, C2, C3, 112, 113) \
    A_M("v[64:79]", V1, "v[116:119]", "v[64:79]") \
    "s_mov_b64 exec, s[72:73]\n" \
    "ds_read_b128 " NV1 ", v130 offset:" XS(OV1) "\n" \
    "ds_read_b128 " NV0 ", v130 offset:" XS(OV0) "\n" \
    "s_mov_b64 exec, -1\n" \
    A_G(C4, C5, C6, C7, 114, 115) \
    A_M(SN, KT, "v[124:127]", SN) \
    A_G(C8, C9, C10, C11, 116, 117) \
    A_M("v[64:79]", V0, "v[112:115]", "v[64:79]") \
    "s_setprio " XS(PRIO) "\n" \
    A_G(C12, C13, C14, C15, 118, 119)
#define A_EVEN(OK0, OK1, OV1, OV0, PRIO) A_STEP(32,33,34,35,36,37,38,39,40,41,42,43,44,45,46,47, "v[48:63]", "v[80:83]", "v[84:87]", "v[88:91]", "v[92:95]", \
    "v[96:97]", "v[98:99]", "v[100:101]", "v[104:107]", "v[108:111]", OK0, OK1, OV1, OV0, PRIO)
#define A_ODD(OK0, OK1, OV1, OV0, PRIO) A_STEP(48,49,50,51,52,53,54,55,56,57,58,59,60,61,62,63, "v[32:47]", "v[96:99]", "v[100:103]", "v[104:107]", "v[108:111]", \
    "v[80:81]", "v[82:83]", "v[84:85]", "v[88:91]", "v[92:95]", OK0, OK1, OV1, OV0, PRIO)

#define A_LD(g, off) "global_load_lds_dwordx4 v131, " g " offset:" XS(off) "\n"
#define A_LDA(off) "s_cbranch_scc1 .Lattn_s" XS(off) "\n" A_LD("s[74:75]", off) ".Lattn_s" XS(off) ":\n"
#define A_BODY \
        "s_mov_b32 m0, s78\n s_cmp_eq_u32 s80, 2\n s_nop 3\n" \
        A_EVEN(5120, 5184, 640, 2560, 2)     A_ODD(7680, 7744, 3200, 5120, 1) \
        A_EVEN(10240, 10304, 5760, 7680, 0)  A_ODD(12800, 12864, 8320, 10240, 3) \
        "s_waitcnt vmcnt(0) lgkmcnt(0)\n s_mov_b64 exec, 1\n ds_add_u32 v127, v86\n s_mov_b64 exec, -1\n" \
        A_EVEN(15360, 15424, 10880, 12800, 2) A_LDA(0) A_ODD(17920, 17984, 13440, 15360, 1) A_LDA(1024) \
        A_EVEN(20480, 20544, 16000, 17920, 0) A_LDA(2048) \
        "s_cmp_eq_u32 s80, 1\n s_cbranch_scc0 .Lattn_skip\n s_mov_b32 m0, s79\n s_nop 0\n" \
        A_LD("s[76:77]", 0) A_LD("s[76:77]", 1024) A_LD("s[76:77]", 2048) ".Lattn_skip:\n" \
        "ds_read_b32 v132, v127\n" \
        A_ODD(23040, 23104, 18560, 20480, 3) \
        "v_add_u32 v128, 0x5000, v128\n v_add_u32 v129, 0x5000, v129\n v_add_u32 v130, 0x5000, v130\n" \
        "s_waitcnt lgkmcnt(0)\n s_nop 0\n"
#define A_OPERANDS \
        : "+{v[32:63]}"(S), "+{v[64:79]}"(O), "+{v[80:111]}"(OPS), "+{v[112:119]}"(P), "+{v[128:131]}"(AD), "={v132}"(cnt) \
        : "{v[120:127]}"(Q), "{s[72:73]}"(vmask), "{s[74:75]}"(ga), "{s[76:77]}"(gb), "{s78}"(ma), "{s79}"(mb), "{s80}"(enb) \
        : "memory", "scc", "s82", "s83"
template <bool FIRST>
DI unsigned attn_chunk(f32x32& S, f32x16& O, u32x32& OPS, u32x8& P, u32x4& AD, const u32x8& Q, unsigned long long vmask,
                   unsigned ma, unsigned long long ga, unsigned mb, unsigned long long gb, unsigned enb) {
    unsigned cnt;
    if (FIRST) asm volatile("s_call_b64 s[82:83], .Lattn_body\n s_branch .Lattn_done\n .p2align 6\n .Lattn_body:\n" A_BODY "s_setpc_b64 s[82:83]\n .Lattn_done:\n" A_OPERANDS);
    else       asm volatile("s_call_b64 s[82:83], .Lattn_body\n" A_OPERANDS);
    return cnt;
}
DI void wait_cnt(char* lds, int off, unsigned cnt, int n) {
    if (__builtin_expect((unsigned)rfl((int)cnt) < (unsigned)(n * 0x3F80), 0)) {
#pragma clang loop unroll(disable)
        do { __builtin_amdgcn_s_sleep(1); cnt = *(volatile LAS unsigned*)(lds + off); } while ((unsigned)rfl((int)cnt) < (unsigned)(n * 0x3F80));
    }
    asm volatile("" ::: "memory");
}

DI void wait_cnt(char* lds, int off, int n) { wait_cnt(lds, off, *(volatile LAS unsigned*)(lds + off), n); }
template <int OFF> DI void add_cnt(unsigned lds0) {
    asm volatile("s_waitcnt lgkmcnt(0)\n\ts_mov_b64 exec, 1\n\tds_add_u32 %0, %1 offset:%2\n\ts_mov_b64 exec, -1" :: "v"(lds0), "v"(0x3F80u), "n"(OFF) : "memory"); }

DI float xhalf_max(float m) { auto rr = __builtin_amdgcn_permlane32_swap(__float_as_uint(m), __float_as_uint(m), false, false);
    return fmaxf(__uint_as_float(rr[0]), __uint_as_float(rr[1])); }
DI float hi_half(float v) {
    auto rr = __builtin_amdgcn_permlane32_swap(__float_as_uint(v), __float_as_uint(v), false, false);
    return __uint_as_float(rr[1]); }
DI float sum16(float v) { auto rr = __builtin_amdgcn_permlane16_swap(__float_as_uint(v), __float_as_uint(v), false, false);
    return __uint_as_float(rr[0]) + __uint_as_float(rr[1]); }
DI float sum32(float v) { auto rr = __builtin_amdgcn_permlane32_swap(__float_as_uint(v), __float_as_uint(v), false, false);
    return __uint_as_float(rr[0]) + __uint_as_float(rr[1]); }
DI f32x4 sum_rows(f32x4 a, f32x4 b) {
    f32x4 s = a + b;
#pragma unroll
    for (int i = 0; i < 4; ++i) s[i] = sum32(sum16(s[i]));
    return s;
}
DI unsigned long long uni64(const void* p) { const unsigned long long a = (unsigned long long)(uintptr_t)p;
    return (unsigned long long)(unsigned)rfl((int)(unsigned)a) | ((unsigned long long)(unsigned)rfl((int)(unsigned)(a >> 32)) << 32); }
DI unsigned full(unsigned v) { return v < EMPTY ? v : EMPTY - 1u; }
DI unsigned umax4(unsigned m, u32x4 v) { return max(max(max(m, v[0]), max(v[1], v[2])), v[3]); }
DI void store_o(rsrc_t rs, const f32x16& o, float f, int rt, int br, int h, int lane) {
    const int rb = (int)OFF_O + (rt * 80 + ((br ? 0 : 4) + (h >> 3)) * 10) * 1024;
    __builtin_amdgcn_raw_buffer_store_b128(u32x4{full(pk2(o[0] * f, o[1] * f)), full(pk2(o[2] * f, o[3] * f)), full(pk2(o[4] * f, o[5] * f)), full(pk2(o[6] * f, o[7] * f))},
                                           rs, lane * 16, rb + (h & 7) * 1024, SC1);
    if (lane < 32) __builtin_amdgcn_raw_buffer_store_b64(u32x2{full(pk2(o[8] * f, o[9] * f)), full(pk2(o[10] * f, o[11] * f))},
                                           rs, lane * 16, rb + (8 + ((h >> 2) & 1)) * 1024 + ((h >> 1) & 1) * 512 + (h & 1) * 8, SC1);
}
DI void empty_o(rsrc_t rs, int rt, int h, int lane) {
    const int rb = (int)OFF_O + (rt * 80 + (h >> 3) * 10) * 1024;
    u32x4 e = {EMPTY, EMPTY, EMPTY, EMPTY};
    __builtin_amdgcn_raw_buffer_store_b128(e, rs, lane * 16, rb + (h & 7) * 1024, SC1);
    if (lane < 32) __builtin_amdgcn_raw_buffer_store_b64(u32x2{e[0], e[1]}, rs, lane * 16, rb + (8 + ((h >> 2) & 1)) * 1024 + ((h >> 1) & 1) * 512 + (h & 1) * 8, SC1);
    asm volatile("" :: "v"(e));
}
DI void set_flag(rsrc_t rs, int off) { __builtin_amdgcn_raw_buffer_store_b32(1u, rs, 0, off, SC1); }
DI unsigned get_flag(rsrc_t rs, int voff, int off) { return __builtin_amdgcn_raw_buffer_load_b32(rs, voff, off, SC1); }
DI unsigned a_ld1(unsigned long long sb, unsigned voff) { unsigned v; asm volatile("global_load_dword %0, %1, %2 sc1" : "=v"(v) : "v"(voff), "s"(sb) : "memory"); return v; }
DI u32x4 a_ld4s(unsigned long long sb, unsigned voff) { u32x4 v; asm volatile("global_load_dwordx4 %0, %1, %2 sc1" : "=v"(v) : "v"(voff), "s"(sb) : "memory"); return v; }
template <int OFF> DI u32x4 a_ld4so(unsigned long long sb, unsigned voff) { u32x4 v;
    asm volatile("global_load_dwordx4 %0, %1, %2 offset:%3 sc1" : "=v"(v) : "v"(voff), "s"(sb), "n"(OFF) : "memory"); return v; }
template <int OFF> DI u32x4 a_ld4(unsigned long long sb, unsigned voff) { u32x4 v;
    asm volatile("global_load_dwordx4 %0, %1, %2 offset:%3" : "=v"(v) : "v"(voff), "s"(sb), "n"(OFF) : "memory"); return v; }
template <class T> DI void landed(T& v) { asm volatile("" : "+v"(v)); }
DI void wait_flag(rsrc_t rs, int voff, int off, unsigned v) {
    if (__builtin_expect(!__all(v == 1u), 0)) {
#pragma clang loop unroll(disable)
        for (int spin = 0; spin < (1 << 22); ++spin) { if (__all(get_flag(rs, voff, off) == 1u)) break; __builtin_amdgcn_s_sleep(1); }
    }
    __builtin_amdgcn_fence(__ATOMIC_ACQUIRE, "wavefront");
    asm volatile("" ::: "memory");
}

#define FILL_C 1
constexpr int L_SP = 136192, L_K0 = 152320, L_SQ = 153600, L_QA = 1280, L_KVF = 11520, L_WO = 13312;

__global__ __launch_bounds__(512, 2) void k2_attn(char* __restrict__ ws, u16* __restrict__ out) {
    extern __shared__ __attribute__((aligned(16))) char lds[];
    int bid = blockIdx.x;
    asm volatile("" : "+s"(bid));
    PSTAMP(1, 0); WSTAMP(0);
    const unsigned pt0 = (unsigned)__builtin_amdgcn_s_memrealtime(), pc0 = (unsigned)__builtin_amdgcn_s_memtime();
    unsigned pt1 = 0, pt2 = 0;
    const int tid = threadIdx.x, lane = tid & 63, wave = rfl(tid >> 6);
    const int h = (bid & 7) * 4 + ((bid >> 3) & 3), qb = bid >> 5;
    const int oq = bid & 7, rb = oq * 4 + ((bid >> 3) & 3), cb = bid >> 5;
    const int r = lane & 31, hh = lane >> 5;
    const unsigned lds0 = (unsigned)(uintptr_t)lds;
    const rsrc_t rs = __builtin_amdgcn_make_buffer_rsrc((void*)ws, 0, WS_USED, 0x00020000);
    const char* kvb = ws + OFF_IB + (size_t)h * 163840;
    const char* qim = ws + OFF_IQ + (size_t)h * 163840 + qb * 20480;
    const int wr = (wave & 3) * 2 + (wave >> 2), rt = qb * 8 + wr;
    const int rc = r < 20 ? r : 19;
    glds16(qim + wr * 2560, lane * 16, lds0 + L_SQ + wave * 1280);
    glds16(qim + wr * 2560 + 256, lane * 16, lds0 + L_SQ + wave * 1280 + 256);
    glds16(kvb + (wave & 1) * 256, lane * 16, lds0 + L_K0 + (wave & 1) * 256);
#pragma unroll
    for (int i = 0; i < 3; ++i) glds16(kvb + (1 + i * 8 + wave) * 1024, lane * 16, lds0 + (1 + i * 8 + wave) * 1024);
    const char* ap = ws + OFF_IA + (size_t)h * 163840 + rt * 2560 + hh * 320 + rc * 16;
    u32x4 a0, a1, b0, b1; float gate;
    asm volatile("global_load_dwordx4 %0, %5, off\n\tglobal_load_dwordx4 %1, %5, off offset:640\n\tglobal_load_dwordx4 %2, %5, off offset:1280\n\t"
                 "global_load_dwordx4 %3, %5, off offset:1920\n\tglobal_load_dword %4, %6, off"
                 : "=&v"(a0), "=&v"(a1), "=&v"(b0), "=&v"(b1), "=&v"(gate) : "v"(ap), "v"((const float*)(ws + OFF_G) + rt * 32 + r) : "memory");
#pragma unroll
    for (int i = 0; i < 3; ++i) glds16(kvb + (25 + i * 8 + wave) * 1024, lane * 16, lds0 + (25 + i * 8 + wave) * 1024);
    if (tid < 4) *(LAS unsigned*)(lds + tid * 4) = 0u;
    const u32x4 ONES = {0x3F803F80u, 0x3F803F80u, 0x3F803F80u, 0x3F803F80u};
    const f32x16 Z16 = {};
    wait_vm_bar<8>();
    PSTAMP(1, 1); WSTAMP(1);
    pt1 = (unsigned)__builtin_amdgcn_s_memrealtime();
    const int g = lane >> 4, q = (lane >> 2) & 3, p = lane & 3;
    const int tro = (g & 1) * 640 + (p & 1) * 320 + q * 16 + (p >> 1) * 8;
    const LAS char* L = (const LAS char*)lds;
    const LAS char* kmp = L + tro + hh * 128;
    const LAS char* ktp = L + tro + 256;
    const LAS char* vp  = L + 1280 + hh * 320 + r * 16;
    const int ent = tid < 126 ? tid : 125;
    u32x8 Q;
    {
        const u32x2 a = trd(kmp + L_SQ + wave * 1280), b = trd(kmp + L_SQ + wave * 1280 + 64), c = trd(ktp + L_SQ + wave * 1280);
        Q[0] = a[0]; Q[1] = a[1]; Q[2] = b[0]; Q[3] = b[1]; Q[4] = hh ? 0u : c[0]; Q[5] = hh ? 0u : c[1]; Q[6] = 0u; Q[7] = 0u;
    }
    const bf16x8 qm = B8(((u32x4){Q[0], Q[1], Q[2], Q[3]}));
    f32x32 S; f32x16 O = {}; u32x32 OPS; u32x8 P = {}; u32x4 AD;
    {
        const u32x2 a = trd(kmp + L_K0), b = trd(kmp + L_K0 + 64), c = trd(ktp + L_K0);
        const bf16x8 k0 = B8(((u32x4){a[0], a[1], b[0], b[1]})), k1 = B8(((u32x4){c[0], c[1], 0x3F80u, 0u}));
        const f32x16 sm = MFMA32(k0, qm, Z16);
        f32x16 s0 = MFMA32(k1, B8(((u32x4){Q[4], Q[5], 0u, 0u})), sm);
        float m = s0[0];
#pragma unroll
        for (int i = 1; i < 16; ++i) m = fmaxf(m, s0[i]);
        m = xhalf_max(m);
        Q[6] = hh ? 0u : (pk2(-m, 0.f) & 0xffffu);
        s0 = MFMA32(k1, B8(((u32x4){Q[4], Q[5], Q[6], 0u})), sm);
#pragma unroll
        for (int i = 0; i < 16; ++i) { S[i] = s0[i]; S[16 + i] = 0.f; }
        const unsigned vc = r == 20 ? 0x3F803F80u : 0u;
#pragma unroll
        for (int i = 0; i < 32; ++i) OPS[i] = (i & 8) ? vc : 0u;
        const u32x2 a1 = trd(kmp + 2560), b1 = trd(kmp + 2560 + 64), c1 = trd(ktp + 2560);
        OPS[0] = a1[0]; OPS[1] = a1[1]; OPS[2] = b1[0]; OPS[3] = b1[1]; OPS[4] = c1[0]; OPS[5] = c1[1]; OPS[6] = 0x3F80u; OPS[22] = 0x3F80u;
        if (r < 20) { const u32x4 v0 = *(const LAS u32x4*)vp; OPS[12] = v0[0]; OPS[13] = v0[1]; OPS[14] = v0[2]; OPS[15] = v0[3]; }
        AD[0] = (unsigned)(uintptr_t)kmp; AD[1] = (unsigned)(uintptr_t)ktp; AD[2] = (unsigned)(uintptr_t)vp; AD[3] = lane * 16;
    }
    {
        asm volatile("s_waitcnt vmcnt(3)" : "+v"(a0), "+v"(a1), "+v"(b0), "+v"(b1), "+v"(gate) :: "memory");
        if (r >= 20) { a0 = u32x4{0u, 0u, 0u, 0u}; a1 = a0; b0 = ONES; b1 = ONES; }
        f32x16 kvacc = MFMA32(B8(a0), B8(b0), Z16); kvacc = MFMA32(B8(a1), B8(b1), kvacc);
        if (r <= 20) {
            LAS f32x4* dst = (LAS f32x4*)(lds + L_SP + ((wave * 2 + hh) * 21 + r) * 48);
            dst[0] = f32x4{kvacc[0], kvacc[1], kvacc[2], kvacc[3]};
            dst[1] = f32x4{kvacc[4], kvacc[5], kvacc[6], kvacc[7]};
            dst[2] = f32x4{kvacc[8], kvacc[9], kvacc[10], kvacc[11]};
        }
    }
    const unsigned long long vmask = 0x000FFFFF000FFFFFull;
    const int j = wave & 3, isE = (wave >> 2) ^ 1;
    const char* wop = ws + OFF_WOT + (size_t)((cb * 4 + j) * 50) * 1024;
    const int flA = (int)OFF_FL + (oq * 4 + j) * 128, flv = (lane & 7) * 16;
    const int ov = ((g >> 1) * 1024 + (g & 1) * 512 + (lane & 15) * 16), os = (int)OFF_O + (rb * 160 + j * 10) * 1024;
    unsigned fl = 0u;
    u32x4 kp[8], wa[20], aa[2][10];
    const unsigned long long wsu = uni64(ws);
    const unsigned long long kb = uni64(ws + OFF_KVP + 4096 + h * 16384), kb2 = uni64(ws + OFF_KVP + 12288 + h * 16384);
    const unsigned long long ob[2][2] = {{uni64(ws + os + 4096), uni64(ws + os + 8192)}, {uni64(ws + os + 81920 + 4096), uni64(ws + os + 81920 + 8192)}};
#define LD_H(A, vo, hf, en, C) asm volatile("s_cmp_lg_u32 %13, 0\n\ts_cselect_b64 exec, -1, 0\n\t" \
        "global_load_dwordx4 %0, %10, %11 offset:-4096 sc1\n\tglobal_load_dwordx4 %1, %10, %11 offset:-3840 sc1\n\t" \
        "global_load_dwordx4 %2, %10, %11 offset:-2048 sc1\n\tglobal_load_dwordx4 %3, %10, %11 offset:-1792 sc1\n\t" \
        "global_load_dwordx4 %4, %10, %11 sc1\n\tglobal_load_dwordx4 %5, %10, %11 offset:256 sc1\n\t" \
        "global_load_dwordx4 %6, %10, %11 offset:2048 sc1\n\tglobal_load_dwordx4 %7, %10, %11 offset:2304 sc1\n\t" \
        "global_load_dwordx4 %8, %10, %12 sc1\n\tglobal_load_dwordx4 %9, %10, %12 offset:256 sc1\n\ts_mov_b64 exec, -1" \
        : C(A[0]), C(A[1]), C(A[2]), C(A[3]), C(A[4]), C(A[5]), C(A[6]), C(A[7]), C(A[8]), C(A[9]) \
        : "v"(vo), "s"(ob[hf][0]), "s"(ob[hf][1]), "s"(en) : "memory", "scc");
#define LD_W4(A, f) asm volatile("s_cmp_lg_u32 %6, 0\n\ts_cselect_b64 exec, -1, 0\n\t" \
        "global_load_dwordx4 %0, %4, %5\n\tglobal_load_dwordx4 %1, %4, %5 offset:1024\n\t" \
        "global_load_dwordx4 %2, %4, %5 offset:2048\n\tglobal_load_dwordx4 %3, %4, %5 offset:3072\n\ts_mov_b64 exec, -1" \
        : "=&v"((A)[0]), "=&v"((A)[1]), "=&v"((A)[2]), "=&v"((A)[3]) : "v"(lane * 16), "s"(uni64(wop + (f) * 1024)), "s"(isE) : "memory", "scc");
#define LD_KP(TAIL) asm volatile("s_cmp_lt_u32 %11, 2\n\ts_cselect_b64 exec, -1, 0\n\t" \
        "global_load_dwordx4 %0, %8, %9 offset:-4096 sc1\n\tglobal_load_dwordx4 %1, %8, %9 offset:-2048 sc1\n\t" \
        "global_load_dwordx4 %2, %8, %9 sc1\n\tglobal_load_dwordx4 %3, %8, %9 offset:2048 sc1\n\t" \
        "global_load_dwordx4 %4, %8, %10 offset:-4096 sc1\n\tglobal_load_dwordx4 %5, %8, %10 offset:-2048 sc1\n\t" \
        "global_load_dwordx4 %6, %8, %10 sc1\n\tglobal_load_dwordx4 %7, %8, %10 offset:2048 sc1\n\t" \
        "s_mov_b64 exec, -1" TAIL \
        : "=&v"(kp[0]), "=&v"(kp[1]), "=&v"(kp[2]), "=&v"(kp[3]), "=&v"(kp[4]), "=&v"(kp[5]), "=&v"(kp[6]), "=&v"(kp[7]) \
        : "v"(ent * 16), "s"(kb), "s"(kb2), "s"(wave) : "memory", "scc")
#pragma unroll
    for (int c = 0; c < 8; ++c) {
        unsigned ma, mb; unsigned long long ga, gb;
        if (c < 3) {
            const int lim = (c == 1 ? L_SP : 163840) - 3072;
            const int oa = (c == 2 ? L_SP : 50176 + c * 49152) + wave * 3072, ob = oa + 24576 < lim ? oa + 24576 : lim;
            ma = lds0 + oa; mb = lds0 + ob; ga = uni64(kvb + oa); gb = uni64(kvb + ob);
        } else {
            ma = lds0 + (c == 7 ? 1024 + j * 3072 : L_WO + ((c - 3) * 8 + wr) * 3072); mb = ma;
            ga = uni64(wop + (c == 7 ? 44 : 20 + (wave >> 2) * 12 + 3 * (c - 3)) * 1024); gb = ga;
        }
        CSTAMP(2 * c);
        unsigned cnt;
        if (c == 0) cnt = attn_chunk<true>(S, O, OPS, P, AD, Q, vmask, rfl(ma), ga, rfl(mb), gb, 1u);
        else cnt = attn_chunk<false>(S, O, OPS, P, AD, Q, vmask, rfl(ma), ga, rfl(mb), gb, c < 3 ? 1u : c < 7 ? 0u : (unsigned)(2 - 2 * isE));
        CSTAMP(2 * c + 1);
        if (c < 7) wait_cnt(lds, 0, cnt, 8 * (c + 1));
        if (c == FILL_C && c != 1) empty_o(rs, rt, h, lane);
        if (c == 0) {
            if (wave < 2) {
                f32x4 s = *(const LAS f32x4*)(L + L_SP + ent * 16);
#pragma unroll
                for (int w = 1; w < 8; ++w) s += *(const LAS f32x4*)(L + L_SP + w * 2016 + ent * 16);
                const u32x4 v = __builtin_bit_cast(u32x4, s);
                if (tid < 126) __builtin_amdgcn_raw_buffer_store_b128(u32x4{full(v[0]), full(v[1]), full(v[2]), full(v[3])}, rs, tid * 16, (int)OFF_KVP + (h * 8 + qb) * 2048, SC1);
            }
            glds16(qim + wr * 2560 + 1280, lane * 16, lds0 + L_QA + wave * 1280);
            glds16(qim + wr * 2560 + 1280 + 256, lane * 16, lds0 + L_QA + wave * 1280 + 256);
        } else if (c == 1) {
            LD_KP("");
            if (FILL_C == 1) empty_o(rs, rt, h, lane);
        } else if (c == 2) {
#pragma unroll
            for (int j = 0; j < 8; ++j) landed(kp[j]);
            if (wave < 2) {
#pragma clang loop unroll(disable)
                for (int spin = 0; spin < (1 << 20); ++spin) {
                    unsigned m = 0u;
#pragma unroll
                    for (int j = 0; j < 8; ++j) m = umax4(m, kp[j]);
                    if (__builtin_expect(__all(m != EMPTY), 1)) break;
                    LD_KP("\n\ts_waitcnt vmcnt(0)");
                }
                f32x4 s = __builtin_bit_cast(f32x4, kp[0]);
#pragma unroll
                for (int j = 1; j < 8; ++j) s += __builtin_bit_cast(f32x4, kp[j]);
                const int e3 = ent / 3;
                *(LAS u32x2*)(lds + L_KVF + e3 * 32 + (ent - e3 * 3) * 8) = u32x2{pk2(s[0], s[1]), pk2(s[2], s[3])};
            }
        } else if (c == 3) {
            const int rr = r <= 20 ? r : 20;
            const u32x4 ka0 = *(const LAS u32x4*)(L + L_KVF + (hh * 21 + rr) * 32);
            const u32x2 ka1 = *(const LAS u32x2*)(L + L_KVF + (hh * 21 + rr) * 32 + 16);
            const LAS char* qa = L + L_QA + wave * 1280 + tro;
            const u32x2 pq0 = trd(qa + hh * 64), pq1 = trd(qa + 128 + hh * 64);
            u32x2 pq2 = trd(qa + 256);
            if (hh) pq2 = u32x2{0u, 0u};
            f32x16 oa = MFMA32(B8(ka0), B8(((u32x4){pq0[0], pq0[1], pq1[0], pq1[1]})), Z16);
            oa = MFMA32(B8(((u32x4){ka1[0], ka1[1], 0u, 0u})), B8(((u32x4){pq2[0], pq2[1], 0u, 0u})), oa);
            const float den = hi_half(oa[8]) + 1e-6f;
            store_o(rs, oa, gate * __builtin_amdgcn_rcpf(den), rt, 0, h, lane);
            LD_W4(wa, 0) LD_W4(wa + 4, 4)
        } else if (c == 4) {
            if (tid == 0) set_flag(rs, (int)OFF_FL + (qb * 4 + (h >> 3)) * 128 + (h & 7) * 16);
            LD_W4(wa + 8, 8) LD_W4(wa + 12, 12)
        } else if (c == 5) {
            fl = a_ld1(wsu, flA + flv);
            LD_W4(wa + 16, 16)
        } else if (c == 6) {
            landed(fl);
            if (wave < 4) wait_flag(rs, flv, flA, fl);
            LD_H(aa[0], ov + 40960, 0, isE, "=&v")
        }
    }
#pragma unroll
    for (int i = 0; i < 10; ++i) { landed(wa[i]); landed(wa[10 + i]); landed(aa[0][i]); }
    u32x4 wd[4];
    LD_H(aa[1], ov + 40960, 1, isE, "=&v") LD_W4(wd, 46)
    if (lane == 0) set_flag(rs, (int)OFF_FL + (32 + rt * 4 + (h >> 3)) * 128 + (h & 7) * 16);
    O = MFMA32(B8(((u32x4){OPS[8], OPS[9], OPS[10], OPS[11]})), B8(((u32x4){P[4], P[5], P[6], P[7]})), O);
    PSTAMP(1, 2); WSTAMP(2);
    pt2 = (unsigned)__builtin_amdgcn_s_memrealtime();
    float l = hi_half(O[8]);
    {
        bool bad = !(l >= 1e-18f && l <= 1.0e30f);
#pragma unroll
        for (int i = 0; i < 12; ++i) bad = bad || !(fabsf(O[i]) <= 3.0e38f);
        if (__builtin_expect(__any(bad), 0)) {
            const bf16x8 qt0 = B8(((u32x4){Q[4], Q[5], 0u, 0u}));
            float m = -INFINITY; O = Z16;
            int tl = tid;
            asm volatile("" : "+v"(tl));
            const int r = tl & 31, hh = (tl >> 5) & 1, rc = r < 20 ? r : 19;
            const int ko = (r >> 4) * 640 + ((r >> 2) & 1) * 320 + ((r >> 3) & 1) * 8 + (r & 3) * 2;
            for (int t = 0; t < 64; ++t) {
                const char* tb = kvb + t * 2560;
                unsigned kw[6];
#pragma unroll
                for (int i = 0; i < 6; ++i) {
                    const int d = i < 4 ? 8 * hh + 2 * i : 16 + 2 * (i - 4);
                    kw[i] = (unsigned)*(const u16*)(tb + ko + d * 16) | ((unsigned)*(const u16*)(tb + ko + d * 16 + 16) << 16);
                }
                u32x4 f0 = *(const u32x4*)(tb + 1280 + hh * 320 + rc * 16), f1 = *(const u32x4*)(tb + 1920 + hh * 320 + rc * 16);
                if (r >= 20) { f0 = ONES; f1 = ONES; }
                f32x16 s = MFMA32(B8(((u32x4){kw[0], kw[1], kw[2], kw[3]})), qm, Z16);
                s = MFMA32(B8(((u32x4){kw[4], kw[5], 0u, 0u})), qt0, s);
                float tm = s[0];
#pragma unroll
                for (int i = 1; i < 16; ++i) tm = fmaxf(tm, s[i]);
                tm = xhalf_max(tm);
                const float mn = fmaxf(m, tm);
                const float al = (m == -INFINITY) ? 0.f : __builtin_amdgcn_exp2f(m - mn);
                m = mn;
#pragma unroll
                for (int i = 0; i < 16; ++i) { O[i] *= al; s[i] = __builtin_amdgcn_exp2f(s[i] - mn); }
                u32x4 p0 = {pk2(s[0], s[1]), pk2(s[2], s[3]), pk2(s[4], s[5]), pk2(s[6], s[7])};
                u32x4 p1 = {pk2(s[8], s[9]), pk2(s[10], s[11]), pk2(s[12], s[13]), pk2(s[14], s[15])};
                O = MFMA32(B8(f0), B8(p0), O); O = MFMA32(B8(f1), B8(p1), O);
            }
            l = hi_half(O[8]);
        }
    }
    store_o(rs, O, (1.f - gate) * __builtin_amdgcn_rcpf(l), rt, 1, h, lane);
    __builtin_amdgcn_s_setprio(0);
    __builtin_amdgcn_sched_barrier(0);
    WSTAMP(3);
    constexpr int L_P0 = L_WO + 98304;
    if (wave < 4) {
        const LAS char* wl = L + j * 6144 + lane * 16;
        auto wfr = [&](int f) -> bf16x8 {
            if (f < 20) return B8(wa[f]);
            if (f < 44) return *(const LAS bf16x8*)(wl + L_WO + (((f - 20) % 12) / 3) * 24576 + ((f - 20) / 12) * 3072 + ((f - 20) % 3) * 1024);
            if (f < 46) return *(const LAS bf16x8*)(L + 1024 + j * 3072 + (f - 44) * 1024 + lane * 16);
            return B8(wd[f - 46]);
        };
        LAS char* wp = (LAS char*)lds + (lane & 15) * 320 + ((g ^ ((lane >> 1) & 3)) << 4);
        f32x4 acc[4][5] = {};
        {
            bf16x8 w4[5];
#pragma unroll
            for (int ni = 0; ni < 5; ++ni) w4[ni] = wfr(20 + ni);
#pragma unroll
            for (int hf = 0; hf < 2; ++hf) {
                if (hf) asm volatile("s_waitcnt vmcnt(3)" : "+v"(aa[1][0]), "+v"(aa[1][1]), "+v"(aa[1][2]), "+v"(aa[1][3]), "+v"(aa[1][4]), "+v"(aa[1][5]), "+v"(aa[1][6]),
                                     "+v"(aa[1][7]), "+v"(aa[1][8]), "+v"(aa[1][9]), "+v"(wd[0]), "+v"(wd[1]), "+v"(wd[2]), "+v"(wd[3]) :: "memory");
#pragma unroll
                for (int k = 0; k < 5; ++k)
#pragma unroll
                    for (int mi = 0; mi < 2; ++mi)
#pragma unroll
                        for (int ni = 0; ni < 5; ++ni) acc[hf * 2 + mi][ni] = MFMA16(k < 4 ? wfr(k * 5 + ni) : w4[ni], B8(aa[hf][2 * k + mi]), acc[hf * 2 + mi][ni]);
                __builtin_amdgcn_sched_barrier(0);
            }
        }
        const int flB = (int)OFF_FL + (32 + rb * 8 + j) * 128;
        u32x4 b0[10], b1[10];
        wait_flag(rs, flv, flB, get_flag(rs, flv, flB));
        WSTAMP(4);
        LD_H(b0, ov, 0, 1, "=&v")
        fl = a_ld1(wsu, flB + 512 + flv);
        wait_cnt(lds, 0, 64);
        asm volatile("s_waitcnt vmcnt(0)" : "+v"(fl) :: "memory");
        wait_flag(rs, flv, flB + 512, fl);
        WSTAMP(5);
        LD_H(b1, ov, 1, 1, "=&v")
#pragma clang loop unroll(disable)
        for (int spin = 0; spin < (1 << 20); ++spin) {
            unsigned m = 0u;
#pragma unroll
            for (int i = 0; i < 10; ++i) { landed(b0[i]); m = umax4(m, b0[i]); }
            if (__builtin_expect(__all(m != EMPTY), 1)) break;
            LD_H(b0, ov, 0, 1, "+v")
            asm volatile("s_waitcnt vmcnt(0)" ::: "memory");
        }
        bf16x8 wb[25];
#pragma unroll
        for (int k = 0; k < 5; ++k) {
#pragma unroll
            for (int ni = 0; ni < 5; ++ni) wb[k * 5 + ni] = wfr(25 + k * 5 + ni);
#pragma unroll
            for (int mi = 0; mi < 2; ++mi)
#pragma unroll
                for (int ni = 0; ni < 5; ++ni) acc[mi][ni] = MFMA16(wb[k * 5 + ni], B8(b0[2 * k + mi]), acc[mi][ni]);
        }
#pragma unroll
        for (int mi = 0; mi < 2; ++mi)
#pragma unroll
            for (int ni = 0; ni < 5; ++ni) *(LAS f32x4*)(wp + L_P0 + j * 10240 + mi * 5120 + ni * 64) = acc[mi][ni];
        add_cnt<12>(lds0);
        f32x4 ac1[2][5];
#pragma clang loop unroll(disable)
        for (int spin = 0; spin < (1 << 20); ++spin) {
            unsigned m = 0u;
#pragma unroll
            for (int k = 0; k < 5; ++k) {
                asm volatile("s_waitcnt vmcnt(%2)" : "+v"(b1[2 * k]), "+v"(b1[2 * k + 1]) : "n"(8 - 2 * k) : "memory");
#pragma unroll
                for (int mi = 0; mi < 2; ++mi)
#pragma unroll
                    for (int ni = 0; ni < 5; ++ni) ac1[mi][ni] = MFMA16(wb[k * 5 + ni], B8(b1[2 * k + mi]), k ? ac1[mi][ni] : acc[2 + mi][ni]);
                m = umax4(umax4(m, b1[2 * k]), b1[2 * k + 1]);
                __builtin_amdgcn_sched_barrier(0);
            }
            if (__builtin_expect(__all(m != EMPTY), 1)) break;
            LD_H(b1, ov, 1, 1, "+v")
        }
#pragma unroll
        for (int mi = 0; mi < 2; ++mi)
#pragma unroll
            for (int ni = 0; ni < 5; ++ni) *(LAS f32x4*)(wp + L_WO + j * 6144 + mi * 24576 + ni * 64) = ac1[mi][ni];
        WSTAMP(6);
        asm volatile("s_waitcnt lgkmcnt(0)\n\ts_barrier" ::: "memory");
    } else {
        const int t = tid - 256;
        const LAS char* rp[2][3]; unsigned oo[3];
#pragma unroll
        for (int i = 0; i < 3; ++i) {
            const int id = i < 2 || t < 128 ? i * 256 + t : 1, row = id / 20, cs = id - row * 20, cc = cs ^ ((row >> 1) & 3);
            rp[0][i] = L + L_P0 + id * 16; rp[1][i] = L + L_WO + id * 16 + (id >= 320 ? 24576 - 5120 : 0);
            oo[i] = (unsigned)(((rb * 64 + row) * D_ + cb * 80 + cc * 4) * 2);
            landed(rp[0][i]); landed(rp[1][i]); landed(oo[i]);
        }
        WSTAMP(4);
#pragma unroll
        for (int hf = 0; hf < 2; ++hf) {
            if (hf == 0) { wait_cnt(lds, 12, 4); WSTAMP(5); }
            else { WSTAMP(6); asm volatile("s_barrier" ::: "memory"); }
            f32x4 pv[3][4];
#pragma unroll
            for (int i = 0; i < 3; ++i)
#pragma unroll
                for (int w = 0; w < 4; ++w) pv[i][w] = *(const LAS f32x4*)(rp[hf][i] + w * (hf ? 6144 : 10240));
            __builtin_amdgcn_sched_barrier(0);
#pragma unroll
            for (int i = 0; i < 3; ++i) {
                const f32x4 v = (pv[i][0] + pv[i][1]) + (pv[i][2] + pv[i][3]);
                if (i < 2 || t < 128) __builtin_amdgcn_raw_buffer_store_b64(u32x2{pk2(v[0], v[1]), pk2(v[2], v[3])},
                    __builtin_amdgcn_make_buffer_rsrc((void*)out, 0, S_ * D_ * 2, 0x00020000), oo[i], hf * (32 * D_ * 2), OUT_AUX);
            }
        }
    }
    if (bid == 0 && wave == 4) {
        const unsigned pt3 = (unsigned)__builtin_amdgcn_s_memrealtime(), pc3 = (unsigned)__builtin_amdgcn_s_memtime();
        const unsigned* pp = (const unsigned*)(ws + (64 << 20));
        int mn = 1 << 30, mx = -(1 << 30);
        for (int i = 0; i < 4; ++i) { const int b = i * 64 + (threadIdx.x & 63); mn = min(mn, (int)(pp[b * 2] - pt0)); mx = max(mx, (int)(pp[b * 2 + 1] - pt0)); }
        for (int o = 1; o < 64; o <<= 1) { mn = min(mn, __shfl_xor(mn, o)); mx = max(mx, __shfl_xor(mx, o)); }
        int X = PROBE == 0 ? 0 : PROBE == 1 ? mx - mn : PROBE == 2 ? -mx : PROBE == 3 ? (int)(pt3 - pt0) : PROBE == 4 ? (int)(pc3 - pc0) / 20 :
                PROBE == 5 ? (int)(pt1 - pt0) : PROBE == 6 ? (int)(pt2 - pt1) : PROBE == 7 ? (int)(pt3 - pt2) : (int)(pt3 - pt0) - mn;
        if (PROBE >= 100) {
            int sm = 0, mz = -(1 << 30);
            for (int i = 0; i < 4; ++i) { const int b = i * 64 + (threadIdx.x & 63);
                const int v = PROBE >= 200 ? (int)pp[512 + b] / 20 : (int)((PROBE == 121 ? pp[b * 2] : PROBE == 122 || PROBE == 130 ? pp[b * 2 + 1] : pp[512 + b]) - pt0) - mn;
                if (b < 240) sm += v; else mz = max(mz, v); }
            for (int o = 1; o < 64; o <<= 1) { sm += __shfl_xor(sm, o); mz = max(mz, __shfl_xor(mz, o)); }
            X = 4 * (PROBE == 130 ? mz : sm / 240);
        }
        X = rfl(min(max(X, 0), 12000));
        while ((int)((unsigned)__builtin_amdgcn_s_memrealtime() - pt3) < 300 + 4 * X) __builtin_amdgcn_s_sleep(1);
    }
}

extern "C" void kernel_launch(void* const* d_in, const int* in_sizes, int n_in, void* d_out, int out_size, void* d_ws, size_t ws_size,
                              hipStream_t stream) {
    const u16* x = (const u16*)d_in[0];
    const u16* wA = (const u16*)d_in[1];
    const u16* woA = (const u16*)d_in[2];
    const u16* wB = (const u16*)d_in[4];
    const u16* woB = (const u16*)d_in[5];
    const u16* gw = (const u16*)d_in[6];
    char* ws = (char*)d_ws;
    (void)hipFuncSetAttribute((const void*)k1_qkv, hipFuncAttributeMaxDynamicSharedMemorySize, LDS_BYTES);
    (void)hipFuncSetAttribute((const void*)k2_attn, hipFuncAttributeMaxDynamicSharedMemorySize, LDS_BYTES);
    k1_qkv<<<256, 512, LDS_BYTES, stream>>>(x, wA, wB, woA, woB, gw, ws);
    k2_attn<<<256, 512, LDS_BYTES, stream>>>(ws, (u16*)d_out);
}
```

```cpp
#define CPF 64
#define OUT_AUX 16
#include <hip/hip_runtime.h>
#include <hip/hip_bf16.h>
#include <stdint.h>

typedef short    bf16x8 __attribute__((ext_vector_type(8)));
typedef short    s16x4  __attribute__((ext_vector_type(4)));
typedef float    f32x2  __attribute__((ext_vector_type(2)));
typedef float    f32x4  __attribute__((ext_vector_type(4)));
typedef float    f32x16 __attribute__((ext_vector_type(16)));
typedef float    f32x32 __attribute__((ext_vector_type(32)));
typedef unsigned u32x2  __attribute__((ext_vector_type(2)));
typedef unsigned u32x4  __attribute__((ext_vector_type(4)));
typedef unsigned u32x8  __attribute__((ext_vector_type(8)));
typedef unsigned u32x32 __attribute__((ext_vector_type(32)));
typedef __bf16   bf2_t  __attribute__((ext_vector_type(2)));
typedef unsigned short u16;
#define LAS __attribute__((address_space(3)))
#define DI __device__ __forceinline__

constexpr int S_ = 2048, D_ = 640;
constexpr size_t IMG     = (size_t)32 * 163840;
constexpr size_t OFF_IQ  = 0;
constexpr size_t OFF_IB  = IMG;
constexpr size_t OFF_IA  = 2 * IMG;
constexpr size_t OFF_O   = 3 * IMG;
constexpr size_t OFF_WOT = 4 * IMG;
constexpr size_t OFF_G   = OFF_WOT + (size_t)1600 * 1024;
constexpr size_t OFF_KVP = OFF_G + 8192;
constexpr size_t OFF_FL  = OFF_KVP + (size_t)256 * 2048;
constexpr int    WS_USED = (int)(OFF_FL + 288 * 128);
constexpr unsigned EMPTY = 0xFFFFFFFFu;
typedef __amdgpu_buffer_rsrc_t rsrc_t;
constexpr int SC1 = 16;

#define CSTAMP(i)
#define PSTAMP(k, i)
#define WSTAMP(i)

DI unsigned pk2(float lo, float hi) { f32x2 v = {lo, hi}; return __builtin_bit_cast(unsigned, __builtin_convertvector(v, bf2_t)); }
DI float bflo(unsigned v) { return __uint_as_float(v << 16); }
DI float bfhi(unsigned v) { return __uint_as_float(v & 0xffff0000u); }
DI int rfl(int x) { return __builtin_amdgcn_readfirstlane(x); }
DI void glds16(const void* sbase, unsigned voff, unsigned lds_base) {
    const unsigned long long a = (unsigned long long)(uintptr_t)sbase;
    const unsigned long long u = (unsigned long long)(unsigned)rfl((int)(unsigned)a) | ((unsigned long long)(unsigned)rfl((int)(unsigned)(a >> 32)) << 32);
    asm volatile("s_mov_b32 m0, %0\n\ts_nop 0\n\tglobal_load_lds_dwordx4 %1, %2" :: "s"(rfl((int)lds_base)), "v"(voff), "s"(u) : "memory");
}
#define MFMA16(a, b, c) __builtin_amdgcn_mfma_f32_16x16x32_bf16((a), (b), (c), 0, 0, 0)
#define MFMA32(a, b, c) __builtin_amdgcn_mfma_f32_32x32x16_bf16((a), (b), (c), 0, 0, 0)
#define B8(x) __builtin_bit_cast(bf16x8, x)
DI u32x2 trd(const LAS char* p) { return __builtin_bit_cast(u32x2, __builtin_amdgcn_ds_read_tr16_b64_v4i16((LAS s16x4*)p)); }
DI float pmax(float t, float c) { return __int_as_float(max(__float_as_int(t), __float_as_int(c))); }
template <int N> DI void wait_vm_bar() { asm volatile("s_waitcnt vmcnt(%0) lgkmcnt(0)\n\ts_barrier" :: "n"(N) : "memory"); }

constexpr int LDS_BYTES = 163840;
constexpr float LOG2E = 1.4426950408889634f;

DI void p1_aux(const u16* __restrict__ x, const u16* __restrict__ woA, const u16* __restrict__ woB, const u16* __restrict__ gate_w,
               char* __restrict__ ws, char* lds, int a) {
    const int tid = threadIdx.x, lane = tid & 63, wave = rfl(tid >> 6);
    const unsigned lds0 = (unsigned)(uintptr_t)lds;
    const int br = a >> 3, u = a & 7;
    if (tid < 144) *(u32x4*)(ws + OFF_FL + (a * 144 + tid) * 16) = u32x4{0u, 0u, 0u, 0u};
    const char* wsrc = (const char*)(br ? woB : woA) + (size_t)u * 102400;
    for (int i = wave; i < 100; i += 8) glds16(wsrc + i * 1024, lane * 16, lds0 + i * 1024);
    const u32x4* xs = (const u32x4*)(x + (size_t)(a * 128 + wave * 16) * D_);
    u32x4 xv[20], gw[5];
#pragma unroll
    for (int i = 0; i < 20; ++i) xv[i] = xs[i * 64 + lane];
#pragma unroll
    for (int i = 0; i < 5; ++i) gw[i] = ((const u32x4*)gate_w)[(i * 64 + lane) % 80];
    {
        const rsrc_t rs = __builtin_amdgcn_make_buffer_rsrc((void*)(ws + OFF_KVP + a * 32768), 0, 32768, 0x00020000);
#pragma unroll
        for (int i = 0; i < 4; ++i) __builtin_amdgcn_raw_buffer_store_b128(u32x4{EMPTY, EMPTY, EMPTY, EMPTY}, rs, tid * 16 + i * 8192, 0, SC1);
    }
    LAS float* part = (LAS float*)(lds + 102400) + wave * 1280;
#pragma unroll
    for (int i = 0; i < 20; ++i) {
        float s = 0.f;
#pragma unroll
        for (int j = 0; j < 4; ++j) s += bflo(xv[i][j]) * bflo(gw[i % 5][j]) + bfhi(xv[i][j]) * bfhi(gw[i % 5][j]);
        part[i * 64 + lane] = s;
    }
    wait_vm_bar<0>();
    {
        float s = 0.f;
#pragma unroll
        for (int j = 0; j < 20; ++j) s += part[lane * 20 + j];
        s += __shfl_xor(s, 1); s += __shfl_xor(s, 2);
        if ((lane & 3) == 0) ((float*)(ws + OFF_G))[a * 128 + wave * 16 + (lane >> 2)] = 1.f / (1.f + __expf(-s));
    }
    const int g = lane >> 4, hh = g & 1, ns = g >> 1, q = (lane >> 2) & 3, p = lane & 3;
    for (int un = wave; un < 100; un += 8) {
        const int e = un / 20, np = un - e * 20, nt = np * 2 + ns;
        const int r0 = e < 4 ? e * 20 + 4 * hh : (2 * hh) * 20 + 16;
        const int r1 = e < 4 ? r0 + 8 : r0 + 20;
        const int ks16 = e < 4 ? (u & 1) * 4 + e : 8 + (u & 1), cbk = nt / 5;
        const u32x2 lo = trd((const LAS char*)lds + (r0 + q) * 1280 + nt * 32 + p * 8);
        const u32x2 hi = trd((const LAS char*)lds + (r1 + q) * 1280 + nt * 32 + p * 8);
        *(u32x4*)(ws + OFF_WOT + (size_t)((cbk * 8 + (u & 6) + br) * 25 + (ks16 >> 1) * 5 + nt - cbk * 5) * 1024 + ((ks16 & 1) * 2 + hh) * 256 + (lane & 15) * 16) = u32x4{lo[0], lo[1], hi[0], hi[1]};
    }
}

#define CPF_N 256
#define CPF_M 0
#define G1_TL 3
#define G1_D 3
#define G1_D0 2
#define G1_DT 10
#define G1_DE 0
#define G1_NX 4
constexpr int G1_NW = 10 - G1_NX, G1_HB = 20 - 2 * G1_TL, G1_WOFF = G1_NX * 16384;
constexpr int g1_t(int h) { return h < 2 * G1_HB ? h >> 1 : h < 2 * G1_HB + 2 * G1_TL ? h - G1_HB : h - G1_HB - 2 * G1_TL; }
constexpr int g1_m(int h) { return h < 2 * G1_HB ? h & 1 : h < 2 * G1_HB + 2 * G1_TL ? 0 : 1; }
constexpr int g1_xi(int h) { return h < 2 * G1_HB ? (h >> 1) & 1 : h & 1; }
constexpr int g1_free(int i) { return i < 10 ? (i < G1_NX ? -1 : 2 * (i - G1_NX) + 2) : (i - 10 < G1_NW ? -1 : i - 10 - G1_NW + 1); }
struct G1Plan { int seq[30]; int cnt[22]; int pos[30]; };
constexpr G1Plan g1_plan() {
    G1Plan p = {}; bool done[30] = {}, seen[10] = {}; int ord[30] = {}, need[21] = {}, n = 0;
    for (int b = 0; b < 20; ++b) { const int u = g1_t(2 * b) >> 1; if (!seen[u]) { seen[u] = true; ord[n++] = u; } need[b] = n; ord[n++] = 10 + b; }
    need[20] = 30; n = 0;
    for (int b = 0; b <= 20; ++b) {
        const int ex = b > G1_DT ? b - G1_DT : 0, lim = 3 * (b >> 1) + 1 + (b & 1) + (b == 0 ? G1_D0 : G1_D) + (ex < G1_DE ? ex : G1_DE);
        for (int k = 0; k < 30 && k <= lim; ++k) { const int i = ord[k]; if (!done[i] && g1_free(i) < b) { done[i] = true; p.pos[i] = n; p.seq[n++] = i; } }
        p.cnt[b] = n;
    }
    return p;
}
constexpr G1Plan G1P = g1_plan();
constexpr int g1_wait(int b) { const int x = G1P.pos[g1_t(2 * b) >> 1], w = G1P.pos[10 + b]; return 4 * (G1P.cnt[b] - 1 - (x > w ? x : w)); }
constexpr bool g1_ok() { for (int b = 0; b < 20; ++b) if (g1_wait(b) < 0 || g1_wait(b) > 60) return false; return G1P.cnt[19] == 30 && G1_NX >= G1_TL && G1_NX <= 4; }
static_assert(g1_ok(), "loader plan");
#define G1_ROT ((0x27057250 >> (4 * xcd)) & 15)

#define LST_P
#define LST_A
template <int T> struct G1Load {
    template <class FX, class FW> static DI void run(FX&& ldx, FW&& ldw LST_P) {
        asm volatile("s_waitcnt vmcnt(%0)\n\ts_barrier" :: "n"(g1_wait(T)) : "memory");
#pragma unroll
        for (int n = G1P.cnt[T]; n < G1P.cnt[T + 1]; ++n) { const int i = G1P.seq[n]; if (i < 10) ldx(i); else ldw(i - 10); }
        G1Load<T + 1>::run(ldx, ldw LST_A);
    }
};
template <> struct G1Load<20> { template <class FX, class FW> static DI void run(FX&&, FW&& LST_P) {} };

struct G1Ctx {
    f32x4 acc[4][8];
    bf16x8 x[2][4]; u32x2 w[2][4][2];
    const LAS char* xa[2]; const LAS char* wa[8];
    int so[4];
    rsrc_t rB; float sc; f32x2 m[4]; u32x4 o;
    unsigned pf0, pf1;
};
#define SB0() __builtin_amdgcn_sched_barrier(0)
#define G1_SP 12
template <int J, int I> DI void g1_side(G1Ctx& c) {
    constexpr int K = (J - (40 - 2 * G1_TL)) * 16 + I;
    if (CPF && J == 20 && I == 12) { asm volatile("s_waitcnt vmcnt(0)" :: "v"(c.pf0), "v"(c.pf1)); SB0(); }
    if (K > 0 && K <= 8 * G1_SP && (K - 1) % G1_SP == 8) asm volatile("" :: "v"(c.o));
    if (K >= 0 && K < 8 * G1_SP && K % G1_SP < 9) {
        constexpr int U = K < 0 ? 0 : K / G1_SP, e = K < 0 ? 0 : K % G1_SP, ni = (U >> 1) & 3, a = U & 1;
        constexpr int mi = e == 0 ? 0 : e == 1 ? 1 : e == 3 ? 2 : e == 5 ? 3 : -1, ci = e == 2 ? 0 : e == 4 ? 1 : e == 6 ? 2 : e == 7 ? 3 : -1;
        if (mi >= 0) { const f32x4 v = c.acc[2 * a + ((mi & 3) >> 1)][ni]; c.m[mi & 3] = f32x2{v[(mi & 1) * 2], v[(mi & 1) * 2 + 1]} * c.sc; }
        if (ci >= 0) c.o[ci & 3] = pk2(c.m[ci & 3][0], c.m[ci & 3][1]);
        if (e == 8) __builtin_amdgcn_raw_buffer_store_b128(c.o, c.rB, c.so[ni] + a * 2560, 0, SC1);
        SB0();
    }
}
template <int J> struct G1Step {
    static DI void run(G1Ctx& c) {
        constexpr int N = J + 1, t = g1_t(N), xo = ((t >> 1) % G1_NX) * 16384, sl = (N >> 1) % G1_NW, wo = (sl & 3) * 16384 + (N & 1) * 8192, wi = (sl >> 2) * 4;
        constexpr int ps = J & 1, ma = g1_m(J) * 4, xc = g1_xi(J), xn = g1_xi(N);
#define G1_M(i) c.acc[(i) & 3][ma + ((i) >> 2)] = MFMA16(c.x[xc][(i) & 3], B8(((u32x4){c.w[ps][(i) >> 2][0][0], c.w[ps][(i) >> 2][0][1], c.w[ps][(i) >> 2][1][0], c.w[ps][(i) >> 2][1][1]})), \
                c.acc[(i) & 3][ma + ((i) >> 2)]); SB0(); g1_side<J, i>(c);
#define G1_X(i) if (N < 40) c.x[xn][i] = *(const LAS bf16x8*)(c.xa[t & 1] + xo + ((i) >> 1) * 4096 + ((i) & 1) * 1024); SB0();
#define G1_W(i, e) if (N < 40) c.w[ps ^ 1][i][e] = trd(c.wa[wi + (i)] + wo + (e) * 1024); SB0();
        if (ps) {
            G1_M(0) G1_M(1) G1_M(2)
            if (N < 40) asm volatile("s_waitcnt lgkmcnt(0)\n\ts_barrier" ::: "memory");
            SB0();
            G1_X(0) G1_M(3) G1_W(0, 0) G1_M(4) G1_W(0, 1) G1_M(5) G1_X(1) G1_M(6) G1_X(2) G1_M(7) G1_X(3) G1_M(8) G1_W(1, 0) G1_M(9) G1_W(1, 1) G1_M(10)
            G1_W(2, 0) G1_M(11) G1_W(2, 1) G1_M(12) G1_W(3, 0) G1_M(13) G1_W(3, 1) G1_M(14) G1_M(15)
        } else if (xn != xc) {
            G1_M(0) G1_X(0) G1_M(1) G1_W(0, 0) G1_M(2) G1_W(0, 1) G1_M(3) G1_X(1) G1_M(4) G1_X(2) G1_M(5) G1_X(3) G1_M(6) G1_W(1, 0) G1_M(7) G1_W(1, 1)
            G1_M(8) G1_W(2, 0) G1_M(9) G1_W(2, 1) G1_M(10) G1_W(3, 0) G1_M(11) G1_W(3, 1) G1_M(12) G1_M(13) G1_M(14) G1_M(15)
        } else {
            G1_M(0) G1_W(0, 0) G1_M(1) G1_W(0, 1) G1_M(2) G1_W(1, 0) G1_M(3) G1_W(1, 1) G1_M(4) G1_W(2, 0) G1_M(5) G1_W(2, 1) G1_M(6) G1_W(3, 0) G1_M(7) G1_W(3, 1)
            G1_M(8) G1_M(9) G1_M(10) G1_M(11) G1_M(12) G1_M(13) G1_M(14) G1_M(15)
        }
        G1Step<J + 1>::run(c);
    }
};
template <> struct G1Step<40> { static DI void run(G1Ctx&) {} };
template <int K> struct G1Rest { static DI void run(G1Ctx& c) { g1_side<40 - 2 * G1_TL + K / 16, K % 16>(c); G1Rest<K + 1>::run(c); } };
template <> struct G1Rest<8 * G1_SP> { static DI void run(G1Ctx&) {} };

DI void phase1(const u16* __restrict__ x, const u16* __restrict__ wA, const u16* __restrict__ wB, char* __restrict__ ws, char* lds, int bid,
               unsigned pf0, unsigned pf1) {
    const int tid = threadIdx.x, lane = tid & 63, wave = rfl(tid >> 6);
    const unsigned lds0 = (unsigned)(uintptr_t)lds;
    const int xcd = bid & 7, j = (xcd >> 1) * 30 + (bid >> 3);
    const int ct = j >> 3, mt = (xcd & 1) * 8 + (j & 7);
    const int type = rfl(ct / 5), rot = rfl(G1_ROT);
    if (__builtin_expect(wave >= 4, 1)) {
        const int lw = wave - 4;
        unsigned xo[4], wo[2];
#pragma unroll
        for (int i = 0; i < 4; ++i) { const int row = (lw * 4 + i) * 8 + (lane >> 3); xo[i] = (unsigned)((mt * 128 + row) * 1280 + (((lane & 7) ^ ((row >> 1) & 3) ^ ((row >> 2) & 4)) << 4)); }
#pragma unroll
        for (int i = 0; i < 2; ++i) { const int k = (lw * 2 + i) * 4 + (lane >> 4), s16 = lane & 15;
            wo[i] = (unsigned)(k * 3840 + ct * 256 + ((s16 ^ ((k & 3) << 2) ^ ((k >> 2) & 2)) << 4)); }
        auto ldx = [&](int u, unsigned st = 0u) {
            int kk = u + rot; kk -= kk >= 10 ? 10 : 0;
            const unsigned slot = lds0 + (u % G1_NX) * 16384 + lw * 4096;
#pragma unroll
            for (int i = 0; i < 4; ++i) glds16((const char*)x + kk * 128, xo[i], slot + i * 1024);
        };
        auto ldw = [&](int s) {
            int kk = (g1_t(2 * s) >> 1) + rot; kk -= kk >= 10 ? 10 : 0;
            const unsigned slot = lds0 + G1_WOFF + (s % G1_NW) * 16384 + lw * 2048;
#pragma unroll
            for (int e = 0; e < 2; ++e) {
                const char* src = (const char*)(g1_m(2 * s + e) ? wA : wB) + (kk * 64 + (g1_t(2 * s + e) & 1) * 32) * 3840;
                glds16(src, wo[0], slot + e * 8192); glds16(src, wo[1], slot + e * 8192 + 1024);
            }
        };
#pragma unroll
        for (int n = 0; n < G1P.cnt[0]; ++n) { const int i = G1P.seq[n]; if (i < 10) ldx(i); else ldw(i - 10); }
        G1Load<0>::run(ldx, ldw LST_A);
        return;
    }
    const int wm = wave & 1, wn = wave >> 1;
    const int i16 = lane & 15, g = lane >> 4, q = (lane >> 2) & 3, p = lane & 3;
    G1Ctx c;
    c.pf0 = pf0; c.pf1 = pf1;
    const int xr = (i16 >> 3) * 16 + (i16 & 7);
#pragma unroll
    for (int ks = 0; ks < 2; ++ks) { c.xa[ks] = (const LAS char*)lds + (wm * 64 + xr) * 128 + (((ks * 4 + g) ^ ((xr >> 1) & 3) ^ ((xr >> 2) & 4)) << 4); asm volatile("" : "+v"(c.xa[ks])); }
#pragma unroll
    for (int i = 0; i < 8; ++i) { c.wa[i] = (const LAS char*)lds + G1_WOFF + (i >> 2) * 65536 + (8 * g + q) * 256 + (((wn * 4 + (i & 3)) ^ (q << 1) ^ (g & 1)) << 5) + p * 8;
        asm volatile("" : "+v"(c.wa[i])); }
#pragma unroll
    for (int i = 0; i < 4; ++i) { const int cc = (ct - type * 5) * 128 + wn * 64 + i * 16 + i16, h = cc / 20; c.so[i] = h * 163840 + g * 320 + (cc - h * 20) * 16; asm volatile("" : "+v"(c.so[i])); }
    constexpr float QS = 0.22360679774997896f * LOG2E;
    int sci = type == 0 ? __builtin_bit_cast(int, QS) : 0x3F800000;
    asm volatile("" : "+s"(sci));
    c.sc = __int_as_float(sci);
    const int tb = (mt * 4 + wm * 2) * 2560;
    const int sB = tb + (type == 0 ? (int)OFF_IQ : (int)OFF_IB + (type - 1) * 1280), sA = tb + (type == 0 ? (int)OFF_IQ + 1280 : (int)OFF_IA + (type - 1) * 1280);
    c.rB = __builtin_amdgcn_make_buffer_rsrc((void*)(ws + sB), 0, (int)IMG, 0x00020000);
    const rsrc_t rA = __builtin_amdgcn_make_buffer_rsrc((void*)(ws + sA), 0, (int)IMG, 0x00020000);
    {
        const float a0 = type < 2 ? 1e-6f : 0.f;
        f32x4 a4 = {a0, a0, a0, a0};
        asm volatile("" : "+v"(a4));
#pragma unroll
        for (int i = 0; i < 16; ++i) { c.acc[i & 3][i >> 2] = f32x4{0.f, 0.f, 0.f, 0.f}; c.acc[i & 3][4 + (i >> 2)] = a4; }
    }
    __builtin_amdgcn_s_setprio(2);
    asm volatile("s_barrier" ::: "memory");
#pragma unroll
    for (int i = 0; i < 4; ++i) {
        c.x[0][i] = *(const LAS bf16x8*)(c.xa[0] + (i >> 1) * 4096 + (i & 1) * 1024);
        if (i == 0) { c.w[0][0][0] = trd(c.wa[0]); c.w[0][0][1] = trd(c.wa[0] + 1024); }
    }
#pragma unroll
    for (int i = 1; i < 4; ++i) { c.w[0][i][0] = trd(c.wa[i]); c.w[0][i][1] = trd(c.wa[i] + 1024); }
    SB0();
    G1Step<0>::run(c);
    PSTAMP(0, 2);
    G1Rest<(32 * G1_TL < 8 * G1_SP ? 32 * G1_TL : 8 * G1_SP)>::run(c);
    if (type < 2) {
#pragma unroll
        for (int i = 0; i < 4; ++i)
#pragma unroll
            for (int a = 0; a < 2; ++a) {
                u32x4 o;
#pragma unroll
                for (int e = 0; e < 4; ++e) {
                    const f32x2 t = {c.acc[2 * a + (e >> 1)][4 + i][(e & 1) * 2], c.acc[2 * a + (e >> 1)][4 + i][(e & 1) * 2 + 1]};
                    const f32x2 m = __builtin_elementwise_fma(t, f32x2{LOG2E, LOG2E}, f32x2{-1e-6f * LOG2E, -1e-6f * LOG2E});
                    const f32x2 ex = {__builtin_amdgcn_fmed3f(__builtin_amdgcn_exp2f(m[0]), 0.f, 1.f), __builtin_amdgcn_fmed3f(__builtin_amdgcn_exp2f(m[1]), 0.f, 1.f)};
                    const f32x2 r = f32x2{pmax(t[0], 1e-6f), pmax(t[1], 1e-6f)} + ex;
                    o[e] = pk2(r[0], r[1]);
                }
                __builtin_amdgcn_raw_buffer_store_b128(o, rA, c.so[i] + a * 2560, 0, SC1);
            }
    } else {
#pragma unroll
        for (int i = 0; i < 4; ++i)
#pragma unroll
            for (int a = 0; a < 2; ++a) {
                const f32x4 u = c.acc[2 * a][4 + i], v = c.acc[2 * a + 1][4 + i];
                __builtin_amdgcn_raw_buffer_store_b128(u32x4{pk2(u[0], u[1]), pk2(u[2], u[3]), pk2(v[0], v[1]), pk2(v[2], v[3])}, rA, c.so[i] + a * 2560, 0, SC1);
            }
    }
}

__global__ __launch_bounds__(512, 2) void k1_qkv(const u16* __restrict__ x, const u16* __restrict__ wA, const u16* __restrict__ wB,
                                                 const u16* __restrict__ woA, const u16* __restrict__ woB, const u16* __restrict__ gate_w,
                                                 char* __restrict__ ws) {
    extern __shared__ __attribute__((aligned(16))) char lds[];
    int bid = blockIdx.x;
    asm volatile("" : "+s"(bid));
    PSTAMP(0, 0);
    unsigned pf0, pf1 = 0u;
    {
        unsigned long long pc;
        asm volatile("s_getpc_b64 %1\n.Lk1_pc:\n\tv_lshl_add_u32 %0, %2, 7, %3\n\tv_min_u32 %0, .Lk2_last-.Lk1_pc, %0\n\t"
                     "v_cmp_gt_u32 vcc, %4, %2\n\ts_mov_b64 exec, vcc\n\tglobal_load_dword %0, %0, %1\n\ts_mov_b64 exec, -1"
                     : "=&v"(pf0), "=&s"(pc) : "v"(threadIdx.x), "s"((bid >> 3) * (CPF * 128) + 64), "n"(CPF) : "memory", "vcc");
    }
    if (__builtin_expect(bid < 240, 1)) phase1(x, wA, wB, ws, lds, bid, pf0, pf1);
    else { p1_aux(x, woA, woB, gate_w, ws, lds, bid - 240); asm volatile("" :: "v"(pf0), "v"(pf1)); }
}

#define STR(x) #x
#define XS(x) STR(x)
#define A_E(i) "v_exp_f32 v" XS(i) ", v" XS(i) "\n"
#define A_C(d, a, b) "v_cvt_pk_bf16_f32 v" XS(d) ", v" XS(a) ", v" XS(b) "\n"
#define A_G(a, b, c, d, p0, p1) A_E(a) A_E(b) A_E(c) A_E(d) A_C(p0, a, b) A_C(p1, c, d)
#define A_M(d, a, b, c) "v_mfma_f32_32x32x16_bf16 " d ", " a ", " b ", " c "\n"
#define A_STEP(C0,C1,C2,C3,C4,C5,C6,C7,C8,C9,C10,C11,C12,C13,C14,C15, SN, KM, KT, V1, V0, NKMa, NKMb, NKT, NV1, NV0, OK0, OK1, OV1, OV0, PRIO) \
    "s_waitcnt lgkmcnt(0)\n" \
    A_M(SN, KM, "v[120:123]", "0") \
    "ds_read_b64_tr_b16 " NKMa ", v128 offset:" XS(OK0) "\n" \
    "ds_read_b64_tr_b16 " NKMb ", v128 offset:" XS(OK1) "\n" \
    "ds_read_b64_tr_b16 " NKT ", v129 offset:" XS(OK0) "\n" \
    A_G(C0, C1, C2, C3, 112, 113) \
    A_M("v[64:79]", V1, "v[116:119]", "v[64:79]") \
    "s_mov_b64 exec, s[72:73]\n" \
    "ds_read_b128 " NV1 ", v130 offset:" XS(OV1) "\n" \
    "ds_read_b128 " NV0 ", v130 offset:" XS(OV0) "\n" \
    "s_mov_b64 exec, -1\n" \
    A_G(C4, C5, C6, C7, 114, 115) \
    A_M(SN, KT, "v[124:127]", SN) \
    A_G(C8, C9, C10, C11, 116, 117) \
    A_M("v[64:79]", V0, "v[112:115]", "v[64:79]") \
    "s_setprio " XS(PRIO) "\n" \
    A_G(C12, C13, C14, C15, 118, 119)
#define A_EVEN(OK0, OK1, OV1, OV0, PRIO) A_STEP(32,33,34,35,36,37,38,39,40,41,42,43,44,45,46,47, "v[48:63]", "v[80:83]", "v[84:87]", "v[88:91]", "v[92:95]", \
    "v[96:97]", "v[98:99]", "v[100:101]", "v[104:107]", "v[108:111]", OK0, OK1, OV1, OV0, PRIO)
#define A_ODD(OK0, OK1, OV1, OV0, PRIO) A_STEP(48,49,50,51,52,53,54,55,56,57,58,59,60,61,62,63, "v[32:47]", "v[96:99]", "v[100:103]", "v[104:107]", "v[108:111]", \
    "v[80:81]", "v[82:83]", "v[84:85]", "v[88:91]", "v[92:95]", OK0, OK1, OV1, OV0, PRIO)

#define A_LD(g, off) "global_load_lds_dwordx4 v131, " g " offset:" XS(off) "\n"
#define A_LDA(off) "s_cbranch_scc1 .Lattn_s" XS(off) "\n" A_LD("s[74:75]", off) ".Lattn_s" XS(off) ":\n"
#define A_BODY \
        "s_mov_b32 m0, s78\n s_cmp_eq_u32 s80, 2\n s_nop 3\n" \
        A_EVEN(5120, 5184, 640, 2560, 2)     A_ODD(7680, 7744, 3200, 5120, 1) \
        A_EVEN(10240, 10304, 5760, 7680, 0)  A_ODD(12800, 12864, 8320, 10240, 3) \
        "s_waitcnt vmcnt(0) lgkmcnt(0)\n s_mov_b64 exec, 1\n ds_add_u32 v127, v86\n s_mov_b64 exec, -1\n" \
        A_EVEN(15360, 15424, 10880, 12800, 2) A_LDA(0) A_ODD(17920, 17984, 13440, 15360, 1) A_LDA(1024) \
        A_EVEN(20480, 20544, 16000, 17920, 0) A_LDA(2048) \
        "s_cmp_eq_u32 s80, 1\n s_cbranch_scc0 .Lattn_skip\n s_mov_b32 m0, s79\n s_nop 0\n" \
        A_LD("s[76:77]", 0) A_LD("s[76:77]", 1024) A_LD("s[76:77]", 2048) ".Lattn_skip:\n" \
        "ds_read_b32 v132, v127\n" \
        A_ODD(23040, 23104, 18560, 20480, 3) \
        "v_add_u32 v128, 0x5000, v128\n v_add_u32 v129, 0x5000, v129\n v_add_u32 v130, 0x5000, v130\n" \
        "s_waitcnt lgkmcnt(0)\n s_nop 0\n"
#define A_OPERANDS \
        : "+{v[32:63]}"(S), "+{v[64:79]}"(O), "+{v[80:111]}"(OPS), "+{v[112:119]}"(P), "+{v[128:131]}"(AD), "={v132}"(cnt) \
        : "{v[120:127]}"(Q), "{s[72:73]}"(vmask), "{s[74:75]}"(ga), "{s[76:77]}"(gb), "{s78}"(ma), "{s79}"(mb), "{s80}"(enb) \
        : "memory", "scc", "s82", "s83"
template <bool FIRST>
DI unsigned attn_chunk(f32x32& S, f32x16& O, u32x32& OPS, u32x8& P, u32x4& AD, const u32x8& Q, unsigned long long vmask,
                   unsigned ma, unsigned long long ga, unsigned mb, unsigned long long gb, unsigned enb) {
    unsigned cnt;
    if (FIRST) asm volatile("s_call_b64 s[82:83], .Lattn_body\n s_branch .Lattn_done\n .p2align 6\n .Lattn_body:\n" A_BODY "s_setpc_b64 s[82:83]\n .Lattn_done:\n" A_OPERANDS);
    else       asm volatile("s_call_b64 s[82:83], .Lattn_body\n" A_OPERANDS);
    return cnt;
}
DI void wait_cnt(char* lds, int off, unsigned cnt, int n) {
    if (__builtin_expect((unsigned)rfl((int)cnt) < (unsigned)(n * 0x3F80), 0)) {
#pragma clang loop unroll(disable)
        do { __builtin_amdgcn_s_sleep(1); cnt = *(volatile LAS unsigned*)(lds + off); } while ((unsigned)rfl((int)cnt) < (unsigned)(n * 0x3F80));
    }
    asm volatile("" ::: "memory");
}

DI void wait_cnt(char* lds, int off, int n) { wait_cnt(lds, off, *(volatile LAS unsigned*)(lds + off), n); }
template <int OFF> DI void add_cnt(unsigned lds0) {
    asm volatile("s_waitcnt lgkmcnt(0)\n\ts_mov_b64 exec, 1\n\tds_add_u32 %0, %1 offset:%2\n\ts_mov_b64 exec, -1" :: "v"(lds0), "v"(0x3F80u), "n"(OFF) : "memory"); }

DI float xhalf_max(float m) { auto rr = __builtin_amdgcn_permlane32_swap(__float_as_uint(m), __float_as_uint(m), false, false);
    return fmaxf(__uint_as_float(rr[0]), __uint_as_float(rr[1])); }
DI float hi_half(float v) {
    auto rr = __builtin_amdgcn_permlane32_swap(__float_as_uint(v), __float_as_uint(v), false, false);
    return __uint_as_float(rr[1]); }
DI float sum16(float v) { auto rr = __builtin_amdgcn_permlane16_swap(__float_as_uint(v), __float_as_uint(v), false, false);
    return __uint_as_float(rr[0]) + __uint_as_float(rr[1]); }
DI float sum32(float v) { auto rr = __builtin_amdgcn_permlane32_swap(__float_as_uint(v), __float_as_uint(v), false, false);
    return __uint_as_float(rr[0]) + __uint_as_float(rr[1]); }
DI f32x4 sum_rows(f32x4 a, f32x4 b) {
    f32x4 s = a + b;
#pragma unroll
    for (int i = 0; i < 4; ++i) s[i] = sum32(sum16(s[i]));
    return s;
}
DI unsigned long long uni64(const void* p) { const unsigned long long a = (unsigned long long)(uintptr_t)p;
    return (unsigned long long)(unsigned)rfl((int)(unsigned)a) | ((unsigned long long)(unsigned)rfl((int)(unsigned)(a >> 32)) << 32); }
DI unsigned full(unsigned v) { return v < EMPTY ? v : EMPTY - 1u; }
DI unsigned umax4(unsigned m, u32x4 v) { return max(max(max(m, v[0]), max(v[1], v[2])), v[3]); }
DI void store_o(rsrc_t rs, const f32x16& o, float f, int rt, int br, int h, int lane) {
    const int rb = (int)OFF_O + (rt * 80 + ((br ? 0 : 4) + (h >> 3)) * 10) * 1024;
    __builtin_amdgcn_raw_buffer_store_b128(u32x4{full(pk2(o[0] * f, o[1] * f)), full(pk2(o[2] * f, o[3] * f)), full(pk2(o[4] * f, o[5] * f)), full(pk2(o[6] * f, o[7] * f))},
                                           rs, lane * 16, rb + (h & 7) * 1024, SC1);
    if (lane < 32) __builtin_amdgcn_raw_buffer_store_b64(u32x2{full(pk2(o[8] * f, o[9] * f)), full(pk2(o[10] * f, o[11] * f))},
                                           rs, lane * 16, rb + (8 + ((h >> 2) & 1)) * 1024 + ((h >> 1) & 1) * 512 + (h & 1) * 8, SC1);
}
DI void empty_o(rsrc_t rs, int rt, int h, int lane) {
    const int rb = (int)OFF_O + (rt * 80 + (h >> 3) * 10) * 1024;
    u32x4 e = {EMPTY, EMPTY, EMPTY, EMPTY};
    __builtin_amdgcn_raw_buffer_store_b128(e, rs, lane * 16, rb + (h & 7) * 1024, SC1);
    if (lane < 32) __builtin_amdgcn_raw_buffer_store_b64(u32x2{e[0], e[1]}, rs, lane * 16, rb + (8 + ((h >> 2) & 1)) * 1024 + ((h >> 1) & 1) * 512 + (h & 1) * 8, SC1);
    asm volatile("" :: "v"(e));
}
DI void set_flag(rsrc_t rs, int off) { __builtin_amdgcn_raw_buffer_store_b32(1u, rs, 0, off, SC1); }
DI unsigned get_flag(rsrc_t rs, int voff, int off) { return __builtin_amdgcn_raw_buffer_load_b32(rs, voff, off, SC1); }
DI unsigned a_ld1(unsigned long long sb, unsigned voff) { unsigned v; asm volatile("global_load_dword %0, %1, %2 sc1" : "=v"(v) : "v"(voff), "s"(sb) : "memory"); return v; }
DI u32x4 a_ld4s(unsigned long long sb, unsigned voff) { u32x4 v; asm volatile("global_load_dwordx4 %0, %1, %2 sc1" : "=v"(v) : "v"(voff), "s"(sb) : "memory"); return v; }
template <int OFF> DI u32x4 a_ld4so(unsigned long long sb, unsigned voff) { u32x4 v;
    asm volatile("global_load_dwordx4 %0, %1, %2 offset:%3 sc1" : "=v"(v) : "v"(voff), "s"(sb), "n"(OFF) : "memory"); return v; }
template <int OFF> DI u32x4 a_ld4(unsigned long long sb, unsigned voff) { u32x4 v;
    asm volatile("global_load_dwordx4 %0, %1, %2 offset:%3" : "=v"(v) : "v"(voff), "s"(sb), "n"(OFF) : "memory"); return v; }
template <class T> DI void landed(T& v) { asm volatile("" : "+v"(v)); }
DI void wait_flag(rsrc_t rs, int voff, int off, unsigned v) {
    if (__builtin_expect(!__all(v == 1u), 0)) {
#pragma clang loop unroll(disable)
        for (int spin = 0; spin < (1 << 22); ++spin) { if (__all(get_flag(rs, voff, off) == 1u)) break; __builtin_amdgcn_s_sleep(1); }
    }
    __builtin_amdgcn_fence(__ATOMIC_ACQUIRE, "wavefront");
    asm volatile("" ::: "memory");
}

#define FILL_C 2
constexpr int L_SP = 136192, L_K0 = 152320, L_SQ = 153600, L_QA = 1280, L_KVF = 11520, L_WO = 13312;

__global__ __launch_bounds__(512, 2) void k2_attn(char* __restrict__ ws, u16* __restrict__ out) {
    extern __shared__ __attribute__((aligned(16))) char lds[];
    int bid = blockIdx.x;
    asm volatile("" : "+s"(bid));
    PSTAMP(1, 0); WSTAMP(0);
    const int tid = threadIdx.x, lane = tid & 63, wave = rfl(tid >> 6);
    const int h = (bid & 7) * 4 + ((bid >> 3) & 3), qb = bid >> 5;
    const int oq = bid & 7, rb = oq * 4 + ((bid >> 3) & 3), cb = bid >> 5;
    const int r = lane & 31, hh = lane >> 5;
    const unsigned lds0 = (unsigned)(uintptr_t)lds;
    const rsrc_t rs = __builtin_amdgcn_make_buffer_rsrc((void*)ws, 0, WS_USED, 0x00020000);
    const char* kvb = ws + OFF_IB + (size_t)h * 163840;
    const char* qim = ws + OFF_IQ + (size_t)h * 163840 + qb * 20480;
    const int wr = (wave & 3) * 2 + (wave >> 2), rt = qb * 8 + wr;
    const int rc = r < 20 ? r : 19;
    glds16(qim + wr * 2560, lane * 16, lds0 + L_SQ + wave * 1280);
    glds16(qim + wr * 2560 + 256, lane * 16, lds0 + L_SQ + wave * 1280 + 256);
    glds16(kvb + (wave & 1) * 256, lane * 16, lds0 + L_K0 + (wave & 1) * 256);
#pragma unroll
    for (int i = 0; i < 3; ++i) glds16(kvb + (1 + i * 8 + wave) * 1024, lane * 16, lds0 + (1 + i * 8 + wave) * 1024);
    const char* ap = ws + OFF_IA + (size_t)h * 163840 + rt * 2560 + hh * 320 + rc * 16;
    u32x4 a0, a1, b0, b1; float gate;
    asm volatile("global_load_dwordx4 %0, %5, off\n\tglobal_load_dwordx4 %1, %5, off offset:640\n\tglobal_load_dwordx4 %2, %5, off offset:1280\n\t"
                 "global_load_dwordx4 %3, %5, off offset:1920\n\tglobal_load_dword %4, %6, off"
                 : "=&v"(a0), "=&v"(a1), "=&v"(b0), "=&v"(b1), "=&v"(gate) : "v"(ap), "v"((const float*)(ws + OFF_G) + rt * 32 + r) : "memory");
#pragma unroll
    for (int i = 0; i < 3; ++i) glds16(kvb + (25 + i * 8 + wave) * 1024, lane * 16, lds0 + (25 + i * 8 + wave) * 1024);
    if (tid < 4) *(LAS unsigned*)(lds + tid * 4) = 0u;
    const u32x4 ONES = {0x3F803F80u, 0x3F803F80u, 0x3F803F80u, 0x3F803F80u};
    const f32x16 Z16 = {};
    wait_vm_bar<8>();
    PSTAMP(1, 1); WSTAMP(1);
    const int g = lane >> 4, q = (lane >> 2) & 3, p = lane & 3;
    const int tro = (g & 1) * 640 + (p & 1) * 320 + q * 16 + (p >> 1) * 8;
    const LAS char* L = (const LAS char*)lds;
    const LAS char* kmp = L + tro + hh * 128;
    const LAS char* ktp = L + tro + 256;
    const LAS char* vp  = L + 1280 + hh * 320 + r * 16;
    const int ent = tid < 126 ? tid : 125;
    u32x8 Q;
    {
        const u32x2 a = trd(kmp + L_SQ + wave * 1280), b = trd(kmp + L_SQ + wave * 1280 + 64), c = trd(ktp + L_SQ + wave * 1280);
        Q[0] = a[0]; Q[1] = a[1]; Q[2] = b[0]; Q[3] = b[1]; Q[4] = hh ? 0u : c[0]; Q[5] = hh ? 0u : c[1]; Q[6] = 0u; Q[7] = 0u;
    }
    const bf16x8 qm = B8(((u32x4){Q[0], Q[1], Q[2], Q[3]}));
    f32x32 S; f32x16 O = {}; u32x32 OPS; u32x8 P = {}; u32x4 AD;
    {
        const u32x2 a = trd(kmp + L_K0), b = trd(kmp + L_K0 + 64), c = trd(ktp + L_K0);
        const bf16x8 k0 = B8(((u32x4){a[0], a[1], b[0], b[1]})), k1 = B8(((u32x4){c[0], c[1], 0x3F80u, 0u}));
        const f32x16 sm = MFMA32(k0, qm, Z16);
        f32x16 s0 = MFMA32(k1, B8(((u32x4){Q[4], Q[5], 0u, 0u})), sm);
        float m = s0[0];
#pragma unroll
        for (int i = 1; i < 16; ++i) m = fmaxf(m, s0[i]);
        m = xhalf_max(m);
        Q[6] = hh ? 0u : (pk2(-m, 0.f) & 0xffffu);
        s0 = MFMA32(k1, B8(((u32x4){Q[4], Q[5], Q[6], 0u})), sm);
#pragma unroll
        for (int i = 0; i < 16; ++i) { S[i] = s0[i]; S[16 + i] = 0.f; }
        const unsigned vc = r == 20 ? 0x3F803F80u : 0u;
#pragma unroll
        for (int i = 0; i < 32; ++i) OPS[i] = (i & 8) ? vc : 0u;
        const u32x2 a1 = trd(kmp + 2560), b1 = trd(kmp + 2560 + 64), c1 = trd(ktp + 2560);
        OPS[0] = a1[0]; OPS[1] = a1[1]; OPS[2] = b1[0]; OPS[3] = b1[1]; OPS[4] = c1[0]; OPS[5] = c1[1]; OPS[6] = 0x3F80u; OPS[22] = 0x3F80u;
        if (r < 20) { const u32x4 v0 = *(const LAS u32x4*)vp; OPS[12] = v0[0]; OPS[13] = v0[1]; OPS[14] = v0[2]; OPS[15] = v0[3]; }
        AD[0] = (unsigned)(uintptr_t)kmp; AD[1] = (unsigned)(uintptr_t)ktp; AD[2] = (unsigned)(uintptr_t)vp; AD[3] = lane * 16;
    }
    {
        asm volatile("s_waitcnt vmcnt(3)" : "+v"(a0), "+v"(a1), "+v"(b0), "+v"(b1), "+v"(gate) :: "memory");
        if (r >= 20) { a0 = u32x4{0u, 0u, 0u, 0u}; a1 = a0; b0 = ONES; b1 = ONES; }
        f32x16 kvacc = MFMA32(B8(a0), B8(b0), Z16); kvacc = MFMA32(B8(a1), B8(b1), kvacc);
        if (r <= 20) {
            LAS f32x4* dst = (LAS f32x4*)(lds + L_SP + ((wave * 2 + hh) * 21 + r) * 48);
            dst[0] = f32x4{kvacc[0], kvacc[1], kvacc[2], kvacc[3]};
            dst[1] = f32x4{kvacc[4], kvacc[5], kvacc[6], kvacc[7]};
            dst[2] = f32x4{kvacc[8], kvacc[9], kvacc[10], kvacc[11]};
        }
    }
    const unsigned long long vmask = 0x000FFFFF000FFFFFull;
    const int j = wave & 3, isE = (wave >> 2) ^ 1;
    const char* wop = ws + OFF_WOT + (size_t)((cb * 4 + j) * 50) * 1024;
    const int flA = (int)OFF_FL + (oq * 4 + j) * 128, flv = (lane & 7) * 16;
    const int ov = ((g >> 1) * 1024 + (g & 1) * 512 + (lane & 15) * 16), os = (int)OFF_O + (rb * 160 + j * 10) * 1024;
    unsigned fl = 0u;
    u32x4 kp[8], wa[20], aa[2][10];
    const unsigned long long wsu = uni64(ws);
    const unsigned long long kb = uni64(ws + OFF_KVP + 4096 + h * 16384), kb2 = uni64(ws + OFF_KVP + 12288 + h * 16384);
    const unsigned long long ob[2][2] = {{uni64(ws + os + 4096), uni64(ws + os + 8192)}, {uni64(ws + os + 81920 + 4096), uni64(ws + os + 81920 + 8192)}};
#define LD_H(A, vo, hf, en, C) asm volatile("s_cmp_lg_u32 %13, 0\n\ts_cselect_b64 exec, -1, 0\n\t" \
        "global_load_dwordx4 %0, %10, %11 offset:-4096 sc1\n\tglobal_load_dwordx4 %1, %10, %11 offset:-3840 sc1\n\t" \
        "global_load_dwordx4 %2, %10, %11 offset:-2048 sc1\n\tglobal_load_dwordx4 %3, %10, %11 offset:-1792 sc1\n\t" \
        "global_load_dwordx4 %4, %10, %11 sc1\n\tglobal_load_dwordx4 %5, %10, %11 offset:256 sc1\n\t" \
        "global_load_dwordx4 %6, %10, %11 offset:2048 sc1\n\tglobal_load_dwordx4 %7, %10, %11 offset:2304 sc1\n\t" \
        "global_load_dwordx4 %8, %10, %12 sc1\n\tglobal_load_dwordx4 %9, %10, %12 offset:256 sc1\n\ts_mov_b64 exec, -1" \
        : C(A[0]), C(A[1]), C(A[2]), C(A[3]), C(A[4]), C(A[5]), C(A[6]), C(A[7]), C(A[8]), C(A[9]) \
        : "v"(vo), "s"(ob[hf][0]), "s"(ob[hf][1]), "s"(en) : "memory", "scc");
#define LD_W4(A, f) asm volatile("s_cmp_lg_u32 %6, 0\n\ts_cselect_b64 exec, -1, 0\n\t" \
        "global_load_dwordx4 %0, %4, %5\n\tglobal_load_dwordx4 %1, %4, %5 offset:1024\n\t" \
        "global_load_dwordx4 %2, %4, %5 offset:2048\n\tglobal_load_dwordx4 %3, %4, %5 offset:3072\n\ts_mov_b64 exec, -1" \
        : "=&v"((A)[0]), "=&v"((A)[1]), "=&v"((A)[2]), "=&v"((A)[3]) : "v"(lane * 16), "s"(uni64(wop + (f) * 1024)), "s"(isE) : "memory", "scc");
#define LD_KP(TAIL) asm volatile("s_cmp_lt_u32 %11, 2\n\ts_cselect_b64 exec, -1, 0\n\t" \
        "global_load_dwordx4 %0, %8, %9 offset:-4096 sc1\n\tglobal_load_dwordx4 %1, %8, %9 offset:-2048 sc1\n\t" \
        "global_load_dwordx4 %2, %8, %9 sc1\n\tglobal_load_dwordx4 %3, %8, %9 offset:2048 sc1\n\t" \
        "global_load_dwordx4 %4, %8, %10 offset:-4096 sc1\n\tglobal_load_dwordx4 %5, %8, %10 offset:-2048 sc1\n\t" \
        "global_load_dwordx4 %6, %8, %10 sc1\n\tglobal_load_dwordx4 %7, %8, %10 offset:2048 sc1\n\t" \
        "s_mov_b64 exec, -1" TAIL \
        : "=&v"(kp[0]), "=&v"(kp[1]), "=&v"(kp[2]), "=&v"(kp[3]), "=&v"(kp[4]), "=&v"(kp[5]), "=&v"(kp[6]), "=&v"(kp[7]) \
        : "v"(ent * 16), "s"(kb), "s"(kb2), "s"(wave) : "memory", "scc")
#pragma unroll
    for (int c = 0; c < 8; ++c) {
        unsigned ma, mb; unsigned long long ga, gb;
        if (c < 3) {
            const int lim = (c == 1 ? L_SP : 163840) - 3072;
            const int oa = (c == 2 ? L_SP : 50176 + c * 49152) + wave * 3072, ob = oa + 24576 < lim ? oa + 24576 : lim;
            ma = lds0 + oa; mb = lds0 + ob; ga = uni64(kvb + oa); gb = uni64(kvb + ob);
        } else {
            ma = lds0 + (c == 7 ? 1024 + j * 3072 : L_WO + ((c - 3) * 8 + wr) * 3072); mb = ma;
            ga = uni64(wop + (c == 7 ? 44 : 20 + (wave >> 2) * 12 + 3 * (c - 3)) * 1024); gb = ga;
        }
        CSTAMP(2 * c);
        unsigned cnt;
        if (c == 0) cnt = attn_chunk<true>(S, O, OPS, P, AD, Q, vmask, rfl(ma), ga, rfl(mb), gb, 1u);
        else cnt = attn_chunk<false>(S, O, OPS, P, AD, Q, vmask, rfl(ma), ga, rfl(mb), gb, c < 3 ? 1u : c < 7 ? 0u : (unsigned)(2 - 2 * isE));
        CSTAMP(2 * c + 1);
        if (c < 7) wait_cnt(lds, 0, cnt, 8 * (c + 1));
        if (c == FILL_C && c != 1) empty_o(rs, rt, h, lane);
        if (c == 0) {
            if (wave < 2) {
                f32x4 s = *(const LAS f32x4*)(L + L_SP + ent * 16);
#pragma unroll
                for (int w = 1; w < 8; ++w) s += *(const LAS f32x4*)(L + L_SP + w * 2016 + ent * 16);
                const u32x4 v = __builtin_bit_cast(u32x4, s);
                if (tid < 126) __builtin_amdgcn_raw_buffer_store_b128(u32x4{full(v[0]), full(v[1]), full(v[2]), full(v[3])}, rs, tid * 16, (int)OFF_KVP + (h * 8 + qb) * 2048, SC1);
            }
            glds16(qim + wr * 2560 + 1280, lane * 16, lds0 + L_QA + wave * 1280);
            glds16(qim + wr * 2560 + 1280 + 256, lane * 16, lds0 + L_QA + wave * 1280 + 256);
        } else if (c == 1) {
            LD_KP("");
            if (FILL_C == 1) empty_o(rs, rt, h, lane);
        } else if (c == 2) {
#pragma unroll
            for (int j = 0; j < 8; ++j) landed(kp[j]);
            if (wave < 2) {
#pragma clang loop unroll(disable)
                for (int spin = 0; spin < (1 << 20); ++spin) {
                    unsigned m = 0u;
#pragma unroll
                    for (int j = 0; j < 8; ++j) m = umax4(m, kp[j]);
                    if (__builtin_expect(__all(m != EMPTY), 1)) break;
                    LD_KP("\n\ts_waitcnt vmcnt(0)");
                }
                f32x4 s = __builtin_bit_cast(f32x4, kp[0]);
#pragma unroll
                for (int j = 1; j < 8; ++j) s += __builtin_bit_cast(f32x4, kp[j]);
                const int e3 = ent / 3;
                *(LAS u32x2*)(lds + L_KVF + e3 * 32 + (ent - e3 * 3) * 8) = u32x2{pk2(s[0], s[1]), pk2(s[2], s[3])};
            }
        } else if (c == 3) {
            const int rr = r <= 20 ? r : 20;
            const u32x4 ka0 = *(const LAS u32x4*)(L + L_KVF + (hh * 21 + rr) * 32);
            const u32x2 ka1 = *(const LAS u32x2*)(L + L_KVF + (hh * 21 + rr) * 32 + 16);
            const LAS char* qa = L + L_QA + wave * 1280 + tro;
            const u32x2 pq0 = trd(qa + hh * 64), pq1 = trd(qa + 128 + hh * 64);
            u32x2 pq2 = trd(qa + 256);
            if (hh) pq2 = u32x2{0u, 0u};
            f32x16 oa = MFMA32(B8(ka0), B8(((u32x4){pq0[0], pq0[1], pq1[0], pq1[1]})), Z16);
            oa = MFMA32(B8(((u32x4){ka1[0], ka1[1], 0u, 0u})), B8(((u32x4){pq2[0], pq2[1], 0u, 0u})), oa);
            const float den = hi_half(oa[8]) + 1e-6f;
            store_o(rs, oa, gate * __builtin_amdgcn_rcpf(den), rt, 0, h, lane);
            LD_W4(wa, 0) LD_W4(wa + 4, 4)
        } else if (c == 4) {
            if (tid == 0) set_flag(rs, (int)OFF_FL + (qb * 4 + (h >> 3)) * 128 + (h & 7) * 16);
            LD_W4(wa + 8, 8) LD_W4(wa + 12, 12)
        } else if (c == 5) {
            fl = a_ld1(wsu, flA + flv);
            LD_W4(wa + 16, 16)
        } else if (c == 6) {
            landed(fl);
            if (wave < 4) wait_flag(rs, flv, flA, fl);
            LD_H(aa[0], ov + 40960, 0, isE, "=&v")
        }
    }
#pragma unroll
    for (int i = 0; i < 10; ++i) { landed(wa[i]); landed(wa[10 + i]); landed(aa[0][i]); }
    u32x4 wd[4];
    LD_H(aa[1], ov + 40960, 1, isE, "=&v") LD_W4(wd, 46)
    if (lane == 0) set_flag(rs, (int)OFF_FL + (32 + rt * 4 + (h >> 3)) * 128 + (h & 7) * 16);
    O = MFMA32(B8(((u32x4){OPS[8], OPS[9], OPS[10], OPS[11]})), B8(((u32x4){P[4], P[5], P[6], P[7]})), O);
    PSTAMP(1, 2); WSTAMP(2);
    float l = hi_half(O[8]);
    {
        bool bad = !(l >= 1e-18f && l <= 1.0e30f);
#pragma unroll
        for (int i = 0; i < 12; ++i) bad = bad || !(fabsf(O[i]) <= 3.0e38f);
        if (__builtin_expect(__any(bad), 0)) {
            const bf16x8 qt0 = B8(((u32x4){Q[4], Q[5], 0u, 0u}));
            float m = -INFINITY; O = Z16;
            int tl = tid;
            asm volatile("" : "+v"(tl));
            const int r = tl & 31, hh = (tl >> 5) & 1, rc = r < 20 ? r : 19;
            const int ko = (r >> 4) * 640 + ((r >> 2) & 1) * 320 + ((r >> 3) & 1) * 8 + (r & 3) * 2;
            for (int t = 0; t < 64; ++t) {
                const char* tb = kvb + t * 2560;
                unsigned kw[6];
#pragma unroll
                for (int i = 0; i < 6; ++i) {
                    const int d = i < 4 ? 8 * hh + 2 * i : 16 + 2 * (i - 4);
                    kw[i] = (unsigned)*(const u16*)(tb + ko + d * 16) | ((unsigned)*(const u16*)(tb + ko + d * 16 + 16) << 16);
                }
                u32x4 f0 = *(const u32x4*)(tb + 1280 + hh * 320 + rc * 16), f1 = *(const u32x4*)(tb + 1920 + hh * 320 + rc * 16);
                if (r >= 20) { f0 = ONES; f1 = ONES; }
                f32x16 s = MFMA32(B8(((u32x4){kw[0], kw[1], kw[2], kw[3]})), qm, Z16);
                s = MFMA32(B8(((u32x4){kw[4], kw[5], 0u, 0u})), qt0, s);
                float tm = s[0];
#pragma unroll
                for (int i = 1; i < 16; ++i) tm = fmaxf(tm, s[i]);
                tm = xhalf_max(tm);
                const float mn = fmaxf(m, tm);
                const float al = (m == -INFINITY) ? 0.f : __builtin_amdgcn_exp2f(m - mn);
                m = mn;
#pragma unroll
                for (int i = 0; i < 16; ++i) { O[i] *= al; s[i] = __builtin_amdgcn_exp2f(s[i] - mn); }
                u32x4 p0 = {pk2(s[0], s[1]), pk2(s[2], s[3]), pk2(s[4], s[5]), pk2(s[6], s[7])};
                u32x4 p1 = {pk2(s[8], s[9]), pk2(s[10], s[11]), pk2(s[12], s[13]), pk2(s[14], s[15])};
                O = MFMA32(B8(f0), B8(p0), O); O = MFMA32(B8(f1), B8(p1), O);
            }
            l = hi_half(O[8]);
        }
    }
    store_o(rs, O, (1.f - gate) * __builtin_amdgcn_rcpf(l), rt, 1, h, lane);
    __builtin_amdgcn_s_setprio(0);
    __builtin_amdgcn_sched_barrier(0);
    WSTAMP(3);
    constexpr int L_P0 = L_WO + 98304;
    if (wave < 4) {
        const LAS char* wl = L + j * 6144 + lane * 16;
        auto wfr = [&](int f) -> bf16x8 {
            if (f < 20) return B8(wa[f]);
            if (f < 44) return *(const LAS bf16x8*)(wl + L_WO + (((f - 20) % 12) / 3) * 24576 + ((f - 20) / 12) * 3072 + ((f - 20) % 3) * 1024);
            if (f < 46) return *(const LAS bf16x8*)(L + 1024 + j * 3072 + (f - 44) * 1024 + lane * 16);
            return B8(wd[f - 46]);
        };
        LAS char* wp = (LAS char*)lds + (lane & 15) * 320 + ((g ^ ((lane >> 1) & 3)) << 4);
        f32x4 acc[4][5] = {};
        {
            bf16x8 w4[5];
#pragma unroll
            for (int ni = 0; ni < 5; ++ni) w4[ni] = wfr(20 + ni);
#pragma unroll
            for (int hf = 0; hf < 2; ++hf) {
                if (hf) asm volatile("s_waitcnt vmcnt(3)" : "+v"(aa[1][0]), "+v"(aa[1][1]), "+v"(aa[1][2]), "+v"(aa[1][3]), "+v"(aa[1][4]), "+v"(aa[1][5]), "+v"(aa[1][6]),
                                     "+v"(aa[1][7]), "+v"(aa[1][8]), "+v"(aa[1][9]), "+v"(wd[0]), "+v"(wd[1]), "+v"(wd[2]), "+v"(wd[3]) :: "memory");
#pragma unroll
                for (int k = 0; k < 5; ++k)
#pragma unroll
                    for (int mi = 0; mi < 2; ++mi)
#pragma unroll
                        for (int ni = 0; ni < 5; ++ni) acc[hf * 2 + mi][ni] = MFMA16(k < 4 ? wfr(k * 5 + ni) : w4[ni], B8(aa[hf][2 * k + mi]), acc[hf * 2 + mi][ni]);
                __builtin_amdgcn_sched_barrier(0);
            }
        }
        const int flB = (int)OFF_FL + (32 + rb * 8 + j) * 128;
        u32x4 b0[10], b1[10];
        wait_flag(rs, flv, flB, get_flag(rs, flv, flB));
        WSTAMP(4);
        LD_H(b0, ov, 0, 1, "=&v")
        fl = a_ld1(wsu, flB + 512 + flv);
        wait_cnt(lds, 0, 64);
        asm volatile("s_waitcnt vmcnt(0)" : "+v"(fl) :: "memory");
        wait_flag(rs, flv, flB + 512, fl);
        WSTAMP(5);
        LD_H(b1, ov, 1, 1, "=&v")
#pragma clang loop unroll(disable)
        for (int spin = 0; spin < (1 << 20); ++spin) {
            unsigned m = 0u;
#pragma unroll
            for (int i = 0; i < 10; ++i) { landed(b0[i]); m = umax4(m, b0[i]); }
            if (__builtin_expect(__all(m != EMPTY), 1)) break;
            LD_H(b0, ov, 0, 1, "+v")
            asm volatile("s_waitcnt vmcnt(0)" ::: "memory");
        }
        bf16x8 wb[25];
#pragma unroll
        for (int k = 0; k < 5; ++k) {
#pragma unroll
            for (int ni = 0; ni < 5; ++ni) wb[k * 5 + ni] = wfr(25 + k * 5 + ni);
#pragma unroll
            for (int mi = 0; mi < 2; ++mi)
#pragma unroll
                for (int ni = 0; ni < 5; ++ni) acc[mi][ni] = MFMA16(wb[k * 5 + ni], B8(b0[2 * k + mi]), acc[mi][ni]);
        }
#pragma unroll
        for (int mi = 0; mi < 2; ++mi)
#pragma unroll
            for (int ni = 0; ni < 5; ++ni) *(LAS f32x4*)(wp + L_P0 + j * 10240 + mi * 5120 + ni * 64) = acc[mi][ni];
        add_cnt<12>(lds0);
        f32x4 ac1[2][5];
#pragma clang loop unroll(disable)
        for (int spin = 0; spin < (1 << 20); ++spin) {
            unsigned m = 0u;
#pragma unroll
            for (int k = 0; k < 5; ++k) {
                asm volatile("s_waitcnt vmcnt(%2)" : "+v"(b1[2 * k]), "+v"(b1[2 * k + 1]) : "n"(8 - 2 * k) : "memory");
#pragma unroll
                for (int mi = 0; mi < 2; ++mi)
#pragma unroll
                    for (int ni = 0; ni < 5; ++ni) ac1[mi][ni] = MFMA16(wb[k * 5 + ni], B8(b1[2 * k + mi]), k ? ac1[mi][ni] : acc[2 + mi][ni]);
                m = umax4(umax4(m, b1[2 * k]), b1[2 * k + 1]);
                __builtin_amdgcn_sched_barrier(0);
            }
            if (__builtin_expect(__all(m != EMPTY), 1)) break;
            LD_H(b1, ov, 1, 1, "+v")
        }
#pragma unroll
        for (int mi = 0; mi < 2; ++mi)
#pragma unroll
            for (int ni = 0; ni < 5; ++ni) *(LAS f32x4*)(wp + L_WO + j * 6144 + mi * 24576 + ni * 64) = ac1[mi][ni];
        WSTAMP(6);
        asm volatile(".Lk2_last:\n\ts_waitcnt lgkmcnt(0)\n\ts_barrier" ::: "memory");
    } else {
        const int t = tid - 256;
        const LAS char* rp[2][3]; unsigned oo[3];
#pragma unroll
        for (int i = 0; i < 3; ++i) {
            const int id = i < 2 || t < 128 ? i * 256 + t : 1, row = id / 20, cs = id - row * 20, cc = cs ^ ((row >> 1) & 3);
            rp[0][i] = L + L_P0 + id * 16; rp[1][i] = L + L_WO + id * 16 + (id >= 320 ? 24576 - 5120 : 0);
            oo[i] = (unsigned)(((rb * 64 + row) * D_ + cb * 80 + cc * 4) * 2);
            landed(rp[0][i]); landed(rp[1][i]); landed(oo[i]);
        }
        WSTAMP(4);
#pragma unroll
        for (int hf = 0; hf < 2; ++hf) {
            if (hf == 0) { wait_cnt(lds, 12, 4); WSTAMP(5); }
            else { WSTAMP(6); asm volatile("s_barrier" ::: "memory"); }
            f32x4 pv[3][4];
#pragma unroll
            for (int i = 0; i < 3; ++i)
#pragma unroll
                for (int w = 0; w < 4; ++w) pv[i][w] = *(const LAS f32x4*)(rp[hf][i] + w * (hf ? 6144 : 10240));
            __builtin_amdgcn_sched_barrier(0);
#pragma unroll
            for (int i = 0; i < 3; ++i) {
                const f32x4 v = (pv[i][0] + pv[i][1]) + (pv[i][2] + pv[i][3]);
                if (i < 2 || t < 128) __builtin_amdgcn_raw_buffer_store_b64(u32x2{pk2(v[0], v[1]), pk2(v[2], v[3])},
                    __builtin_amdgcn_make_buffer_rsrc((void*)out, 0, S_ * D_ * 2, 0x00020000), oo[i], hf * (32 * D_ * 2), OUT_AUX);
            }
        }
    }
}

__global__ void k_nop(char* ws) { if (ws == nullptr) __builtin_trap(); }
extern "C" void kernel_launch(void* const* d_in, const int* in_sizes, int n_in, void* d_out, int out_size, void* d_ws, size_t ws_size,
                              hipStream_t stream) {
    const u16* x = (const u16*)d_in[0];
    const u16* wA = (const u16*)d_in[1];
    const u16* woA = (const u16*)d_in[2];
    const u16* wB = (const u16*)d_in[4];
    const u16* woB = (const u16*)d_in[5];
    const u16* gw = (const u16*)d_in[6];
    char* ws = (char*)d_ws;
    (void)hipFuncSetAttribute((const void*)k1_qkv, hipFuncAttributeMaxDynamicSharedMemorySize, LDS_BYTES);
    (void)hipFuncSetAttribute((const void*)k2_attn, hipFuncAttributeMaxDynamicSharedMemorySize, LDS_BYTES);
    k1_qkv<<<256, 512, LDS_BYTES, stream>>>(x, wA, wB, woA, woB, gw, ws);
    k2_attn<<<256, 512, LDS_BYTES, stream>>>(ws, (u16*)d_out);
}
```

```cpp
#define OUT_AUX 16
#include <hip/hip_runtime.h>
#include <hip/hip_bf16.h>
#include <stdint.h>

typedef short    bf16x8 __attribute__((ext_vector_type(8)));
typedef short    s16x4  __attribute__((ext_vector_type(4)));
typedef float    f32x2  __attribute__((ext_vector_type(2)));
typedef float    f32x4  __attribute__((ext_vector_type(4)));
typedef float    f32x16 __attribute__((ext_vector_type(16)));
typedef float    f32x32 __attribute__((ext_vector_type(32)));
typedef unsigned u32x2  __attribute__((ext_vector_type(2)));
typedef unsigned u32x4  __attribute__((ext_vector_type(4)));
typedef unsigned u32x8  __attribute__((ext_vector_type(8)));
typedef unsigned u32x32 __attribute__((ext_vector_type(32)));
typedef __bf16   bf2_t  __attribute__((ext_vector_type(2)));
typedef unsigned short u16;
#define LAS __attribute__((address_space(3)))
#define DI __device__ __forceinline__

constexpr int S_ = 2048, D_ = 640;
constexpr size_t IMG     = (size_t)32 * 163840;
constexpr size_t OFF_IQ  = 0;
constexpr size_t OFF_IB  = IMG;
constexpr size_t OFF_IA  = 2 * IMG;
constexpr size_t OFF_O   = 3 * IMG;
constexpr size_t OFF_WOT = 4 * IMG;
constexpr size_t OFF_G   = OFF_WOT + (size_t)1600 * 1024;
constexpr size_t OFF_KVP = OFF_G + 8192;
constexpr size_t OFF_FL  = OFF_KVP + (size_t)256 * 2048;
constexpr int    WS_USED = (int)(OFF_FL + 288 * 128);
constexpr unsigned EMPTY = 0xFFFFFFFFu;
typedef __amdgpu_buffer_rsrc_t rsrc_t;
constexpr int SC1 = 16;

#define CSTAMP(i)
#define PSTAMP(k, i)
#define WSTAMP(i)

DI unsigned pk2(float lo, float hi) { f32x2 v = {lo, hi}; return __builtin_bit_cast(unsigned, __builtin_convertvector(v, bf2_t)); }
DI float bflo(unsigned v) { return __uint_as_float(v << 16); }
DI float bfhi(unsigned v) { return __uint_as_float(v & 0xffff0000u); }
DI int rfl(int x) { return __builtin_amdgcn_readfirstlane(x); }
DI void glds16(const void* sbase, unsigned voff, unsigned lds_base) {
    const unsigned long long a = (unsigned long long)(uintptr_t)sbase;
    const unsigned long long u = (unsigned long long)(unsigned)rfl((int)(unsigned)a) | ((unsigned long long)(unsigned)rfl((int)(unsigned)(a >> 32)) << 32);
    asm volatile("s_mov_b32 m0, %0\n\ts_nop 0\n\tglobal_load_lds_dwordx4 %1, %2" :: "s"(rfl((int)lds_base)), "v"(voff), "s"(u) : "memory");
}
#define MFMA16(a, b, c) __builtin_amdgcn_mfma_f32_16x16x32_bf16((a), (b), (c), 0, 0, 0)
#define MFMA32(a, b, c) __builtin_amdgcn_mfma_f32_32x32x16_bf16((a), (b), (c), 0, 0, 0)
#define B8(x) __builtin_bit_cast(bf16x8, x)
DI u32x2 trd(const LAS char* p) { return __builtin_bit_cast(u32x2, __builtin_amdgcn_ds_read_tr16_b64_v4i16((LAS s16x4*)p)); }
DI float pmax(float t, float c) { return __int_as_float(max(__float_as_int(t), __float_as_int(c))); }
template <int N> DI void wait_vm_bar() { asm volatile("s_waitcnt vmcnt(%0) lgkmcnt(0)\n\ts_barrier" :: "n"(N) : "memory"); }

constexpr int LDS_BYTES = 163840;
constexpr float LOG2E = 1.4426950408889634f;

DI void p1_aux(const u16* __restrict__ x, const u16* __restrict__ woA, const u16* __restrict__ woB, const u16* __restrict__ gate_w,
               char* __restrict__ ws, char* lds, int a) {
    const int tid = threadIdx.x, lane = tid & 63, wave = rfl(tid >> 6);
    const unsigned lds0 = (unsigned)(uintptr_t)lds;
    const int br = a >> 3, u = a & 7;
    if (tid < 144) *(u32x4*)(ws + OFF_FL + (a * 144 + tid) * 16) = u32x4{0u, 0u, 0u, 0u};
    const char* wsrc = (const char*)(br ? woB : woA) + (size_t)u * 102400;
    for (int i = wave; i < 100; i += 8) glds16(wsrc + i * 1024, lane * 16, lds0 + i * 1024);
    const u32x4* xs = (const u32x4*)(x + (size_t)(a * 128 + wave * 16) * D_);
    u32x4 xv[20], gw[5];
#pragma unroll
    for (int i = 0; i < 20; ++i) xv[i] = xs[i * 64 + lane];
#pragma unroll
    for (int i = 0; i < 5; ++i) gw[i] = ((const u32x4*)gate_w)[(i * 64 + lane) % 80];
    {
        const rsrc_t rs = __builtin_amdgcn_make_buffer_rsrc((void*)(ws + OFF_KVP + a * 32768), 0, 32768, 0x00020000);
#pragma unroll
        for (int i = 0; i < 4; ++i) __builtin_amdgcn_raw_buffer_store_b128(u32x4{EMPTY, EMPTY, EMPTY, EMPTY}, rs, tid * 16 + i * 8192, 0, SC1);
    }
    LAS float* part = (LAS float*)(lds + 102400) + wave * 1280;
#pragma unroll
    for (int i = 0; i < 20; ++i) {
        float s = 0.f;
#pragma unroll
        for (int j = 0; j < 4; ++j) s += bflo(xv[i][j]) * bflo(gw[i % 5][j]) + bfhi(xv[i][j]) * bfhi(gw[i % 5][j]);
        part[i * 64 + lane] = s;
    }
    wait_vm_bar<0>();
    {
        float s = 0.f;
#pragma unroll
        for (int j = 0; j < 20; ++j) s += part[lane * 20 + j];
        s += __shfl_xor(s, 1); s += __shfl_xor(s, 2);
        if ((lane & 3) == 0) ((float*)(ws + OFF_G))[a * 128 + wave * 16 + (lane >> 2)] = 1.f / (1.f + __expf(-s));
    }
    const int g = lane >> 4, hh = g & 1, ns = g >> 1, q = (lane >> 2) & 3, p = lane & 3;
    for (int un = wave; un < 100; un += 8) {
        const int e = un / 20, np = un - e * 20, nt = np * 2 + ns;
        const int r0 = e < 4 ? e * 20 + 4 * hh : (2 * hh) * 20 + 16;
        const int r1 = e < 4 ? r0 + 8 : r0 + 20;
        const int ks16 = e < 4 ? (u & 1) * 4 + e : 8 + (u & 1), cbk = nt / 5;
        const u32x2 lo = trd((const LAS char*)lds + (r0 + q) * 1280 + nt * 32 + p * 8);
        const u32x2 hi = trd((const LAS char*)lds + (r1 + q) * 1280 + nt * 32 + p * 8);
        *(u32x4*)(ws + OFF_WOT + (size_t)((cbk * 8 + (u & 6) + br) * 25 + (ks16 >> 1) * 5 + nt - cbk * 5) * 1024 + ((ks16 & 1) * 2 + hh) * 256 + (lane & 15) * 16) = u32x4{lo[0], lo[1], hi[0], hi[1]};
    }
}

#define CPF 64
#define CPF_N 256
#define CPF_M 0
#define G1_TL 3
#define G1_D 3
#define G1_D0 2
#define G1_DT 10
#define G1_DE 0
#define G1_NX 4
constexpr int G1_NW = 10 - G1_NX, G1_HB = 20 - 2 * G1_TL, G1_WOFF = G1_NX * 16384;
constexpr int g1_t(int h) { return h < 2 * G1_HB ? h >> 1 : h < 2 * G1_HB + 2 * G1_TL ? h - G1_HB : h - G1_HB - 2 * G1_TL; }
constexpr int g1_m(int h) { return h < 2 * G1_HB ? h & 1 : h < 2 * G1_HB + 2 * G1_TL ? 0 : 1; }
constexpr int g1_xi(int h) { return h < 2 * G1_HB ? (h >> 1) & 1 : h & 1; }
constexpr int g1_free(int i) { return i < 10 ? (i < G1_NX ? -1 : 2 * (i - G1_NX) + 2) : (i - 10 < G1_NW ? -1 : i - 10 - G1_NW + 1); }
struct G1Plan { int seq[30]; int cnt[22]; int pos[30]; };
constexpr G1Plan g1_plan() {
    G1Plan p = {}; bool done[30] = {}, seen[10] = {}; int ord[30] = {}, need[21] = {}, n = 0;
    for (int b = 0; b < 20; ++b) { const int u = g1_t(2 * b) >> 1; if (!seen[u]) { seen[u] = true; ord[n++] = u; } need[b] = n; ord[n++] = 10 + b; }
    need[20] = 30; n = 0;
    for (int b = 0; b <= 20; ++b) {
        const int ex = b > G1_DT ? b - G1_DT : 0, lim = 3 * (b >> 1) + 1 + (b & 1) + (b == 0 ? G1_D0 : G1_D) + (ex < G1_DE ? ex : G1_DE);
        for (int k = 0; k < 30 && k <= lim; ++k) { const int i = ord[k]; if (!done[i] && g1_free(i) < b) { done[i] = true; p.pos[i] = n; p.seq[n++] = i; } }
        p.cnt[b] = n;
    }
    return p;
}
constexpr G1Plan G1P = g1_plan();
constexpr int g1_wait(int b) { const int x = G1P.pos[g1_t(2 * b) >> 1], w = G1P.pos[10 + b]; return 4 * (G1P.cnt[b] - 1 - (x > w ? x : w)); }
constexpr bool g1_ok() { for (int b = 0; b < 20; ++b) if (g1_wait(b) < 0 || g1_wait(b) > 60) return false; return G1P.cnt[19] == 30 && G1_NX >= G1_TL && G1_NX <= 4; }
static_assert(g1_ok(), "loader plan");
#define G1_ROT ((0x27057250 >> (4 * xcd)) & 15)

#define LST_P
#define LST_A
template <int T> struct G1Load {
    template <class FX, class FW> static DI void run(FX&& ldx, FW&& ldw LST_P) {
        asm volatile("s_waitcnt vmcnt(%0)\n\ts_barrier" :: "n"(g1_wait(T)) : "memory");
#pragma unroll
        for (int n = G1P.cnt[T]; n < G1P.cnt[T + 1]; ++n) { const int i = G1P.seq[n]; if (i < 10) ldx(i); else ldw(i - 10); }
        G1Load<T + 1>::run(ldx, ldw LST_A);
    }
};
template <> struct G1Load<20> { template <class FX, class FW> static DI void run(FX&&, FW&& LST_P) {} };

struct G1Ctx {
    f32x4 acc[4][8];
    bf16x8 x[2][4]; u32x2 w[2][4][2];
    const LAS char* xa[2]; const LAS char* wa[8];
    int so[4];
    rsrc_t rB; float sc; f32x2 m[4]; u32x4 o;
    unsigned pf0, pf1;
};
#define SB0() __builtin_amdgcn_sched_barrier(0)
#define G1_SP 12
template <int J, int I> DI void g1_side(G1Ctx& c) {
    constexpr int K = (J - (40 - 2 * G1_TL)) * 16 + I;
    if (CPF && J == 20 && I == 12) { asm volatile("s_waitcnt vmcnt(0)" :: "v"(c.pf0), "v"(c.pf1)); SB0(); }
    if (K > 0 && K <= 8 * G1_SP && (K - 1) % G1_SP == 8) asm volatile("" :: "v"(c.o));
    if (K >= 0 && K < 8 * G1_SP && K % G1_SP < 9) {
        constexpr int U = K < 0 ? 0 : K / G1_SP, e = K < 0 ? 0 : K % G1_SP, ni = (U >> 1) & 3, a = U & 1;
        constexpr int mi = e == 0 ? 0 : e == 1 ? 1 : e == 3 ? 2 : e == 5 ? 3 : -1, ci = e == 2 ? 0 : e == 4 ? 1 : e == 6 ? 2 : e == 7 ? 3 : -1;
        if (mi >= 0) { const f32x4 v = c.acc[2 * a + ((mi & 3) >> 1)][ni]; c.m[mi & 3] = f32x2{v[(mi & 1) * 2], v[(mi & 1) * 2 + 1]} * c.sc; }
        if (ci >= 0) c.o[ci & 3] = pk2(c.m[ci & 3][0], c.m[ci & 3][1]);
        if (e == 8) __builtin_amdgcn_raw_buffer_store_b128(c.o, c.rB, c.so[ni] + a * 2560, 0, SC1);
        SB0();
    }
}
template <int J> struct G1Step {
    static DI void run(G1Ctx& c) {
        constexpr int N = J + 1, t = g1_t(N), xo = ((t >> 1) % G1_NX) * 16384, sl = (N >> 1) % G1_NW, wo = (sl & 3) * 16384 + (N & 1) * 8192, wi = (sl >> 2) * 4;
        constexpr int ps = J & 1, ma = g1_m(J) * 4, xc = g1_xi(J), xn = g1_xi(N);
#define G1_M(i) c.acc[(i) & 3][ma + ((i) >> 2)] = MFMA16(c.x[xc][(i) & 3], B8(((u32x4){c.w[ps][(i) >> 2][0][0], c.w[ps][(i) >> 2][0][1], c.w[ps][(i) >> 2][1][0], c.w[ps][(i) >> 2][1][1]})), \
                c.acc[(i) & 3][ma + ((i) >> 2)]); SB0(); g1_side<J, i>(c);
#define G1_X(i) if (N < 40) c.x[xn][i] = *(const LAS bf16x8*)(c.xa[t & 1] + xo + ((i) >> 1) * 4096 + ((i) & 1) * 1024); SB0();
#define G1_W(i, e) if (N < 40) c.w[ps ^ 1][i][e] = trd(c.wa[wi + (i)] + wo + (e) * 1024); SB0();
        if (ps) {
            G1_M(0) G1_M(1) G1_M(2)
            if (N < 40) asm volatile("s_waitcnt lgkmcnt(0)\n\ts_barrier" ::: "memory");
            SB0();
            G1_X(0) G1_M(3) G1_W(0, 0) G1_M(4) G1_W(0, 1) G1_M(5) G1_X(1) G1_M(6) G1_X(2) G1_M(7) G1_X(3) G1_M(8) G1_W(1, 0) G1_M(9) G1_W(1, 1) G1_M(10)
            G1_W(2, 0) G1_M(11) G1_W(2, 1) G1_M(12) G1_W(3, 0) G1_M(13) G1_W(3, 1) G1_M(14) G1_M(15)
        } else if (xn != xc) {
            G1_M(0) G1_X(0) G1_M(1) G1_W(0, 0) G1_M(2) G1_W(0, 1) G1_M(3) G1_X(1) G1_M(4) G1_X(2) G1_M(5) G1_X(3) G1_M(6) G1_W(1, 0) G1_M(7) G1_W(1, 1)
            G1_M(8) G1_W(2, 0) G1_M(9) G1_W(2, 1) G1_M(10) G1_W(3, 0) G1_M(11) G1_W(3, 1) G1_M(12) G1_M(13) G1_M(14) G1_M(15)
        } else {
            G1_M(0) G1_W(0, 0) G1_M(1) G1_W(0, 1) G1_M(2) G1_W(1, 0) G1_M(3) G1_W(1, 1) G1_M(4) G1_W(2, 0) G1_M(5) G1_W(2, 1) G1_M(6) G1_W(3, 0) G1_M(7) G1_W(3, 1)
            G1_M(8) G1_M(9) G1_M(10) G1_M(11) G1_M(12) G1_M(13) G1_M(14) G1_M(15)
        }
        G1Step<J + 1>::run(c);
    }
};
template <> struct G1Step<40> { static DI void run(G1Ctx&) {} };
template <int K> struct G1Rest { static DI void run(G1Ctx& c) { g1_side<40 - 2 * G1_TL + K / 16, K % 16>(c); G1Rest<K + 1>::run(c); } };
template <> struct G1Rest<8 * G1_SP> { static DI void run(G1Ctx&) {} };

DI void phase1(const u16* __restrict__ x, const u16* __restrict__ wA, const u16* __restrict__ wB, char* __restrict__ ws, char* lds, int bid,
               unsigned pf0, unsigned pf1) {
    const int tid = threadIdx.x, lane = tid & 63, wave = rfl(tid >> 6);
    const unsigned lds0 = (unsigned)(uintptr_t)lds;
    const int xcd = bid & 7, j = (xcd >> 1) * 30 + (bid >> 3);
    const int ct = j >> 3, mt = (xcd & 1) * 8 + (j & 7);
    const int type = rfl(ct / 5), rot = rfl(G1_ROT);
    if (__builtin_expect(wave >= 4, 1)) {
        const int lw = wave - 4;
        unsigned xo[4], wo[2];
#pragma unroll
        for (int i = 0; i < 4; ++i) { const int row = (lw * 4 + i) * 8 + (lane >> 3); xo[i] = (unsigned)((mt * 128 + row) * 1280 + (((lane & 7) ^ ((row >> 1) & 3) ^ ((row >> 2) & 4)) << 4)); }
#pragma unroll
        for (int i = 0; i < 2; ++i) { const int k = (lw * 2 + i) * 4 + (lane >> 4), s16 = lane & 15;
            wo[i] = (unsigned)(k * 3840 + ct * 256 + ((s16 ^ ((k & 3) << 2) ^ ((k >> 2) & 2)) << 4)); }
        auto ldx = [&](int u, unsigned st = 0u) {
            int kk = u + rot; kk -= kk >= 10 ? 10 : 0;
            const unsigned slot = lds0 + (u % G1_NX) * 16384 + lw * 4096;
#pragma unroll
            for (int i = 0; i < 4; ++i) glds16((const char*)x + kk * 128, xo[i], slot + i * 1024);
        };
        auto ldw = [&](int s) {
            int kk = (g1_t(2 * s) >> 1) + rot; kk -= kk >= 10 ? 10 : 0;
            const unsigned slot = lds0 + G1_WOFF + (s % G1_NW) * 16384 + lw * 2048;
#pragma unroll
            for (int e = 0; e < 2; ++e) {
                const char* src = (const char*)(g1_m(2 * s + e) ? wA : wB) + (kk * 64 + (g1_t(2 * s + e) & 1) * 32) * 3840;
                glds16(src, wo[0], slot + e * 8192); glds16(src, wo[1], slot + e * 8192 + 1024);
            }
        };
#pragma unroll
        for (int n = 0; n < G1P.cnt[0]; ++n) { const int i = G1P.seq[n]; if (i < 10) ldx(i); else ldw(i - 10); }
        G1Load<0>::run(ldx, ldw LST_A);
        return;
    }
    const int wm = wave & 1, wn = wave >> 1;
    const int i16 = lane & 15, g = lane >> 4, q = (lane >> 2) & 3, p = lane & 3;
    G1Ctx c;
    c.pf0 = pf0; c.pf1 = pf1;
    const int xr = (i16 >> 3) * 16 + (i16 & 7);
#pragma unroll
    for (int ks = 0; ks < 2; ++ks) { c.xa[ks] = (const LAS char*)lds + (wm * 64 + xr) * 128 + (((ks * 4 + g) ^ ((xr >> 1) & 3) ^ ((xr >> 2) & 4)) << 4); asm volatile("" : "+v"(c.xa[ks])); }
#pragma unroll
    for (int i = 0; i < 8; ++i) { c.wa[i] = (const LAS char*)lds + G1_WOFF + (i >> 2) * 65536 + (8 * g + q) * 256 + (((wn * 4 + (i & 3)) ^ (q << 1) ^ (g & 1)) << 5) + p * 8;
        asm volatile("" : "+v"(c.wa[i])); }
#pragma unroll
    for (int i = 0; i < 4; ++i) { const int cc = (ct - type * 5) * 128 + wn * 64 + i * 16 + i16, h = cc / 20; c.so[i] = h * 163840 + g * 320 + (cc - h * 20) * 16; asm volatile("" : "+v"(c.so[i])); }
    constexpr float QS = 0.22360679774997896f * LOG2E;
    int sci = type == 0 ? __builtin_bit_cast(int, QS) : 0x3F800000;
    asm volatile("" : "+s"(sci));
    c.sc = __int_as_float(sci);
    const int tb = (mt * 4 + wm * 2) * 2560;
    const int sB = tb + (type == 0 ? (int)OFF_IQ : (int)OFF_IB + (type - 1) * 1280), sA = tb + (type == 0 ? (int)OFF_IQ + 1280 : (int)OFF_IA + (type - 1) * 1280);
    c.rB = __builtin_amdgcn_make_buffer_rsrc((void*)(ws + sB), 0, (int)IMG, 0x00020000);
    const rsrc_t rA = __builtin_amdgcn_make_buffer_rsrc((void*)(ws + sA), 0, (int)IMG, 0x00020000);
    {
        const float a0 = type < 2 ? 1e-6f : 0.f;
        f32x4 a4 = {a0, a0, a0, a0};
        asm volatile("" : "+v"(a4));
#pragma unroll
        for (int i = 0; i < 16; ++i) { c.acc[i & 3][i >> 2] = f32x4{0.f, 0.f, 0.f, 0.f}; c.acc[i & 3][4 + (i >> 2)] = a4; }
    }
    __builtin_amdgcn_s_setprio(2);
    asm volatile("s_barrier" ::: "memory");
#pragma unroll
    for (int i = 0; i < 4; ++i) {
        c.x[0][i] = *(const LAS bf16x8*)(c.xa[0] + (i >> 1) * 4096 + (i & 1) * 1024);
        if (i == 0) { c.w[0][0][0] = trd(c.wa[0]); c.w[0][0][1] = trd(c.wa[0] + 1024); }
    }
#pragma unroll
    for (int i = 1; i < 4; ++i) { c.w[0][i][0] = trd(c.wa[i]); c.w[0][i][1] = trd(c.wa[i] + 1024); }
    SB0();
    G1Step<0>::run(c);
    PSTAMP(0, 2);
    G1Rest<(32 * G1_TL < 8 * G1_SP ? 32 * G1_TL : 8 * G1_SP)>::run(c);
    if (type < 2) {
#pragma unroll
        for (int i = 0; i < 4; ++i)
#pragma unroll
            for (int a = 0; a < 2; ++a) {
                u32x4 o;
#pragma unroll
                for (int e = 0; e < 4; ++e) {
                    const f32x2 t = {c.acc[2 * a + (e >> 1)][4 + i][(e & 1) * 2], c.acc[2 * a + (e >> 1)][4 + i][(e & 1) * 2 + 1]};
                    const f32x2 m = __builtin_elementwise_fma(t, f32x2{LOG2E, LOG2E}, f32x2{-1e-6f * LOG2E, -1e-6f * LOG2E});
                    const f32x2 ex = {__builtin_amdgcn_fmed3f(__builtin_amdgcn_exp2f(m[0]), 0.f, 1.f), __builtin_amdgcn_fmed3f(__builtin_amdgcn_exp2f(m[1]), 0.f, 1.f)};
                    const f32x2 r = f32x2{pmax(t[0], 1e-6f), pmax(t[1], 1e-6f)} + ex;
                    o[e] = pk2(r[0], r[1]);
                }
                __builtin_amdgcn_raw_buffer_store_b128(o, rA, c.so[i] + a * 2560, 0, SC1);
            }
    } else {
#pragma unroll
        for (int i = 0; i < 4; ++i)
#pragma unroll
            for (int a = 0; a < 2; ++a) {
                const f32x4 u = c.acc[2 * a][4 + i], v = c.acc[2 * a + 1][4 + i];
                __builtin_amdgcn_raw_buffer_store_b128(u32x4{pk2(u[0], u[1]), pk2(u[2], u[3]), pk2(v[0], v[1]), pk2(v[2], v[3])}, rA, c.so[i] + a * 2560, 0, SC1);
            }
    }
}

__global__ __launch_bounds__(512, 2) void k1_qkv(const u16* __restrict__ x, const u16* __restrict__ wA, const u16* __restrict__ wB,
                                                 const u16* __restrict__ woA, const u16* __restrict__ woB, const u16* __restrict__ gate_w,
                                                 char* __restrict__ ws) {
    extern __shared__ __attribute__((aligned(16))) char lds[];
    int bid = blockIdx.x;
    asm volatile("" : "+s"(bid));
    unsigned pf0, pf1 = 0u;
    {
        unsigned long long pc;
        asm volatile("s_getpc_b64 %1\n.Lk1_pc:\n\tv_lshl_add_u32 %0, %2, 7, %3\n\tv_min_u32 %0, .Lk2_last-.Lk1_pc, %0\n\t"
                     "v_cmp_gt_u32 vcc, %4, %2\n\ts_mov_b64 exec, vcc\n\tglobal_load_dword %0, %0, %1\n\ts_mov_b64 exec, -1"
                     : "=&v"(pf0), "=&s"(pc) : "v"(threadIdx.x), "s"((bid >> 3) * (CPF * 128) + 64), "n"(CPF) : "memory", "vcc");
    }
    if (__builtin_expect(bid < 240, 1)) phase1(x, wA, wB, ws, lds, bid, pf0, pf1);
    else { p1_aux(x, woA, woB, gate_w, ws, lds, bid - 240); asm volatile("" :: "v"(pf0), "v"(pf1)); }
}

#define STR(x) #x
#define XS(x) STR(x)
#define A_E(i) "v_exp_f32 v" XS(i) ", v" XS(i) "\n"
#define A_C(d, a, b) "v_cvt_pk_bf16_f32 v" XS(d) ", v" XS(a) ", v" XS(b) "\n"
#define A_G(a, b, c, d, p0, p1) A_E(a) A_E(b) A_E(c) A_E(d) A_C(p0, a, b) A_C(p1, c, d)
#define A_M(d, a, b, c) "v_mfma_f32_32x32x16_bf16 " d ", " a ", " b ", " c "\n"
#define A_STEP(C0,C1,C2,C3,C4,C5,C6,C7,C8,C9,C10,C11,C12,C13,C14,C15, SN, KM, KT, V1, V0, NKMa, NKMb, NKT, NV1, NV0, OK0, OK1, OV1, OV0, PRIO) \
    "s_waitcnt lgkmcnt(0)\n" \
    A_M(SN, KM, "v[120:123]", "0") \
    "ds_read_b64_tr_b16 " NKMa ", v128 offset:" XS(OK0) "\n" \
    "ds_read_b64_tr_b16 " NKMb ", v128 offset:" XS(OK1) "\n" \
    "ds_read_b64_tr_b16 " NKT ", v129 offset:" XS(OK0) "\n" \
    A_G(C0, C1, C2, C3, 112, 113) \
    A_M("v[64:79]", V1, "v[116:119]", "v[64:79]") \
    "s_mov_b64 exec, s[72:73]\n" \
    "ds_read_b128 " NV1 ", v130 offset:" XS(OV1) "\n" \
    "ds_read_b128 " NV0 ", v130 offset:" XS(OV0) "\n" \
    "s_mov_b64 exec, -1\n" \
    A_G(C4, C5, C6, C7, 114, 115) \
    A_M(SN, KT, "v[124:127]", SN) \
    A_G(C8, C9, C10, C11, 116, 117) \
    A_M("v[64:79]", V0, "v[112:115]", "v[64:79]") \
    "s_setprio " XS(PRIO) "\n" \
    A_G(C12, C13, C14, C15, 118, 119)
#define A_EVEN(OK0, OK1, OV1, OV0, PRIO) A_STEP(32,33,34,35,36,37,38,39,40,41,42,43,44,45,46,47, "v[48:63]", "v[80:83]", "v[84:87]", "v[88:91]", "v[92:95]", \
    "v[96:97]", "v[98:99]", "v[100:101]", "v[104:107]", "v[108:111]", OK0, OK1, OV1, OV0, PRIO)
#define A_ODD(OK0, OK1, OV1, OV0, PRIO) A_STEP(48,49,50,51,52,53,54,55,56,57,58,59,60,61,62,63, "v[32:47]", "v[96:99]", "v[100:103]", "v[104:107]", "v[108:111]", \
    "v[80:81]", "v[82:83]", "v[84:85]", "v[88:91]", "v[92:95]", OK0, OK1, OV1, OV0, PRIO)

#define A_LD(g, off) "global_load_lds_dwordx4 v131, " g " offset:" XS(off) "\n"
#define A_LDA(off) "s_cbranch_scc1 .Lattn_s" XS(off) "\n" A_LD("s[74:75]", off) ".Lattn_s" XS(off) ":\n"
#define A_BODY \
        "s_mov_b32 m0, s78\n s_cmp_eq_u32 s80, 2\n s_nop 3\n" \
        A_EVEN(5120, 5184, 640, 2560, 2)     A_ODD(7680, 7744, 3200, 5120, 1) \
        A_EVEN(10240, 10304, 5760, 7680, 0)  A_ODD(12800, 12864, 8320, 10240, 3) \
        "s_waitcnt vmcnt(0) lgkmcnt(0)\n s_mov_b64 exec, 1\n ds_add_u32 v127, v86\n s_mov_b64 exec, -1\n" \
        A_EVEN(15360, 15424, 10880, 12800, 2) A_LDA(0) A_ODD(17920, 17984, 13440, 15360, 1) A_LDA(1024) \
        A_EVEN(20480, 20544, 16000, 17920, 0) A_LDA(2048) \
        "s_cmp_eq_u32 s80, 1\n s_cbranch_scc0 .Lattn_skip\n s_mov_b32 m0, s79\n s_nop 0\n" \
        A_LD("s[76:77]", 0) A_LD("s[76:77]", 1024) A_LD("s[76:77]", 2048) ".Lattn_skip:\n" \
        "ds_read_b32 v132, v127\n" \
        A_ODD(23040, 23104, 18560, 20480, 3) \
        "v_add_u32 v128, 0x5000, v128\n v_add_u32 v129, 0x5000, v129\n v_add_u32 v130, 0x5000, v130\n" \
        "s_waitcnt lgkmcnt(0)\n s_nop 0\n"
#define A_OPERANDS \
        : "+{v[32:63]}"(S), "+{v[64:79]}"(O), "+{v[80:111]}"(OPS), "+{v[112:119]}"(P), "+{v[128:131]}"(AD), "={v132}"(cnt) \
        : "{v[120:127]}"(Q), "{s[72:73]}"(vmask), "{s[74:75]}"(ga), "{s[76:77]}"(gb), "{s78}"(ma), "{s79}"(mb), "{s80}"(enb) \
        : "memory", "scc", "s82", "s83"
template <bool FIRST>
DI unsigned attn_chunk(f32x32& S, f32x16& O, u32x32& OPS, u32x8& P, u32x4& AD, const u32x8& Q, unsigned long long vmask,
                   unsigned ma, unsigned long long ga, unsigned mb, unsigned long long gb, unsigned enb) {
    unsigned cnt;
    if (FIRST) asm volatile("s_call_b64 s[82:83], .Lattn_body\n s_branch .Lattn_done\n .p2align 6\n .Lattn_body:\n" A_BODY "s_setpc_b64 s[82:83]\n .Lattn_done:\n" A_OPERANDS);
    else       asm volatile("s_call_b64 s[82:83], .Lattn_body\n" A_OPERANDS);
    return cnt;
}
DI void wait_cnt(char* lds, int off, unsigned cnt, int n) {
    if (__builtin_expect((unsigned)rfl((int)cnt) < (unsigned)(n * 0x3F80), 0)) {
#pragma clang loop unroll(disable)
        do { __builtin_amdgcn_s_sleep(1); cnt = *(volatile LAS unsigned*)(lds + off); } while ((unsigned)rfl((int)cnt) < (unsigned)(n * 0x3F80));
    }
    asm volatile("" ::: "memory");
}

DI void wait_cnt(char* lds, int off, int n) { wait_cnt(lds, off, *(volatile LAS unsigned*)(lds + off), n); }
template <int OFF> DI void add_cnt(unsigned lds0) {
    asm volatile("s_waitcnt lgkmcnt(0)\n\ts_mov_b64 exec, 1\n\tds_add_u32 %0, %1 offset:%2\n\ts_mov_b64 exec, -1" :: "v"(lds0), "v"(0x3F80u), "n"(OFF) : "memory"); }

DI float xhalf_max(float m) { auto rr = __builtin_amdgcn_permlane32_swap(__float_as_uint(m), __float_as_uint(m), false, false);
    return fmaxf(__uint_as_float(rr[0]), __uint_as_float(rr[1])); }
DI float hi_half(float v) {
    auto rr = __builtin_amdgcn_permlane32_swap(__float_as_uint(v), __float_as_uint(v), false, false);
    return __uint_as_float(rr[1]); }
DI float sum16(float v) { auto rr = __builtin_amdgcn_permlane16_swap(__float_as_uint(v), __float_as_uint(v), false, false);
    return __uint_as_float(rr[0]) + __uint_as_float(rr[1]); }
DI float sum32(float v) { auto rr = __builtin_amdgcn_permlane32_swap(__float_as_uint(v), __float_as_uint(v), false, false);
    return __uint_as_float(rr[0]) + __uint_as_float(rr[1]); }
DI f32x4 sum_rows(f32x4 a, f32x4 b) {
    f32x4 s = a + b;
#pragma unroll
    for (int i = 0; i < 4; ++i) s[i] = sum32(sum16(s[i]));
    return s;
}
DI unsigned long long uni64(const void* p) { const unsigned long long a = (unsigned long long)(uintptr_t)p;
    return (unsigned long long)(unsigned)rfl((int)(unsigned)a) | ((unsigned long long)(unsigned)rfl((int)(unsigned)(a >> 32)) << 32); }
DI unsigned full(unsigned v) { return v < EMPTY ? v : EMPTY - 1u; }
DI unsigned umax4(unsigned m, u32x4 v) { return max(max(max(m, v[0]), max(v[1], v[2])), v[3]); }
DI void store_o(rsrc_t rs, const f32x16& o, float f, int rt, int br, int h, int lane) {
    const int rb = (int)OFF_O + (rt * 80 + ((br ? 0 : 4) + (h >> 3)) * 10) * 1024;
    __builtin_amdgcn_raw_buffer_store_b128(u32x4{full(pk2(o[0] * f, o[1] * f)), full(pk2(o[2] * f, o[3] * f)), full(pk2(o[4] * f, o[5] * f)), full(pk2(o[6] * f, o[7] * f))},
                                           rs, lane * 16, rb + (h & 7) * 1024, SC1);
    if (lane < 32) __builtin_amdgcn_raw_buffer_store_b64(u32x2{full(pk2(o[8] * f, o[9] * f)), full(pk2(o[10] * f, o[11] * f))},
                                           rs, lane * 16, rb + (8 + ((h >> 2) & 1)) * 1024 + ((h >> 1) & 1) * 512 + (h & 1) * 8, SC1);
}
DI void empty_o(rsrc_t rs, int rt, int h, int lane) {
    const int rb = (int)OFF_O + (rt * 80 + (h >> 3) * 10) * 1024;
    u32x4 e = {EMPTY, EMPTY, EMPTY, EMPTY};
    __builtin_amdgcn_raw_buffer_store_b128(e, rs, lane * 16, rb + (h & 7) * 1024, SC1);
    if (lane < 32) __builtin_amdgcn_raw_buffer_store_b64(u32x2{e[0], e[1]}, rs, lane * 16, rb + (8 + ((h >> 2) & 1)) * 1024 + ((h >> 1) & 1) * 512 + (h & 1) * 8, SC1);
    asm volatile("" :: "v"(e));
}
DI void set_flag(rsrc_t rs, int off) { __builtin_amdgcn_raw_buffer_store_b32(1u, rs, 0, off, SC1); }
DI unsigned get_flag(rsrc_t rs, int voff, int off) { return __builtin_amdgcn_raw_buffer_load_b32(rs, voff, off, SC1); }
DI unsigned a_ld1(unsigned long long sb, unsigned voff) { unsigned v; asm volatile("global_load_dword %0, %1, %2 sc1" : "=v"(v) : "v"(voff), "s"(sb) : "memory"); return v; }
DI u32x4 a_ld4s(unsigned long long sb, unsigned voff) { u32x4 v; asm volatile("global_load_dwordx4 %0, %1, %2 sc1" : "=v"(v) : "v"(voff), "s"(sb) : "memory"); return v; }
template <int OFF> DI u32x4 a_ld4so(unsigned long long sb, unsigned voff) { u32x4 v;
    asm volatile("global_load_dwordx4 %0, %1, %2 offset:%3 sc1" : "=v"(v) : "v"(voff), "s"(sb), "n"(OFF) : "memory"); return v; }
template <int OFF> DI u32x4 a_ld4(unsigned long long sb, unsigned voff) { u32x4 v;
    asm volatile("global_load_dwordx4 %0, %1, %2 offset:%3" : "=v"(v) : "v"(voff), "s"(sb), "n"(OFF) : "memory"); return v; }
template <class T> DI void landed(T& v) { asm volatile("" : "+v"(v)); }
DI void wait_flag(rsrc_t rs, int voff, int off, unsigned v) {
    if (__builtin_expect(!__all(v == 1u), 0)) {
#pragma clang loop unroll(disable)
        for (int spin = 0; spin < (1 << 22); ++spin) { if (__all(get_flag(rs, voff, off) == 1u)) break; __builtin_amdgcn_s_sleep(1); }
    }
    __builtin_amdgcn_fence(__ATOMIC_ACQUIRE, "wavefront");
    asm volatile("" ::: "memory");
}

#define FILL_C 2
constexpr int L_SP = 136192, L_K0 = 152320, L_SQ = 153600, L_QA = 1280, L_KVF = 11520, L_WO = 13312;

__global__ __launch_bounds__(512, 2) void k2_attn(char* __restrict__ ws, u16* __restrict__ out) {
    extern __shared__ __attribute__((aligned(16))) char lds[];
    int bid = blockIdx.x;
    asm volatile("" : "+s"(bid));
    const int tid = threadIdx.x, lane = tid & 63, wave = rfl(tid >> 6);
    const int h = (bid & 7) * 4 + ((bid >> 3) & 3), qb = bid >> 5;
    const int oq = bid & 7, rb = oq * 4 + ((bid >> 3) & 3), cb = bid >> 5;
    const int r = lane & 31, hh = lane >> 5;
    const unsigned lds0 = (unsigned)(uintptr_t)lds;
    const rsrc_t rs = __builtin_amdgcn_make_buffer_rsrc((void*)ws, 0, WS_USED, 0x00020000);
    const char* kvb = ws + OFF_IB + (size_t)h * 163840;
    const char* qim = ws + OFF_IQ + (size_t)h * 163840 + qb * 20480;
    const int wr = (wave & 3) * 2 + (wave >> 2), rt = qb * 8 + wr;
    const int rc = r < 20 ? r : 19;
    glds16(qim + wr * 2560, lane * 16, lds0 + L_SQ + wave * 1280);
    glds16(qim + wr * 2560 + 256, lane * 16, lds0 + L_SQ + wave * 1280 + 256);
    glds16(kvb + (wave & 1) * 256, lane * 16, lds0 + L_K0 + (wave & 1) * 256);
#pragma unroll
    for (int i = 0; i < 3; ++i) glds16(kvb + (1 + i * 8 + wave) * 1024, lane * 16, lds0 + (1 + i * 8 + wave) * 1024);
    const char* ap = ws + OFF_IA + (size_t)h * 163840 + rt * 2560 + hh * 320 + rc * 16;
    u32x4 a0, a1, b0, b1; float gate;
    asm volatile("global_load_dwordx4 %0, %5, off\n\tglobal_load_dwordx4 %1, %5, off offset:640\n\tglobal_load_dwordx4 %2, %5, off offset:1280\n\t"
                 "global_load_dwordx4 %3, %5, off offset:1920\n\tglobal_load_dword %4, %6, off"
                 : "=&v"(a0), "=&v"(a1), "=&v"(b0), "=&v"(b1), "=&v"(gate) : "v"(ap), "v"((const float*)(ws + OFF_G) + rt * 32 + r) : "memory");
#pragma unroll
    for (int i = 0; i < 3; ++i) glds16(kvb + (25 + i * 8 + wave) * 1024, lane * 16, lds0 + (25 + i * 8 + wave) * 1024);
    if (tid < 4) *(LAS unsigned*)(lds + tid * 4) = 0u;
    const u32x4 ONES = {0x3F803F80u, 0x3F803F80u, 0x3F803F80u, 0x3F803F80u};
    const f32x16 Z16 = {};
    wait_vm_bar<8>();
    const int g = lane >> 4, q = (lane >> 2) & 3, p = lane & 3;
    const int tro = (g & 1) * 640 + (p & 1) * 320 + q * 16 + (p >> 1) * 8;
    const LAS char* L = (const LAS char*)lds;
    const LAS char* kmp = L + tro + hh * 128;
    const LAS char* ktp = L + tro + 256;
    const LAS char* vp  = L + 1280 + hh * 320 + r * 16;
    const int ent = tid < 126 ? tid : 125;
    u32x8 Q;
    {
        const u32x2 a = trd(kmp + L_SQ + wave * 1280), b = trd(kmp + L_SQ + wave * 1280 + 64), c = trd(ktp + L_SQ + wave * 1280);
        Q[0] = a[0]; Q[1] = a[1]; Q[2] = b[0]; Q[3] = b[1]; Q[4] = hh ? 0u : c[0]; Q[5] = hh ? 0u : c[1]; Q[6] = 0u; Q[7] = 0u;
    }
    const bf16x8 qm = B8(((u32x4){Q[0], Q[1], Q[2], Q[3]}));
    f32x32 S; f32x16 O = {}; u32x32 OPS; u32x8 P = {}; u32x4 AD;
    {
        const u32x2 a = trd(kmp + L_K0), b = trd(kmp + L_K0 + 64), c = trd(ktp + L_K0);
        const bf16x8 k0 = B8(((u32x4){a[0], a[1], b[0], b[1]})), k1 = B8(((u32x4){c[0], c[1], 0x3F80u, 0u}));
        const f32x16 sm = MFMA32(k0, qm, Z16);
        f32x16 s0 = MFMA32(k1, B8(((u32x4){Q[4], Q[5], 0u, 0u})), sm);
        float m = s0[0];
#pragma unroll
        for (int i = 1; i < 16; ++i) m = fmaxf(m, s0[i]);
        m = xhalf_max(m);
        Q[6] = hh ? 0u : (pk2(-m, 0.f) & 0xffffu);
        s0 = MFMA32(k1, B8(((u32x4){Q[4], Q[5], Q[6], 0u})), sm);
#pragma unroll
        for (int i = 0; i < 16; ++i) { S[i] = s0[i]; S[16 + i] = 0.f; }
        const unsigned vc = r == 20 ? 0x3F803F80u : 0u;
#pragma unroll
        for (int i = 0; i < 32; ++i) OPS[i] = (i & 8) ? vc : 0u;
        const u32x2 a1 = trd(kmp + 2560), b1 = trd(kmp + 2560 + 64), c1 = trd(ktp + 2560);
        OPS[0] = a1[0]; OPS[1] = a1[1]; OPS[2] = b1[0]; OPS[3] = b1[1]; OPS[4] = c1[0]; OPS[5] = c1[1]; OPS[6] = 0x3F80u; OPS[22] = 0x3F80u;
        if (r < 20) { const u32x4 v0 = *(const LAS u32x4*)vp; OPS[12] = v0[0]; OPS[13] = v0[1]; OPS[14] = v0[2]; OPS[15] = v0[3]; }
        AD[0] = (unsigned)(uintptr_t)kmp; AD[1] = (unsigned)(uintptr_t)ktp; AD[2] = (unsigned)(uintptr_t)vp; AD[3] = lane * 16;
    }
    {
        asm volatile("s_waitcnt vmcnt(3)" : "+v"(a0), "+v"(a1), "+v"(b0), "+v"(b1), "+v"(gate) :: "memory");
        if (r >= 20) { a0 = u32x4{0u, 0u, 0u, 0u}; a1 = a0; b0 = ONES; b1 = ONES; }
        f32x16 kvacc = MFMA32(B8(a0), B8(b0), Z16); kvacc = MFMA32(B8(a1), B8(b1), kvacc);
        if (r <= 20) {
            LAS f32x4* dst = (LAS f32x4*)(lds + L_SP + ((wave * 2 + hh) * 21 + r) * 48);
            dst[0] = f32x4{kvacc[0], kvacc[1], kvacc[2], kvacc[3]};
            dst[1] = f32x4{kvacc[4], kvacc[5], kvacc[6], kvacc[7]};
            dst[2] = f32x4{kvacc[8], kvacc[9], kvacc[10], kvacc[11]};
        }
    }
    const unsigned long long vmask = 0x000FFFFF000FFFFFull;
    const int j = wave & 3, isE = (wave >> 2) ^ 1;
    const char* wop = ws + OFF_WOT + (size_t)((cb * 4 + j) * 50) * 1024;
    const int flA = (int)OFF_FL + (oq * 4 + j) * 128, flv = (lane & 7) * 16;
    const int ov = ((g >> 1) * 1024 + (g & 1) * 512 + (lane & 15) * 16), os = (int)OFF_O + (rb * 160 + j * 10) * 1024;
    unsigned fl = 0u;
    u32x4 kp[8], wa[20], aa[2][10];
    const unsigned long long wsu = uni64(ws);
    const unsigned long long kb = uni64(ws + OFF_KVP + 4096 + h * 16384), kb2 = uni64(ws + OFF_KVP + 12288 + h * 16384);
    const unsigned long long ob[2][2] = {{uni64(ws + os + 4096), uni64(ws + os + 8192)}, {uni64(ws + os + 81920 + 4096), uni64(ws + os + 81920 + 8192)}};
#define LD_H(A, vo, hf, en, C) asm volatile("s_cmp_lg_u32 %13, 0\n\ts_cselect_b64 exec, -1, 0\n\t" \
        "global_load_dwordx4 %0, %10, %11 offset:-4096 sc1\n\tglobal_load_dwordx4 %1, %10, %11 offset:-3840 sc1\n\t" \
        "global_load_dwordx4 %2, %10, %11 offset:-2048 sc1\n\tglobal_load_dwordx4 %3, %10, %11 offset:-1792 sc1\n\t" \
        "global_load_dwordx4 %4, %10, %11 sc1\n\tglobal_load_dwordx4 %5, %10, %11 offset:256 sc1\n\t" \
        "global_load_dwordx4 %6, %10, %11 offset:2048 sc1\n\tglobal_load_dwordx4 %7, %10, %11 offset:2304 sc1\n\t" \
        "global_load_dwordx4 %8, %10, %12 sc1\n\tglobal_load_dwordx4 %9, %10, %12 offset:256 sc1\n\ts_mov_b64 exec, -1" \
        : C(A[0]), C(A[1]), C(A[2]), C(A[3]), C(A[4]), C(A[5]), C(A[6]), C(A[7]), C(A[8]), C(A[9]) \
        : "v"(vo), "s"(ob[hf][0]), "s"(ob[hf][1]), "s"(en) : "memory", "scc");
#define LD_W4(A, f) asm volatile("s_cmp_lg_u32 %6, 0\n\ts_cselect_b64 exec, -1, 0\n\t" \
        "global_load_dwordx4 %0, %4, %5\n\tglobal_load_dwordx4 %1, %4, %5 offset:1024\n\t" \
        "global_load_dwordx4 %2, %4, %5 offset:2048\n\tglobal_load_dwordx4 %3, %4, %5 offset:3072\n\ts_mov_b64 exec, -1" \
        : "=&v"((A)[0]), "=&v"((A)[1]), "=&v"((A)[2]), "=&v"((A)[3]) : "v"(lane * 16), "s"(uni64(wop + (f) * 1024)), "s"(isE) : "memory", "scc");
#define LD_KP(TAIL) asm volatile("s_cmp_lt_u32 %11, 2\n\ts_cselect_b64 exec, -1, 0\n\t" \
        "global_load_dwordx4 %0, %8, %9 offset:-4096 sc1\n\tglobal_load_dwordx4 %1, %8, %9 offset:-2048 sc1\n\t" \
        "global_load_dwordx4 %2, %8, %9 sc1\n\tglobal_load_dwordx4 %3, %8, %9 offset:2048 sc1\n\t" \
        "global_load_dwordx4 %4, %8, %10 offset:-4096 sc1\n\tglobal_load_dwordx4 %5, %8, %10 offset:-2048 sc1\n\t" \
        "global_load_dwordx4 %6, %8, %10 sc1\n\tglobal_load_dwordx4 %7, %8, %10 offset:2048 sc1\n\t" \
        "s_mov_b64 exec, -1" TAIL \
        : "=&v"(kp[0]), "=&v"(kp[1]), "=&v"(kp[2]), "=&v"(kp[3]), "=&v"(kp[4]), "=&v"(kp[5]), "=&v"(kp[6]), "=&v"(kp[7]) \
        : "v"(ent * 16), "s"(kb), "s"(kb2), "s"(wave) : "memory", "scc")
#pragma unroll
    for (int c = 0; c < 8; ++c) {
        unsigned ma, mb; unsigned long long ga, gb;
        if (c < 3) {
            const int lim = (c == 1 ? L_SP : 163840) - 3072;
            const int oa = (c == 2 ? L_SP : 50176 + c * 49152) + wave * 3072, ob = oa + 24576 < lim ? oa + 24576 : lim;
            ma = lds0 + oa; mb = lds0 + ob; ga = uni64(kvb + oa); gb = uni64(kvb + ob);
        } else {
            ma = lds0 + (c == 7 ? 1024 + j * 3072 : L_WO + ((c - 3) * 8 + wr) * 3072); mb = ma;
            ga = uni64(wop + (c == 7 ? 44 : 20 + (wave >> 2) * 12 + 3 * (c - 3)) * 1024); gb = ga;
        }
        CSTAMP(2 * c);
        unsigned cnt;
        if (c == 0) cnt = attn_chunk<true>(S, O, OPS, P, AD, Q, vmask, rfl(ma), ga, rfl(mb), gb, 1u);
        else cnt = attn_chunk<false>(S, O, OPS, P, AD, Q, vmask, rfl(ma), ga, rfl(mb), gb, c < 3 ? 1u : c < 7 ? 0u : (unsigned)(2 - 2 * isE));
        CSTAMP(2 * c + 1);
        if (c < 7) wait_cnt(lds, 0, cnt, 8 * (c + 1));
        if (c == FILL_C && c != 1) empty_o(rs, rt, h, lane);
        if (c == 0) {
            if (wave < 2) {
                f32x4 s = *(const LAS f32x4*)(L + L_SP + ent * 16);
#pragma unroll
                for (int w = 1; w < 8; ++w) s += *(const LAS f32x4*)(L + L_SP + w * 2016 + ent * 16);
                const u32x4 v = __builtin_bit_cast(u32x4, s);
                if (tid < 126) __builtin_amdgcn_raw_buffer_store_b128(u32x4{full(v[0]), full(v[1]), full(v[2]), full(v[3])}, rs, tid * 16, (int)OFF_KVP + (h * 8 + qb) * 2048, SC1);
            }
            glds16(qim + wr * 2560 + 1280, lane * 16, lds0 + L_QA + wave * 1280);
            glds16(qim + wr * 2560 + 1280 + 256, lane * 16, lds0 + L_QA + wave * 1280 + 256);
        } else if (c == 1) {
            LD_KP("");
            if (FILL_C == 1) empty_o(rs, rt, h, lane);
        } else if (c == 2) {
#pragma unroll
            for (int j = 0; j < 8; ++j) landed(kp[j]);
            if (wave < 2) {
#pragma clang loop unroll(disable)
                for (int spin = 0; spin < (1 << 20); ++spin) {
                    unsigned m = 0u;
#pragma unroll
                    for (int j = 0; j < 8; ++j) m = umax4(m, kp[j]);
                    if (__builtin_expect(__all(m != EMPTY), 1)) break;
                    LD_KP("\n\ts_waitcnt vmcnt(0)");
                }
                f32x4 s = __builtin_bit_cast(f32x4, kp[0]);
#pragma unroll
                for (int j = 1; j < 8; ++j) s += __builtin_bit_cast(f32x4, kp[j]);
                const int e3 = ent / 3;
                *(LAS u32x2*)(lds + L_KVF + e3 * 32 + (ent - e3 * 3) * 8) = u32x2{pk2(s[0], s[1]), pk2(s[2], s[3])};
            }
        } else if (c == 3) {
            const int rr = r <= 20 ? r : 20;
            const u32x4 ka0 = *(const LAS u32x4*)(L + L_KVF + (hh * 21 + rr) * 32);
            const u32x2 ka1 = *(const LAS u32x2*)(L + L_KVF + (hh * 21 + rr) * 32 + 16);
            const LAS char* qa = L + L_QA + wave * 1280 + tro;
            const u32x2 pq0 = trd(qa + hh * 64), pq1 = trd(qa + 128 + hh * 64);
            u32x2 pq2 = trd(qa + 256);
            if (hh) pq2 = u32x2{0u, 0u};
            f32x16 oa = MFMA32(B8(ka0), B8(((u32x4){pq0[0], pq0[1], pq1[0], pq1[1]})), Z16);
            oa = MFMA32(B8(((u32x4){ka1[0], ka1[1], 0u, 0u})), B8(((u32x4){pq2[0], pq2[1], 0u, 0u})), oa);
            const float den = hi_half(oa[8]) + 1e-6f;
            store_o(rs, oa, gate * __builtin_amdgcn_rcpf(den), rt, 0, h, lane);
            LD_W4(wa, 0) LD_W4(wa + 4, 4)
        } else if (c == 4) {
            if (tid == 0) set_flag(rs, (int)OFF_FL + (qb * 4 + (h >> 3)) * 128 + (h & 7) * 16);
            LD_W4(wa + 8, 8) LD_W4(wa + 12, 12)
        } else if (c == 5) {
            fl = a_ld1(wsu, flA + flv);
            LD_W4(wa + 16, 16)
        } else if (c == 6) {
            landed(fl);
            if (wave < 4) wait_flag(rs, flv, flA, fl);
            LD_H(aa[0], ov + 40960, 0, isE, "=&v")
        }
    }
#pragma unroll
    for (int i = 0; i < 10; ++i) { landed(wa[i]); landed(wa[10 + i]); landed(aa[0][i]); }
    u32x4 wd[4];
    LD_H(aa[1], ov + 40960, 1, isE, "=&v") LD_W4(wd, 46)
    if (lane == 0) set_flag(rs, (int)OFF_FL + (32 + rt * 4 + (h >> 3)) * 128 + (h & 7) * 16);
    O = MFMA32(B8(((u32x4){OPS[8], OPS[9], OPS[10], OPS[11]})), B8(((u32x4){P[4], P[5], P[6], P[7]})), O);
    PSTAMP(1, 2); WSTAMP(2);
    float l = hi_half(O[8]);
    {
        bool bad = !(l >= 1e-18f && l <= 1.0e30f);
#pragma unroll
        for (int i = 0; i < 12; ++i) bad = bad || !(fabsf(O[i]) <= 3.0e38f);
        if (__builtin_expect(__any(bad), 0)) {
            const bf16x8 qt0 = B8(((u32x4){Q[4], Q[5], 0u, 0u}));
            float m = -INFINITY; O = Z16;
            int tl = tid;
            asm volatile("" : "+v"(tl));
            const int r = tl & 31, hh = (tl >> 5) & 1, rc = r < 20 ? r : 19;
            const int ko = (r >> 4) * 640 + ((r >> 2) & 1) * 320 + ((r >> 3) & 1) * 8 + (r & 3) * 2;
            for (int t = 0; t < 64; ++t) {
                const char* tb = kvb + t * 2560;
                unsigned kw[6];
#pragma unroll
                for (int i = 0; i < 6; ++i) {
                    const int d = i < 4 ? 8 * hh + 2 * i : 16 + 2 * (i - 4);
                    kw[i] = (unsigned)*(const u16*)(tb + ko + d * 16) | ((unsigned)*(const u16*)(tb + ko + d * 16 + 16) << 16);
                }
                u32x4 f0 = *(const u32x4*)(tb + 1280 + hh * 320 + rc * 16), f1 = *(const u32x4*)(tb + 1920 + hh * 320 + rc * 16);
                if (r >= 20) { f0 = ONES; f1 = ONES; }
                f32x16 s = MFMA32(B8(((u32x4){kw[0], kw[1], kw[2], kw[3]})), qm, Z16);
                s = MFMA32(B8(((u32x4){kw[4], kw[5], 0u, 0u})), qt0, s);
                float tm = s[0];
#pragma unroll
                for (int i = 1; i < 16; ++i) tm = fmaxf(tm, s[i]);
                tm = xhalf_max(tm);
                const float mn = fmaxf(m, tm);
                const float al = (m == -INFINITY) ? 0.f : __builtin_amdgcn_exp2f(m - mn);
                m = mn;
#pragma unroll
                for (int i = 0; i < 16; ++i) { O[i] *= al; s[i] = __builtin_amdgcn_exp2f(s[i] - mn); }
                u32x4 p0 = {pk2(s[0], s[1]), pk2(s[2], s[3]), pk2(s[4], s[5]), pk2(s[6], s[7])};
                u32x4 p1 = {pk2(s[8], s[9]), pk2(s[10], s[11]), pk2(s[12], s[13]), pk2(s[14], s[15])};
                O = MFMA32(B8(f0), B8(p0), O); O = MFMA32(B8(f1), B8(p1), O);
            }
            l = hi_half(O[8]);
        }
    }
    store_o(rs, O, (1.f - gate) * __builtin_amdgcn_rcpf(l), rt, 1, h, lane);
    __builtin_amdgcn_s_setprio(0);
    __builtin_amdgcn_sched_barrier(0);
    WSTAMP(3);
    constexpr int L_P0 = L_WO + 98304;
    if (wave < 4) {
        const LAS char* wl = L + j * 6144 + lane * 16;
        auto wfr = [&](int f) -> bf16x8 {
            if (f < 20) return B8(wa[f]);
            if (f < 44) return *(const LAS bf16x8*)(wl + L_WO + (((f - 20) % 12) / 3) * 24576 + ((f - 20) / 12) * 3072 + ((f - 20) % 3) * 1024);
            if (f < 46) return *(const LAS bf16x8*)(L + 1024 + j * 3072 + (f - 44) * 1024 + lane * 16);
            return B8(wd[f - 46]);
        };
        LAS char* wp = (LAS char*)lds + (lane & 15) * 320 + ((g ^ ((lane >> 1) & 3)) << 4);
        f32x4 acc[4][5] = {};
        {
            bf16x8 w4[5];
#pragma unroll
            for (int ni = 0; ni < 5; ++ni) w4[ni] = wfr(20 + ni);
#pragma unroll
            for (int hf = 0; hf < 2; ++hf) {
                if (hf) asm volatile("s_waitcnt vmcnt(3)" : "+v"(aa[1][0]), "+v"(aa[1][1]), "+v"(aa[1][2]), "+v"(aa[1][3]), "+v"(aa[1][4]), "+v"(aa[1][5]), "+v"(aa[1][6]),
                                     "+v"(aa[1][7]), "+v"(aa[1][8]), "+v"(aa[1][9]), "+v"(wd[0]), "+v"(wd[1]), "+v"(wd[2]), "+v"(wd[3]) :: "memory");
#pragma unroll
                for (int k = 0; k < 5; ++k)
#pragma unroll
                    for (int mi = 0; mi < 2; ++mi)
#pragma unroll
                        for (int ni = 0; ni < 5; ++ni) acc[hf * 2 + mi][ni] = MFMA16(k < 4 ? wfr(k * 5 + ni) : w4[ni], B8(aa[hf][2 * k + mi]), acc[hf * 2 + mi][ni]);
                __builtin_amdgcn_sched_barrier(0);
            }
        }
        const int flB = (int)OFF_FL + (32 + rb * 8 + j) * 128;
        u32x4 b0[10], b1[10];
        wait_flag(rs, flv, flB, get_flag(rs, flv, flB));
        WSTAMP(4);
        LD_H(b0, ov, 0, 1, "=&v")
        fl = a_ld1(wsu, flB + 512 + flv);
        wait_cnt(lds, 0, 64);
        asm volatile("s_waitcnt vmcnt(0)" : "+v"(fl) :: "memory");
        wait_flag(rs, flv, flB + 512, fl);
        WSTAMP(5);
        LD_H(b1, ov, 1, 1, "=&v")
#pragma clang loop unroll(disable)
        for (int spin = 0; spin < (1 << 20); ++spin) {
            unsigned m = 0u;
#pragma unroll
            for (int i = 0; i < 10; ++i) { landed(b0[i]); m = umax4(m, b0[i]); }
            if (__builtin_expect(__all(m != EMPTY), 1)) break;
            LD_H(b0, ov, 0, 1, "+v")
            asm volatile("s_waitcnt vmcnt(0)" ::: "memory");
        }
        bf16x8 wb[25];
#pragma unroll
        for (int k = 0; k < 5; ++k) {
#pragma unroll
            for (int ni = 0; ni < 5; ++ni) wb[k * 5 + ni] = wfr(25 + k * 5 + ni);
#pragma unroll
            for (int mi = 0; mi < 2; ++mi)
#pragma unroll
                for (int ni = 0; ni < 5; ++ni) acc[mi][ni] = MFMA16(wb[k * 5 + ni], B8(b0[2 * k + mi]), acc[mi][ni]);
        }
#pragma unroll
        for (int mi = 0; mi < 2; ++mi)
#pragma unroll
            for (int ni = 0; ni < 5; ++ni) *(LAS f32x4*)(wp + L_P0 + j * 10240 + mi * 5120 + ni * 64) = acc[mi][ni];
        add_cnt<12>(lds0);
        f32x4 ac1[2][5];
#pragma clang loop unroll(disable)
        for (int spin = 0; spin < (1 << 20); ++spin) {
            unsigned m = 0u;
#pragma unroll
            for (int k = 0; k < 5; ++k) {
                asm volatile("s_waitcnt vmcnt(%2)" : "+v"(b1[2 * k]), "+v"(b1[2 * k + 1]) : "n"(8 - 2 * k) : "memory");
#pragma unroll
                for (int mi = 0; mi < 2; ++mi)
#pragma unroll
                    for (int ni = 0; ni < 5; ++ni) ac1[mi][ni] = MFMA16(wb[k * 5 + ni], B8(b1[2 * k + mi]), k ? ac1[mi][ni] : acc[2 + mi][ni]);
                m = umax4(umax4(m, b1[2 * k]), b1[2 * k + 1]);
                __builtin_amdgcn_sched_barrier(0);
            }
            if (__builtin_expect(__all(m != EMPTY), 1)) break;
            LD_H(b1, ov, 1, 1, "+v")
        }
#pragma unroll
        for (int mi = 0; mi < 2; ++mi)
#pragma unroll
            for (int ni = 0; ni < 5; ++ni) *(LAS f32x4*)(wp + L_WO + j * 6144 + mi * 24576 + ni * 64) = ac1[mi][ni];
        WSTAMP(6);
        asm volatile(".Lk2_last:\n\ts_waitcnt lgkmcnt(0)\n\ts_barrier" ::: "memory");
    } else {
        const int t = tid - 256;
        const LAS char* rp[2][3]; unsigned oo[3];
#pragma unroll
        for (int i = 0; i < 3; ++i) {
            const int id = i < 2 || t < 128 ? i * 256 + t : 1, row = id / 20, cs = id - row * 20, cc = cs ^ ((row >> 1) & 3);
            rp[0][i] = L + L_P0 + id * 16; rp[1][i] = L + L_WO + id * 16 + (id >= 320 ? 24576 - 5120 : 0);
            oo[i] = (unsigned)(((rb * 64 + row) * D_ + cb * 80 + cc * 4) * 2);
            landed(rp[0][i]); landed(rp[1][i]); landed(oo[i]);
        }
        WSTAMP(4);
#pragma unroll
        for (int hf = 0; hf < 2; ++hf) {
            if (hf == 0) { wait_cnt(lds, 12, 4); WSTAMP(5); }
            else { WSTAMP(6); asm volatile("s_barrier" ::: "memory"); }
            f32x4 pv[3][4];
#pragma unroll
            for (int i = 0; i < 3; ++i)
#pragma unroll
                for (int w = 0; w < 4; ++w) pv[i][w] = *(const LAS f32x4*)(rp[hf][i] + w * (hf ? 6144 : 10240));
            __builtin_amdgcn_sched_barrier(0);
#pragma unroll
            for (int i = 0; i < 3; ++i) {
                const f32x4 v = (pv[i][0] + pv[i][1]) + (pv[i][2] + pv[i][3]);
                if (i < 2 || t < 128) __builtin_amdgcn_raw_buffer_store_b64(u32x2{pk2(v[0], v[1]), pk2(v[2], v[3])},
                    __builtin_amdgcn_make_buffer_rsrc((void*)out, 0, S_ * D_ * 2, 0x00020000), oo[i], hf * (32 * D_ * 2), OUT_AUX);
            }
        }
    }
}

extern "C" void kernel_launch(void* const* d_in, const int* in_sizes, int n_in, void* d_out, int out_size, void* d_ws, size_t ws_size,
                              hipStream_t stream) {
    const u16* x = (const u16*)d_in[0];
    const u16* wA = (const u16*)d_in[1];
    const u16* woA = (const u16*)d_in[2];
    const u16* wB = (const u16*)d_in[4];
    const u16* woB = (const u16*)d_in[5];
    const u16* gw = (const u16*)d_in[6];
    char* ws = (char*)d_ws;
    (void)hipFuncSetAttribute((const void*)k1_qkv, hipFuncAttributeMaxDynamicSharedMemorySize, LDS_BYTES);
    (void)hipFuncSetAttribute((const void*)k2_attn, hipFuncAttributeMaxDynamicSharedMemorySize, LDS_BYTES);
    k1_qkv<<<256, 512, LDS_BYTES, stream>>>(x, wA, wB, woA, woB, gw, ws);
    k2_attn<<<256, 512, LDS_BYTES, stream>>>(ws, (u16*)d_out);
}
```

```cpp
#define K2_W0 8
#define OUT_AUX 16
#include <hip/hip_runtime.h>
#include <hip/hip_bf16.h>
#include <stdint.h>

typedef short    bf16x8 __attribute__((ext_vector_type(8)));
typedef short    s16x4  __attribute__((ext_vector_type(4)));
typedef float    f32x2  __attribute__((ext_vector_type(2)));
typedef float    f32x4  __attribute__((ext_vector_type(4)));
typedef float    f32x16 __attribute__((ext_vector_type(16)));
typedef float    f32x32 __attribute__((ext_vector_type(32)));
typedef unsigned u32x2  __attribute__((ext_vector_type(2)));
typedef unsigned u32x4  __attribute__((ext_vector_type(4)));
typedef unsigned u32x8  __attribute__((ext_vector_type(8)));
typedef unsigned u32x32 __attribute__((ext_vector_type(32)));
typedef __bf16   bf2_t  __attribute__((ext_vector_type(2)));
typedef unsigned short u16;
#define LAS __attribute__((address_space(3)))
#define DI __device__ __forceinline__

constexpr int S_ = 2048, D_ = 640;
constexpr size_t IMG     = (size_t)32 * 163840;
constexpr size_t OFF_IQ  = 0;
constexpr size_t OFF_IB  = IMG;
constexpr size_t OFF_IA  = 2 * IMG;
constexpr size_t OFF_O   = 3 * IMG;
constexpr size_t OFF_WOT = 4 * IMG;
constexpr size_t OFF_G   = OFF_WOT + (size_t)1600 * 1024;
constexpr size_t OFF_KVP = OFF_G + 8192;
constexpr size_t OFF_FL  = OFF_KVP + (size_t)256 * 2048;
constexpr int    WS_USED = (int)(OFF_FL + 288 * 128);
constexpr unsigned EMPTY = 0xFFFFFFFFu;
typedef __amdgpu_buffer_rsrc_t rsrc_t;
constexpr int SC1 = 16;

#define CSTAMP(i)
#define PSTAMP(k, i)
#define WSTAMP(i)

DI unsigned pk2(float lo, float hi) { f32x2 v = {lo, hi}; return __builtin_bit_cast(unsigned, __builtin_convertvector(v, bf2_t)); }
DI float bflo(unsigned v) { return __uint_as_float(v << 16); }
DI float bfhi(unsigned v) { return __uint_as_float(v & 0xffff0000u); }
DI int rfl(int x) { return __builtin_amdgcn_readfirstlane(x); }
DI void glds16(const void* sbase, unsigned voff, unsigned lds_base) {
    const unsigned long long a = (unsigned long long)(uintptr_t)sbase;
    const unsigned long long u = (unsigned long long)(unsigned)rfl((int)(unsigned)a) | ((unsigned long long)(unsigned)rfl((int)(unsigned)(a >> 32)) << 32);
    asm volatile("s_mov_b32 m0, %0\n\ts_nop 0\n\tglobal_load_lds_dwordx4 %1, %2" :: "s"(rfl((int)lds_base)), "v"(voff), "s"(u) : "memory");
}
#define MFMA16(a, b, c) __builtin_amdgcn_mfma_f32_16x16x32_bf16((a), (b), (c), 0, 0, 0)
#define MFMA32(a, b, c) __builtin_amdgcn_mfma_f32_32x32x16_bf16((a), (b), (c), 0, 0, 0)
#define B8(x) __builtin_bit_cast(bf16x8, x)
DI u32x2 trd(const LAS char* p) { return __builtin_bit_cast(u32x2, __builtin_amdgcn_ds_read_tr16_b64_v4i16((LAS s16x4*)p)); }
DI float pmax(float t, float c) { return __int_as_float(max(__float_as_int(t), __float_as_int(c))); }
template <int N> DI void wait_vm_bar() { asm volatile("s_waitcnt vmcnt(%0) lgkmcnt(0)\n\ts_barrier" :: "n"(N) : "memory"); }

constexpr int LDS_BYTES = 163840;
constexpr float LOG2E = 1.4426950408889634f;

DI void p1_aux(const u16* __restrict__ x, const u16* __restrict__ woA, const u16* __restrict__ woB, const u16* __restrict__ gate_w,
               char* __restrict__ ws, char* lds, int a) {
    const int tid = threadIdx.x, lane = tid & 63, wave = rfl(tid >> 6);
    const unsigned lds0 = (unsigned)(uintptr_t)lds;
    const int br = a >> 3, u = a & 7;
    if (tid < 144) *(u32x4*)(ws + OFF_FL + (a * 144 + tid) * 16) = u32x4{0u, 0u, 0u, 0u};
    const char* wsrc = (const char*)(br ? woB : woA) + (size_t)u * 102400;
    for (int i = wave; i < 100; i += 8) glds16(wsrc + i * 1024, lane * 16, lds0 + i * 1024);
    const u32x4* xs = (const u32x4*)(x + (size_t)(a * 128 + wave * 16) * D_);
    u32x4 xv[20], gw[5];
#pragma unroll
    for (int i = 0; i < 20; ++i) xv[i] = xs[i * 64 + lane];
#pragma unroll
    for (int i = 0; i < 5; ++i) gw[i] = ((const u32x4*)gate_w)[(i * 64 + lane) % 80];
    {
        const rsrc_t rs = __builtin_amdgcn_make_buffer_rsrc((void*)(ws + OFF_KVP + a * 32768), 0, 32768, 0x00020000);
#pragma unroll
        for (int i = 0; i < 4; ++i) __builtin_amdgcn_raw_buffer_store_b128(u32x4{EMPTY, EMPTY, EMPTY, EMPTY}, rs, tid * 16 + i * 8192, 0, SC1);
    }
    LAS float* part = (LAS float*)(lds + 102400) + wave * 1280;
#pragma unroll
    for (int i = 0; i < 20; ++i) {
        float s = 0.f;
#pragma unroll
        for (int j = 0; j < 4; ++j) s += bflo(xv[i][j]) * bflo(gw[i % 5][j]) + bfhi(xv[i][j]) * bfhi(gw[i % 5][j]);
        part[i * 64 + lane] = s;
    }
    wait_vm_bar<0>();
    {
        float s = 0.f;
#pragma unroll
        for (int j = 0; j < 20; ++j) s += part[lane * 20 + j];
        s += __shfl_xor(s, 1); s += __shfl_xor(s, 2);
        if ((lane & 3) == 0) ((float*)(ws + OFF_G))[a * 128 + wave * 16 + (lane >> 2)] = 1.f / (1.f + __expf(-s));
    }
    const int g = lane >> 4, hh = g & 1, ns = g >> 1, q = (lane >> 2) & 3, p = lane & 3;
    for (int un = wave; un < 100; un += 8) {
        const int e = un / 20, np = un - e * 20, nt = np * 2 + ns;
        const int r0 = e < 4 ? e * 20 + 4 * hh : (2 * hh) * 20 + 16;
        const int r1 = e < 4 ? r0 + 8 : r0 + 20;
        const int ks16 = e < 4 ? (u & 1) * 4 + e : 8 + (u & 1), cbk = nt / 5;
        const u32x2 lo = trd((const LAS char*)lds + (r0 + q) * 1280 + nt * 32 + p * 8);
        const u32x2 hi = trd((const LAS char*)lds + (r1 + q) * 1280 + nt * 32 + p * 8);
        *(u32x4*)(ws + OFF_WOT + (size_t)((cbk * 8 + (u & 6) + br) * 25 + (ks16 >> 1) * 5 + nt - cbk * 5) * 1024 + ((ks16 & 1) * 2 + hh) * 256 + (lane & 15) * 16) = u32x4{lo[0], lo[1], hi[0], hi[1]};
    }
}

#define CPF 64
#define CPF_N 256
#define CPF_M 0
#define G1_TL 3
#define G1_D 3
#define G1_D0 2
#define G1_DT 10
#define G1_DE 0
#define G1_NX 4
constexpr int G1_NW = 10 - G1_NX, G1_HB = 20 - 2 * G1_TL, G1_WOFF = G1_NX * 16384;
constexpr int g1_t(int h) { return h < 2 * G1_HB ? h >> 1 : h < 2 * G1_HB + 2 * G1_TL ? h - G1_HB : h - G1_HB - 2 * G1_TL; }
constexpr int g1_m(int h) { return h < 2 * G1_HB ? h & 1 : h < 2 * G1_HB + 2 * G1_TL ? 0 : 1; }
constexpr int g1_xi(int h) { return h < 2 * G1_HB ? (h >> 1) & 1 : h & 1; }
constexpr int g1_free(int i) { return i < 10 ? (i < G1_NX ? -1 : 2 * (i - G1_NX) + 2) : (i - 10 < G1_NW ? -1 : i - 10 - G1_NW + 1); }
struct G1Plan { int seq[30]; int cnt[22]; int pos[30]; };
constexpr G1Plan g1_plan() {
    G1Plan p = {}; bool done[30] = {}, seen[10] = {}; int ord[30] = {}, need[21] = {}, n = 0;
    for (int b = 0; b < 20; ++b) { const int u = g1_t(2 * b) >> 1; if (!seen[u]) { seen[u] = true; ord[n++] = u; } need[b] = n; ord[n++] = 10 + b; }
    need[20] = 30; n = 0;
    for (int b = 0; b <= 20; ++b) {
        const int ex = b > G1_DT ? b - G1_DT : 0, lim = 3 * (b >> 1) + 1 + (b & 1) + (b == 0 ? G1_D0 : G1_D) + (ex < G1_DE ? ex : G1_DE);
        for (int k = 0; k < 30 && k <= lim; ++k) { const int i = ord[k]; if (!done[i] && g1_free(i) < b) { done[i] = true; p.pos[i] = n; p.seq[n++] = i; } }
        p.cnt[b] = n;
    }
    return p;
}
constexpr G1Plan G1P = g1_plan();
constexpr int g1_wait(int b) { const int x = G1P.pos[g1_t(2 * b) >> 1], w = G1P.pos[10 + b]; return 4 * (G1P.cnt[b] - 1 - (x > w ? x : w)); }
constexpr bool g1_ok() { for (int b = 0; b < 20; ++b) if (g1_wait(b) < 0 || g1_wait(b) > 60) return false; return G1P.cnt[19] == 30 && G1_NX >= G1_TL && G1_NX <= 4; }
static_assert(g1_ok(), "loader plan");
#define G1_ROT ((0x27057250 >> (4 * xcd)) & 15)

#define LST_P
#define LST_A
template <int T> struct G1Load {
    template <class FX, class FW> static DI void run(FX&& ldx, FW&& ldw LST_P) {
        asm volatile("s_waitcnt vmcnt(%0)\n\ts_barrier" :: "n"(g1_wait(T)) : "memory");
#pragma unroll
        for (int n = G1P.cnt[T]; n < G1P.cnt[T + 1]; ++n) { const int i = G1P.seq[n]; if (i < 10) ldx(i); else ldw(i - 10); }
        G1Load<T + 1>::run(ldx, ldw LST_A);
    }
};
template <> struct G1Load<20> { template <class FX, class FW> static DI void run(FX&&, FW&& LST_P) {} };

struct G1Ctx {
    f32x4 acc[4][8];
    bf16x8 x[2][4]; u32x2 w[2][4][2];
    const LAS char* xa[2]; const LAS char* wa[8];
    int so[4];
    rsrc_t rB; float sc; f32x2 m[4]; u32x4 o;
    unsigned pf0, pf1;
};
#define SB0() __builtin_amdgcn_sched_barrier(0)
#define G1_SP 12
template <int J, int I> DI void g1_side(G1Ctx& c) {
    constexpr int K = (J - (40 - 2 * G1_TL)) * 16 + I;
    if (CPF && J == 20 && I == 12) { asm volatile("s_waitcnt vmcnt(0)" :: "v"(c.pf0), "v"(c.pf1)); SB0(); }
    if (K > 0 && K <= 8 * G1_SP && (K - 1) % G1_SP == 8) asm volatile("" :: "v"(c.o));
    if (K >= 0 && K < 8 * G1_SP && K % G1_SP < 9) {
        constexpr int U = K < 0 ? 0 : K / G1_SP, e = K < 0 ? 0 : K % G1_SP, ni = (U >> 1) & 3, a = U & 1;
        constexpr int mi = e == 0 ? 0 : e == 1 ? 1 : e == 3 ? 2 : e == 5 ? 3 : -1, ci = e == 2 ? 0 : e == 4 ? 1 : e == 6 ? 2 : e == 7 ? 3 : -1;
        if (mi >= 0) { const f32x4 v = c.acc[2 * a + ((mi & 3) >> 1)][ni]; c.m[mi & 3] = f32x2{v[(mi & 1) * 2], v[(mi & 1) * 2 + 1]} * c.sc; }
        if (ci >= 0) c.o[ci & 3] = pk2(c.m[ci & 3][0], c.m[ci & 3][1]);
        if (e == 8) __builtin_amdgcn_raw_buffer_store_b128(c.o, c.rB, c.so[ni] + a * 2560, 0, SC1);
        SB0();
    }
}
template <int J> struct G1Step {
    static DI void run(G1Ctx& c) {
        constexpr int N = J + 1, t = g1_t(N), xo = ((t >> 1) % G1_NX) * 16384, sl = (N >> 1) % G1_NW, wo = (sl & 3) * 16384 + (N & 1) * 8192, wi = (sl >> 2) * 4;
        constexpr int ps = J & 1, ma = g1_m(J) * 4, xc = g1_xi(J), xn = g1_xi(N);
#define G1_M(i) c.acc[(i) & 3][ma + ((i) >> 2)] = MFMA16(c.x[xc][(i) & 3], B8(((u32x4){c.w[ps][(i) >> 2][0][0], c.w[ps][(i) >> 2][0][1], c.w[ps][(i) >> 2][1][0], c.w[ps][(i) >> 2][1][1]})), \
                c.acc[(i) & 3][ma + ((i) >> 2)]); SB0(); g1_side<J, i>(c);
#define G1_X(i) if (N < 40) c.x[xn][i] = *(const LAS bf16x8*)(c.xa[t & 1] + xo + ((i) >> 1) * 4096 + ((i) & 1) * 1024); SB0();
#define G1_W(i, e) if (N < 40) c.w[ps ^ 1][i][e] = trd(c.wa[wi + (i)] + wo + (e) * 1024); SB0();
        if (ps) {
            G1_M(0) G1_M(1) G1_M(2)
            if (N < 40) asm volatile("s_waitcnt lgkmcnt(0)\n\ts_barrier" ::: "memory");
            SB0();
            G1_X(0) G1_M(3) G1_W(0, 0) G1_M(4) G1_W(0, 1) G1_M(5) G1_X(1) G1_M(6) G1_X(2) G1_M(7) G1_X(3) G1_M(8) G1_W(1, 0) G1_M(9) G1_W(1, 1) G1_M(10)
            G1_W(2, 0) G1_M(11) G1_W(2, 1) G1_M(12) G1_W(3, 0) G1_M(13) G1_W(3, 1) G1_M(14) G1_M(15)
        } else if (xn != xc) {
            G1_M(0) G1_X(0) G1_M(1) G1_W(0, 0) G1_M(2) G1_W(0, 1) G1_M(3) G1_X(1) G1_M(4) G1_X(2) G1_M(5) G1_X(3) G1_M(6) G1_W(1, 0) G1_M(7) G1_W(1, 1)
            G1_M(8) G1_W(2, 0) G1_M(9) G1_W(2, 1) G1_M(10) G1_W(3, 0) G1_M(11) G1_W(3, 1) G1_M(12) G1_M(13) G1_M(14) G1_M(15)
        } else {
            G1_M(0) G1_W(0, 0) G1_M(1) G1_W(0, 1) G1_M(2) G1_W(1, 0) G1_M(3) G1_W(1, 1) G1_M(4) G1_W(2, 0) G1_M(5) G1_W(2, 1) G1_M(6) G1_W(3, 0) G1_M(7) G1_W(3, 1)
            G1_M(8) G1_M(9) G1_M(10) G1_M(11) G1_M(12) G1_M(13) G1_M(14) G1_M(15)
        }
        G1Step<J + 1>::run(c);
    }
};
template <> struct G1Step<40> { static DI void run(G1Ctx&) {} };
template <int K> struct G1Rest { static DI void run(G1Ctx& c) { g1_side<40 - 2 * G1_TL + K / 16, K % 16>(c); G1Rest<K + 1>::run(c); } };
template <> struct G1Rest<8 * G1_SP> { static DI void run(G1Ctx&) {} };

DI void phase1(const u16* __restrict__ x, const u16* __restrict__ wA, const u16* __restrict__ wB, char* __restrict__ ws, char* lds, int bid,
               unsigned pf0, unsigned pf1) {
    const int tid = threadIdx.x, lane = tid & 63, wave = rfl(tid >> 6);
    const unsigned lds0 = (unsigned)(uintptr_t)lds;
    const int xcd = bid & 7, j = (xcd >> 1) * 30 + (bid >> 3);
    const int ct = j >> 3, mt = (xcd & 1) * 8 + (j & 7);
    const int type = rfl(ct / 5), rot = rfl(G1_ROT);
    if (__builtin_expect(wave >= 4, 1)) {
        const int lw = wave - 4;
        unsigned xo[4], wo[2];
#pragma unroll
        for (int i = 0; i < 4; ++i) { const int row = (lw * 4 + i) * 8 + (lane >> 3); xo[i] = (unsigned)((mt * 128 + row) * 1280 + (((lane & 7) ^ ((row >> 1) & 3) ^ ((row >> 2) & 4)) << 4)); }
#pragma unroll
        for (int i = 0; i < 2; ++i) { const int k = (lw * 2 + i) * 4 + (lane >> 4), s16 = lane & 15;
            wo[i] = (unsigned)(k * 3840 + ct * 256 + ((s16 ^ ((k & 3) << 2) ^ ((k >> 2) & 2)) << 4)); }
        auto ldx = [&](int u, unsigned st = 0u) {
            int kk = u + rot; kk -= kk >= 10 ? 10 : 0;
            const unsigned slot = lds0 + (u % G1_NX) * 16384 + lw * 4096;
#pragma unroll
            for (int i = 0; i < 4; ++i) glds16((const char*)x + kk * 128, xo[i], slot + i * 1024);
        };
        auto ldw = [&](int s) {
            int kk = (g1_t(2 * s) >> 1) + rot; kk -= kk >= 10 ? 10 : 0;
            const unsigned slot = lds0 + G1_WOFF + (s % G1_NW) * 16384 + lw * 2048;
#pragma unroll
            for (int e = 0; e < 2; ++e) {
                const char* src = (const char*)(g1_m(2 * s + e) ? wA : wB) + (kk * 64 + (g1_t(2 * s + e) & 1) * 32) * 3840;
                glds16(src, wo[0], slot + e * 8192); glds16(src, wo[1], slot + e * 8192 + 1024);
            }
        };
#pragma unroll
        for (int n = 0; n < G1P.cnt[0]; ++n) { const int i = G1P.seq[n]; if (i < 10) ldx(i); else ldw(i - 10); }
        G1Load<0>::run(ldx, ldw LST_A);
        return;
    }
    const int wm = wave & 1, wn = wave >> 1;
    const int i16 = lane & 15, g = lane >> 4, q = (lane >> 2) & 3, p = lane & 3;
    G1Ctx c;
    c.pf0 = pf0; c.pf1 = pf1;
    const int xr = (i16 >> 3) * 16 + (i16 & 7);
#pragma unroll
    for (int ks = 0; ks < 2; ++ks) { c.xa[ks] = (const LAS char*)lds + (wm * 64 + xr) * 128 + (((ks * 4 + g) ^ ((xr >> 1) & 3) ^ ((xr >> 2) & 4)) << 4); asm volatile("" : "+v"(c.xa[ks])); }
#pragma unroll
    for (int i = 0; i < 8; ++i) { c.wa[i] = (const LAS char*)lds + G1_WOFF + (i >> 2) * 65536 + (8 * g + q) * 256 + (((wn * 4 + (i & 3)) ^ (q << 1) ^ (g & 1)) << 5) + p * 8;
        asm volatile("" : "+v"(c.wa[i])); }
#pragma unroll
    for (int i = 0; i < 4; ++i) { const int cc = (ct - type * 5) * 128 + wn * 64 + i * 16 + i16, h = cc / 20; c.so[i] = h * 163840 + g * 320 + (cc - h * 20) * 16; asm volatile("" : "+v"(c.so[i])); }
    constexpr float QS = 0.22360679774997896f * LOG2E;
    int sci = type == 0 ? __builtin_bit_cast(int, QS) : 0x3F800000;
    asm volatile("" : "+s"(sci));
    c.sc = __int_as_float(sci);
    const int tb = (mt * 4 + wm * 2) * 2560;
    const int sB = tb + (type == 0 ? (int)OFF_IQ : (int)OFF_IB + (type - 1) * 1280), sA = tb + (type == 0 ? (int)OFF_IQ + 1280 : (int)OFF_IA + (type - 1) * 1280);
    c.rB = __builtin_amdgcn_make_buffer_rsrc((void*)(ws + sB), 0, (int)IMG, 0x00020000);
    const rsrc_t rA = __builtin_amdgcn_make_buffer_rsrc((void*)(ws + sA), 0, (int)IMG, 0x00020000);
    {
        const float a0 = type < 2 ? 1e-6f : 0.f;
        f32x4 a4 = {a0, a0, a0, a0};
        asm volatile("" : "+v"(a4));
#pragma unroll
        for (int i = 0; i < 16; ++i) { c.acc[i & 3][i >> 2] = f32x4{0.f, 0.f, 0.f, 0.f}; c.acc[i & 3][4 + (i >> 2)] = a4; }
    }
    __builtin_amdgcn_s_setprio(2);
    asm volatile("s_barrier" ::: "memory");
#pragma unroll
    for (int i = 0; i < 4; ++i) {
        c.x[0][i] = *(const LAS bf16x8*)(c.xa[0] + (i >> 1) * 4096 + (i & 1) * 1024);
        if (i == 0) { c.w[0][0][0] = trd(c.wa[0]); c.w[0][0][1] = trd(c.wa[0] + 1024); }
    }
#pragma unroll
    for (int i = 1; i < 4; ++i) { c.w[0][i][0] = trd(c.wa[i]); c.w[0][i][1] = trd(c.wa[i] + 1024); }
    SB0();
    G1Step<0>::run(c);
    PSTAMP(0, 2);
    G1Rest<(32 * G1_TL < 8 * G1_SP ? 32 * G1_TL : 8 * G1_SP)>::run(c);
    if (type < 2) {
#pragma unroll
        for (int i = 0; i < 4; ++i)
#pragma unroll
            for (int a = 0; a < 2; ++a) {
                u32x4 o;
#pragma unroll
                for (int e = 0; e < 4; ++e) {
                    const f32x2 t = {c.acc[2 * a + (e >> 1)][4 + i][(e & 1) * 2], c.acc[2 * a + (e >> 1)][4 + i][(e & 1) * 2 + 1]};
                    const f32x2 m = __builtin_elementwise_fma(t, f32x2{LOG2E, LOG2E}, f32x2{-1e-6f * LOG2E, -1e-6f * LOG2E});
                    const f32x2 ex = {__builtin_amdgcn_fmed3f(__builtin_amdgcn_exp2f(m[0]), 0.f, 1.f), __builtin_amdgcn_fmed3f(__builtin_amdgcn_exp2f(m[1]), 0.f, 1.f)};
                    const f32x2 r = f32x2{pmax(t[0], 1e-6f), pmax(t[1], 1e-6f)} + ex;
                    o[e] = pk2(r[0], r[1]);
                }
                __builtin_amdgcn_raw_buffer_store_b128(o, rA, c.so[i] + a * 2560, 0, SC1);
            }
    } else {
#pragma unroll
        for (int i = 0; i < 4; ++i)
#pragma unroll
            for (int a = 0; a < 2; ++a) {
                const f32x4 u = c.acc[2 * a][4 + i], v = c.acc[2 * a + 1][4 + i];
                __builtin_amdgcn_raw_buffer_store_b128(u32x4{pk2(u[0], u[1]), pk2(u[2], u[3]), pk2(v[0], v[1]), pk2(v[2], v[3])}, rA, c.so[i] + a * 2560, 0, SC1);
            }
    }
}

__global__ __launch_bounds__(512, 2) void k1_qkv(const u16* __restrict__ x, const u16* __restrict__ wA, const u16* __restrict__ wB,
                                                 const u16* __restrict__ woA, const u16* __restrict__ woB, const u16* __restrict__ gate_w,
                                                 char* __restrict__ ws) {
    extern __shared__ __attribute__((aligned(16))) char lds[];
    int bid = blockIdx.x;
    asm volatile("" : "+s"(bid));
    unsigned pf0, pf1 = 0u;
    {
        unsigned long long pc;
        asm volatile("s_getpc_b64 %1\n.Lk1_pc:\n\tv_lshl_add_u32 %0, %2, 7, %3\n\tv_min_u32 %0, .Lk2_last-.Lk1_pc, %0\n\t"
                     "v_cmp_gt_u32 vcc, %4, %2\n\ts_mov_b64 exec, vcc\n\tglobal_load_dword %0, %0, %1\n\ts_mov_b64 exec, -1"
                     : "=&v"(pf0), "=&s"(pc) : "v"(threadIdx.x), "s"((bid >> 3) * (CPF * 128) + 64), "n"(CPF) : "memory", "vcc");
    }
    if (__builtin_expect(bid < 240, 1)) phase1(x, wA, wB, ws, lds, bid, pf0, pf1);
    else { p1_aux(x, woA, woB, gate_w, ws, lds, bid - 240); asm volatile("" :: "v"(pf0), "v"(pf1)); }
}

#define STR(x) #x
#define XS(x) STR(x)
#define A_E(i) "v_exp_f32 v" XS(i) ", v" XS(i) "\n"
#define A_C(d, a, b) "v_cvt_pk_bf16_f32 v" XS(d) ", v" XS(a) ", v" XS(b) "\n"
#define A_G(a, b, c, d, p0, p1) A_E(a) A_E(b) A_E(c) A_E(d) A_C(p0, a, b) A_C(p1, c, d)
#define A_M(d, a, b, c) "v_mfma_f32_32x32x16_bf16 " d ", " a ", " b ", " c "\n"
#define A_STEP(C0,C1,C2,C3,C4,C5,C6,C7,C8,C9,C10,C11,C12,C13,C14,C15, SN, KM, KT, V1, V0, NKMa, NKMb, NKT, NV1, NV0, OK0, OK1, OV1, OV0, PRIO) \
    "s_waitcnt lgkmcnt(0)\n" \
    A_M(SN, KM, "v[120:123]", "0") \
    "ds_read_b64_tr_b16 " NKMa ", v128 offset:" XS(OK0) "\n" \
    "ds_read_b64_tr_b16 " NKMb ", v128 offset:" XS(OK1) "\n" \
    "ds_read_b64_tr_b16 " NKT ", v129 offset:" XS(OK0) "\n" \
    A_G(C0, C1, C2, C3, 112, 113) \
    A_M("v[64:79]", V1, "v[116:119]", "v[64:79]") \
    "s_mov_b64 exec, s[72:73]\n" \
    "ds_read_b128 " NV1 ", v130 offset:" XS(OV1) "\n" \
    "ds_read_b128 " NV0 ", v130 offset:" XS(OV0) "\n" \
    "s_mov_b64 exec, -1\n" \
    A_G(C4, C5, C6, C7, 114, 115) \
    A_M(SN, KT, "v[124:127]", SN) \
    A_G(C8, C9, C10, C11, 116, 117) \
    A_M("v[64:79]", V0, "v[112:115]", "v[64:79]") \
    "s_setprio " XS(PRIO) "\n" \
    A_G(C12, C13, C14, C15, 118, 119)
#define A_EVEN(OK0, OK1, OV1, OV0, PRIO) A_STEP(32,33,34,35,36,37,38,39,40,41,42,43,44,45,46,47, "v[48:63]", "v[80:83]", "v[84:87]", "v[88:91]", "v[92:95]", \
    "v[96:97]", "v[98:99]", "v[100:101]", "v[104:107]", "v[108:111]", OK0, OK1, OV1, OV0, PRIO)
#define A_ODD(OK0, OK1, OV1, OV0, PRIO) A_STEP(48,49,50,51,52,53,54,55,56,57,58,59,60,61,62,63, "v[32:47]", "v[96:99]", "v[100:103]", "v[104:107]", "v[108:111]", \
    "v[80:81]", "v[82:83]", "v[84:85]", "v[88:91]", "v[92:95]", OK0, OK1, OV1, OV0, PRIO)

#define A_LD(g, off) "global_load_lds_dwordx4 v131, " g " offset:" XS(off) "\n"
#define A_LDA(off) "s_cbranch_scc1 .Lattn_s" XS(off) "\n" A_LD("s[74:75]", off) ".Lattn_s" XS(off) ":\n"
#define A_BODY \
        "s_mov_b32 m0, s78\n s_cmp_eq_u32 s80, 2\n s_nop 3\n" \
        A_EVEN(5120, 5184, 640, 2560, 2)     A_ODD(7680, 7744, 3200, 5120, 1) \
        A_EVEN(10240, 10304, 5760, 7680, 0)  A_ODD(12800, 12864, 8320, 10240, 3) \
        "s_waitcnt vmcnt(0) lgkmcnt(0)\n s_mov_b64 exec, 1\n ds_add_u32 v127, v86\n s_mov_b64 exec, -1\n" \
        A_EVEN(15360, 15424, 10880, 12800, 2) A_LDA(0) A_ODD(17920, 17984, 13440, 15360, 1) A_LDA(1024) \
        A_EVEN(20480, 20544, 16000, 17920, 0) A_LDA(2048) \
        "s_cmp_eq_u32 s80, 1\n s_cbranch_scc0 .Lattn_skip\n s_mov_b32 m0, s79\n s_nop 0\n" \
        A_LD("s[76:77]", 0) A_LD("s[76:77]", 1024) A_LD("s[76:77]", 2048) ".Lattn_skip:\n" \
        "ds_read_b32 v132, v127\n" \
        A_ODD(23040, 23104, 18560, 20480, 3) \
        "v_add_u32 v128, 0x5000, v128\n v_add_u32 v129, 0x5000, v129\n v_add_u32 v130, 0x5000, v130\n" \
        "s_waitcnt lgkmcnt(0)\n s_nop 0\n"
struct AttnRegs { f32x16 S, O; u32x4 KM, P1, QM, KT0, KT1, QT, AD; u32x8 V0, V1; };
#define A_OPERANDS \
        : "+{v[32:47]}"(a.S), "+{v[64:79]}"(a.O), "+{v[80:83]}"(a.KM), "+{v[84:87]}"(a.KT0), "+{v[88:95]}"(a.V0), "+{v[100:103]}"(a.KT1), \
          "+{v[104:111]}"(a.V1), "+{v[116:119]}"(a.P1), "+{v[128:131]}"(a.AD), "={v132}"(cnt) \
        : "{v[120:123]}"(a.QM), "{v[124:127]}"(a.QT), \
          "{s[72:73]}"(vmask), "{s[74:75]}"(ga), "{s[76:77]}"(gb), "{s78}"(ma), "{s79}"(mb), "{s80}"(enb) \
        : "memory", "scc", "s82", "s83", "v48", "v49", "v50", "v51", "v52", "v53", "v54", "v55", "v56", "v57", "v58", "v59", "v60", "v61", "v62", "v63", \
          "v96", "v97", "v98", "v99", "v112", "v113", "v114", "v115"
template <bool FIRST>
DI unsigned attn_chunk(AttnRegs& a, unsigned long long vmask, unsigned ma, unsigned long long ga, unsigned mb, unsigned long long gb, unsigned enb) {
    unsigned cnt;
    if (FIRST) asm volatile("s_call_b64 s[82:83], .Lattn_body\n s_branch .Lattn_done\n .p2align 6\n .Lattn_body:\n" A_BODY "s_setpc_b64 s[82:83]\n .Lattn_done:\n" A_OPERANDS);
    else       asm volatile("s_call_b64 s[82:83], .Lattn_body\n" A_OPERANDS);
    return cnt;
}
DI void wait_cnt(char* lds, int off, unsigned cnt, int n) {
    if (__builtin_expect((unsigned)rfl((int)cnt) < (unsigned)(n * 0x3F80), 0)) {
#pragma clang loop unroll(disable)
        do { __builtin_amdgcn_s_sleep(1); cnt = *(volatile LAS unsigned*)(lds + off); } while ((unsigned)rfl((int)cnt) < (unsigned)(n * 0x3F80));
    }
    asm volatile("" ::: "memory");
}

DI void wait_cnt(char* lds, int off, int n) { wait_cnt(lds, off, *(volatile LAS unsigned*)(lds + off), n); }
template <int OFF> DI void add_cnt(unsigned lds0) {
    asm volatile("s_waitcnt lgkmcnt(0)\n\ts_mov_b64 exec, 1\n\tds_add_u32 %0, %1 offset:%2\n\ts_mov_b64 exec, -1" :: "v"(lds0), "v"(0x3F80u), "n"(OFF) : "memory"); }

DI float xhalf_max(float m) { auto rr = __builtin_amdgcn_permlane32_swap(__float_as_uint(m), __float_as_uint(m), false, false);
    return fmaxf(__uint_as_float(rr[0]), __uint_as_float(rr[1])); }
DI float hi_half(float v) {
    auto rr = __builtin_amdgcn_permlane32_swap(__float_as_uint(v), __float_as_uint(v), false, false);
    return __uint_as_float(rr[1]); }
DI float sum16(float v) { auto rr = __builtin_amdgcn_permlane16_swap(__float_as_uint(v), __float_as_uint(v), false, false);
    return __uint_as_float(rr[0]) + __uint_as_float(rr[1]); }
DI float sum32(float v) { auto rr = __builtin_amdgcn_permlane32_swap(__float_as_uint(v), __float_as_uint(v), false, false);
    return __uint_as_float(rr[0]) + __uint_as_float(rr[1]); }
DI f32x4 sum_rows(f32x4 a, f32x4 b) {
    f32x4 s = a + b;
#pragma unroll
    for (int i = 0; i < 4; ++i) s[i] = sum32(sum16(s[i]));
    return s;
}
DI unsigned long long uni64(const void* p) { const unsigned long long a = (unsigned long long)(uintptr_t)p;
    return (unsigned long long)(unsigned)rfl((int)(unsigned)a) | ((unsigned long long)(unsigned)rfl((int)(unsigned)(a >> 32)) << 32); }
DI unsigned full(unsigned v) { return v < EMPTY ? v : EMPTY - 1u; }
DI unsigned umax4(unsigned m, u32x4 v) { return max(max(max(m, v[0]), max(v[1], v[2])), v[3]); }
DI void store_o(rsrc_t rs, const f32x16& o, float f, int rt, int br, int h, int lane) {
    const int rb = (int)OFF_O + (rt * 80 + ((br ? 0 : 4) + (h >> 3)) * 10) * 1024;
    __builtin_amdgcn_raw_buffer_store_b128(u32x4{full(pk2(o[0] * f, o[1] * f)), full(pk2(o[2] * f, o[3] * f)), full(pk2(o[4] * f, o[5] * f)), full(pk2(o[6] * f, o[7] * f))},
                                           rs, lane * 16, rb + (h & 7) * 1024, SC1);
    if (lane < 32) __builtin_amdgcn_raw_buffer_store_b64(u32x2{full(pk2(o[8] * f, o[9] * f)), full(pk2(o[10] * f, o[11] * f))},
                                           rs, lane * 16, rb + (8 + ((h >> 2) & 1)) * 1024 + ((h >> 1) & 1) * 512 + (h & 1) * 8, SC1);
}
DI void empty_o(rsrc_t rs, int rt, int h, int lane) {
    const int rb = (int)OFF_O + (rt * 80 + (h >> 3) * 10) * 1024;
    u32x4 e = {EMPTY, EMPTY, EMPTY, EMPTY};
    __builtin_amdgcn_raw_buffer_store_b128(e, rs, lane * 16, rb + (h & 7) * 1024, SC1);
    if (lane < 32) __builtin_amdgcn_raw_buffer_store_b64(u32x2{e[0], e[1]}, rs, lane * 16, rb + (8 + ((h >> 2) & 1)) * 1024 + ((h >> 1) & 1) * 512 + (h & 1) * 8, SC1);
    asm volatile("" :: "v"(e));
}
DI void set_flag(rsrc_t rs, int off) { __builtin_amdgcn_raw_buffer_store_b32(1u, rs, 0, off, SC1); }
DI unsigned get_flag(rsrc_t rs, int voff, int off) { return __builtin_amdgcn_raw_buffer_load_b32(rs, voff, off, SC1); }
DI unsigned a_ld1(unsigned long long sb, unsigned voff) { unsigned v; asm volatile("global_load_dword %0, %1, %2 sc1" : "=v"(v) : "v"(voff), "s"(sb) : "memory"); return v; }
DI u32x4 a_ld4s(unsigned long long sb, unsigned voff) { u32x4 v; asm volatile("global_load_dwordx4 %0, %1, %2 sc1" : "=v"(v) : "v"(voff), "s"(sb) : "memory"); return v; }
template <int OFF> DI u32x4 a_ld4so(unsigned long long sb, unsigned voff) { u32x4 v;
    asm volatile("global_load_dwordx4 %0, %1, %2 offset:%3 sc1" : "=v"(v) : "v"(voff), "s"(sb), "n"(OFF) : "memory"); return v; }
template <int OFF> DI u32x4 a_ld4(unsigned long long sb, unsigned voff) { u32x4 v;
    asm volatile("global_load_dwordx4 %0, %1, %2 offset:%3" : "=v"(v) : "v"(voff), "s"(sb), "n"(OFF) : "memory"); return v; }
template <class T> DI void landed(T& v) { asm volatile("" : "+v"(v)); }
DI void wait_flag(rsrc_t rs, int voff, int off, unsigned v) {
    if (__builtin_expect(!__all(v == 1u), 0)) {
#pragma clang loop unroll(disable)
        for (int spin = 0; spin < (1 << 22); ++spin) { if (__all(get_flag(rs, voff, off) == 1u)) break; __builtin_amdgcn_s_sleep(1); }
    }
    __builtin_amdgcn_fence(__ATOMIC_ACQUIRE, "wavefront");
    asm volatile("" ::: "memory");
}

#define K2_BAR
#define FILL_C 2
constexpr int L_SP = 136192, L_K0 = 152320, L_SQ = 153600, L_QA = 1280, L_KVF = 11520, L_WO = 13312;

__global__ __launch_bounds__(512, 2) void k2_attn(char* ws_arg, u16* __restrict__ out) {
    extern __shared__ __attribute__((aligned(16))) char lds[];
    char* ws;
    asm volatile("s_load_dwordx2 %0, %1, 0x0" : "={s[20:21]}"(ws) : "s"(__builtin_amdgcn_kernarg_segment_ptr()));
    int bid = blockIdx.x;
    asm volatile("" : "+s"(bid));
    const int tid = threadIdx.x, lane = tid & 63, wave = rfl(tid >> 6);
    const int h = (bid & 7) * 4 + ((bid >> 3) & 3), qb = bid >> 5;
    const int oq = bid & 7, rb = oq * 4 + ((bid >> 3) & 3), cb = bid >> 5;
    const int r = lane & 31, hh = lane >> 5;
    const unsigned lds0 = (unsigned)(uintptr_t)lds;
    const int wr = (wave & 3) * 2 + (wave >> 2), rt = qb * 8 + wr;
    unsigned long long vmask = 0x000FFFFF000FFFFFull;
    const u32x4 ONES = {0x3F803F80u, 0x3F803F80u, 0x3F803F80u, 0x3F803F80u};
    const f32x16 Z16 = {};
    const int g = lane >> 4, q = (lane >> 2) & 3, p = lane & 3;
    const int tro = (g & 1) * 640 + (p & 1) * 320 + q * 16 + (p >> 1) * 8;
    const LAS char* L = (const LAS char*)lds;
    const int ent = tid < 126 ? tid : 125;
    AttnRegs ar;
    u32x2 c0, c1, c2;
    unsigned aq, ak, aqt, akt, asp, vi[6];
    u32x4 a0 = {0u, 0u, 0u, 0u}, a1 = a0, b0 = ONES, b1 = ONES; float gate;
    {
        if (tid < 8) *(LAS unsigned*)(lds + tid * 4) = 0u;
        const unsigned vc = r == 20 ? 0x3F803F80u : 0u;
        ar.O = Z16; ar.P1 = u32x4{0u, 0u, 0u, 0u}; c0 = u32x2{0x3F80u, 0u}; c1 = c0; c2 = u32x2{0u, 0u};
        ar.V0 = u32x8{vc, vc, vc, vc, vc, vc, vc, vc}; ar.V1 = ar.V0;
        ar.AD = u32x4{lds0 + tro + hh * 128, lds0 + tro + 256, lds0 + 1280 + hh * 320 + r * 16, (unsigned)lane * 16};
        aq = ar.AD[0] + L_SQ + wave * 1280; ak = ar.AD[0] + L_K0; asp = lds0 + L_SP + ((wave * 2 + hh) * 21 + r) * 48;
        aqt = hh ? lds0 + 16 : ar.AD[1] + L_SQ + wave * 1280; akt = ar.AD[1] + L_K0;
        asm volatile("" : "+{v[64:79]}"(ar.O), "+{v[86:87]}"(c0), "+{v[88:95]}"(ar.V0), "+{v[102:103]}"(c1), "+{v[104:111]}"(ar.V1),
                          "+{v[116:119]}"(ar.P1), "+{v[128:131]}"(ar.AD), "+{v[126:127]}"(c2),
                          "+v"(aq), "+v"(ak), "+v"(aqt), "+v"(akt), "+v"(asp), "+{s[72:73]}"(vmask));
        const unsigned hb = h * 163840 + lane * 16;
        unsigned vq = (unsigned)OFF_IQ + hb + rt * 2560, vk = (unsigned)OFF_IB + hb + (wave & 1) * 256;
        unsigned va = (unsigned)OFF_IA + h * 163840 + rt * 2560 + hh * 320 + r * 16, vg = (unsigned)OFF_G + rt * 128 + r * 4;
        unsigned mq = lds0 + L_SQ + wave * 1280, mk = lds0 + L_K0 + (wave & 1) * 256, mi = lds0 + (1 + wave) * 1024;
#pragma unroll
        for (int i = 0; i < 6; ++i) vi[i] = (unsigned)OFF_IB + hb + (1 + i * 8 + wave) * 1024;
        asm volatile("" : "+v"(vq), "+v"(vk), "+v"(va), "+v"(vg), "+v"(vi[0]), "+v"(vi[1]), "+v"(vi[2]), "+v"(vi[3]), "+v"(vi[4]), "+v"(vi[5]),
                          "+v"(a0), "+v"(a1), "+v"(b0), "+v"(b1), "+s"(mq), "+s"(mk), "+s"(mi));
        asm volatile("s_mov_b32 m0, %13\n\ts_waitcnt lgkmcnt(0)\n\tglobal_load_lds_dwordx4 %6, %5\n\tglobal_load_lds_dwordx4 %6, %5 offset:256\n\t"
                     "s_mov_b32 m0, %14\n\ts_nop 0\n\tglobal_load_lds_dwordx4 %7, %5\n\t"
                     "s_mov_b32 m0, %15\n\ts_nop 0\n\tglobal_load_lds_dwordx4 %10, %5\n\t"
                     "s_add_u32 m0, m0, 0x2000\n\ts_nop 0\n\tglobal_load_lds_dwordx4 %11, %5\n\t"
                     "s_add_u32 m0, m0, 0x2000\n\ts_nop 0\n\tglobal_load_lds_dwordx4 %12, %5\n\t"
                     "global_load_dword %4, %9, %5\n\ts_mov_b64 exec, %16\n\t"
                     "global_load_dwordx4 %0, %8, %5\n\tglobal_load_dwordx4 %1, %8, %5 offset:640\n\tglobal_load_dwordx4 %2, %8, %5 offset:1280\n\t"
                     "global_load_dwordx4 %3, %8, %5 offset:1920\n\ts_mov_b64 exec, -1"
                     : "+v"(a0), "+v"(a1), "+v"(b0), "+v"(b1), "=&v"(gate), "+{s[20:21]}"(ws)
                     : "v"(vq), "v"(vk), "v"(va), "v"(vg), "v"(vi[0]), "v"(vi[1]), "v"(vi[2]), "s"(mq), "s"(mk), "s"(mi), "s"(vmask) : "memory", "scc");
    }
    const rsrc_t rs = __builtin_amdgcn_make_buffer_rsrc((void*)ws, 0, WS_USED, 0x00020000);
    const char* kvb = ws + OFF_IB + (size_t)h * 163840;
    const char* qim = ws + OFF_IQ + (size_t)h * 163840 + qb * 20480;
    const int j = wave & 3, isE = (wave >> 2) ^ 1;
    const char* wop = ws + OFF_WOT + (size_t)((cb * 4 + j) * 50) * 1024;
    const int flA = (int)OFF_FL + (oq * 4 + j) * 128, flv = (lane & 7) * 16;
    const int ov = ((g >> 1) * 1024 + (g & 1) * 512 + (lane & 15) * 16), os = (int)OFF_O + (rb * 160 + j * 10) * 1024;
    unsigned fl = 0u;
    u32x4 kp[8], wa[20], aa[2][10];
    const unsigned long long wsu = uni64(ws);
    unsigned long long kb = uni64(ws + OFF_KVP + 4096 + h * 16384), kb2 = uni64(ws + OFF_KVP + 12288 + h * 16384);
    unsigned long long ob[2][2] = {{uni64(ws + os + 4096), uni64(ws + os + 8192)}, {uni64(ws + os + 81920 + 4096), uni64(ws + os + 81920 + 8192)}};
#define LD_H(A, vo, hf, en, C) asm volatile("s_cmp_lg_u32 %13, 0\n\ts_cselect_b64 exec, -1, 0\n\t" \
        "global_load_dwordx4 %0, %10, %11 offset:-4096 sc1\n\tglobal_load_dwordx4 %1, %10, %11 offset:-3840 sc1\n\t" \
        "global_load_dwordx4 %2, %10, %11 offset:-2048 sc1\n\tglobal_load_dwordx4 %3, %10, %11 offset:-1792 sc1\n\t" \
        "global_load_dwordx4 %4, %10, %11 sc1\n\tglobal_load_dwordx4 %5, %10, %11 offset:256 sc1\n\t" \
        "global_load_dwordx4 %6, %10, %11 offset:2048 sc1\n\tglobal_load_dwordx4 %7, %10, %11 offset:2304 sc1\n\t" \
        "global_load_dwordx4 %8, %10, %12 sc1\n\tglobal_load_dwordx4 %9, %10, %12 offset:256 sc1\n\ts_mov_b64 exec, -1" \
        : C(A[0]), C(A[1]), C(A[2]), C(A[3]), C(A[4]), C(A[5]), C(A[6]), C(A[7]), C(A[8]), C(A[9]) \
        : "v"(vo), "s"(ob[hf][0]), "s"(ob[hf][1]), "s"(en) : "memory", "scc");
#define LD_W4(A, f) asm volatile("s_cmp_lg_u32 %6, 0\n\ts_cselect_b64 exec, -1, 0\n\t" \
        "global_load_dwordx4 %0, %4, %5\n\tglobal_load_dwordx4 %1, %4, %5 offset:1024\n\t" \
        "global_load_dwordx4 %2, %4, %5 offset:2048\n\tglobal_load_dwordx4 %3, %4, %5 offset:3072\n\ts_mov_b64 exec, -1" \
        : "=&v"((A)[0]), "=&v"((A)[1]), "=&v"((A)[2]), "=&v"((A)[3]) : "v"(lane * 16), "s"(uni64(wop + (f) * 1024)), "s"(isE) : "memory", "scc");
#define LD_KP(TAIL) asm volatile("s_cmp_lt_u32 %11, 2\n\ts_cselect_b64 exec, -1, 0\n\t" \
        "global_load_dwordx4 %0, %8, %9 offset:-4096 sc1\n\tglobal_load_dwordx4 %1, %8, %9 offset:-2048 sc1\n\t" \
        "global_load_dwordx4 %2, %8, %9 sc1\n\tglobal_load_dwordx4 %3, %8, %9 offset:2048 sc1\n\t" \
        "global_load_dwordx4 %4, %8, %10 offset:-4096 sc1\n\tglobal_load_dwordx4 %5, %8, %10 offset:-2048 sc1\n\t" \
        "global_load_dwordx4 %6, %8, %10 sc1\n\tglobal_load_dwordx4 %7, %8, %10 offset:2048 sc1\n\t" \
        "s_mov_b64 exec, -1" TAIL \
        : "=&v"(kp[0]), "=&v"(kp[1]), "=&v"(kp[2]), "=&v"(kp[3]), "=&v"(kp[4]), "=&v"(kp[5]), "=&v"(kp[6]), "=&v"(kp[7]) \
        : "v"(ent * 16), "s"(kb), "s"(kb2), "s"(wave) : "memory", "scc")
    asm volatile("" : "+s"(kb), "+s"(kb2), "+s"(ob[0][0]), "+s"(ob[0][1]), "+s"(ob[1][0]), "+s"(ob[1][1]));
    unsigned ma0 = rfl(lds0 + 50176 + wave * 3072), mb0 = rfl(ma0 + 24576); unsigned long long ga0 = uni64(kvb + 50176 + wave * 3072), gb0 = ga0 + 24576;
    asm volatile("" : "+{s[74:75]}"(ga0), "+{s[76:77]}"(gb0), "+{s78}"(ma0), "+{s79}"(mb0));
    asm volatile("s_mov_b32 m0, %4\n\ts_nop 0\n\tglobal_load_lds_dwordx4 %0, %3\n\t"
                 "s_add_u32 m0, m0, 0x2000\n\ts_nop 0\n\tglobal_load_lds_dwordx4 %1, %3\n\t"
                 "s_add_u32 m0, m0, 0x2000\n\ts_nop 0\n\tglobal_load_lds_dwordx4 %2, %3"
                 :: "v"(vi[3]), "v"(vi[4]), "v"(vi[5]), "s"(ws), "s"(lds0 + (25 + wave) * 1024) : "memory", "scc");
    wait_vm_bar<K2_W0>();
    {
        f32x16 kvacc;
        u32x2 t0, t1, t2;
        asm volatile("ds_read_b64_tr_b16 v[96:97], %15\n\tds_read_b64_tr_b16 v[98:99], %15 offset:64\n\t"
                     "ds_read_b64_tr_b16 v[120:121], %14\n\tds_read_b64_tr_b16 v[122:123], %14 offset:64\n\t"
                     "ds_read_b64_tr_b16 v[124:125], %16\n\tds_read_b64_tr_b16 v[100:101], %17\n\t"
                     "ds_read_b64_tr_b16 v[84:85], v129 offset:2560\n\ts_mov_b64 exec, %19\n\tds_read_b128 v[92:95], v130\n\ts_mov_b64 exec, -1\n\t"
                     "ds_read_b64_tr_b16 v[80:81], v128 offset:2560\n\tds_read_b64_tr_b16 v[82:83], v128 offset:2624\n\ts_waitcnt lgkmcnt(6)\n\t"
                     "v_mfma_f32_32x32x16_bf16 v[32:47], v[96:99], v[120:123], 0\n\ts_waitcnt lgkmcnt(4)\n\t"
                     "v_mfma_f32_32x32x16_bf16 v[48:63], v[100:103], v[124:127], v[32:47]\n\t"
                     "s_waitcnt vmcnt(3)\n\t" K2_BAR
                     "v_mfma_f32_32x32x16_bf16 %8, %9, %11, 0\n\tv_mfma_f32_32x32x16_bf16 %8, %10, %12, %8\n\ts_nop 7\n\ts_nop 0\n\t"
                     "v_max3_f32 v96, v48, v49, v50\n\tv_max3_f32 v97, v51, v52, v53\n\tv_max3_f32 v96, v96, v54, v55\n\tv_max3_f32 v97, v97, v56, v57\n\t"
                     "v_max3_f32 v96, v96, v58, v59\n\tv_max3_f32 v97, v97, v60, v61\n\tv_max3_f32 v96, v96, v62, v63\n\tv_max_f32 v96, v96, v97\n\t"
                     "v_mov_b32 v97, v96\n\ts_nop 1\n\tv_permlane32_swap_b32 v96, v97\n\tv_max_f32 v96, v96, v97\n\t"
                     "s_mov_b32 exec_hi, 0\n\tv_cvt_pk_bf16_f32 v126, -v96, 0\n\ts_mov_b32 exec_hi, -1\n\ts_nop 0\n\t"
                     "v_mfma_f32_32x32x16_bf16 v[32:47], v[100:103], v[124:127], v[32:47]"
                     : "=&{v[32:47]}"(ar.S), "=&{v[120:123]}"(ar.QM), "=&{v[124:125]}"(t2), "+{v[126:127]}"(c2), "=&{v[100:101]}"(t1), "=&{v[84:85]}"(t0),
                       "+{v[88:95]}"(ar.V0), "=&{v[80:83]}"(ar.KM), "=&v"(kvacc), "+v"(a0), "+v"(a1), "+v"(b0), "+v"(b1), "+v"(gate)
                     : "v"(aq), "v"(ak), "v"(aqt), "v"(akt), "{v[128:131]}"(ar.AD), "s"(vmask), "{v[102:103]}"(c1)
                     : "memory", "v48", "v49", "v50", "v51", "v52", "v53", "v54", "v55", "v56", "v57", "v58", "v59", "v60", "v61", "v62", "v63",
                       "v96", "v97", "v98", "v99");
        ar.KT0 = u32x4{t0[0], t0[1], c0[0], c0[1]}; ar.KT1 = u32x4{t1[0], t1[1], c1[0], c1[1]}; ar.QT = u32x4{t2[0], t2[1], c2[0], c2[1]};
        if (r <= 20) {
            LAS f32x4* dst = (LAS f32x4*)(uintptr_t)asp;
            dst[0] = f32x4{kvacc[0], kvacc[1], kvacc[2], kvacc[3]};
            dst[1] = f32x4{kvacc[4], kvacc[5], kvacc[6], kvacc[7]};
            dst[2] = f32x4{kvacc[8], kvacc[9], kvacc[10], kvacc[11]};
        }
    }
#pragma unroll
    for (int c = 0; c < 8; ++c) {
        unsigned ma, mb; unsigned long long ga, gb;
        if (c < 3) {
            const int lim = (c == 1 ? L_SP : 163840) - 3072;
            const int oa = (c == 2 ? L_SP : 50176 + c * 49152) + wave * 3072, ob = oa + 24576 < lim ? oa + 24576 : lim;
            ma = lds0 + oa; mb = lds0 + ob; ga = uni64(kvb + oa); gb = uni64(kvb + ob);
        } else {
            ma = lds0 + (c == 7 ? 1024 + j * 3072 : L_WO + ((c - 3) * 8 + wr) * 3072); mb = ma;
            ga = uni64(wop + (c == 7 ? 44 : 20 + (wave >> 2) * 12 + 3 * (c - 3)) * 1024); gb = ga;
        }
        CSTAMP(2 * c);
        unsigned cnt;
        if (c == 0) cnt = attn_chunk<true>(ar, vmask, ma0, ga0, mb0, gb0, 1u);
        else cnt = attn_chunk<false>(ar, vmask, rfl(ma), ga, rfl(mb), gb, c < 3 ? 1u : c < 7 ? 0u : (unsigned)(2 - 2 * isE));
        CSTAMP(2 * c + 1);
        if (c < 7) wait_cnt(lds, 0, cnt, 8 * (c + 1));
        if (c == FILL_C && c != 1) empty_o(rs, rt, h, lane);
        if (c == 0) {
            if (wave < 2) {
                f32x4 s = *(const LAS f32x4*)(L + L_SP + ent * 16);
#pragma unroll
                for (int w = 1; w < 8; ++w) s += *(const LAS f32x4*)(L + L_SP + w * 2016 + ent * 16);
                const u32x4 v = __builtin_bit_cast(u32x4, s);
                if (tid < 126) __builtin_amdgcn_raw_buffer_store_b128(u32x4{full(v[0]), full(v[1]), full(v[2]), full(v[3])}, rs, tid * 16, (int)OFF_KVP + (h * 8 + qb) * 2048, SC1);
            }
            glds16(qim + wr * 2560 + 1280, lane * 16, lds0 + L_QA + wave * 1280);
            glds16(qim + wr * 2560 + 1280 + 256, lane * 16, lds0 + L_QA + wave * 1280 + 256);
        } else if (c == 1) {
            LD_KP("");
            if (FILL_C == 1) empty_o(rs, rt, h, lane);
        } else if (c == 2) {
#pragma unroll
            for (int j = 0; j < 8; ++j) landed(kp[j]);
            if (wave < 2) {
#pragma clang loop unroll(disable)
                for (int spin = 0; spin < (1 << 20); ++spin) {
                    unsigned m = 0u;
#pragma unroll
                    for (int j = 0; j < 8; ++j) m = umax4(m, kp[j]);
                    if (__builtin_expect(__all(m != EMPTY), 1)) break;
                    LD_KP("\n\ts_waitcnt vmcnt(0)");
                }
                f32x4 s = __builtin_bit_cast(f32x4, kp[0]);
#pragma unroll
                for (int j = 1; j < 8; ++j) s += __builtin_bit_cast(f32x4, kp[j]);
                const int e3 = ent / 3;
                *(LAS u32x2*)(lds + L_KVF + e3 * 32 + (ent - e3 * 3) * 8) = u32x2{pk2(s[0], s[1]), pk2(s[2], s[3])};
            }
        } else if (c == 3) {
            const int rr = r <= 20 ? r : 20;
            const u32x4 ka0 = *(const LAS u32x4*)(L + L_KVF + (hh * 21 + rr) * 32);
            const u32x2 ka1 = *(const LAS u32x2*)(L + L_KVF + (hh * 21 + rr) * 32 + 16);
            const LAS char* qa = L + L_QA + wave * 1280 + tro;
            const u32x2 pq0 = trd(qa + hh * 64), pq1 = trd(qa + 128 + hh * 64);
            u32x2 pq2 = trd(qa + 256);
            if (hh) pq2 = u32x2{0u, 0u};
            f32x16 oa = MFMA32(B8(ka0), B8(((u32x4){pq0[0], pq0[1], pq1[0], pq1[1]})), Z16);
            oa = MFMA32(B8(((u32x4){ka1[0], ka1[1], 0u, 0u})), B8(((u32x4){pq2[0], pq2[1], 0u, 0u})), oa);
            const float den = hi_half(oa[8]) + 1e-6f;
            store_o(rs, oa, gate * __builtin_amdgcn_rcpf(den), rt, 0, h, lane);
            LD_W4(wa, 0) LD_W4(wa + 4, 4)
        } else if (c == 4) {
            if (tid == 0) set_flag(rs, (int)OFF_FL + (qb * 4 + (h >> 3)) * 128 + (h & 7) * 16);
            LD_W4(wa + 8, 8) LD_W4(wa + 12, 12)
        } else if (c == 5) {
            fl = a_ld1(wsu, flA + flv);
            LD_W4(wa + 16, 16)
        } else if (c == 6) {
            landed(fl);
            if (wave < 4) wait_flag(rs, flv, flA, fl);
            LD_H(aa[0], ov + 40960, 0, isE, "=&v")
        }
    }
#pragma unroll
    for (int i = 0; i < 10; ++i) { landed(wa[i]); landed(wa[10 + i]); landed(aa[0][i]); }
    u32x4 wd[4];
    LD_H(aa[1], ov + 40960, 1, isE, "=&v") LD_W4(wd, 46)
    if (lane == 0) set_flag(rs, (int)OFF_FL + (32 + rt * 4 + (h >> 3)) * 128 + (h & 7) * 16);
    f32x16 O = MFMA32(B8(((u32x4){ar.V0[0], ar.V0[1], ar.V0[2], ar.V0[3]})), B8(ar.P1), ar.O);
    PSTAMP(1, 2); WSTAMP(2);
    float l = hi_half(O[8]);
    {
        bool bad = !(l >= 1e-18f && l <= 1.0e30f);
#pragma unroll
        for (int i = 0; i < 12; ++i) bad = bad || !(fabsf(O[i]) <= 3.0e38f);
        if (__builtin_expect(__any(bad), 0)) {
            float m = -INFINITY; O = Z16;
            int tl = tid;
            asm volatile("" : "+v"(tl));
            const int r = tl & 31, hh = (tl >> 5) & 1, rc = r < 20 ? r : 19;
            const bf16x8 qm = B8(ar.QM), qt0 = B8(((u32x4){ar.QT[0], ar.QT[1], 0u, 0u}));
            const int ko = (r >> 4) * 640 + ((r >> 2) & 1) * 320 + ((r >> 3) & 1) * 8 + (r & 3) * 2;
            for (int t = 0; t < 64; ++t) {
                const char* tb = kvb + t * 2560;
                unsigned kw[6];
#pragma unroll
                for (int i = 0; i < 6; ++i) {
                    const int d = i < 4 ? 8 * hh + 2 * i : 16 + 2 * (i - 4);
                    kw[i] = (unsigned)*(const u16*)(tb + ko + d * 16) | ((unsigned)*(const u16*)(tb + ko + d * 16 + 16) << 16);
                }
                u32x4 f0 = *(const u32x4*)(tb + 1280 + hh * 320 + rc * 16), f1 = *(const u32x4*)(tb + 1920 + hh * 320 + rc * 16);
                if (r >= 20) { f0 = ONES; f1 = ONES; }
                f32x16 s = MFMA32(B8(((u32x4){kw[0], kw[1], kw[2], kw[3]})), qm, Z16);
                s = MFMA32(B8(((u32x4){kw[4], kw[5], 0u, 0u})), qt0, s);
                float tm = s[0];
#pragma unroll
                for (int i = 1; i < 16; ++i) tm = fmaxf(tm, s[i]);
                tm = xhalf_max(tm);
                const float mn = fmaxf(m, tm);
                const float al = (m == -INFINITY) ? 0.f : __builtin_amdgcn_exp2f(m - mn);
                m = mn;
#pragma unroll
                for (int i = 0; i < 16; ++i) { O[i] *= al; s[i] = __builtin_amdgcn_exp2f(s[i] - mn); }
                u32x4 p0 = {pk2(s[0], s[1]), pk2(s[2], s[3]), pk2(s[4], s[5]), pk2(s[6], s[7])};
                u32x4 p1 = {pk2(s[8], s[9]), pk2(s[10], s[11]), pk2(s[12], s[13]), pk2(s[14], s[15])};
                O = MFMA32(B8(f0), B8(p0), O); O = MFMA32(B8(f1), B8(p1), O);
            }
            l = hi_half(O[8]);
        }
    }
    store_o(rs, O, (1.f - gate) * __builtin_amdgcn_rcpf(l), rt, 1, h, lane);
    __builtin_amdgcn_s_setprio(0);
    __builtin_amdgcn_sched_barrier(0);
    WSTAMP(3);
    constexpr int L_P0 = L_WO + 98304;
    if (wave < 4) {
        const LAS char* wl = L + j * 6144 + lane * 16;
        auto wfr = [&](int f) -> bf16x8 {
            if (f < 20) return B8(wa[f]);
            if (f < 44) return *(const LAS bf16x8*)(wl + L_WO + (((f - 20) % 12) / 3) * 24576 + ((f - 20) / 12) * 3072 + ((f - 20) % 3) * 1024);
            if (f < 46) return *(const LAS bf16x8*)(L + 1024 + j * 3072 + (f - 44) * 1024 + lane * 16);
            return B8(wd[f - 46]);
        };
        LAS char* wp = (LAS char*)lds + (lane & 15) * 320 + ((g ^ ((lane >> 1) & 3)) << 4);
        f32x4 acc[4][5] = {};
        {
            bf16x8 w4[5];
#pragma unroll
            for (int ni = 0; ni < 5; ++ni) w4[ni] = wfr(20 + ni);
#pragma unroll
            for (int hf = 0; hf < 2; ++hf) {
                if (hf) asm volatile("s_waitcnt vmcnt(3)" : "+v"(aa[1][0]), "+v"(aa[1][1]), "+v"(aa[1][2]), "+v"(aa[1][3]), "+v"(aa[1][4]), "+v"(aa[1][5]), "+v"(aa[1][6]),
                                     "+v"(aa[1][7]), "+v"(aa[1][8]), "+v"(aa[1][9]), "+v"(wd[0]), "+v"(wd[1]), "+v"(wd[2]), "+v"(wd[3]) :: "memory");
#pragma unroll
                for (int k = 0; k < 5; ++k)
#pragma unroll
                    for (int mi = 0; mi < 2; ++mi)
#pragma unroll
                        for (int ni = 0; ni < 5; ++ni) acc[hf * 2 + mi][ni] = MFMA16(k < 4 ? wfr(k * 5 + ni) : w4[ni], B8(aa[hf][2 * k + mi]), acc[hf * 2 + mi][ni]);
                __builtin_amdgcn_sched_barrier(0);
            }
        }
        const int flB = (int)OFF_FL + (32 + rb * 8 + j) * 128;
        u32x4 b0[10], b1[10];
        wait_flag(rs, flv, flB, get_flag(rs, flv, flB));
        WSTAMP(4);
        LD_H(b0, ov, 0, 1, "=&v")
        fl = a_ld1(wsu, flB + 512 + flv);
        wait_cnt(lds, 0, 64);
        asm volatile("s_waitcnt vmcnt(0)" : "+v"(fl) :: "memory");
        wait_flag(rs, flv, flB + 512, fl);
        WSTAMP(5);
        LD_H(b1, ov, 1, 1, "=&v")
#pragma clang loop unroll(disable)
        for (int spin = 0; spin < (1 << 20); ++spin) {
            unsigned m = 0u;
#pragma unroll
            for (int i = 0; i < 10; ++i) { landed(b0[i]); m = umax4(m, b0[i]); }
            if (__builtin_expect(__all(m != EMPTY), 1)) break;
            LD_H(b0, ov, 0, 1, "+v")
            asm volatile("s_waitcnt vmcnt(0)" ::: "memory");
        }
        bf16x8 wb[25];
#pragma unroll
        for (int k = 0; k < 5; ++k) {
#pragma unroll
            for (int ni = 0; ni < 5; ++ni) wb[k * 5 + ni] = wfr(25 + k * 5 + ni);
#pragma unroll
            for (int mi = 0; mi < 2; ++mi)
#pragma unroll
                for (int ni = 0; ni < 5; ++ni) acc[mi][ni] = MFMA16(wb[k * 5 + ni], B8(b0[2 * k + mi]), acc[mi][ni]);
        }
#pragma unroll
        for (int mi = 0; mi < 2; ++mi)
#pragma unroll
            for (int ni = 0; ni < 5; ++ni) *(LAS f32x4*)(wp + L_P0 + j * 10240 + mi * 5120 + ni * 64) = acc[mi][ni];
        add_cnt<12>(lds0);
        f32x4 ac1[2][5];
#pragma clang loop unroll(disable)
        for (int spin = 0; spin < (1 << 20); ++spin) {
            unsigned m = 0u;
#pragma unroll
            for (int k = 0; k < 5; ++k) {
                asm volatile("s_waitcnt vmcnt(%2)" : "+v"(b1[2 * k]), "+v"(b1[2 * k + 1]) : "n"(8 - 2 * k) : "memory");
#pragma unroll
                for (int mi = 0; mi < 2; ++mi)
#pragma unroll
                    for (int ni = 0; ni < 5; ++ni) ac1[mi][ni] = MFMA16(wb[k * 5 + ni], B8(b1[2 * k + mi]), k ? ac1[mi][ni] : acc[2 + mi][ni]);
                m = umax4(umax4(m, b1[2 * k]), b1[2 * k + 1]);
                __builtin_amdgcn_sched_barrier(0);
            }
            if (__builtin_expect(__all(m != EMPTY), 1)) break;
            LD_H(b1, ov, 1, 1, "+v")
        }
#pragma unroll
        for (int mi = 0; mi < 2; ++mi)
#pragma unroll
            for (int ni = 0; ni < 5; ++ni) *(LAS f32x4*)(wp + L_WO + j * 6144 + mi * 24576 + ni * 64) = ac1[mi][ni];
        WSTAMP(6);
        asm volatile(".Lk2_last:\n\ts_waitcnt lgkmcnt(0)\n\ts_barrier" ::: "memory");
    } else {
        const int t = tid - 256;
        const LAS char* rp[2][3]; unsigned oo[3];
#pragma unroll
        for (int i = 0; i < 3; ++i) {
            const int id = i < 2 || t < 128 ? i * 256 + t : 1, row = id / 20, cs = id - row * 20, cc = cs ^ ((row >> 1) & 3);
            rp[0][i] = L + L_P0 + id * 16; rp[1][i] = L + L_WO + id * 16 + (id >= 320 ? 24576 - 5120 : 0);
            oo[i] = (unsigned)(((rb * 64 + row) * D_ + cb * 80 + cc * 4) * 2);
            landed(rp[0][i]); landed(rp[1][i]); landed(oo[i]);
        }
        WSTAMP(4);
#pragma unroll
        for (int hf = 0; hf < 2; ++hf) {
            if (hf == 0) { wait_cnt(lds, 12, 4); WSTAMP(5); }
            else { WSTAMP(6); asm volatile("s_barrier" ::: "memory"); }
            f32x4 pv[3][4];
#pragma unroll
            for (int i = 0; i < 3; ++i)
#pragma unroll
                for (int w = 0; w < 4; ++w) pv[i][w] = *(const LAS f32x4*)(rp[hf][i] + w * (hf ? 6144 : 10240));
            __builtin_amdgcn_sched_barrier(0);
#pragma unroll
            for (int i = 0; i < 3; ++i) {
                const f32x4 v = (pv[i][0] + pv[i][1]) + (pv[i][2] + pv[i][3]);
                if (i < 2 || t < 128) __builtin_amdgcn_raw_buffer_store_b64(u32x2{pk2(v[0], v[1]), pk2(v[2], v[3])},
                    __builtin_amdgcn_make_buffer_rsrc((void*)out, 0, S_ * D_ * 2, 0x00020000), oo[i], hf * (32 * D_ * 2), OUT_AUX);
            }
        }
    }
}

extern "C" void kernel_launch(void* const* d_in, const int* in_sizes, int n_in, void* d_out, int out_size, void* d_ws, size_t ws_size,
                              hipStream_t stream) {
    const u16* x = (const u16*)d_in[0];
    const u16* wA = (const u16*)d_in[1];
    const u16* woA = (const u16*)d_in[2];
    const u16* wB = (const u16*)d_in[4];
    const u16* woB = (const u16*)d_in[5];
    const u16* gw = (const u16*)d_in[6];
    char* ws = (char*)d_ws;
    (void)hipFuncSetAttribute((const void*)k1_qkv, hipFuncAttributeMaxDynamicSharedMemorySize, LDS_BYTES);
    (void)hipFuncSetAttribute((const void*)k2_attn, hipFuncAttributeMaxDynamicSharedMemorySize, LDS_BYTES);
    k1_qkv<<<256, 512, LDS_BYTES, stream>>>(x, wA, wB, woA, woB, gw, ws);
    k2_attn<<<256, 512, LDS_BYTES, stream>>>(ws, (u16*)d_out);
}
```

```cpp
#define PROBE 0
#define K2_W0 8
#define OUT_AUX 16
#include <hip/hip_runtime.h>
#include <hip/hip_bf16.h>
#include <stdint.h>

typedef short    bf16x8 __attribute__((ext_vector_type(8)));
typedef short    s16x4  __attribute__((ext_vector_type(4)));
typedef float    f32x2  __attribute__((ext_vector_type(2)));
typedef float    f32x4  __attribute__((ext_vector_type(4)));
typedef float    f32x16 __attribute__((ext_vector_type(16)));
typedef float    f32x32 __attribute__((ext_vector_type(32)));
typedef unsigned u32x2  __attribute__((ext_vector_type(2)));
typedef unsigned u32x4  __attribute__((ext_vector_type(4)));
typedef unsigned u32x8  __attribute__((ext_vector_type(8)));
typedef unsigned u32x32 __attribute__((ext_vector_type(32)));
typedef __bf16   bf2_t  __attribute__((ext_vector_type(2)));
typedef unsigned short u16;
#define LAS __attribute__((address_space(3)))
#define DI __device__ __forceinline__

constexpr int S_ = 2048, D_ = 640;
constexpr size_t IMG     = (size_t)32 * 163840;
constexpr size_t OFF_IQ  = 0;
constexpr size_t OFF_IB  = IMG;
constexpr size_t OFF_IA  = 2 * IMG;
constexpr size_t OFF_O   = 3 * IMG;
constexpr size_t OFF_WOT = 4 * IMG;
constexpr size_t OFF_G   = OFF_WOT + (size_t)1600 * 1024;
constexpr size_t OFF_KVP = OFF_G + 8192;
constexpr size_t OFF_FL  = OFF_KVP + (size_t)256 * 2048;
constexpr int    WS_USED = (int)(OFF_FL + 288 * 128);
constexpr unsigned EMPTY = 0xFFFFFFFFu;
typedef __amdgpu_buffer_rsrc_t rsrc_t;
constexpr int SC1 = 16;

#define CSTAMP(i)
#define PSTAMP(k, i)
#define WSTAMP(i)

DI unsigned pk2(float lo, float hi) { f32x2 v = {lo, hi}; return __builtin_bit_cast(unsigned, __builtin_convertvector(v, bf2_t)); }
DI float bflo(unsigned v) { return __uint_as_float(v << 16); }
DI float bfhi(unsigned v) { return __uint_as_float(v & 0xffff0000u); }
DI int rfl(int x) { return __builtin_amdgcn_readfirstlane(x); }
DI void glds16(const void* sbase, unsigned voff, unsigned lds_base) {
    const unsigned long long a = (unsigned long long)(uintptr_t)sbase;
    const unsigned long long u = (unsigned long long)(unsigned)rfl((int)(unsigned)a) | ((unsigned long long)(unsigned)rfl((int)(unsigned)(a >> 32)) << 32);
    asm volatile("s_mov_b32 m0, %0\n\ts_nop 0\n\tglobal_load_lds_dwordx4 %1, %2" :: "s"(rfl((int)lds_base)), "v"(voff), "s"(u) : "memory");
}
#define MFMA16(a, b, c) __builtin_amdgcn_mfma_f32_16x16x32_bf16((a), (b), (c), 0, 0, 0)
#define MFMA32(a, b, c) __builtin_amdgcn_mfma_f32_32x32x16_bf16((a), (b), (c), 0, 0, 0)
#define B8(x) __builtin_bit_cast(bf16x8, x)
DI u32x2 trd(const LAS char* p) { return __builtin_bit_cast(u32x2, __builtin_amdgcn_ds_read_tr16_b64_v4i16((LAS s16x4*)p)); }
DI float pmax(float t, float c) { return __int_as_float(max(__float_as_int(t), __float_as_int(c))); }
template <int N> DI void wait_vm_bar() { asm volatile("s_waitcnt vmcnt(%0) lgkmcnt(0)\n\ts_barrier" :: "n"(N) : "memory"); }

constexpr int LDS_BYTES = 163840;
constexpr float LOG2E = 1.4426950408889634f;

DI void p1_aux(const u16* __restrict__ x, const u16* __restrict__ woA, const u16* __restrict__ woB, const u16* __restrict__ gate_w,
               char* __restrict__ ws, char* lds, int a) {
    const int tid = threadIdx.x, lane = tid & 63, wave = rfl(tid >> 6);
    const unsigned lds0 = (unsigned)(uintptr_t)lds;
    const int br = a >> 3, u = a & 7;
    if (tid < 144) *(u32x4*)(ws + OFF_FL + (a * 144 + tid) * 16) = u32x4{0u, 0u, 0u, 0u};
    const char* wsrc = (const char*)(br ? woB : woA) + (size_t)u * 102400;
    for (int i = wave; i < 100; i += 8) glds16(wsrc + i * 1024, lane * 16, lds0 + i * 1024);
    const u32x4* xs = (const u32x4*)(x + (size_t)(a * 128 + wave * 16) * D_);
    u32x4 xv[20], gw[5];
#pragma unroll
    for (int i = 0; i < 20; ++i) xv[i] = xs[i * 64 + lane];
#pragma unroll
    for (int i = 0; i < 5; ++i) gw[i] = ((const u32x4*)gate_w)[(i * 64 + lane) % 80];
    {
        const rsrc_t rs = __builtin_amdgcn_make_buffer_rsrc((void*)(ws + OFF_KVP + a * 32768), 0, 32768, 0x00020000);
#pragma unroll
        for (int i = 0; i < 4; ++i) __builtin_amdgcn_raw_buffer_store_b128(u32x4{EMPTY, EMPTY, EMPTY, EMPTY}, rs, tid * 16 + i * 8192, 0, SC1);
    }
    LAS float* part = (LAS float*)(lds + 102400) + wave * 1280;
#pragma unroll
    for (int i = 0; i < 20; ++i) {
        float s = 0.f;
#pragma unroll
        for (int j = 0; j < 4; ++j) s += bflo(xv[i][j]) * bflo(gw[i % 5][j]) + bfhi(xv[i][j]) * bfhi(gw[i % 5][j]);
        part[i * 64 + lane] = s;
    }
    wait_vm_bar<0>();
    {
        float s = 0.f;
#pragma unroll
        for (int j = 0; j < 20; ++j) s += part[lane * 20 + j];
        s += __shfl_xor(s, 1); s += __shfl_xor(s, 2);
        if ((lane & 3) == 0) ((float*)(ws + OFF_G))[a * 128 + wave * 16 + (lane >> 2)] = 1.f / (1.f + __expf(-s));
    }
    const int g = lane >> 4, hh = g & 1, ns = g >> 1, q = (lane >> 2) & 3, p = lane & 3;
    for (int un = wave; un < 100; un += 8) {
        const int e = un / 20, np = un - e * 20, nt = np * 2 + ns;
        const int r0 = e < 4 ? e * 20 + 4 * hh : (2 * hh) * 20 + 16;
        const int r1 = e < 4 ? r0 + 8 : r0 + 20;
        const int ks16 = e < 4 ? (u & 1) * 4 + e : 8 + (u & 1), cbk = nt / 5;
        const u32x2 lo = trd((const LAS char*)lds + (r0 + q) * 1280 + nt * 32 + p * 8);
        const u32x2 hi = trd((const LAS char*)lds + (r1 + q) * 1280 + nt * 32 + p * 8);
        *(u32x4*)(ws + OFF_WOT + (size_t)((cbk * 8 + (u & 6) + br) * 25 + (ks16 >> 1) * 5 + nt - cbk * 5) * 1024 + ((ks16 & 1) * 2 + hh) * 256 + (lane & 15) * 16) = u32x4{lo[0], lo[1], hi[0], hi[1]};
    }
}

#define CPF 64
#define CPF_N 256
#define CPF_M 0
#define G1_TL 3
#define G1_D 3
#define G1_D0 2
#define G1_DT 10
#define G1_DE 0
#define G1_NX 4
constexpr int G1_NW = 10 - G1_NX, G1_HB = 20 - 2 * G1_TL, G1_WOFF = G1_NX * 16384;
constexpr int g1_t(int h) { return h < 2 * G1_HB ? h >> 1 : h < 2 * G1_HB + 2 * G1_TL ? h - G1_HB : h - G1_HB - 2 * G1_TL; }
constexpr int g1_m(int h) { return h < 2 * G1_HB ? h & 1 : h < 2 * G1_HB + 2 * G1_TL ? 0 : 1; }
constexpr int g1_xi(int h) { return h < 2 * G1_HB ? (h >> 1) & 1 : h & 1; }
constexpr int g1_free(int i) { return i < 10 ? (i < G1_NX ? -1 : 2 * (i - G1_NX) + 2) : (i - 10 < G1_NW ? -1 : i - 10 - G1_NW + 1); }
struct G1Plan { int seq[30]; int cnt[22]; int pos[30]; };
constexpr G1Plan g1_plan() {
    G1Plan p = {}; bool done[30] = {}, seen[10] = {}; int ord[30] = {}, need[21] = {}, n = 0;
    for (int b = 0; b < 20; ++b) { const int u = g1_t(2 * b) >> 1; if (!seen[u]) { seen[u] = true; ord[n++] = u; } need[b] = n; ord[n++] = 10 + b; }
    need[20] = 30; n = 0;
    for (int b = 0; b <= 20; ++b) {
        const int ex = b > G1_DT ? b - G1_DT : 0, lim = 3 * (b >> 1) + 1 + (b & 1) + (b == 0 ? G1_D0 : G1_D) + (ex < G1_DE ? ex : G1_DE);
        for (int k = 0; k < 30 && k <= lim; ++k) { const int i = ord[k]; if (!done[i] && g1_free(i) < b) { done[i] = true; p.pos[i] = n; p.seq[n++] = i; } }
        p.cnt[b] = n;
    }
    return p;
}
constexpr G1Plan G1P = g1_plan();
constexpr int g1_wait(int b) { const int x = G1P.pos[g1_t(2 * b) >> 1], w = G1P.pos[10 + b]; return 4 * (G1P.cnt[b] - 1 - (x > w ? x : w)); }
constexpr bool g1_ok() { for (int b = 0; b < 20; ++b) if (g1_wait(b) < 0 || g1_wait(b) > 60) return false; return G1P.cnt[19] == 30 && G1_NX >= G1_TL && G1_NX <= 4; }
static_assert(g1_ok(), "loader plan");
#define G1_ROT ((0x27057250 >> (4 * xcd)) & 15)

#define LST_P
#define LST_A
template <int T> struct G1Load {
    template <class FX, class FW> static DI void run(FX&& ldx, FW&& ldw LST_P) {
        asm volatile("s_waitcnt vmcnt(%0)\n\ts_barrier" :: "n"(g1_wait(T)) : "memory");
#pragma unroll
        for (int n = G1P.cnt[T]; n < G1P.cnt[T + 1]; ++n) { const int i = G1P.seq[n]; if (i < 10) ldx(i); else ldw(i - 10); }
        G1Load<T + 1>::run(ldx, ldw LST_A);
    }
};
template <> struct G1Load<20> { template <class FX, class FW> static DI void run(FX&&, FW&& LST_P) {} };

struct G1Ctx {
    f32x4 acc[4][8];
    bf16x8 x[2][4]; u32x2 w[2][4][2];
    const LAS char* xa[2]; const LAS char* wa[8];
    int so[4];
    rsrc_t rB; float sc; f32x2 m[4]; u32x4 o;
    unsigned pf0, pf1;
};
#define SB0() __builtin_amdgcn_sched_barrier(0)
#define G1_SP 12
template <int J, int I> DI void g1_side(G1Ctx& c) {
    constexpr int K = (J - (40 - 2 * G1_TL)) * 16 + I;
    if (CPF && J == 20 && I == 12) { asm volatile("s_waitcnt vmcnt(0)" :: "v"(c.pf0), "v"(c.pf1)); SB0(); }
    if (K > 0 && K <= 8 * G1_SP && (K - 1) % G1_SP == 8) asm volatile("" :: "v"(c.o));
    if (K >= 0 && K < 8 * G1_SP && K % G1_SP < 9) {
        constexpr int U = K < 0 ? 0 : K / G1_SP, e = K < 0 ? 0 : K % G1_SP, ni = (U >> 1) & 3, a = U & 1;
        constexpr int mi = e == 0 ? 0 : e == 1 ? 1 : e == 3 ? 2 : e == 5 ? 3 : -1, ci = e == 2 ? 0 : e == 4 ? 1 : e == 6 ? 2 : e == 7 ? 3 : -1;
        if (mi >= 0) { const f32x4 v = c.acc[2 * a + ((mi & 3) >> 1)][ni]; c.m[mi & 3] = f32x2{v[(mi & 1) * 2], v[(mi & 1) * 2 + 1]} * c.sc; }
        if (ci >= 0) c.o[ci & 3] = pk2(c.m[ci & 3][0], c.m[ci & 3][1]);
        if (e == 8) __builtin_amdgcn_raw_buffer_store_b128(c.o, c.rB, c.so[ni] + a * 2560, 0, SC1);
        SB0();
    }
}
template <int J> struct G1Step {
    static DI void run(G1Ctx& c) {
        constexpr int N = J + 1, t = g1_t(N), xo = ((t >> 1) % G1_NX) * 16384, sl = (N >> 1) % G1_NW, wo = (sl & 3) * 16384 + (N & 1) * 8192, wi = (sl >> 2) * 4;
        constexpr int ps = J & 1, ma = g1_m(J) * 4, xc = g1_xi(J), xn = g1_xi(N);
#define G1_M(i) c.acc[(i) & 3][ma + ((i) >> 2)] = MFMA16(c.x[xc][(i) & 3], B8(((u32x4){c.w[ps][(i) >> 2][0][0], c.w[ps][(i) >> 2][0][1], c.w[ps][(i) >> 2][1][0], c.w[ps][(i) >> 2][1][1]})), \
                c.acc[(i) & 3][ma + ((i) >> 2)]); SB0(); g1_side<J, i>(c);
#define G1_X(i) if (N < 40) c.x[xn][i] = *(const LAS bf16x8*)(c.xa[t & 1] + xo + ((i) >> 1) * 4096 + ((i) & 1) * 1024); SB0();
#define G1_W(i, e) if (N < 40) c.w[ps ^ 1][i][e] = trd(c.wa[wi + (i)] + wo + (e) * 1024); SB0();
        if (ps) {
            G1_M(0) G1_M(1) G1_M(2)
            if (N < 40) asm volatile("s_waitcnt lgkmcnt(0)\n\ts_barrier" ::: "memory");
            SB0();
            G1_X(0) G1_M(3) G1_W(0, 0) G1_M(4) G1_W(0, 1) G1_M(5) G1_X(1) G1_M(6) G1_X(2) G1_M(7) G1_X(3) G1_M(8) G1_W(1, 0) G1_M(9) G1_W(1, 1) G1_M(10)
            G1_W(2, 0) G1_M(11) G1_W(2, 1) G1_M(12) G1_W(3, 0) G1_M(13) G1_W(3, 1) G1_M(14) G1_M(15)
        } else if (xn != xc) {
            G1_M(0) G1_X(0) G1_M(1) G1_W(0, 0) G1_M(2) G1_W(0, 1) G1_M(3) G1_X(1) G1_M(4) G1_X(2) G1_M(5) G1_X(3) G1_M(6) G1_W(1, 0) G1_M(7) G1_W(1, 1)
            G1_M(8) G1_W(2, 0) G1_M(9) G1_W(2, 1) G1_M(10) G1_W(3, 0) G1_M(11) G1_W(3, 1) G1_M(12) G1_M(13) G1_M(14) G1_M(15)
        } else {
            G1_M(0) G1_W(0, 0) G1_M(1) G1_W(0, 1) G1_M(2) G1_W(1, 0) G1_M(3) G1_W(1, 1) G1_M(4) G1_W(2, 0) G1_M(5) G1_W(2, 1) G1_M(6) G1_W(3, 0) G1_M(7) G1_W(3, 1)
            G1_M(8) G1_M(9) G1_M(10) G1_M(11) G1_M(12) G1_M(13) G1_M(14) G1_M(15)
        }
        G1Step<J + 1>::run(c);
    }
};
template <> struct G1Step<40> { static DI void run(G1Ctx&) {} };
template <int K> struct G1Rest { static DI void run(G1Ctx& c) { g1_side<40 - 2 * G1_TL + K / 16, K % 16>(c); G1Rest<K + 1>::run(c); } };
template <> struct G1Rest<8 * G1_SP> { static DI void run(G1Ctx&) {} };

DI void phase1(const u16* __restrict__ x, const u16* __restrict__ wA, const u16* __restrict__ wB, char* __restrict__ ws, char* lds, int bid,
               unsigned pf0, unsigned pf1) {
    const int tid = threadIdx.x, lane = tid & 63, wave = rfl(tid >> 6);
    const unsigned lds0 = (unsigned)(uintptr_t)lds;
    const int xcd = bid & 7, j = (xcd >> 1) * 30 + (bid >> 3);
    const int ct = j >> 3, mt = (xcd & 1) * 8 + (j & 7);
    const int type = rfl(ct / 5), rot = rfl(G1_ROT);
    const unsigned pcs = (unsigned)__builtin_amdgcn_s_memtime();
    if (__builtin_expect(wave >= 4, 1)) {
        const int lw = wave - 4;
        unsigned xo[4], wo[2];
#pragma unroll
        for (int i = 0; i < 4; ++i) { const int row = (lw * 4 + i) * 8 + (lane >> 3); xo[i] = (unsigned)((mt * 128 + row) * 1280 + (((lane & 7) ^ ((row >> 1) & 3) ^ ((row >> 2) & 4)) << 4)); }
#pragma unroll
        for (int i = 0; i < 2; ++i) { const int k = (lw * 2 + i) * 4 + (lane >> 4), s16 = lane & 15;
            wo[i] = (unsigned)(k * 3840 + ct * 256 + ((s16 ^ ((k & 3) << 2) ^ ((k >> 2) & 2)) << 4)); }
        auto ldx = [&](int u, unsigned st = 0u) {
            if (u < 0) { if (tid == 256) ((unsigned*)(ws + (64 << 20)))[512 + bid] = PROBE >= 200 ? st - pcs : st; return; }
            int kk = u + rot; kk -= kk >= 10 ? 10 : 0;
            const unsigned slot = lds0 + (u % G1_NX) * 16384 + lw * 4096;
#pragma unroll
            for (int i = 0; i < 4; ++i) glds16((const char*)x + kk * 128, xo[i], slot + i * 1024);
        };
        auto ldw = [&](int s) {
            int kk = (g1_t(2 * s) >> 1) + rot; kk -= kk >= 10 ? 10 : 0;
            const unsigned slot = lds0 + G1_WOFF + (s % G1_NW) * 16384 + lw * 2048;
#pragma unroll
            for (int e = 0; e < 2; ++e) {
                const char* src = (const char*)(g1_m(2 * s + e) ? wA : wB) + (kk * 64 + (g1_t(2 * s + e) & 1) * 32) * 3840;
                glds16(src, wo[0], slot + e * 8192); glds16(src, wo[1], slot + e * 8192 + 1024);
            }
        };
#pragma unroll
        for (int n = 0; n < G1P.cnt[0]; ++n) { const int i = G1P.seq[n]; if (i < 10) ldx(i); else ldw(i - 10); }
        G1Load<0>::run(ldx, ldw LST_A);
        return;
    }
    const int wm = wave & 1, wn = wave >> 1;
    const int i16 = lane & 15, g = lane >> 4, q = (lane >> 2) & 3, p = lane & 3;
    G1Ctx c;
    c.pf0 = pf0; c.pf1 = pf1;
    const int xr = (i16 >> 3) * 16 + (i16 & 7);
#pragma unroll
    for (int ks = 0; ks < 2; ++ks) { c.xa[ks] = (const LAS char*)lds + (wm * 64 + xr) * 128 + (((ks * 4 + g) ^ ((xr >> 1) & 3) ^ ((xr >> 2) & 4)) << 4); asm volatile("" : "+v"(c.xa[ks])); }
#pragma unroll
    for (int i = 0; i < 8; ++i) { c.wa[i] = (const LAS char*)lds + G1_WOFF + (i >> 2) * 65536 + (8 * g + q) * 256 + (((wn * 4 + (i & 3)) ^ (q << 1) ^ (g & 1)) << 5) + p * 8;
        asm volatile("" : "+v"(c.wa[i])); }
#pragma unroll
    for (int i = 0; i < 4; ++i) { const int cc = (ct - type * 5) * 128 + wn * 64 + i * 16 + i16, h = cc / 20; c.so[i] = h * 163840 + g * 320 + (cc - h * 20) * 16; asm volatile("" : "+v"(c.so[i])); }
    constexpr float QS = 0.22360679774997896f * LOG2E;
    int sci = type == 0 ? __builtin_bit_cast(int, QS) : 0x3F800000;
    asm volatile("" : "+s"(sci));
    c.sc = __int_as_float(sci);
    const int tb = (mt * 4 + wm * 2) * 2560;
    const int sB = tb + (type == 0 ? (int)OFF_IQ : (int)OFF_IB + (type - 1) * 1280), sA = tb + (type == 0 ? (int)OFF_IQ + 1280 : (int)OFF_IA + (type - 1) * 1280);
    c.rB = __builtin_amdgcn_make_buffer_rsrc((void*)(ws + sB), 0, (int)IMG, 0x00020000);
    const rsrc_t rA = __builtin_amdgcn_make_buffer_rsrc((void*)(ws + sA), 0, (int)IMG, 0x00020000);
    {
        const float a0 = type < 2 ? 1e-6f : 0.f;
        f32x4 a4 = {a0, a0, a0, a0};
        asm volatile("" : "+v"(a4));
#pragma unroll
        for (int i = 0; i < 16; ++i) { c.acc[i & 3][i >> 2] = f32x4{0.f, 0.f, 0.f, 0.f}; c.acc[i & 3][4 + (i >> 2)] = a4; }
    }
    __builtin_amdgcn_s_setprio(2);
    asm volatile("s_barrier" ::: "memory");
#pragma unroll
    for (int i = 0; i < 4; ++i) {
        c.x[0][i] = *(const LAS bf16x8*)(c.xa[0] + (i >> 1) * 4096 + (i & 1) * 1024);
        if (i == 0) { c.w[0][0][0] = trd(c.wa[0]); c.w[0][0][1] = trd(c.wa[0] + 1024); }
    }
#pragma unroll
    for (int i = 1; i < 4; ++i) { c.w[0][i][0] = trd(c.wa[i]); c.w[0][i][1] = trd(c.wa[i] + 1024); }
    SB0();
    G1Step<0>::run(c);
    PSTAMP(0, 2);
    G1Rest<(32 * G1_TL < 8 * G1_SP ? 32 * G1_TL : 8 * G1_SP)>::run(c);
    if (type < 2) {
#pragma unroll
        for (int i = 0; i < 4; ++i)
#pragma unroll
            for (int a = 0; a < 2; ++a) {
                u32x4 o;
#pragma unroll
                for (int e = 0; e < 4; ++e) {
                    const f32x2 t = {c.acc[2 * a + (e >> 1)][4 + i][(e & 1) * 2], c.acc[2 * a + (e >> 1)][4 + i][(e & 1) * 2 + 1]};
                    const f32x2 m = __builtin_elementwise_fma(t, f32x2{LOG2E, LOG2E}, f32x2{-1e-6f * LOG2E, -1e-6f * LOG2E});
                    const f32x2 ex = {__builtin_amdgcn_fmed3f(__builtin_amdgcn_exp2f(m[0]), 0.f, 1.f), __builtin_amdgcn_fmed3f(__builtin_amdgcn_exp2f(m[1]), 0.f, 1.f)};
                    const f32x2 r = f32x2{pmax(t[0], 1e-6f), pmax(t[1], 1e-6f)} + ex;
                    o[e] = pk2(r[0], r[1]);
                }
                __builtin_amdgcn_raw_buffer_store_b128(o, rA, c.so[i] + a * 2560, 0, SC1);
            }
    } else {
#pragma unroll
        for (int i = 0; i < 4; ++i)
#pragma unroll
            for (int a = 0; a < 2; ++a) {
                const f32x4 u = c.acc[2 * a][4 + i], v = c.acc[2 * a + 1][4 + i];
                __builtin_amdgcn_raw_buffer_store_b128(u32x4{pk2(u[0], u[1]), pk2(u[2], u[3]), pk2(v[0], v[1]), pk2(v[2], v[3])}, rA, c.so[i] + a * 2560, 0, SC1);
            }
    }
}

__global__ __launch_bounds__(512, 2) void k1_qkv(const u16* __restrict__ x, const u16* __restrict__ wA, const u16* __restrict__ wB,
                                                 const u16* __restrict__ woA, const u16* __restrict__ woB, const u16* __restrict__ gate_w,
                                                 char* __restrict__ ws) {
    extern __shared__ __attribute__((aligned(16))) char lds[];
    int bid = blockIdx.x;
    asm volatile("" : "+s"(bid));
    const unsigned pt0 = (unsigned)__builtin_amdgcn_s_memrealtime();
    unsigned pf0, pf1 = 0u;
    {
        unsigned long long pc;
        asm volatile("s_getpc_b64 %1\n.Lk1_pc:\n\tv_lshl_add_u32 %0, %2, 7, %3\n\tv_min_u32 %0, .Lk2_last-.Lk1_pc, %0\n\t"
                     "v_cmp_gt_u32 vcc, %4, %2\n\ts_mov_b64 exec, vcc\n\tglobal_load_dword %0, %0, %1\n\ts_mov_b64 exec, -1"
                     : "=&v"(pf0), "=&s"(pc) : "v"(threadIdx.x), "s"((bid >> 3) * (CPF * 128) + 64), "n"(CPF) : "memory", "vcc");
    }
    if (__builtin_expect(bid < 240, 1)) phase1(x, wA, wB, ws, lds, bid, pf0, pf1);
    else { p1_aux(x, woA, woB, gate_w, ws, lds, bid - 240); asm volatile("" :: "v"(pf0), "v"(pf1)); }
    if (threadIdx.x == 0) { asm volatile("s_waitcnt vmcnt(0)"); unsigned* pp = (unsigned*)(ws + (64 << 20)) + bid * 2; pp[0] = pt0; pp[1] = (unsigned)__builtin_amdgcn_s_memrealtime(); }
}

#define STR(x) #x
#define XS(x) STR(x)
#define A_E(i) "v_exp_f32 v" XS(i) ", v" XS(i) "\n"
#define A_C(d, a, b) "v_cvt_pk_bf16_f32 v" XS(d) ", v" XS(a) ", v" XS(b) "\n"
#define A_G(a, b, c, d, p0, p1) A_E(a) A_E(b) A_E(c) A_E(d) A_C(p0, a, b) A_C(p1, c, d)
#define A_M(d, a, b, c) "v_mfma_f32_32x32x16_bf16 " d ", " a ", " b ", " c "\n"
#define A_STEP(C0,C1,C2,C3,C4,C5,C6,C7,C8,C9,C10,C11,C12,C13,C14,C15, SN, KM, KT, V1, V0, NKMa, NKMb, NKT, NV1, NV0, OK0, OK1, OV1, OV0, PRIO) \
    "s_waitcnt lgkmcnt(0)\n" \
    A_M(SN, KM, "v[120:123]", "0") \
    "ds_read_b64_tr_b16 " NKMa ", v128 offset:" XS(OK0) "\n" \
    "ds_read_b64_tr_b16 " NKMb ", v128 offset:" XS(OK1) "\n" \
    "ds_read_b64_tr_b16 " NKT ", v129 offset:" XS(OK0) "\n" \
    A_G(C0, C1, C2, C3, 112, 113) \
    A_M("v[64:79]", V1, "v[116:119]", "v[64:79]") \
    "s_mov_b64 exec, s[72:73]\n" \
    "ds_read_b128 " NV1 ", v130 offset:" XS(OV1) "\n" \
    "ds_read_b128 " NV0 ", v130 offset:" XS(OV0) "\n" \
    "s_mov_b64 exec, -1\n" \
    A_G(C4, C5, C6, C7, 114, 115) \
    A_M(SN, KT, "v[124:127]", SN) \
    A_G(C8, C9, C10, C11, 116, 117) \
    A_M("v[64:79]", V0, "v[112:115]", "v[64:79]") \
    "s_setprio " XS(PRIO) "\n" \
    A_G(C12, C13, C14, C15, 118, 119)
#define A_EVEN(OK0, OK1, OV1, OV0, PRIO) A_STEP(32,33,34,35,36,37,38,39,40,41,42,43,44,45,46,47, "v[48:63]", "v[80:83]", "v[84:87]", "v[88:91]", "v[92:95]", \
    "v[96:97]", "v[98:99]", "v[100:101]", "v[104:107]", "v[108:111]", OK0, OK1, OV1, OV0, PRIO)
#define A_ODD(OK0, OK1, OV1, OV0, PRIO) A_STEP(48,49,50,51,52,53,54,55,56,57,58,59,60,61,62,63, "v[32:47]", "v[96:99]", "v[100:103]", "v[104:107]", "v[108:111]", \
    "v[80:81]", "v[82:83]", "v[84:85]", "v[88:91]", "v[92:95]", OK0, OK1, OV1, OV0, PRIO)

#define A_LD(g, off) "global_load_lds_dwordx4 v131, " g " offset:" XS(off) "\n"
#define A_LDA(off) "s_cbranch_scc1 .Lattn_s" XS(off) "\n" A_LD("s[74:75]", off) ".Lattn_s" XS(off) ":\n"
#define A_BODY \
        "s_mov_b32 m0, s78\n s_cmp_eq_u32 s80, 2\n s_nop 3\n" \
        A_EVEN(5120, 5184, 640, 2560, 2)     A_ODD(7680, 7744, 3200, 5120, 1) \
        A_EVEN(10240, 10304, 5760, 7680, 0)  A_ODD(12800, 12864, 8320, 10240, 3) \
        "s_waitcnt vmcnt(0) lgkmcnt(0)\n s_mov_b64 exec, 1\n ds_add_u32 v127, v86\n s_mov_b64 exec, -1\n" \
        A_EVEN(15360, 15424, 10880, 12800, 2) A_LDA(0) A_ODD(17920, 17984, 13440, 15360, 1) A_LDA(1024) \
        A_EVEN(20480, 20544, 16000, 17920, 0) A_LDA(2048) \
        "s_cmp_eq_u32 s80, 1\n s_cbranch_scc0 .Lattn_skip\n s_mov_b32 m0, s79\n s_nop 0\n" \
        A_LD("s[76:77]", 0) A_LD("s[76:77]", 1024) A_LD("s[76:77]", 2048) ".Lattn_skip:\n" \
        "ds_read_b32 v132, v127\n" \
        A_ODD(23040, 23104, 18560, 20480, 3) \
        "v_add_u32 v128, 0x5000, v128\n v_add_u32 v129, 0x5000, v129\n v_add_u32 v130, 0x5000, v130\n" \
        "s_waitcnt lgkmcnt(0)\n s_nop 0\n"
struct AttnRegs { f32x16 S, O; u32x4 KM, P1, QM, KT0, KT1, QT, AD; u32x8 V0, V1; };
#define A_OPERANDS \
        : "+{v[32:47]}"(a.S), "+{v[64:79]}"(a.O), "+{v[80:83]}"(a.KM), "+{v[84:87]}"(a.KT0), "+{v[88:95]}"(a.V0), "+{v[100:103]}"(a.KT1), \
          "+{v[104:111]}"(a.V1), "+{v[116:119]}"(a.P1), "+{v[128:131]}"(a.AD), "={v132}"(cnt) \
        : "{v[120:123]}"(a.QM), "{v[124:127]}"(a.QT), \
          "{s[72:73]}"(vmask), "{s[74:75]}"(ga), "{s[76:77]}"(gb), "{s78}"(ma), "{s79}"(mb), "{s80}"(enb) \
        : "memory", "scc", "s82", "s83", "v48", "v49", "v50", "v51", "v52", "v53", "v54", "v55", "v56", "v57", "v58", "v59", "v60", "v61", "v62", "v63", \
          "v96", "v97", "v98", "v99", "v112", "v113", "v114", "v115"
template <bool FIRST>
DI unsigned attn_chunk(AttnRegs& a, unsigned long long vmask, unsigned ma, unsigned long long ga, unsigned mb, unsigned long long gb, unsigned enb) {
    unsigned cnt;
    if (FIRST) asm volatile("s_call_b64 s[82:83], .Lattn_body\n s_branch .Lattn_done\n .p2align 6\n .Lattn_body:\n" A_BODY "s_setpc_b64 s[82:83]\n .Lattn_done:\n" A_OPERANDS);
    else       asm volatile("s_call_b64 s[82:83], .Lattn_body\n" A_OPERANDS);
    return cnt;
}
DI void wait_cnt(char* lds, int off, unsigned cnt, int n) {
    if (__builtin_expect((unsigned)rfl((int)cnt) < (unsigned)(n * 0x3F80), 0)) {
#pragma clang loop unroll(disable)
        do { __builtin_amdgcn_s_sleep(1); cnt = *(volatile LAS unsigned*)(lds + off); } while ((unsigned)rfl((int)cnt) < (unsigned)(n * 0x3F80));
    }
    asm volatile("" ::: "memory");
}

DI void wait_cnt(char* lds, int off, int n) { wait_cnt(lds, off, *(volatile LAS unsigned*)(lds + off), n); }
template <int OFF> DI void add_cnt(unsigned lds0) {
    asm volatile("s_waitcnt lgkmcnt(0)\n\ts_mov_b64 exec, 1\n\tds_add_u32 %0, %1 offset:%2\n\ts_mov_b64 exec, -1" :: "v"(lds0), "v"(0x3F80u), "n"(OFF) : "memory"); }

DI float xhalf_max(float m) { auto rr = __builtin_amdgcn_permlane32_swap(__float_as_uint(m), __float_as_uint(m), false, false);
    return fmaxf(__uint_as_float(rr[0]), __uint_as_float(rr[1])); }
DI float hi_half(float v) {
    auto rr = __builtin_amdgcn_permlane32_swap(__float_as_uint(v), __float_as_uint(v), false, false);
    return __uint_as_float(rr[1]); }
DI float sum16(float v) { auto rr = __builtin_amdgcn_permlane16_swap(__float_as_uint(v), __float_as_uint(v), false, false);
    return __uint_as_float(rr[0]) + __uint_as_float(rr[1]); }
DI float sum32(float v) { auto rr = __builtin_amdgcn_permlane32_swap(__float_as_uint(v), __float_as_uint(v), false, false);
    return __uint_as_float(rr[0]) + __uint_as_float(rr[1]); }
DI f32x4 sum_rows(f32x4 a, f32x4 b) {
    f32x4 s = a + b;
#pragma unroll
    for (int i = 0; i < 4; ++i) s[i] = sum32(sum16(s[i]));
    return s;
}
DI unsigned long long uni64(const void* p) { const unsigned long long a = (unsigned long long)(uintptr_t)p;
    return (unsigned long long)(unsigned)rfl((int)(unsigned)a) | ((unsigned long long)(unsigned)rfl((int)(unsigned)(a >> 32)) << 32); }
DI unsigned full(unsigned v) { return v < EMPTY ? v : EMPTY - 1u; }
DI unsigned umax4(unsigned m, u32x4 v) { return max(max(max(m, v[0]), max(v[1], v[2])), v[3]); }
DI void store_o(rsrc_t rs, const f32x16& o, float f, int rt, int br, int h, int lane) {
    const int rb = (int)OFF_O + (rt * 80 + ((br ? 0 : 4) + (h >> 3)) * 10) * 1024;
    __builtin_amdgcn_raw_buffer_store_b128(u32x4{full(pk2(o[0] * f, o[1] * f)), full(pk2(o[2] * f, o[3] * f)), full(pk2(o[4] * f, o[5] * f)), full(pk2(o[6] * f, o[7] * f))},
                                           rs, lane * 16, rb + (h & 7) * 1024, SC1);
    if (lane < 32) __builtin_amdgcn_raw_buffer_store_b64(u32x2{full(pk2(o[8] * f, o[9] * f)), full(pk2(o[10] * f, o[11] * f))},
                                           rs, lane * 16, rb + (8 + ((h >> 2) & 1)) * 1024 + ((h >> 1) & 1) * 512 + (h & 1) * 8, SC1);
}
DI void empty_o(rsrc_t rs, int rt, int h, int lane) {
    const int rb = (int)OFF_O + (rt * 80 + (h >> 3) * 10) * 1024;
    u32x4 e = {EMPTY, EMPTY, EMPTY, EMPTY};
    __builtin_amdgcn_raw_buffer_store_b128(e, rs, lane * 16, rb + (h & 7) * 1024, SC1);
    if (lane < 32) __builtin_amdgcn_raw_buffer_store_b64(u32x2{e[0], e[1]}, rs, lane * 16, rb + (8 + ((h >> 2) & 1)) * 1024 + ((h >> 1) & 1) * 512 + (h & 1) * 8, SC1);
    asm volatile("" :: "v"(e));
}
DI void set_flag(rsrc_t rs, int off) { __builtin_amdgcn_raw_buffer_store_b32(1u, rs, 0, off, SC1); }
DI unsigned get_flag(rsrc_t rs, int voff, int off) { return __builtin_amdgcn_raw_buffer_load_b32(rs, voff, off, SC1); }
DI unsigned a_ld1(unsigned long long sb, unsigned voff) { unsigned v; asm volatile("global_load_dword %0, %1, %2 sc1" : "=v"(v) : "v"(voff), "s"(sb) : "memory"); return v; }
DI u32x4 a_ld4s(unsigned long long sb, unsigned voff) { u32x4 v; asm volatile("global_load_dwordx4 %0, %1, %2 sc1" : "=v"(v) : "v"(voff), "s"(sb) : "memory"); return v; }
template <int OFF> DI u32x4 a_ld4so(unsigned long long sb, unsigned voff) { u32x4 v;
    asm volatile("global_load_dwordx4 %0, %1, %2 offset:%3 sc1" : "=v"(v) : "v"(voff), "s"(sb), "n"(OFF) : "memory"); return v; }
template <int OFF> DI u32x4 a_ld4(unsigned long long sb, unsigned voff) { u32x4 v;
    asm volatile("global_load_dwordx4 %0, %1, %2 offset:%3" : "=v"(v) : "v"(voff), "s"(sb), "n"(OFF) : "memory"); return v; }
template <class T> DI void landed(T& v) { asm volatile("" : "+v"(v)); }
DI void wait_flag(rsrc_t rs, int voff, int off, unsigned v) {
    if (__builtin_expect(!__all(v == 1u), 0)) {
#pragma clang loop unroll(disable)
        for (int spin = 0; spin < (1 << 22); ++spin) { if (__all(get_flag(rs, voff, off) == 1u)) break; __builtin_amdgcn_s_sleep(1); }
    }
    __builtin_amdgcn_fence(__ATOMIC_ACQUIRE, "wavefront");
    asm volatile("" ::: "memory");
}

#define K2_BAR
#define FILL_C 2
constexpr int L_SP = 136192, L_K0 = 152320, L_SQ = 153600, L_QA = 1280, L_KVF = 11520, L_WO = 13312;

__global__ __launch_bounds__(512, 2) void k2_attn(char* ws_arg, u16* __restrict__ out) {
    extern __shared__ __attribute__((aligned(16))) char lds[];
    char* ws;
    asm volatile("s_load_dwordx2 %0, %1, 0x0" : "={s[20:21]}"(ws) : "s"(__builtin_amdgcn_kernarg_segment_ptr()));
    int bid = blockIdx.x;
    asm volatile("" : "+s"(bid));
    const unsigned pt0 = (unsigned)__builtin_amdgcn_s_memrealtime(), pc0 = (unsigned)__builtin_amdgcn_s_memtime();
    unsigned pt1 = 0, pt2 = 0;
    const int tid = threadIdx.x, lane = tid & 63, wave = rfl(tid >> 6);
    const int h = (bid & 7) * 4 + ((bid >> 3) & 3), qb = bid >> 5;
    const int oq = bid & 7, rb = oq * 4 + ((bid >> 3) & 3), cb = bid >> 5;
    const int r = lane & 31, hh = lane >> 5;
    const unsigned lds0 = (unsigned)(uintptr_t)lds;
    const int wr = (wave & 3) * 2 + (wave >> 2), rt = qb * 8 + wr;
    unsigned long long vmask = 0x000FFFFF000FFFFFull;
    const u32x4 ONES = {0x3F803F80u, 0x3F803F80u, 0x3F803F80u, 0x3F803F80u};
    const f32x16 Z16 = {};
    const int g = lane >> 4, q = (lane >> 2) & 3, p = lane & 3;
    const int tro = (g & 1) * 640 + (p & 1) * 320 + q * 16 + (p >> 1) * 8;
    const LAS char* L = (const LAS char*)lds;
    const int ent = tid < 126 ? tid : 125;
    AttnRegs ar;
    u32x2 c0, c1, c2;
    unsigned aq, ak, aqt, akt, asp, vi[6];
    u32x4 a0 = {0u, 0u, 0u, 0u}, a1 = a0, b0 = ONES, b1 = ONES; float gate;
    {
        if (tid < 8) *(LAS unsigned*)(lds + tid * 4) = 0u;
        const unsigned vc = r == 20 ? 0x3F803F80u : 0u;
        ar.O = Z16; ar.P1 = u32x4{0u, 0u, 0u, 0u}; c0 = u32x2{0x3F80u, 0u}; c1 = c0; c2 = u32x2{0u, 0u};
        ar.V0 = u32x8{vc, vc, vc, vc, vc, vc, vc, vc}; ar.V1 = ar.V0;
        ar.AD = u32x4{lds0 + tro + hh * 128, lds0 + tro + 256, lds0 + 1280 + hh * 320 + r * 16, (unsigned)lane * 16};
        aq = ar.AD[0] + L_SQ + wave * 1280; ak = ar.AD[0] + L_K0; asp = lds0 + L_SP + ((wave * 2 + hh) * 21 + r) * 48;
        aqt = hh ? lds0 + 16 : ar.AD[1] + L_SQ + wave * 1280; akt = ar.AD[1] + L_K0;
        asm volatile("" : "+{v[64:79]}"(ar.O), "+{v[86:87]}"(c0), "+{v[88:95]}"(ar.V0), "+{v[102:103]}"(c1), "+{v[104:111]}"(ar.V1),
                          "+{v[116:119]}"(ar.P1), "+{v[128:131]}"(ar.AD), "+{v[126:127]}"(c2),
                          "+v"(aq), "+v"(ak), "+v"(aqt), "+v"(akt), "+v"(asp), "+{s[72:73]}"(vmask));
        const unsigned hb = h * 163840 + lane * 16;
        unsigned vq = (unsigned)OFF_IQ + hb + rt * 2560, vk = (unsigned)OFF_IB + hb + (wave & 1) * 256;
        unsigned va = (unsigned)OFF_IA + h * 163840 + rt * 2560 + hh * 320 + r * 16, vg = (unsigned)OFF_G + rt * 128 + r * 4;
        unsigned mq = lds0 + L_SQ + wave * 1280, mk = lds0 + L_K0 + (wave & 1) * 256, mi = lds0 + (1 + wave) * 1024;
#pragma unroll
        for (int i = 0; i < 6; ++i) vi[i] = (unsigned)OFF_IB + hb + (1 + i * 8 + wave) * 1024;
        asm volatile("" : "+v"(vq), "+v"(vk), "+v"(va), "+v"(vg), "+v"(vi[0]), "+v"(vi[1]), "+v"(vi[2]), "+v"(vi[3]), "+v"(vi[4]), "+v"(vi[5]),
                          "+v"(a0), "+v"(a1), "+v"(b0), "+v"(b1), "+s"(mq), "+s"(mk), "+s"(mi));
        asm volatile("s_mov_b32 m0, %13\n\ts_waitcnt lgkmcnt(0)\n\tglobal_load_lds_dwordx4 %6, %5\n\tglobal_load_lds_dwordx4 %6, %5 offset:256\n\t"
                     "s_mov_b32 m0, %14\n\ts_nop 0\n\tglobal_load_lds_dwordx4 %7, %5\n\t"
                     "s_mov_b32 m0, %15\n\ts_nop 0\n\tglobal_load_lds_dwordx4 %10, %5\n\t"
                     "s_add_u32 m0, m0, 0x2000\n\ts_nop 0\n\tglobal_load_lds_dwordx4 %11, %5\n\t"
                     "s_add_u32 m0, m0, 0x2000\n\ts_nop 0\n\tglobal_load_lds_dwordx4 %12, %5\n\t"
                     "global_load_dword %4, %9, %5\n\ts_mov_b64 exec, %16\n\t"
                     "global_load_dwordx4 %0, %8, %5\n\tglobal_load_dwordx4 %1, %8, %5 offset:640\n\tglobal_load_dwordx4 %2, %8, %5 offset:1280\n\t"
                     "global_load_dwordx4 %3, %8, %5 offset:1920\n\ts_mov_b64 exec, -1"
                     : "+v"(a0), "+v"(a1), "+v"(b0), "+v"(b1), "=&v"(gate), "+{s[20:21]}"(ws)
                     : "v"(vq), "v"(vk), "v"(va), "v"(vg), "v"(vi[0]), "v"(vi[1]), "v"(vi[2]), "s"(mq), "s"(mk), "s"(mi), "s"(vmask) : "memory", "scc");
    }
    const rsrc_t rs = __builtin_amdgcn_make_buffer_rsrc((void*)ws, 0, WS_USED, 0x00020000);
    const char* kvb = ws + OFF_IB + (size_t)h * 163840;
    const char* qim = ws + OFF_IQ + (size_t)h * 163840 + qb * 20480;
    const int j = wave & 3, isE = (wave >> 2) ^ 1;
    const char* wop = ws + OFF_WOT + (size_t)((cb * 4 + j) * 50) * 1024;
    const int flA = (int)OFF_FL + (oq * 4 + j) * 128, flv = (lane & 7) * 16;
    const int ov = ((g >> 1) * 1024 + (g & 1) * 512 + (lane & 15) * 16), os = (int)OFF_O + (rb * 160 + j * 10) * 1024;
    unsigned fl = 0u;
    u32x4 kp[8], wa[20], aa[2][10];
    const unsigned long long wsu = uni64(ws);
    unsigned long long kb = uni64(ws + OFF_KVP + 4096 + h * 16384), kb2 = uni64(ws + OFF_KVP + 12288 + h * 16384);
    unsigned long long ob[2][2] = {{uni64(ws + os + 4096), uni64(ws + os + 8192)}, {uni64(ws + os + 81920 + 4096), uni64(ws + os + 81920 + 8192)}};
#define LD_H(A, vo, hf, en, C) asm volatile("s_cmp_lg_u32 %13, 0\n\ts_cselect_b64 exec, -1, 0\n\t" \
        "global_load_dwordx4 %0, %10, %11 offset:-4096 sc1\n\tglobal_load_dwordx4 %1, %10, %11 offset:-3840 sc1\n\t" \
        "global_load_dwordx4 %2, %10, %11 offset:-2048 sc1\n\tglobal_load_dwordx4 %3, %10, %11 offset:-1792 sc1\n\t" \
        "global_load_dwordx4 %4, %10, %11 sc1\n\tglobal_load_dwordx4 %5, %10, %11 offset:256 sc1\n\t" \
        "global_load_dwordx4 %6, %10, %11 offset:2048 sc1\n\tglobal_load_dwordx4 %7, %10, %11 offset:2304 sc1\n\t" \
        "global_load_dwordx4 %8, %10, %12 sc1\n\tglobal_load_dwordx4 %9, %10, %12 offset:256 sc1\n\ts_mov_b64 exec, -1" \
        : C(A[0]), C(A[1]), C(A[2]), C(A[3]), C(A[4]), C(A[5]), C(A[6]), C(A[7]), C(A[8]), C(A[9]) \
        : "v"(vo), "s"(ob[hf][0]), "s"(ob[hf][1]), "s"(en) : "memory", "scc");
#define LD_W4(A, f) asm volatile("s_cmp_lg_u32 %6, 0\n\ts_cselect_b64 exec, -1, 0\n\t" \
        "global_load_dwordx4 %0, %4, %5\n\tglobal_load_dwordx4 %1, %4, %5 offset:1024\n\t" \
        "global_load_dwordx4 %2, %4, %5 offset:2048\n\tglobal_load_dwordx4 %3, %4, %5 offset:3072\n\ts_mov_b64 exec, -1" \
        : "=&v"((A)[0]), "=&v"((A)[1]), "=&v"((A)[2]), "=&v"((A)[3]) : "v"(lane * 16), "s"(uni64(wop + (f) * 1024)), "s"(isE) : "memory", "scc");
#define LD_KP(TAIL) asm volatile("s_cmp_lt_u32 %11, 2\n\ts_cselect_b64 exec, -1, 0\n\t" \
        "global_load_dwordx4 %0, %8, %9 offset:-4096 sc1\n\tglobal_load_dwordx4 %1, %8, %9 offset:-2048 sc1\n\t" \
        "global_load_dwordx4 %2, %8, %9 sc1\n\tglobal_load_dwordx4 %3, %8, %9 offset:2048 sc1\n\t" \
        "global_load_dwordx4 %4, %8, %10 offset:-4096 sc1\n\tglobal_load_dwordx4 %5, %8, %10 offset:-2048 sc1\n\t" \
        "global_load_dwordx4 %6, %8, %10 sc1\n\tglobal_load_dwordx4 %7, %8, %10 offset:2048 sc1\n\t" \
        "s_mov_b64 exec, -1" TAIL \
        : "=&v"(kp[0]), "=&v"(kp[1]), "=&v"(kp[2]), "=&v"(kp[3]), "=&v"(kp[4]), "=&v"(kp[5]), "=&v"(kp[6]), "=&v"(kp[7]) \
        : "v"(ent * 16), "s"(kb), "s"(kb2), "s"(wave) : "memory", "scc")
    asm volatile("" : "+s"(kb), "+s"(kb2), "+s"(ob[0][0]), "+s"(ob[0][1]), "+s"(ob[1][0]), "+s"(ob[1][1]));
    unsigned ma0 = rfl(lds0 + 50176 + wave * 3072), mb0 = rfl(ma0 + 24576); unsigned long long ga0 = uni64(kvb + 50176 + wave * 3072), gb0 = ga0 + 24576;
    asm volatile("" : "+{s[74:75]}"(ga0), "+{s[76:77]}"(gb0), "+{s78}"(ma0), "+{s79}"(mb0));
    asm volatile("s_mov_b32 m0, %4\n\ts_nop 0\n\tglobal_load_lds_dwordx4 %0, %3\n\t"
                 "s_add_u32 m0, m0, 0x2000\n\ts_nop 0\n\tglobal_load_lds_dwordx4 %1, %3\n\t"
                 "s_add_u32 m0, m0, 0x2000\n\ts_nop 0\n\tglobal_load_lds_dwordx4 %2, %3"
                 :: "v"(vi[3]), "v"(vi[4]), "v"(vi[5]), "s"(ws), "s"(lds0 + (25 + wave) * 1024) : "memory", "scc");
    wait_vm_bar<K2_W0>();
    pt1 = (unsigned)__builtin_amdgcn_s_memrealtime();
    {
        f32x16 kvacc;
        u32x2 t0, t1, t2;
        asm volatile("ds_read_b64_tr_b16 v[96:97], %15\n\tds_read_b64_tr_b16 v[98:99], %15 offset:64\n\t"
                     "ds_read_b64_tr_b16 v[120:121], %14\n\tds_read_b64_tr_b16 v[122:123], %14 offset:64\n\t"
                     "ds_read_b64_tr_b16 v[124:125], %16\n\tds_read_b64_tr_b16 v[100:101], %17\n\t"
                     "ds_read_b64_tr_b16 v[84:85], v129 offset:2560\n\ts_mov_b64 exec, %19\n\tds_read_b128 v[92:95], v130\n\ts_mov_b64 exec, -1\n\t"
                     "ds_read_b64_tr_b16 v[80:81], v128 offset:2560\n\tds_read_b64_tr_b16 v[82:83], v128 offset:2624\n\ts_waitcnt lgkmcnt(6)\n\t"
                     "v_mfma_f32_32x32x16_bf16 v[32:47], v[96:99], v[120:123], 0\n\ts_waitcnt lgkmcnt(4)\n\t"
                     "v_mfma_f32_32x32x16_bf16 v[48:63], v[100:103], v[124:127], v[32:47]\n\t"
                     "s_waitcnt vmcnt(3)\n\t" K2_BAR
                     "v_mfma_f32_32x32x16_bf16 %8, %9, %11, 0\n\tv_mfma_f32_32x32x16_bf16 %8, %10, %12, %8\n\ts_nop 7\n\ts_nop 0\n\t"
                     "v_max3_f32 v96, v48, v49, v50\n\tv_max3_f32 v97, v51, v52, v53\n\tv_max3_f32 v96, v96, v54, v55\n\tv_max3_f32 v97, v97, v56, v57\n\t"
                     "v_max3_f32 v96, v96, v58, v59\n\tv_max3_f32 v97, v97, v60, v61\n\tv_max3_f32 v96, v96, v62, v63\n\tv_max_f32 v96, v96, v97\n\t"
                     "v_mov_b32 v97, v96\n\ts_nop 1\n\tv_permlane32_swap_b32 v96, v97\n\tv_max_f32 v96, v96, v97\n\t"
                     "s_mov_b32 exec_hi, 0\n\tv_cvt_pk_bf16_f32 v126, -v96, 0\n\ts_mov_b32 exec_hi, -1\n\ts_nop 0\n\t"
                     "v_mfma_f32_32x32x16_bf16 v[32:47], v[100:103], v[124:127], v[32:47]"
                     : "=&{v[32:47]}"(ar.S), "=&{v[120:123]}"(ar.QM), "=&{v[124:125]}"(t2), "+{v[126:127]}"(c2), "=&{v[100:101]}"(t1), "=&{v[84:85]}"(t0),
                       "+{v[88:95]}"(ar.V0), "=&{v[80:83]}"(ar.KM), "=&v"(kvacc), "+v"(a0), "+v"(a1), "+v"(b0), "+v"(b1), "+v"(gate)
                     : "v"(aq), "v"(ak), "v"(aqt), "v"(akt), "{v[128:131]}"(ar.AD), "s"(vmask), "{v[102:103]}"(c1)
                     : "memory", "v48", "v49", "v50", "v51", "v52", "v53", "v54", "v55", "v56", "v57", "v58", "v59", "v60", "v61", "v62", "v63",
                       "v96", "v97", "v98", "v99");
        ar.KT0 = u32x4{t0[0], t0[1], c0[0], c0[1]}; ar.KT1 = u32x4{t1[0], t1[1], c1[0], c1[1]}; ar.QT = u32x4{t2[0], t2[1], c2[0], c2[1]};
        if (r <= 20) {
            LAS f32x4* dst = (LAS f32x4*)(uintptr_t)asp;
            dst[0] = f32x4{kvacc[0], kvacc[1], kvacc[2], kvacc[3]};
            dst[1] = f32x4{kvacc[4], kvacc[5], kvacc[6], kvacc[7]};
            dst[2] = f32x4{kvacc[8], kvacc[9], kvacc[10], kvacc[11]};
        }
    }
#pragma unroll
    for (int c = 0; c < 8; ++c) {
        unsigned ma, mb; unsigned long long ga, gb;
        if (c < 3) {
            const int lim = (c == 1 ? L_SP : 163840) - 3072;
            const int oa = (c == 2 ? L_SP : 50176 + c * 49152) + wave * 3072, ob = oa + 24576 < lim ? oa + 24576 : lim;
            ma = lds0 + oa; mb = lds0 + ob; ga = uni64(kvb + oa); gb = uni64(kvb + ob);
        } else {
            ma = lds0 + (c == 7 ? 1024 + j * 3072 : L_WO + ((c - 3) * 8 + wr) * 3072); mb = ma;
            ga = uni64(wop + (c == 7 ? 44 : 20 + (wave >> 2) * 12 + 3 * (c - 3)) * 1024); gb = ga;
        }
        CSTAMP(2 * c);
        unsigned cnt;
        if (c == 0) cnt = attn_chunk<true>(ar, vmask, ma0, ga0, mb0, gb0, 1u);
        else cnt = attn_chunk<false>(ar, vmask, rfl(ma), ga, rfl(mb), gb, c < 3 ? 1u : c < 7 ? 0u : (unsigned)(2 - 2 * isE));
        CSTAMP(2 * c + 1);
        if (c < 7) wait_cnt(lds, 0, cnt, 8 * (c + 1));
        if (c == FILL_C && c != 1) empty_o(rs, rt, h, lane);
        if (c == 0) {
            if (wave < 2) {
                f32x4 s = *(const LAS f32x4*)(L + L_SP + ent * 16);
#pragma unroll
                for (int w = 1; w < 8; ++w) s += *(const LAS f32x4*)(L + L_SP + w * 2016 + ent * 16);
                const u32x4 v = __builtin_bit_cast(u32x4, s);
                if (tid < 126) __builtin_amdgcn_raw_buffer_store_b128(u32x4{full(v[0]), full(v[1]), full(v[2]), full(v[3])}, rs, tid * 16, (int)OFF_KVP + (h * 8 + qb) * 2048, SC1);
            }
            glds16(qim + wr * 2560 + 1280, lane * 16, lds0 + L_QA + wave * 1280);
            glds16(qim + wr * 2560 + 1280 + 256, lane * 16, lds0 + L_QA + wave * 1280 + 256);
        } else if (c == 1) {
            LD_KP("");
            if (FILL_C == 1) empty_o(rs, rt, h, lane);
        } else if (c == 2) {
#pragma unroll
            for (int j = 0; j < 8; ++j) landed(kp[j]);
            if (wave < 2) {
#pragma clang loop unroll(disable)
                for (int spin = 0; spin < (1 << 20); ++spin) {
                    unsigned m = 0u;
#pragma unroll
                    for (int j = 0; j < 8; ++j) m = umax4(m, kp[j]);
                    if (__builtin_expect(__all(m != EMPTY), 1)) break;
                    LD_KP("\n\ts_waitcnt vmcnt(0)");
                }
                f32x4 s = __builtin_bit_cast(f32x4, kp[0]);
#pragma unroll
                for (int j = 1; j < 8; ++j) s += __builtin_bit_cast(f32x4, kp[j]);
                const int e3 = ent / 3;
                *(LAS u32x2*)(lds + L_KVF + e3 * 32 + (ent - e3 * 3) * 8) = u32x2{pk2(s[0], s[1]), pk2(s[2], s[3])};
            }
        } else if (c == 3) {
            const int rr = r <= 20 ? r : 20;
            const u32x4 ka0 = *(const LAS u32x4*)(L + L_KVF + (hh * 21 + rr) * 32);
            const u32x2 ka1 = *(const LAS u32x2*)(L + L_KVF + (hh * 21 + rr) * 32 + 16);
            const LAS char* qa = L + L_QA + wave * 1280 + tro;
            const u32x2 pq0 = trd(qa + hh * 64), pq1 = trd(qa + 128 + hh * 64);
            u32x2 pq2 = trd(qa + 256);
            if (hh) pq2 = u32x2{0u, 0u};
            f32x16 oa = MFMA32(B8(ka0), B8(((u32x4){pq0[0], pq0[1], pq1[0], pq1[1]})), Z16);
            oa = MFMA32(B8(((u32x4){ka1[0], ka1[1], 0u, 0u})), B8(((u32x4){pq2[0], pq2[1], 0u, 0u})), oa);
            const float den = hi_half(oa[8]) + 1e-6f;
            store_o(rs, oa, gate * __builtin_amdgcn_rcpf(den), rt, 0, h, lane);
            LD_W4(wa, 0) LD_W4(wa + 4, 4)
        } else if (c == 4) {
            if (tid == 0) set_flag(rs, (int)OFF_FL + (qb * 4 + (h >> 3)) * 128 + (h & 7) * 16);
            LD_W4(wa + 8, 8) LD_W4(wa + 12, 12)
        } else if (c == 5) {
            fl = a_ld1(wsu, flA + flv);
            LD_W4(wa + 16, 16)
        } else if (c == 6) {
            landed(fl);
            if (wave < 4) wait_flag(rs, flv, flA, fl);
            LD_H(aa[0], ov + 40960, 0, isE, "=&v")
        }
    }
#pragma unroll
    for (int i = 0; i < 10; ++i) { landed(wa[i]); landed(wa[10 + i]); landed(aa[0][i]); }
    u32x4 wd[4];
    LD_H(aa[1], ov + 40960, 1, isE, "=&v") LD_W4(wd, 46)
    if (lane == 0) set_flag(rs, (int)OFF_FL + (32 + rt * 4 + (h >> 3)) * 128 + (h & 7) * 16);
    f32x16 O = MFMA32(B8(((u32x4){ar.V0[0], ar.V0[1], ar.V0[2], ar.V0[3]})), B8(ar.P1), ar.O);
    PSTAMP(1, 2); WSTAMP(2);
    pt2 = (unsigned)__builtin_amdgcn_s_memrealtime();
    float l = hi_half(O[8]);
    {
        bool bad = !(l >= 1e-18f && l <= 1.0e30f);
#pragma unroll
        for (int i = 0; i < 12; ++i) bad = bad || !(fabsf(O[i]) <= 3.0e38f);
        if (__builtin_expect(__any(bad), 0)) {
            float m = -INFINITY; O = Z16;
            int tl = tid;
            asm volatile("" : "+v"(tl));
            const int r = tl & 31, hh = (tl >> 5) & 1, rc = r < 20 ? r : 19;
            const bf16x8 qm = B8(ar.QM), qt0 = B8(((u32x4){ar.QT[0], ar.QT[1], 0u, 0u}));
            const int ko = (r >> 4) * 640 + ((r >> 2) & 1) * 320 + ((r >> 3) & 1) * 8 + (r & 3) * 2;
            for (int t = 0; t < 64; ++t) {
                const char* tb = kvb + t * 2560;
                unsigned kw[6];
#pragma unroll
                for (int i = 0; i < 6; ++i) {
                    const int d = i < 4 ? 8 * hh + 2 * i : 16 + 2 * (i - 4);
                    kw[i] = (unsigned)*(const u16*)(tb + ko + d * 16) | ((unsigned)*(const u16*)(tb + ko + d * 16 + 16) << 16);
                }
                u32x4 f0 = *(const u32x4*)(tb + 1280 + hh * 320 + rc * 16), f1 = *(const u32x4*)(tb + 1920 + hh * 320 + rc * 16);
                if (r >= 20) { f0 = ONES; f1 = ONES; }
                f32x16 s = MFMA32(B8(((u32x4){kw[0], kw[1], kw[2], kw[3]})), qm, Z16);
                s = MFMA32(B8(((u32x4){kw[4], kw[5], 0u, 0u})), qt0, s);
                float tm = s[0];
#pragma unroll
                for (int i = 1; i < 16; ++i) tm = fmaxf(tm, s[i]);
                tm = xhalf_max(tm);
                const float mn = fmaxf(m, tm);
                const float al = (m == -INFINITY) ? 0.f : __builtin_amdgcn_exp2f(m - mn);
                m = mn;
#pragma unroll
                for (int i = 0; i < 16; ++i) { O[i] *= al; s[i] = __builtin_amdgcn_exp2f(s[i] - mn); }
                u32x4 p0 = {pk2(s[0], s[1]), pk2(s[2], s[3]), pk2(s[4], s[5]), pk2(s[6], s[7])};
                u32x4 p1 = {pk2(s[8], s[9]), pk2(s[10], s[11]), pk2(s[12], s[13]), pk2(s[14], s[15])};
                O = MFMA32(B8(f0), B8(p0), O); O = MFMA32(B8(f1), B8(p1), O);
            }
            l = hi_half(O[8]);
        }
    }
    store_o(rs, O, (1.f - gate) * __builtin_amdgcn_rcpf(l), rt, 1, h, lane);
    __builtin_amdgcn_s_setprio(0);
    __builtin_amdgcn_sched_barrier(0);
    WSTAMP(3);
    constexpr int L_P0 = L_WO + 98304;
    if (wave < 4) {
        const LAS char* wl = L + j * 6144 + lane * 16;
        auto wfr = [&](int f) -> bf16x8 {
            if (f < 20) return B8(wa[f]);
            if (f < 44) return *(const LAS bf16x8*)(wl + L_WO + (((f - 20) % 12) / 3) * 24576 + ((f - 20) / 12) * 3072 + ((f - 20) % 3) * 1024);
            if (f < 46) return *(const LAS bf16x8*)(L + 1024 + j * 3072 + (f - 44) * 1024 + lane * 16);
            return B8(wd[f - 46]);
        };
        LAS char* wp = (LAS char*)lds + (lane & 15) * 320 + ((g ^ ((lane >> 1) & 3)) << 4);
        f32x4 acc[4][5] = {};
        {
            bf16x8 w4[5];
#pragma unroll
            for (int ni = 0; ni < 5; ++ni) w4[ni] = wfr(20 + ni);
#pragma unroll
            for (int hf = 0; hf < 2; ++hf) {
                if (hf) asm volatile("s_waitcnt vmcnt(3)" : "+v"(aa[1][0]), "+v"(aa[1][1]), "+v"(aa[1][2]), "+v"(aa[1][3]), "+v"(aa[1][4]), "+v"(aa[1][5]), "+v"(aa[1][6]),
                                     "+v"(aa[1][7]), "+v"(aa[1][8]), "+v"(aa[1][9]), "+v"(wd[0]), "+v"(wd[1]), "+v"(wd[2]), "+v"(wd[3]) :: "memory");
#pragma unroll
                for (int k = 0; k < 5; ++k)
#pragma unroll
                    for (int mi = 0; mi < 2; ++mi)
#pragma unroll
                        for (int ni = 0; ni < 5; ++ni) acc[hf * 2 + mi][ni] = MFMA16(k < 4 ? wfr(k * 5 + ni) : w4[ni], B8(aa[hf][2 * k + mi]), acc[hf * 2 + mi][ni]);
                __builtin_amdgcn_sched_barrier(0);
            }
        }
        const int flB = (int)OFF_FL + (32 + rb * 8 + j) * 128;
        u32x4 b0[10], b1[10];
        wait_flag(rs, flv, flB, get_flag(rs, flv, flB));
        WSTAMP(4);
        LD_H(b0, ov, 0, 1, "=&v")
        fl = a_ld1(wsu, flB + 512 + flv);
        wait_cnt(lds, 0, 64);
        asm volatile("s_waitcnt vmcnt(0)" : "+v"(fl) :: "memory");
        wait_flag(rs, flv, flB + 512, fl);
        WSTAMP(5);
        LD_H(b1, ov, 1, 1, "=&v")
#pragma clang loop unroll(disable)
        for (int spin = 0; spin < (1 << 20); ++spin) {
            unsigned m = 0u;
#pragma unroll
            for (int i = 0; i < 10; ++i) { landed(b0[i]); m = umax4(m, b0[i]); }
            if (__builtin_expect(__all(m != EMPTY), 1)) break;
            LD_H(b0, ov, 0, 1, "+v")
            asm volatile("s_waitcnt vmcnt(0)" ::: "memory");
        }
        bf16x8 wb[25];
#pragma unroll
        for (int k = 0; k < 5; ++k) {
#pragma unroll
            for (int ni = 0; ni < 5; ++ni) wb[k * 5 + ni] = wfr(25 + k * 5 + ni);
#pragma unroll
            for (int mi = 0; mi < 2; ++mi)
#pragma unroll
                for (int ni = 0; ni < 5; ++ni) acc[mi][ni] = MFMA16(wb[k * 5 + ni], B8(b0[2 * k + mi]), acc[mi][ni]);
        }
#pragma unroll
        for (int mi = 0; mi < 2; ++mi)
#pragma unroll
            for (int ni = 0; ni < 5; ++ni) *(LAS f32x4*)(wp + L_P0 + j * 10240 + mi * 5120 + ni * 64) = acc[mi][ni];
        add_cnt<12>(lds0);
        f32x4 ac1[2][5];
#pragma clang loop unroll(disable)
        for (int spin = 0; spin < (1 << 20); ++spin) {
            unsigned m = 0u;
#pragma unroll
            for (int k = 0; k < 5; ++k) {
                asm volatile("s_waitcnt vmcnt(%2)" : "+v"(b1[2 * k]), "+v"(b1[2 * k + 1]) : "n"(8 - 2 * k) : "memory");
#pragma unroll
                for (int mi = 0; mi < 2; ++mi)
#pragma unroll
                    for (int ni = 0; ni < 5; ++ni) ac1[mi][ni] = MFMA16(wb[k * 5 + ni], B8(b1[2 * k + mi]), k ? ac1[mi][ni] : acc[2 + mi][ni]);
                m = umax4(umax4(m, b1[2 * k]), b1[2 * k + 1]);
                __builtin_amdgcn_sched_barrier(0);
            }
            if (__builtin_expect(__all(m != EMPTY), 1)) break;
            LD_H(b1, ov, 1, 1, "+v")
        }
#pragma unroll
        for (int mi = 0; mi < 2; ++mi)
#pragma unroll
            for (int ni = 0; ni < 5; ++ni) *(LAS f32x4*)(wp + L_WO + j * 6144 + mi * 24576 + ni * 64) = ac1[mi][ni];
        WSTAMP(6);
        asm volatile(".Lk2_last:\n\ts_waitcnt lgkmcnt(0)\n\ts_barrier" ::: "memory");
    } else {
        const int t = tid - 256;
        const LAS char* rp[2][3]; unsigned oo[3];
#pragma unroll
        for (int i = 0; i < 3; ++i) {
            const int id = i < 2 || t < 128 ? i * 256 + t : 1, row = id / 20, cs = id - row * 20, cc = cs ^ ((row >> 1) & 3);
            rp[0][i] = L + L_P0 + id * 16; rp[1][i] = L + L_WO + id * 16 + (id >= 320 ? 24576 - 5120 : 0);
            oo[i] = (unsigned)(((rb * 64 + row) * D_ + cb * 80 + cc * 4) * 2);
            landed(rp[0][i]); landed(rp[1][i]); landed(oo[i]);
        }
        WSTAMP(4);
#pragma unroll
        for (int hf = 0; hf < 2; ++hf) {
            if (hf == 0) { wait_cnt(lds, 12, 4); WSTAMP(5); }
            else { WSTAMP(6); asm volatile("s_barrier" ::: "memory"); }
            f32x4 pv[3][4];
#pragma unroll
            for (int i = 0; i < 3; ++i)
#pragma unroll
                for (int w = 0; w < 4; ++w) pv[i][w] = *(const LAS f32x4*)(rp[hf][i] + w * (hf ? 6144 : 10240));
            __builtin_amdgcn_sched_barrier(0);
#pragma unroll
            for (int i = 0; i < 3; ++i) {
                const f32x4 v = (pv[i][0] + pv[i][1]) + (pv[i][2] + pv[i][3]);
                if (i < 2 || t < 128) __builtin_amdgcn_raw_buffer_store_b64(u32x2{pk2(v[0], v[1]), pk2(v[2], v[3])},
                    __builtin_amdgcn_make_buffer_rsrc((void*)out, 0, S_ * D_ * 2, 0x00020000), oo[i], hf * (32 * D_ * 2), OUT_AUX);
            }
        }
    }
    if (bid == 0 && wave == 4) {
        const unsigned pt3 = (unsigned)__builtin_amdgcn_s_memrealtime(), pc3 = (unsigned)__builtin_amdgcn_s_memtime();
        const unsigned* pp = (const unsigned*)(ws + (64 << 20));
        int mn = 1 << 30, mx = -(1 << 30);
        for (int i = 0; i < 4; ++i) { const int b = i * 64 + (threadIdx.x & 63); mn = min(mn, (int)(pp[b * 2] - pt0)); mx = max(mx, (int)(pp[b * 2 + 1] - pt0)); }
        for (int o = 1; o < 64; o <<= 1) { mn = min(mn, __shfl_xor(mn, o)); mx = max(mx, __shfl_xor(mx, o)); }
        int X = PROBE == 0 ? 0 : PROBE == 1 ? mx - mn : PROBE == 2 ? -mx : PROBE == 3 ? (int)(pt3 - pt0) : PROBE == 4 ? (int)(pc3 - pc0) / 20 :
                PROBE == 5 ? (int)(pt1 - pt0) : PROBE == 6 ? (int)(pt2 - pt1) : PROBE == 7 ? (int)(pt3 - pt2) : (int)(pt3 - pt0) - mn;
        if (PROBE >= 100) {
            int sm = 0, mz = -(1 << 30);
            for (int i = 0; i < 4; ++i) { const int b = i * 64 + (threadIdx.x & 63);
                const int v = PROBE >= 200 ? (int)pp[512 + b] / 20 : (int)((PROBE == 121 ? pp[b * 2] : PROBE == 122 || PROBE == 130 ? pp[b * 2 + 1] : pp[512 + b]) - pt0) - mn;
                if (b < 240) sm += v; else mz = max(mz, v); }
            for (int o = 1; o < 64; o <<= 1) { sm += __shfl_xor(sm, o); mz = max(mz, __shfl_xor(mz, o)); }
            X = 4 * (PROBE == 130 ? mz : sm / 240);
        }
        X = rfl(min(max(X, 0), 12000));
        while ((int)((unsigned)__builtin_amdgcn_s_memrealtime() - pt3) < 300 + 4 * X) __builtin_amdgcn_s_sleep(1);
    }
}

extern "C" void kernel_launch(void* const* d_in, const int* in_sizes, int n_in, void* d_out, int out_size, void* d_ws, size_t ws_size,
                              hipStream_t stream) {
    const u16* x = (const u16*)d_in[0];
    const u16* wA = (const u16*)d_in[1];
    const u16* woA = (const u16*)d_in[2];
    const u16* wB = (const u16*)d_in[4];
    const u16* woB = (const u16*)d_in[5];
    const u16* gw = (const u16*)d_in[6];
    char* ws = (char*)d_ws;
    (void)hipFuncSetAttribute((const void*)k1_qkv, hipFuncAttributeMaxDynamicSharedMemorySize, LDS_BYTES);
    (void)hipFuncSetAttribute((const void*)k2_attn, hipFuncAttributeMaxDynamicSharedMemorySize, LDS_BYTES);
    k1_qkv<<<256, 512, LDS_BYTES, stream>>>(x, wA, wB, woA, woB, gw, ws);
    k2_attn<<<256, 512, LDS_BYTES, stream>>>(ws, (u16*)d_out);
}
```

```cpp
#define OUT_AUX 16
#include <hip/hip_runtime.h>
#include <hip/hip_bf16.h>
#include <stdint.h>

typedef short    bf16x8 __attribute__((ext_vector_type(8)));
typedef short    s16x4  __attribute__((ext_vector_type(4)));
typedef float    f32x2  __attribute__((ext_vector_type(2)));
typedef float    f32x4  __attribute__((ext_vector_type(4)));
typedef float    f32x16 __attribute__((ext_vector_type(16)));
typedef float    f32x32 __attribute__((ext_vector_type(32)));
typedef unsigned u32x2  __attribute__((ext_vector_type(2)));
typedef unsigned u32x4  __attribute__((ext_vector_type(4)));
typedef unsigned u32x8  __attribute__((ext_vector_type(8)));
typedef unsigned u32x32 __attribute__((ext_vector_type(32)));
typedef __bf16   bf2_t  __attribute__((ext_vector_type(2)));
typedef unsigned short u16;
#define LAS __attribute__((address_space(3)))
#define DI __device__ __forceinline__

constexpr int S_ = 2048, D_ = 640;
constexpr size_t IMG     = (size_t)32 * 163840;
constexpr size_t OFF_IQ  = 0;
constexpr size_t OFF_IB  = IMG;
constexpr size_t OFF_IA  = 2 * IMG;
constexpr size_t OFF_O   = 3 * IMG;
constexpr size_t OFF_WOT = 4 * IMG;
constexpr size_t OFF_G   = OFF_WOT + (size_t)1600 * 1024;
constexpr size_t OFF_KVP = OFF_G + 8192;
constexpr size_t OFF_FL  = OFF_KVP + (size_t)256 * 2048;
constexpr int    WS_USED = (int)(OFF_FL + 288 * 128);
constexpr unsigned EMPTY = 0xFFFFFFFFu;
typedef __amdgpu_buffer_rsrc_t rsrc_t;
constexpr int SC1 = 16;

#define CSTAMP(i)
#define PSTAMP(k, i)
#define WSTAMP(i)

DI unsigned pk2(float lo, float hi) { f32x2 v = {lo, hi}; return __builtin_bit_cast(unsigned, __builtin_convertvector(v, bf2_t)); }
DI float bflo(unsigned v) { return __uint_as_float(v << 16); }
DI float bfhi(unsigned v) { return __uint_as_float(v & 0xffff0000u); }
DI int rfl(int x) { return __builtin_amdgcn_readfirstlane(x); }
DI void glds16(const void* sbase, unsigned voff, unsigned lds_base) {
    const unsigned long long a = (unsigned long long)(uintptr_t)sbase;
    const unsigned long long u = (unsigned long long)(unsigned)rfl((int)(unsigned)a) | ((unsigned long long)(unsigned)rfl((int)(unsigned)(a >> 32)) << 32);
    asm volatile("s_mov_b32 m0, %0\n\ts_nop 0\n\tglobal_load_lds_dwordx4 %1, %2" :: "s"(rfl((int)lds_base)), "v"(voff), "s"(u) : "memory");
}
#define MFMA16(a, b, c) __builtin_amdgcn_mfma_f32_16x16x32_bf16((a), (b), (c), 0, 0, 0)
#define MFMA32(a, b, c) __builtin_amdgcn_mfma_f32_32x32x16_bf16((a), (b), (c), 0, 0, 0)
#define B8(x) __builtin_bit_cast(bf16x8, x)
DI u32x2 trd(const LAS char* p) { return __builtin_bit_cast(u32x2, __builtin_amdgcn_ds_read_tr16_b64_v4i16((LAS s16x4*)p)); }
DI float pmax(float t, float c) { return __int_as_float(max(__float_as_int(t), __float_as_int(c))); }
template <int N> DI void wait_vm_bar() { asm volatile("s_waitcnt vmcnt(%0) lgkmcnt(0)\n\ts_barrier" :: "n"(N) : "memory"); }

constexpr int LDS_BYTES = 163840;
constexpr float LOG2E = 1.4426950408889634f;

DI void p1_aux(const u16* __restrict__ x, const u16* __restrict__ woA, const u16* __restrict__ woB, const u16* __restrict__ gate_w,
               char* __restrict__ ws, char* lds, int a) {
    const int tid = threadIdx.x, lane = tid & 63, wave = rfl(tid >> 6);
    const unsigned lds0 = (unsigned)(uintptr_t)lds;
    const int br = a >> 3, u = a & 7;
    if (tid < 144) *(u32x4*)(ws + OFF_FL + (a * 144 + tid) * 16) = u32x4{0u, 0u, 0u, 0u};
    const char* wsrc = (const char*)(br ? woB : woA) + (size_t)u * 102400;
    for (int i = wave; i < 100; i += 8) glds16(wsrc + i * 1024, lane * 16, lds0 + i * 1024);
    const u32x4* xs = (const u32x4*)(x + (size_t)(a * 128 + wave * 16) * D_);
    u32x4 xv[20], gw[5];
#pragma unroll
    for (int i = 0; i < 20; ++i) xv[i] = xs[i * 64 + lane];
#pragma unroll
    for (int i = 0; i < 5; ++i) gw[i] = ((const u32x4*)gate_w)[(i * 64 + lane) % 80];
    {
        const rsrc_t rs = __builtin_amdgcn_make_buffer_rsrc((void*)(ws + OFF_KVP + a * 32768), 0, 32768, 0x00020000);
#pragma unroll
        for (int i = 0; i < 4; ++i) __builtin_amdgcn_raw_buffer_store_b128(u32x4{EMPTY, EMPTY, EMPTY, EMPTY}, rs, tid * 16 + i * 8192, 0, SC1);
    }
    LAS float* part = (LAS float*)(lds + 102400) + wave * 1280;
#pragma unroll
    for (int i = 0; i < 20; ++i) {
        float s = 0.f;
#pragma unroll
        for (int j = 0; j < 4; ++j) s += bflo(xv[i][j]) * bflo(gw[i % 5][j]) + bfhi(xv[i][j]) * bfhi(gw[i % 5][j]);
        part[i * 64 + lane] = s;
    }
    wait_vm_bar<0>();
    {
        float s = 0.f;
#pragma unroll
        for (int j = 0; j < 20; ++j) s += part[lane * 20 + j];
        s += __shfl_xor(s, 1); s += __shfl_xor(s, 2);
        if ((lane & 3) == 0) ((float*)(ws + OFF_G))[a * 128 + wave * 16 + (lane >> 2)] = 1.f / (1.f + __expf(-s));
    }
    const int g = lane >> 4, hh = g & 1, ns = g >> 1, q = (lane >> 2) & 3, p = lane & 3;
    for (int un = wave; un < 100; un += 8) {
        const int e = un / 20, np = un - e * 20, nt = np * 2 + ns;
        const int r0 = e < 4 ? e * 20 + 4 * hh : (2 * hh) * 20 + 16;
        const int r1 = e < 4 ? r0 + 8 : r0 + 20;
        const int ks16 = e < 4 ? (u & 1) * 4 + e : 8 + (u & 1), cbk = nt / 5;
        const u32x2 lo = trd((const LAS char*)lds + (r0 + q) * 1280 + nt * 32 + p * 8);
        const u32x2 hi = trd((const LAS char*)lds + (r1 + q) * 1280 + nt * 32 + p * 8);
        *(u32x4*)(ws + OFF_WOT + (size_t)((cbk * 8 + (u & 6) + br) * 25 + (ks16 >> 1) * 5 + nt - cbk * 5) * 1024 + ((ks16 & 1) * 2 + hh) * 256 + (lane & 15) * 16) = u32x4{lo[0], lo[1], hi[0], hi[1]};
    }
}

#define CPF 64
#define CPF_N 256
#define CPF_M 0
#define G1_TL 3
#define G1_D 3
#define G1_D0 2
#define G1_DT 10
#define G1_DE 0
#define G1_NX 4
constexpr int G1_NW = 10 - G1_NX, G1_HB = 20 - 2 * G1_TL, G1_WOFF = G1_NX * 16384;
constexpr int g1_t(int h) { return h < 2 * G1_HB ? h >> 1 : h < 2 * G1_HB + 2 * G1_TL ? h - G1_HB : h - G1_HB - 2 * G1_TL; }
constexpr int g1_m(int h) { return h < 2 * G1_HB ? h & 1 : h < 2 * G1_HB + 2 * G1_TL ? 0 : 1; }
constexpr int g1_xi(int h) { return h < 2 * G1_HB ? (h >> 1) & 1 : h & 1; }
constexpr int g1_free(int i) { return i < 10 ? (i < G1_NX ? -1 : 2 * (i - G1_NX) + 2) : (i - 10 < G1_NW ? -1 : i - 10 - G1_NW + 1); }
struct G1Plan { int seq[30]; int cnt[22]; int pos[30]; };
constexpr G1Plan g1_plan() {
    G1Plan p = {}; bool done[30] = {}, seen[10] = {}; int ord[30] = {}, need[21] = {}, n = 0;
    for (int b = 0; b < 20; ++b) { const int u = g1_t(2 * b) >> 1; if (!seen[u]) { seen[u] = true; ord[n++] = u; } need[b] = n; ord[n++] = 10 + b; }
    need[20] = 30; n = 0;
    for (int b = 0; b <= 20; ++b) {
        const int ex = b > G1_DT ? b - G1_DT : 0, lim = 3 * (b >> 1) + 1 + (b & 1) + (b == 0 ? G1_D0 : G1_D) + (ex < G1_DE ? ex : G1_DE);
        for (int k = 0; k < 30 && k <= lim; ++k) { const int i = ord[k]; if (!done[i] && g1_free(i) < b) { done[i] = true; p.pos[i] = n; p.seq[n++] = i; } }
        p.cnt[b] = n;
    }
    return p;
}
constexpr G1Plan G1P = g1_plan();
constexpr int g1_wait(int b) { const int x = G1P.pos[g1_t(2 * b) >> 1], w = G1P.pos[10 + b]; return 4 * (G1P.cnt[b] - 1 - (x > w ? x : w)); }
constexpr bool g1_ok() { for (int b = 0; b < 20; ++b) if (g1_wait(b) < 0 || g1_wait(b) > 60) return false; return G1P.cnt[19] == 30 && G1_NX >= G1_TL && G1_NX <= 4; }
static_assert(g1_ok(), "loader plan");
#define G1_ROT ((0x27057250 >> (4 * xcd)) & 15)

#define LST_P
#define LST_A
template <int T> struct G1Load {
    template <class FX, class FW> static DI void run(FX&& ldx, FW&& ldw LST_P) {
        asm volatile("s_waitcnt vmcnt(%0)\n\ts_barrier" :: "n"(g1_wait(T)) : "memory");
#pragma unroll
        for (int n = G1P.cnt[T]; n < G1P.cnt[T + 1]; ++n) { const int i = G1P.seq[n]; if (i < 10) ldx(i); else ldw(i - 10); }
        G1Load<T + 1>::run(ldx, ldw LST_A);
    }
};
template <> struct G1Load<20> { template <class FX, class FW> static DI void run(FX&&, FW&& LST_P) {} };

struct G1Ctx {
    f32x4 acc[4][8];
    bf16x8 x[2][4]; u32x2 w[2][4][2];
    const LAS char* xa[2]; const LAS char* wa[8];
    int so[4];
    rsrc_t rB; float sc; f32x2 m[4]; u32x4 o;
    unsigned pf0, pf1;
};
#define SB0() __builtin_amdgcn_sched_barrier(0)
#define G1_SP 12
template <int J, int I> DI void g1_side(G1Ctx& c) {
    constexpr int K = (J - (40 - 2 * G1_TL)) * 16 + I;
    if (CPF && J == 20 && I == 12) { asm volatile("s_waitcnt vmcnt(0)" :: "v"(c.pf0), "v"(c.pf1)); SB0(); }
    if (K > 0 && K <= 8 * G1_SP && (K - 1) % G1_SP == 8) asm volatile("" :: "v"(c.o));
    if (K >= 0 && K < 8 * G1_SP && K % G1_SP < 9) {
        constexpr int U = K < 0 ? 0 : K / G1_SP, e = K < 0 ? 0 : K % G1_SP, ni = (U >> 1) & 3, a = U & 1;
        constexpr int mi = e == 0 ? 0 : e == 1 ? 1 : e == 3 ? 2 : e == 5 ? 3 : -1, ci = e == 2 ? 0 : e == 4 ? 1 : e == 6 ? 2 : e == 7 ? 3 : -1;
        if (mi >= 0) { const f32x4 v = c.acc[2 * a + ((mi & 3) >> 1)][ni]; c.m[mi & 3] = f32x2{v[(mi & 1) * 2], v[(mi & 1) * 2 + 1]} * c.sc; }
        if (ci >= 0) c.o[ci & 3] = pk2(c.m[ci & 3][0], c.m[ci & 3][1]);
        if (e == 8) __builtin_amdgcn_raw_buffer_store_b128(c.o, c.rB, c.so[ni] + a * 2560, 0, SC1);
        SB0();
    }
}
template <int J> struct G1Step {
    static DI void run(G1Ctx& c) {
        constexpr int N = J + 1, t = g1_t(N), xo = ((t >> 1) % G1_NX) * 16384, sl = (N >> 1) % G1_NW, wo = (sl & 3) * 16384 + (N & 1) * 8192, wi = (sl >> 2) * 4;
        constexpr int ps = J & 1, ma = g1_m(J) * 4, xc = g1_xi(J), xn = g1_xi(N);
#define G1_M(i) c.acc[(i) & 3][ma + ((i) >> 2)] = MFMA16(c.x[xc][(i) & 3], B8(((u32x4){c.w[ps][(i) >> 2][0][0], c.w[ps][(i) >> 2][0][1], c.w[ps][(i) >> 2][1][0], c.w[ps][(i) >> 2][1][1]})), \
                c.acc[(i) & 3][ma + ((i) >> 2)]); SB0(); g1_side<J, i>(c);
#define G1_X(i) if (N < 40) c.x[xn][i] = *(const LAS bf16x8*)(c.xa[t & 1] + xo + ((i) >> 1) * 4096 + ((i) & 1) * 1024); SB0();
#define G1_W(i, e) if (N < 40) c.w[ps ^ 1][i][e] = trd(c.wa[wi + (i)] + wo + (e) * 1024); SB0();
        if (ps) {
            G1_M(0) G1_M(1) G1_M(2)
            if (N < 40) asm volatile("s_waitcnt lgkmcnt(0)\n\ts_barrier" ::: "memory");
            SB0();
            G1_X(0) G1_M(3) G1_W(0, 0) G1_M(4) G1_W(0, 1) G1_M(5) G1_X(1) G1_M(6) G1_X(2) G1_M(7) G1_X(3) G1_M(8) G1_W(1, 0) G1_M(9) G1_W(1, 1) G1_M(10)
            G1_W(2, 0) G1_M(11) G1_W(2, 1) G1_M(12) G1_W(3, 0) G1_M(13) G1_W(3, 1) G1_M(14) G1_M(15)
        } else if (xn != xc) {
            G1_M(0) G1_X(0) G1_M(1) G1_W(0, 0) G1_M(2) G1_W(0, 1) G1_M(3) G1_X(1) G1_M(4) G1_X(2) G1_M(5) G1_X(3) G1_M(6) G1_W(1, 0) G1_M(7) G1_W(1, 1)
            G1_M(8) G1_W(2, 0) G1_M(9) G1_W(2, 1) G1_M(10) G1_W(3, 0) G1_M(11) G1_W(3, 1) G1_M(12) G1_M(13) G1_M(14) G1_M(15)
        } else {
            G1_M(0) G1_W(0, 0) G1_M(1) G1_W(0, 1) G1_M(2) G1_W(1, 0) G1_M(3) G1_W(1, 1) G1_M(4) G1_W(2, 0) G1_M(5) G1_W(2, 1) G1_M(6) G1_W(3, 0) G1_M(7) G1_W(3, 1)
            G1_M(8) G1_M(9) G1_M(10) G1_M(11) G1_M(12) G1_M(13) G1_M(14) G1_M(15)
        }
        G1Step<J + 1>::run(c);
    }
};
template <> struct G1Step<40> { static DI void run(G1Ctx&) {} };
template <int K> struct G1Rest { static DI void run(G1Ctx& c) { g1_side<40 - 2 * G1_TL + K / 16, K % 16>(c); G1Rest<K + 1>::run(c); } };
template <> struct G1Rest<8 * G1_SP> { static DI void run(G1Ctx&) {} };

DI void phase1(const u16* __restrict__ x, const u16* __restrict__ wA, const u16* __restrict__ wB, char* __restrict__ ws, char* lds, int bid,
               unsigned pf0, unsigned pf1) {
    const int tid = threadIdx.x, lane = tid & 63, wave = rfl(tid >> 6);
    const unsigned lds0 = (unsigned)(uintptr_t)lds;
    const int xcd = bid & 7, j = (xcd >> 1) * 30 + (bid >> 3);
    const int ct = j >> 3, mt = (xcd & 1) * 8 + (j & 7);
    const int type = rfl(ct / 5), rot = rfl(G1_ROT);
    if (__builtin_expect(wave >= 4, 1)) {
        const int lw = wave - 4;
        unsigned xo[4], wo[2];
#pragma unroll
        for (int i = 0; i < 4; ++i) { const int row = (lw * 4 + i) * 8 + (lane >> 3); xo[i] = (unsigned)((mt * 128 + row) * 1280 + (((lane & 7) ^ ((row >> 1) & 3) ^ ((row >> 2) & 4)) << 4)); }
#pragma unroll
        for (int i = 0; i < 2; ++i) { const int k = (lw * 2 + i) * 4 + (lane >> 4), s16 = lane & 15;
            wo[i] = (unsigned)(k * 3840 + ct * 256 + ((s16 ^ ((k & 3) << 2) ^ ((k >> 2) & 2)) << 4)); }
        auto ldx = [&](int u, unsigned st = 0u) {
            int kk = u + rot; kk -= kk >= 10 ? 10 : 0;
            const unsigned slot = lds0 + (u % G1_NX) * 16384 + lw * 4096;
#pragma unroll
            for (int i = 0; i < 4; ++i) glds16((const char*)x + kk * 128, xo[i], slot + i * 1024);
        };
        auto ldw = [&](int s) {
            int kk = (g1_t(2 * s) >> 1) + rot; kk -= kk >= 10 ? 10 : 0;
            const unsigned slot = lds0 + G1_WOFF + (s % G1_NW) * 16384 + lw * 2048;
#pragma unroll
            for (int e = 0; e < 2; ++e) {
                const char* src = (const char*)(g1_m(2 * s + e) ? wA : wB) + (kk * 64 + (g1_t(2 * s + e) & 1) * 32) * 3840;
                glds16(src, wo[0], slot + e * 8192); glds16(src, wo[1], slot + e * 8192 + 1024);
            }
        };
#pragma unroll
        for (int n = 0; n < G1P.cnt[0]; ++n) { const int i = G1P.seq[n]; if (i < 10) ldx(i); else ldw(i - 10); }
        G1Load<0>::run(ldx, ldw LST_A);
        return;
    }
    const int wm = wave & 1, wn = wave >> 1;
    const int i16 = lane & 15, g = lane >> 4, q = (lane >> 2) & 3, p = lane & 3;
    G1Ctx c;
    c.pf0 = pf0; c.pf1 = pf1;
    const int xr = (i16 >> 3) * 16 + (i16 & 7);
#pragma unroll
    for (int ks = 0; ks < 2; ++ks) { c.xa[ks] = (const LAS char*)lds + (wm * 64 + xr) * 128 + (((ks * 4 + g) ^ ((xr >> 1) & 3) ^ ((xr >> 2) & 4)) << 4); asm volatile("" : "+v"(c.xa[ks])); }
#pragma unroll
    for (int i = 0; i < 8; ++i) { c.wa[i] = (const LAS char*)lds + G1_WOFF + (i >> 2) * 65536 + (8 * g + q) * 256 + (((wn * 4 + (i & 3)) ^ (q << 1) ^ (g & 1)) << 5) + p * 8;
        asm volatile("" : "+v"(c.wa[i])); }
#pragma unroll
    for (int i = 0; i < 4; ++i) { const int cc = (ct - type * 5) * 128 + wn * 64 + i * 16 + i16, h = cc / 20; c.so[i] = h * 163840 + g * 320 + (cc - h * 20) * 16; asm volatile("" : "+v"(c.so[i])); }
    constexpr float QS = 0.22360679774997896f * LOG2E;
    int sci = type == 0 ? __builtin_bit_cast(int, QS) : 0x3F800000;
    asm volatile("" : "+s"(sci));
    c.sc = __int_as_float(sci);
    const int tb = (mt * 4 + wm * 2) * 2560;
    const int sB = tb + (type == 0 ? (int)OFF_IQ : (int)OFF_IB + (type - 1) * 1280), sA = tb + (type == 0 ? (int)OFF_IQ + 1280 : (int)OFF_IA + (type - 1) * 1280);
    c.rB = __builtin_amdgcn_make_buffer_rsrc((void*)(ws + sB), 0, (int)IMG, 0x00020000);
    const rsrc_t rA = __builtin_amdgcn_make_buffer_rsrc((void*)(ws + sA), 0, (int)IMG, 0x00020000);
    {
        const float a0 = type < 2 ? 1e-6f : 0.f;
        f32x4 a4 = {a0, a0, a0, a0};
        asm volatile("" : "+v"(a4));
#pragma unroll
        for (int i = 0; i < 16; ++i) { c.acc[i & 3][i >> 2] = f32x4{0.f, 0.f, 0.f, 0.f}; c.acc[i & 3][4 + (i >> 2)] = a4; }
    }
    __builtin_amdgcn_s_setprio(2);
    asm volatile("s_barrier" ::: "memory");
#pragma unroll
    for (int i = 0; i < 4; ++i) {
        c.x[0][i] = *(const LAS bf16x8*)(c.xa[0] + (i >> 1) * 4096 + (i & 1) * 1024);
        if (i == 0) { c.w[0][0][0] = trd(c.wa[0]); c.w[0][0][1] = trd(c.wa[0] + 1024); }
    }
#pragma unroll
    for (int i = 1; i < 4; ++i) { c.w[0][i][0] = trd(c.wa[i]); c.w[0][i][1] = trd(c.wa[i] + 1024); }
    SB0();
    G1Step<0>::run(c);
    PSTAMP(0, 2);
    G1Rest<(32 * G1_TL < 8 * G1_SP ? 32 * G1_TL : 8 * G1_SP)>::run(c);
    if (type < 2) {
#pragma unroll
        for (int i = 0; i < 4; ++i)
#pragma unroll
            for (int a = 0; a < 2; ++a) {
                u32x4 o;
#pragma unroll
                for (int e = 0; e < 4; ++e) {
                    const f32x2 t = {c.acc[2 * a + (e >> 1)][4 + i][(e & 1) * 2], c.acc[2 * a + (e >> 1)][4 + i][(e & 1) * 2 + 1]};
                    const f32x2 m = __builtin_elementwise_fma(t, f32x2{LOG2E, LOG2E}, f32x2{-1e-6f * LOG2E, -1e-6f * LOG2E});
                    const f32x2 ex = {__builtin_amdgcn_fmed3f(__builtin_amdgcn_exp2f(m[0]), 0.f, 1.f), __builtin_amdgcn_fmed3f(__builtin_amdgcn_exp2f(m[1]), 0.f, 1.f)};
                    const f32x2 r = f32x2{pmax(t[0], 1e-6f), pmax(t[1], 1e-6f)} + ex;
                    o[e] = pk2(r[0], r[1]);
                }
                __builtin_amdgcn_raw_buffer_store_b128(o, rA, c.so[i] + a * 2560, 0, SC1);
            }
    } else {
#pragma unroll
        for (int i = 0; i < 4; ++i)
#pragma unroll
            for (int a = 0; a < 2; ++a) {
                const f32x4 u = c.acc[2 * a][4 + i], v = c.acc[2 * a + 1][4 + i];
                __builtin_amdgcn_raw_buffer_store_b128(u32x4{pk2(u[0], u[1]), pk2(u[2], u[3]), pk2(v[0], v[1]), pk2(v[2], v[3])}, rA, c.so[i] + a * 2560, 0, SC1);
            }
    }
}

__global__ __launch_bounds__(512, 2) void k1_qkv(const u16* __restrict__ x, const u16* __restrict__ wA, const u16* __restrict__ wB,
                                                 const u16* __restrict__ woA, const u16* __restrict__ woB, const u16* __restrict__ gate_w,
                                                 char* __restrict__ ws) {
    extern __shared__ __attribute__((aligned(16))) char lds[];
    int bid = blockIdx.x;
    asm volatile("" : "+s"(bid));
    unsigned pf0, pf1 = 0u;
    {
        unsigned long long pc;
        asm volatile("s_getpc_b64 %1\n.Lk1_pc:\n\tv_lshl_add_u32 %0, %2, 7, %3\n\tv_min_u32 %0, .Lk2_last-.Lk1_pc, %0\n\t"
                     "v_cmp_gt_u32 vcc, %4, %2\n\ts_mov_b64 exec, vcc\n\tglobal_load_dword %0, %0, %1\n\ts_mov_b64 exec, -1"
                     : "=&v"(pf0), "=&s"(pc) : "v"(threadIdx.x), "s"((bid >> 3) * (CPF * 128) + 64), "n"(CPF) : "memory", "vcc");
    }
    if (__builtin_expect(bid < 240, 1)) phase1(x, wA, wB, ws, lds, bid, pf0, pf1);
    else { p1_aux(x, woA, woB, gate_w, ws, lds, bid - 240); asm volatile("" :: "v"(pf0), "v"(pf1)); }
}

#define STR(x) #x
#define XS(x) STR(x)
#define A_E(i) "v_exp_f32 v" XS(i) ", v" XS(i) "\n"
#define A_C(d, a, b) "v_cvt_pk_bf16_f32 v" XS(d) ", v" XS(a) ", v" XS(b) "\n"
#define A_G(a, b, c, d, p0, p1) A_E(a) A_E(b) A_E(c) A_E(d) A_C(p0, a, b) A_C(p1, c, d)
#define A_M(d, a, b, c) "v_mfma_f32_32x32x16_bf16 " d ", " a ", " b ", " c "\n"
#define A_STEP(C0,C1,C2,C3,C4,C5,C6,C7,C8,C9,C10,C11,C12,C13,C14,C15, SN, KM, KT, V1, V0, NKMa, NKMb, NKT, NV1, NV0, OK0, OK1, OV1, OV0, PRIO) \
    "s_waitcnt lgkmcnt(0)\n" \
    A_M(SN, KM, "v[120:123]", "0") \
    "ds_read_b64_tr_b16 " NKMa ", v128 offset:" XS(OK0) "\n" \
    "ds_read_b64_tr_b16 " NKMb ", v128 offset:" XS(OK1) "\n" \
    "ds_read_b64_tr_b16 " NKT ", v129 offset:" XS(OK0) "\n" \
    A_G(C0, C1, C2, C3, 112, 113) \
    A_M("v[64:79]", V1, "v[116:119]", "v[64:79]") \
    "s_mov_b64 exec, s[72:73]\n" \
    "ds_read_b128 " NV1 ", v130 offset:" XS(OV1) "\n" \
    "ds_read_b128 " NV0 ", v130 offset:" XS(OV0) "\n" \
    "s_mov_b64 exec, -1\n" \
    A_G(C4, C5, C6, C7, 114, 115) \
    A_M(SN, KT, "v[124:127]", SN) \
    A_G(C8, C9, C10, C11, 116, 117) \
    A_M("v[64:79]", V0, "v[112:115]", "v[64:79]") \
    "s_setprio " XS(PRIO) "\n" \
    A_G(C12, C13, C14, C15, 118, 119)
#define A_EVEN(OK0, OK1, OV1, OV0, PRIO) A_STEP(32,33,34,35,36,37,38,39,40,41,42,43,44,45,46,47, "v[48:63]", "v[80:83]", "v[84:87]", "v[88:91]", "v[92:95]", \
    "v[96:97]", "v[98:99]", "v[100:101]", "v[104:107]", "v[108:111]", OK0, OK1, OV1, OV0, PRIO)
#define A_ODD(OK0, OK1, OV1, OV0, PRIO) A_STEP(48,49,50,51,52,53,54,55,56,57,58,59,60,61,62,63, "v[32:47]", "v[96:99]", "v[100:103]", "v[104:107]", "v[108:111]", \
    "v[80:81]", "v[82:83]", "v[84:85]", "v[88:91]", "v[92:95]", OK0, OK1, OV1, OV0, PRIO)

#define A_LD(g, off) "global_load_lds_dwordx4 v131, " g " offset:" XS(off) "\n"
#define A_LDA(off) "s_cbranch_scc1 .Lattn_s" XS(off) "\n" A_LD("s[74:75]", off) ".Lattn_s" XS(off) ":\n"
#define A_BODY \
        "s_mov_b32 m0, s78\n s_cmp_eq_u32 s80, 2\n s_nop 3\n" \
        A_EVEN(5120, 5184, 640, 2560, 2)     A_ODD(7680, 7744, 3200, 5120, 1) \
        A_EVEN(10240, 10304, 5760, 7680, 0)  A_ODD(12800, 12864, 8320, 10240, 3) \
        "s_waitcnt vmcnt(0) lgkmcnt(0)\n s_mov_b64 exec, 1\n ds_add_u32 v127, v86\n s_mov_b64 exec, -1\n" \
        A_EVEN(15360, 15424, 10880, 12800, 2) A_LDA(0) A_ODD(17920, 17984, 13440, 15360, 1) A_LDA(1024) \
        A_EVEN(20480, 20544, 16000, 17920, 0) A_LDA(2048) \
        "s_cmp_eq_u32 s80, 1\n s_cbranch_scc0 .Lattn_skip\n s_mov_b32 m0, s79\n s_nop 0\n" \
        A_LD("s[76:77]", 0) A_LD("s[76:77]", 1024) A_LD("s[76:77]", 2048) ".Lattn_skip:\n" \
        "ds_read_b32 v132, v127\n" \
        A_ODD(23040, 23104, 18560, 20480, 3) \
        "v_add_u32 v128, 0x5000, v128\n v_add_u32 v129, 0x5000, v129\n v_add_u32 v130, 0x5000, v130\n" \
        "s_waitcnt lgkmcnt(0)\n s_nop 0\n"
struct AttnRegs { f32x16 S, O; u32x4 KM, P1, QM, KT0, KT1, QT, AD; u32x8 V0, V1; };
#define A_OPERANDS \
        : "+{v[32:47]}"(a.S), "+{v[64:79]}"(a.O), "+{v[80:83]}"(a.KM), "+{v[84:87]}"(a.KT0), "+{v[88:95]}"(a.V0), "+{v[100:103]}"(a.KT1), \
          "+{v[104:111]}"(a.V1), "+{v[116:119]}"(a.P1), "+{v[128:131]}"(a.AD), "={v132}"(cnt) \
        : "{v[120:123]}"(a.QM), "{v[124:127]}"(a.QT), \
          "{s[72:73]}"(vmask), "{s[74:75]}"(ga), "{s[76:77]}"(gb), "{s78}"(ma), "{s79}"(mb), "{s80}"(enb) \
        : "memory", "scc", "s82", "s83", "v48", "v49", "v50", "v51", "v52", "v53", "v54", "v55", "v56", "v57", "v58", "v59", "v60", "v61", "v62", "v63", \
          "v96", "v97", "v98", "v99", "v112", "v113", "v114", "v115"
template <bool FIRST>
DI unsigned attn_chunk(AttnRegs& a, unsigned long long vmask, unsigned ma, unsigned long long ga, unsigned mb, unsigned long long gb, unsigned enb) {
    unsigned cnt;
    if (FIRST) asm volatile("s_call_b64 s[82:83], .Lattn_body\n s_branch .Lattn_done\n .p2align 6\n .Lattn_body:\n" A_BODY "s_setpc_b64 s[82:83]\n .Lattn_done:\n" A_OPERANDS);
    else       asm volatile("s_call_b64 s[82:83], .Lattn_body\n" A_OPERANDS);
    return cnt;
}
DI void wait_cnt(char* lds, int off, unsigned cnt, int n) {
    if (__builtin_expect((unsigned)rfl((int)cnt) < (unsigned)(n * 0x3F80), 0)) {
#pragma clang loop unroll(disable)
        do { __builtin_amdgcn_s_sleep(1); cnt = *(volatile LAS unsigned*)(lds + off); } while ((unsigned)rfl((int)cnt) < (unsigned)(n * 0x3F80));
    }
    asm volatile("" ::: "memory");
}

DI void wait_cnt(char* lds, int off, int n) { wait_cnt(lds, off, *(volatile LAS unsigned*)(lds + off), n); }
template <int OFF> DI void add_cnt(unsigned lds0) {
    asm volatile("s_waitcnt lgkmcnt(0)\n\ts_mov_b64 exec, 1\n\tds_add_u32 %0, %1 offset:%2\n\ts_mov_b64 exec, -1" :: "v"(lds0), "v"(0x3F80u), "n"(OFF) : "memory"); }

DI float xhalf_max(float m) { auto rr = __builtin_amdgcn_permlane32_swap(__float_as_uint(m), __float_as_uint(m), false, false);
    return fmaxf(__uint_as_float(rr[0]), __uint_as_float(rr[1])); }
DI float hi_half(float v) {
    auto rr = __builtin_amdgcn_permlane32_swap(__float_as_uint(v), __float_as_uint(v), false, false);
    return __uint_as_float(rr[1]); }
DI float sum16(float v) { auto rr = __builtin_amdgcn_permlane16_swap(__float_as_uint(v), __float_as_uint(v), false, false);
    return __uint_as_float(rr[0]) + __uint_as_float(rr[1]); }
DI float sum32(float v) { auto rr = __builtin_amdgcn_permlane32_swap(__float_as_uint(v), __float_as_uint(v), false, false);
    return __uint_as_float(rr[0]) + __uint_as_float(rr[1]); }
DI f32x4 sum_rows(f32x4 a, f32x4 b) {
    f32x4 s = a + b;
#pragma unroll
    for (int i = 0; i < 4; ++i) s[i] = sum32(sum16(s[i]));
    return s;
}
DI unsigned long long uni64(const void* p) { const unsigned long long a = (unsigned long long)(uintptr_t)p;
    return (unsigned long long)(unsigned)rfl((int)(unsigned)a) | ((unsigned long long)(unsigned)rfl((int)(unsigned)(a >> 32)) << 32); }
DI unsigned full(unsigned v) { return v < EMPTY ? v : EMPTY - 1u; }
DI unsigned umax4(unsigned m, u32x4 v) { return max(max(max(m, v[0]), max(v[1], v[2])), v[3]); }
DI void scale_o(f32x2 (&s)[6], const f32x16& o, float f) {
#pragma unroll
    for (int i = 0; i < 6; ++i) s[i] = f32x2{o[2 * i], o[2 * i + 1]} * f;
}
DI void store_o(rsrc_t rs, const f32x2 (&s)[6], int rt, int br, int h, int lane) {
    const int rb = (int)OFF_O + (rt * 80 + ((br ? 0 : 4) + (h >> 3)) * 10) * 1024;
    const u32x2 t = {full(pk2(s[4][0], s[4][1])), full(pk2(s[5][0], s[5][1]))};
    __builtin_amdgcn_raw_buffer_store_b128(u32x4{full(pk2(s[0][0], s[0][1])), full(pk2(s[1][0], s[1][1])), full(pk2(s[2][0], s[2][1])), full(pk2(s[3][0], s[3][1]))},
                                           rs, lane * 16, rb + (h & 7) * 1024, SC1);
    __builtin_amdgcn_raw_buffer_store_b64(t, rs, lane < 32 ? lane * 16 : (int)0x7F000000, rb + (8 + ((h >> 2) & 1)) * 1024 + ((h >> 1) & 1) * 512 + (h & 1) * 8, SC1);
}
DI void empty_o(rsrc_t rs, int rt, int h, int lane) {
    const int rb = (int)OFF_O + (rt * 80 + (h >> 3) * 10) * 1024;
    u32x4 e = {EMPTY, EMPTY, EMPTY, EMPTY};
    __builtin_amdgcn_raw_buffer_store_b128(e, rs, lane * 16, rb + (h & 7) * 1024, SC1);
    if (lane < 32) __builtin_amdgcn_raw_buffer_store_b64(u32x2{e[0], e[1]}, rs, lane * 16, rb + (8 + ((h >> 2) & 1)) * 1024 + ((h >> 1) & 1) * 512 + (h & 1) * 8, SC1);
    asm volatile("" :: "v"(e));
}
DI void set_flag(rsrc_t rs, int off) { __builtin_amdgcn_raw_buffer_store_b32(1u, rs, 0, off, SC1); }
DI unsigned get_flag(rsrc_t rs, int voff, int off) { return __builtin_amdgcn_raw_buffer_load_b32(rs, voff, off, SC1); }
DI unsigned a_ld1(unsigned long long sb, unsigned voff) { unsigned v; asm volatile("global_load_dword %0, %1, %2 sc1" : "=v"(v) : "v"(voff), "s"(sb) : "memory"); return v; }
DI u32x4 a_ld4s(unsigned long long sb, unsigned voff) { u32x4 v; asm volatile("global_load_dwordx4 %0, %1, %2 sc1" : "=v"(v) : "v"(voff), "s"(sb) : "memory"); return v; }
template <int OFF> DI u32x4 a_ld4so(unsigned long long sb, unsigned voff) { u32x4 v;
    asm volatile("global_load_dwordx4 %0, %1, %2 offset:%3 sc1" : "=v"(v) : "v"(voff), "s"(sb), "n"(OFF) : "memory"); return v; }
template <int OFF> DI u32x4 a_ld4(unsigned long long sb, unsigned voff) { u32x4 v;
    asm volatile("global_load_dwordx4 %0, %1, %2 offset:%3" : "=v"(v) : "v"(voff), "s"(sb), "n"(OFF) : "memory"); return v; }
template <int I> struct IC { static constexpr int v = I; };
template <int I, int N, class F> DI void sfor(F&& f) { if constexpr (I < N) { f(IC<I>{}); sfor<I + 1, N>(f); } }
#define SFOR(i, a, b) sfor<a, b>([&](auto i##_) __attribute__((always_inline)) { constexpr int i = decltype(i##_)::v;
#define SEND });
template <bool FIRST> DI void mfma_ip(f32x4& c, bf16x8 a, bf16x8 b) {
    if constexpr (FIRST) asm volatile("v_mfma_f32_16x16x32_bf16 %0, %1, %2, 0" : "=&v"(c) : "v"(a), "v"(b));
    else asm volatile("v_mfma_f32_16x16x32_bf16 %0, %1, %2, %0" : "+v"(c) : "v"(a), "v"(b));
}
template <int N> DI void wait_vm(unsigned& x) { asm volatile("s_waitcnt vmcnt(%1)" : "+v"(x) : "n"(N) : "memory"); }
template <int N> DI void wait_vm(u32x4& x, u32x4& y) { asm volatile("s_waitcnt vmcnt(%2)" : "+v"(x), "+v"(y) : "n"(N) : "memory"); }
template <int K0> DI void ld_ob(u32x4 (&b)[10], unsigned vo, const unsigned long long (&ob)[2]) {
    if constexpr (K0 < 5) {
        asm volatile("global_load_dwordx4 %0, %2, %3 offset:%4 sc1\n\tglobal_load_dwordx4 %1, %2, %3 offset:%5 sc1" : "=&v"(b[2 * K0]), "=&v"(b[2 * K0 + 1])
                     : "v"(vo), "s"(ob[K0 >> 2]), "n"(K0 < 4 ? K0 * 2048 - 4096 : 0), "n"(K0 < 4 ? K0 * 2048 - 3840 : 256) : "memory");
        ld_ob<K0 + 1>(b, vo, ob);
    }
}
template <class T> DI void landed(T& v) { asm volatile("" : "+v"(v)); }
DI void wait_flag(rsrc_t rs, int voff, int off, unsigned v) {
    if (__builtin_expect(!__all(v == 1u), 0)) {
#pragma clang loop unroll(disable)
        for (int spin = 0; spin < (1 << 22); ++spin) { if (__all(get_flag(rs, voff, off) == 1u)) break; __builtin_amdgcn_s_sleep(1); }
    }
    __builtin_amdgcn_fence(__ATOMIC_ACQUIRE, "wavefront");
    asm volatile("" ::: "memory");
}

#define K2_W0 8
#define K2_BAR
#define K2_TOUCH 1
#define K2_S0 2
#define FILL_C 2
constexpr int L_SP = 136192, L_K0 = 152320, L_SQ = 153600, L_QA = 1280, L_KVF = 11520, L_WO = 13312;

__global__ __launch_bounds__(512, 2) void k2_attn(char* ws_arg, u16* out_arg) {
    extern __shared__ __attribute__((aligned(16))) char lds[];
    __attribute__((address_space(1))) char* wsg; u16* out;
    asm volatile("s_load_dwordx4 s[20:23], %2, 0x0" : "={s[20:21]}"(wsg), "={s[22:23]}"(out) : "s"(__builtin_amdgcn_kernarg_segment_ptr()));
    int bid = blockIdx.x;
    asm volatile("" : "+s"(bid));
    const int tid = threadIdx.x, lane = tid & 63, wave = rfl(tid >> 6);
    const int h = (bid & 7) * 4 + ((bid >> 3) & 3), qb = bid >> 5;
    const int oq = bid & 7, rb = oq * 4 + ((bid >> 3) & 3), cb = bid >> 5;
    const int r = lane & 31, hh = lane >> 5;
    const unsigned lds0 = (unsigned)(uintptr_t)lds;
    const int wr = (wave & 3) * 2 + (wave >> 2), rt = qb * 8 + wr;
    unsigned long long vmask = 0x000FFFFF000FFFFFull;
    const u32x4 ONES = {0x3F803F80u, 0x3F803F80u, 0x3F803F80u, 0x3F803F80u};
    const f32x16 Z16 = {};
    const int g = lane >> 4, q = (lane >> 2) & 3, p = lane & 3;
    const int tro = (g & 1) * 640 + (p & 1) * 320 + q * 16 + (p >> 1) * 8;
    const LAS char* L = (const LAS char*)lds;
    const int ent = tid < 126 ? tid : 125;
    AttnRegs ar;
    u32x2 c0, c1, c2;
    unsigned aq, ak, aqt, akt, asp, vi[6];
    u32x4 a0 = {0u, 0u, 0u, 0u}, a1 = a0, b0 = ONES, b1 = ONES; float gate;
    {
        if (tid < 12) *(LAS unsigned*)(lds + tid * 4) = 0u;
        const unsigned vc = r == 20 ? 0x3F803F80u : 0u;
        ar.O = Z16; ar.P1 = u32x4{0u, 0u, 0u, 0u}; c0 = u32x2{0x3F80u, 0u}; c1 = c0; c2 = u32x2{0u, 0u};
        ar.V0 = u32x8{vc, vc, vc, vc, vc, vc, vc, vc}; ar.V1 = ar.V0;
        ar.AD = u32x4{lds0 + tro + hh * 128, lds0 + tro + 256, lds0 + 1280 + hh * 320 + r * 16, (unsigned)lane * 16};
        aq = ar.AD[0] + L_SQ + wave * 1280; ak = ar.AD[0] + L_K0; asp = lds0 + L_SP + ((wave * 2 + hh) * 21 + r) * 48;
        aqt = hh ? lds0 + 16 : ar.AD[1] + L_SQ + wave * 1280; akt = ar.AD[1] + L_K0;
        asm volatile("" : "+{v[64:79]}"(ar.O), "+{v[86:87]}"(c0), "+{v[88:95]}"(ar.V0), "+{v[102:103]}"(c1), "+{v[104:111]}"(ar.V1),
                          "+{v[116:119]}"(ar.P1), "+{v[128:131]}"(ar.AD), "+{v[126:127]}"(c2),
                          "+v"(aq), "+v"(ak), "+v"(aqt), "+v"(akt), "+v"(asp), "+{s[72:73]}"(vmask));
        const unsigned hb = h * 163840 + lane * 16;
        unsigned vq = (unsigned)OFF_IQ + hb + rt * 2560, vk = (unsigned)OFF_IB + hb + (wave & 1) * 256;
        unsigned va = (unsigned)OFF_IA + h * 163840 + rt * 2560 + hh * 320 + r * 16, vg = (unsigned)OFF_G + rt * 128 + r * 4;
        unsigned mq = lds0 + L_SQ + wave * 1280, mk = lds0 + L_K0 + (wave & 1) * 256, mi = lds0 + (1 + wave) * 1024;
#pragma unroll
        for (int i = 0; i < 6; ++i) vi[i] = (unsigned)OFF_IB + hb + (1 + i * 8 + wave) * 1024;
        asm volatile("" : "+v"(vq), "+v"(vk), "+v"(va), "+v"(vg), "+v"(vi[0]), "+v"(vi[1]), "+v"(vi[2]), "+v"(vi[3]), "+v"(vi[4]), "+v"(vi[5]),
                          "+v"(a0), "+v"(a1), "+v"(b0), "+v"(b1), "+s"(mq), "+s"(mk), "+s"(mi));
        asm volatile("s_mov_b32 m0, %14\n\ts_waitcnt lgkmcnt(0)\n\tglobal_load_lds_dwordx4 %7, %5\n\tglobal_load_lds_dwordx4 %7, %5 offset:256\n\t"
                     "s_mov_b32 m0, %15\n\ts_nop 0\n\tglobal_load_lds_dwordx4 %8, %5\n\t"
                     "s_mov_b32 m0, %16\n\ts_nop 0\n\tglobal_load_lds_dwordx4 %11, %5\n\t"
                     "s_add_u32 m0, m0, 0x2000\n\ts_nop 0\n\tglobal_load_lds_dwordx4 %12, %5\n\t"
                     "s_add_u32 m0, m0, 0x2000\n\ts_nop 0\n\tglobal_load_lds_dwordx4 %13, %5\n\t"
                     "global_load_dword %4, %10, %5\n\ts_mov_b64 exec, %17\n\t"
                     "global_load_dwordx4 %0, %9, %5\n\tglobal_load_dwordx4 %1, %9, %5 offset:640\n\tglobal_load_dwordx4 %2, %9, %5 offset:1280\n\t"
                     "global_load_dwordx4 %3, %9, %5 offset:1920\n\ts_mov_b64 exec, -1"
                     : "+v"(a0), "+v"(a1), "+v"(b0), "+v"(b1), "=&v"(gate), "+{s[20:21]}"(wsg), "+{s[22:23]}"(out)
                     : "v"(vq), "v"(vk), "v"(va), "v"(vg), "v"(vi[0]), "v"(vi[1]), "v"(vi[2]), "s"(mq), "s"(mk), "s"(mi), "s"(vmask) : "memory", "scc");
    }
    char* const ws = (char*)wsg;
    const rsrc_t rs = __builtin_amdgcn_make_buffer_rsrc((void*)ws, 0, WS_USED, 0x00020000);
    const char* kvb = ws + OFF_IB + (size_t)h * 163840;
    const char* qim = ws + OFF_IQ + (size_t)h * 163840 + qb * 20480;
    const int j = wave & 3, isE = (wave >> 2) ^ 1;
    const char* wop = ws + OFF_WOT + (size_t)((cb * 4 + j) * 50) * 1024;
    const int flA = (int)OFF_FL + (oq * 4 + j) * 128, flv = (lane & 7) * 16;
    const int ov = ((g >> 1) * 1024 + (g & 1) * 512 + (lane & 15) * 16), os = (int)OFF_O + (rb * 160 + j * 10) * 1024;
    unsigned fl = 0u;
    u32x4 kp[8], wa[20], aa[2][10];
    const unsigned long long wsu = uni64(ws);
    unsigned long long kb = uni64(ws + OFF_KVP + 4096 + h * 16384), kb2 = uni64(ws + OFF_KVP + 12288 + h * 16384);
    unsigned long long ob[2][2] = {{uni64(ws + os + 4096), uni64(ws + os + 8192)}, {uni64(ws + os + 81920 + 4096), uni64(ws + os + 81920 + 8192)}};
#define LD_H(A, vo, hf, en, C) asm volatile("s_cmp_lg_u32 %13, 0\n\ts_cbranch_scc0 .Lsk%=\n\t" \
        "global_load_dwordx4 %0, %10, %11 offset:-4096 sc1\n\tglobal_load_dwordx4 %1, %10, %11 offset:-3840 sc1\n\t" \
        "global_load_dwordx4 %2, %10, %11 offset:-2048 sc1\n\tglobal_load_dwordx4 %3, %10, %11 offset:-1792 sc1\n\t" \
        "global_load_dwordx4 %4, %10, %11 sc1\n\tglobal_load_dwordx4 %5, %10, %11 offset:256 sc1\n\t" \
        "global_load_dwordx4 %6, %10, %11 offset:2048 sc1\n\tglobal_load_dwordx4 %7, %10, %11 offset:2304 sc1\n\t" \
        "global_load_dwordx4 %8, %10, %12 sc1\n\tglobal_load_dwordx4 %9, %10, %12 offset:256 sc1\n.Lsk%=:" \
        : C(A[0]), C(A[1]), C(A[2]), C(A[3]), C(A[4]), C(A[5]), C(A[6]), C(A[7]), C(A[8]), C(A[9]) \
        : "v"(vo), "s"(ob[hf][0]), "s"(ob[hf][1]), "s"(en) : "memory", "scc");
#define LD_W4(A, f) asm volatile("s_cmp_lg_u32 %6, 0\n\ts_cbranch_scc0 .Lsk%=\n\t" \
        "global_load_dwordx4 %0, %4, %5\n\tglobal_load_dwordx4 %1, %4, %5 offset:1024\n\t" \
        "global_load_dwordx4 %2, %4, %5 offset:2048\n\tglobal_load_dwordx4 %3, %4, %5 offset:3072\n.Lsk%=:" \
        : "=&v"((A)[0]), "=&v"((A)[1]), "=&v"((A)[2]), "=&v"((A)[3]) : "v"(lane * 16), "s"(uni64(wop + (f) * 1024)), "s"(isE) : "memory", "scc");
#define LD_KP(TAIL) asm volatile("s_cmp_lt_u32 %11, 2\n\ts_cbranch_scc0 .Lsk%=\n\t" \
        "global_load_dwordx4 %0, %8, %9 offset:-4096 sc1\n\tglobal_load_dwordx4 %1, %8, %9 offset:-2048 sc1\n\t" \
        "global_load_dwordx4 %2, %8, %9 sc1\n\tglobal_load_dwordx4 %3, %8, %9 offset:2048 sc1\n\t" \
        "global_load_dwordx4 %4, %8, %10 offset:-4096 sc1\n\tglobal_load_dwordx4 %5, %8, %10 offset:-2048 sc1\n\t" \
        "global_load_dwordx4 %6, %8, %10 sc1\n\tglobal_load_dwordx4 %7, %8, %10 offset:2048 sc1\n" \
        ".Lsk%=:" TAIL \
        : "=&v"(kp[0]), "=&v"(kp[1]), "=&v"(kp[2]), "=&v"(kp[3]), "=&v"(kp[4]), "=&v"(kp[5]), "=&v"(kp[6]), "=&v"(kp[7]) \
        : "v"(ent * 16), "s"(kb), "s"(kb2), "s"(wave) : "memory", "scc")
    asm volatile("" : "+s"(kb), "+s"(kb2), "+s"(ob[0][0]), "+s"(ob[0][1]), "+s"(ob[1][0]), "+s"(ob[1][1]));
    unsigned ma0 = rfl(lds0 + 50176 + wave * 3072), mb0 = rfl(ma0 + 24576); unsigned long long ga0 = uni64(kvb + 50176 + wave * 3072), gb0 = ga0 + 24576;
    asm volatile("" : "+{s[74:75]}"(ga0), "+{s[76:77]}"(gb0), "+{s78}"(ma0), "+{s79}"(mb0));
    asm volatile("s_mov_b32 m0, %4\n\ts_nop 0\n\tglobal_load_lds_dwordx4 %0, %3\n\t"
                 "s_add_u32 m0, m0, 0x2000\n\ts_nop 0\n\tglobal_load_lds_dwordx4 %1, %3\n\t"
                 "s_add_u32 m0, m0, 0x2000\n\ts_nop 0\n\tglobal_load_lds_dwordx4 %2, %3"
                 :: "v"(vi[3]), "v"(vi[4]), "v"(vi[5]), "s"(ws), "s"(lds0 + (25 + wave) * 1024) : "memory", "scc");
    wait_vm_bar<K2_W0>();
    {
        f32x16 kvacc;
        u32x2 t0, t1, t2;
        asm volatile("ds_read_b64_tr_b16 v[96:97], %15\n\tds_read_b64_tr_b16 v[98:99], %15 offset:64\n\t"
                     "ds_read_b64_tr_b16 v[120:121], %14\n\tds_read_b64_tr_b16 v[122:123], %14 offset:64\n\t"
                     "ds_read_b64_tr_b16 v[124:125], %16\n\tds_read_b64_tr_b16 v[100:101], %17\n\t"
                     "ds_read_b64_tr_b16 v[84:85], v129 offset:2560\n\ts_mov_b64 exec, %19\n\tds_read_b128 v[92:95], v130\n\ts_mov_b64 exec, -1\n\t"
                     "ds_read_b64_tr_b16 v[80:81], v128 offset:2560\n\tds_read_b64_tr_b16 v[82:83], v128 offset:2624\n\ts_waitcnt lgkmcnt(6)\n\t"
                     "v_mfma_f32_32x32x16_bf16 v[32:47], v[96:99], v[120:123], 0\n\ts_waitcnt lgkmcnt(4)\n\t"
                     "v_mfma_f32_32x32x16_bf16 v[48:63], v[100:103], v[124:127], v[32:47]\n\t"
                     "s_waitcnt vmcnt(3)\n\t" K2_BAR
                     "v_mfma_f32_32x32x16_bf16 %8, %9, %11, 0\n\tv_mfma_f32_32x32x16_bf16 %8, %10, %12, %8\n\ts_nop 7\n\ts_nop 0\n\t"
                     "v_max3_f32 v96, v48, v49, v50\n\tv_max3_f32 v97, v51, v52, v53\n\tv_max3_f32 v96, v96, v54, v55\n\tv_max3_f32 v97, v97, v56, v57\n\t"
                     "v_max3_f32 v96, v96, v58, v59\n\tv_max3_f32 v97, v97, v60, v61\n\tv_max3_f32 v96, v96, v62, v63\n\tv_max_f32 v96, v96, v97\n\t"
                     "v_mov_b32 v97, v96\n\ts_nop 1\n\tv_permlane32_swap_b32 v96, v97\n\tv_max_f32 v96, v96, v97\n\t"
                     "s_mov_b32 exec_hi, 0\n\tv_cvt_pk_bf16_f32 v126, -v96, 0\n\ts_mov_b32 exec_hi, -1\n\ts_nop 0\n\t"
                     "v_mfma_f32_32x32x16_bf16 v[32:47], v[100:103], v[124:127], v[32:47]"
                     : "=&{v[32:47]}"(ar.S), "=&{v[120:123]}"(ar.QM), "=&{v[124:125]}"(t2), "+{v[126:127]}"(c2), "=&{v[100:101]}"(t1), "=&{v[84:85]}"(t0),
                       "+{v[88:95]}"(ar.V0), "=&{v[80:83]}"(ar.KM), "=&v"(kvacc), "+v"(a0), "+v"(a1), "+v"(b0), "+v"(b1), "+v"(gate)
                     : "v"(aq), "v"(ak), "v"(aqt), "v"(akt), "{v[128:131]}"(ar.AD), "s"(vmask), "{v[102:103]}"(c1)
                     : "memory", "v48", "v49", "v50", "v51", "v52", "v53", "v54", "v55", "v56", "v57", "v58", "v59", "v60", "v61", "v62", "v63",
                       "v96", "v97", "v98", "v99");
        ar.KT0 = u32x4{t0[0], t0[1], c0[0], c0[1]}; ar.KT1 = u32x4{t1[0], t1[1], c1[0], c1[1]}; ar.QT = u32x4{t2[0], t2[1], c2[0], c2[1]};
        if (r <= 20) {
            LAS f32x4* dst = (LAS f32x4*)(uintptr_t)asp;
            dst[0] = f32x4{kvacc[0], kvacc[1], kvacc[2], kvacc[3]};
            dst[1] = f32x4{kvacc[4], kvacc[5], kvacc[6], kvacc[7]};
            dst[2] = f32x4{kvacc[8], kvacc[9], kvacc[10], kvacc[11]};
        }
    }
#pragma unroll
    for (int c = 0; c < 8; ++c) {
        unsigned ma, mb; unsigned long long ga, gb;
        if (c < 3) {
            const int lim = (c == 1 ? L_SP : 163840) - 3072;
            const int oa = (c == 2 ? L_SP : 50176 + c * 49152) + wave * 3072, ob = oa + 24576 < lim ? oa + 24576 : lim;
            ma = lds0 + oa; mb = lds0 + ob; ga = uni64(kvb + oa); gb = uni64(kvb + ob);
        } else {
            ma = lds0 + (c == 7 ? 1024 + j * 3072 : L_WO + ((c - 3) * 8 + wr) * 3072); mb = ma;
            ga = uni64(wop + (c == 7 ? 44 : 20 + (wave >> 2) * 12 + 3 * (c - 3)) * 1024); gb = ga;
        }
        CSTAMP(2 * c);
        unsigned cnt;
        if (c == 0) cnt = attn_chunk<true>(ar, vmask, ma0, ga0, mb0, gb0, 1u);
        else cnt = attn_chunk<false>(ar, vmask, rfl(ma), ga, rfl(mb), gb, c < 3 ? 1u : c < 7 ? 0u : (unsigned)(2 - 2 * isE));
        CSTAMP(2 * c + 1);
        if (c < 7) wait_cnt(lds, 0, cnt, 8 * (c + 1));
        if (c == FILL_C && c != 1) empty_o(rs, rt, h, lane);
        if (c == 0) {
            if (wave < 2) {
                f32x4 s = *(const LAS f32x4*)(L + L_SP + ent * 16);
#pragma unroll
                for (int w = 1; w < 8; ++w) s += *(const LAS f32x4*)(L + L_SP + w * 2016 + ent * 16);
                const u32x4 v = __builtin_bit_cast(u32x4, s);
                if (tid < 126) __builtin_amdgcn_raw_buffer_store_b128(u32x4{full(v[0]), full(v[1]), full(v[2]), full(v[3])}, rs, tid * 16, (int)OFF_KVP + (h * 8 + qb) * 2048, SC1);
            }
            glds16(qim + wr * 2560 + 1280, lane * 16, lds0 + L_QA + wave * 1280);
            glds16(qim + wr * 2560 + 1280 + 256, lane * 16, lds0 + L_QA + wave * 1280 + 256);
        } else if (c == 1) {
            LD_KP("");
            if (FILL_C == 1) empty_o(rs, rt, h, lane);
        } else if (c == 2) {
#pragma unroll
            for (int j = 0; j < 8; ++j) landed(kp[j]);
            if (wave < 2) {
#pragma clang loop unroll(disable)
                for (int spin = 0; spin < (1 << 20); ++spin) {
                    unsigned m = 0u;
#pragma unroll
                    for (int j = 0; j < 8; ++j) m = umax4(m, kp[j]);
                    if (__builtin_expect(__all(m != EMPTY), 1)) break;
                    LD_KP("\n\ts_waitcnt vmcnt(0)");
                }
                f32x4 s = __builtin_bit_cast(f32x4, kp[0]);
#pragma unroll
                for (int j = 1; j < 8; ++j) s += __builtin_bit_cast(f32x4, kp[j]);
                const int e3 = ent / 3;
                *(LAS u32x2*)(lds + L_KVF + e3 * 32 + (ent - e3 * 3) * 8) = u32x2{pk2(s[0], s[1]), pk2(s[2], s[3])};
            }
        } else if (c == 3) {
            const int rr = r <= 20 ? r : 20;
            const u32x4 ka0 = *(const LAS u32x4*)(L + L_KVF + (hh * 21 + rr) * 32);
            const u32x2 ka1 = *(const LAS u32x2*)(L + L_KVF + (hh * 21 + rr) * 32 + 16);
            const LAS char* qa = L + L_QA + wave * 1280 + tro;
            const u32x2 pq0 = trd(qa + hh * 64), pq1 = trd(qa + 128 + hh * 64);
            u32x2 pq2 = trd(qa + 256);
            if (hh) pq2 = u32x2{0u, 0u};
            f32x16 oa = MFMA32(B8(ka0), B8(((u32x4){pq0[0], pq0[1], pq1[0], pq1[1]})), Z16);
            oa = MFMA32(B8(((u32x4){ka1[0], ka1[1], 0u, 0u})), B8(((u32x4){pq2[0], pq2[1], 0u, 0u})), oa);
            const float den = hi_half(oa[8]) + 1e-6f;
            f32x2 sv[6];
            scale_o(sv, oa, gate * __builtin_amdgcn_rcpf(den));
            store_o(rs, sv, rt, 0, h, lane);
            LD_W4(wa, 0) LD_W4(wa + 4, 4)
        } else if (c == 4) {
            if (tid == 0) set_flag(rs, (int)OFF_FL + (qb * 4 + (h >> 3)) * 128 + (h & 7) * 16);
            LD_W4(wa + 8, 8) LD_W4(wa + 12, 12)
        } else if (c == 5) {
            fl = a_ld1(isE ? wsu : uni64(out), isE ? flA + flv : (rb * 64 + lane) * (D_ * 2) + cb * 160);
            LD_W4(wa + 16, 16)
        } else if (c == 6) {
            landed(fl);
            if (wave < 4) wait_flag(rs, flv, flA, fl);
            LD_H(aa[0], ov + 40960, 0, isE, "=&v")
        }
    }
#pragma unroll
    for (int i = 0; i < 10; ++i) { landed(wa[i]); landed(wa[10 + i]); landed(aa[0][i]); }
    u32x4 wd[4];
    f32x16 O = MFMA32(B8(((u32x4){ar.V0[0], ar.V0[1], ar.V0[2], ar.V0[3]})), B8(ar.P1), ar.O);
    landed(O);
    if (lane == 0) set_flag(rs, (int)OFF_FL + (32 + rt * 4 + (h >> 3)) * 128 + (h & 7) * 16);
    LD_H(aa[1], ov + 40960, 1, isE, "=&v")
    WSTAMP(2);
    f32x2 sv[6];
    {
        const float l = hi_half(O[8]);
        scale_o(sv, O, (1.f - gate) * __builtin_amdgcn_rcpf(l));
        const f32x2 t = ((sv[0] + sv[1]) + (sv[2] + sv[3])) + (sv[4] + sv[5]);
        const unsigned long long ok = __builtin_amdgcn_ballot_w64(l >= 1e-18f) & __builtin_amdgcn_ballot_w64(l <= 1.0e30f) &
                                      __builtin_amdgcn_ballot_w64(fabsf(t[0] + t[1]) <= 3.0e38f);
        if (__builtin_expect(ok != ~0ull, 0)) {
            float m = -INFINITY; O = Z16;
            int tl = tid;
            asm volatile("" : "+v"(tl));
            const int r = tl & 31, hh = (tl >> 5) & 1, rc = r < 20 ? r : 19;
            const bf16x8 qm = B8(ar.QM), qt0 = B8(((u32x4){ar.QT[0], ar.QT[1], 0u, 0u}));
            const int ko = (r >> 4) * 640 + ((r >> 2) & 1) * 320 + ((r >> 3) & 1) * 8 + (r & 3) * 2;
            for (int t = 0; t < 64; ++t) {
                const char* tb = kvb + t * 2560;
                unsigned kw[6];
#pragma unroll
                for (int i = 0; i < 6; ++i) {
                    const int d = i < 4 ? 8 * hh + 2 * i : 16 + 2 * (i - 4);
                    kw[i] = (unsigned)*(const u16*)(tb + ko + d * 16) | ((unsigned)*(const u16*)(tb + ko + d * 16 + 16) << 16);
                }
                u32x4 f0 = *(const u32x4*)(tb + 1280 + hh * 320 + rc * 16), f1 = *(const u32x4*)(tb + 1920 + hh * 320 + rc * 16);
                if (r >= 20) { f0 = ONES; f1 = ONES; }
                f32x16 s = MFMA32(B8(((u32x4){kw[0], kw[1], kw[2], kw[3]})), qm, Z16);
                s = MFMA32(B8(((u32x4){kw[4], kw[5], 0u, 0u})), qt0, s);
                float tm = s[0];
#pragma unroll
                for (int i = 1; i < 16; ++i) tm = fmaxf(tm, s[i]);
                tm = xhalf_max(tm);
                const float mn = fmaxf(m, tm);
                const float al = (m == -INFINITY) ? 0.f : __builtin_amdgcn_exp2f(m - mn);
                m = mn;
#pragma unroll
                for (int i = 0; i < 16; ++i) { O[i] *= al; s[i] = __builtin_amdgcn_exp2f(s[i] - mn); }
                u32x4 p0 = {pk2(s[0], s[1]), pk2(s[2], s[3]), pk2(s[4], s[5]), pk2(s[6], s[7])};
                u32x4 p1 = {pk2(s[8], s[9]), pk2(s[10], s[11]), pk2(s[12], s[13]), pk2(s[14], s[15])};
                O = MFMA32(B8(f0), B8(p0), O); O = MFMA32(B8(f1), B8(p1), O);
            }
            scale_o(sv, O, (1.f - gate) * __builtin_amdgcn_rcpf(hi_half(O[8])));
        }
    }
    store_o(rs, sv, rt, 1, h, lane);
    __builtin_amdgcn_s_setprio(0);
    __builtin_amdgcn_sched_barrier(0);
    WSTAMP(3);
    constexpr int L_P0 = L_WO + 98304;
    if (wave < 4) {
        const LAS char* wl[3] = {L + L_WO + j * 6144 + lane * 16, L + L_WO + 49152 + j * 6144 + lane * 16, L + 1024 + j * 3072 + lane * 16};
        LAS char* wp[2] = {(LAS char*)lds + (lane & 15) * 320 + ((g ^ ((lane >> 1) & 3)) << 4) + L_P0 + j * 10240, nullptr};
        wp[1] = wp[0] + (L_WO - L_P0 - j * 4096);
        unsigned fa[2] = {(unsigned)OFF_FL + (32 + rb * 8 + j) * 128 + flv, 0u};
        fa[1] = fa[0] + 512;
        landed(wl[0]); landed(wl[1]); landed(wl[2]); landed(wp[0]); landed(wp[1]); landed(fa[0]); landed(fa[1]);
        auto wfr = [&](int f) -> bf16x8 {
            if (f < 20) return B8(wa[f]);
            if (f < 44) return *(const LAS bf16x8*)(wl[((f - 20) % 12) / 6] + ((((f - 20) % 12) / 3) & 1) * 24576 + ((f - 20) / 12) * 3072 + ((f - 20) % 3) * 1024);
            if (f < 46) return *(const LAS bf16x8*)(wl[2] + (f - 44) * 1024);
            return B8(wd[f - 46]);
        };
        f32x4 acc[4][5];
        u32x4 b[2][10];
        bf16x8 wb[30];
        wait_cnt(lds + j * 4, 32, 1);
        fl = a_ld1(wsu, fa[0]);
        asm volatile("s_waitcnt vmcnt(3)" : "+v"(aa[1][0]), "+v"(aa[1][1]), "+v"(aa[1][2]), "+v"(aa[1][3]), "+v"(aa[1][4]), "+v"(aa[1][5]), "+v"(aa[1][6]),
                     "+v"(aa[1][7]), "+v"(aa[1][8]), "+v"(aa[1][9]) :: "memory");
        SB0();
        unsigned c64 = 0u, m = 0u;
        const unsigned l16 = lane * 16;
        const unsigned long long wdp = uni64(wop + 46 * 1024);
        SFOR(st, 0, 15)
            constexpr int v = st - 4, vh = v < 5 ? 0 : 1, vk = v < 0 ? 0 : v % 5, n = st < 5 ? 20 : 10;
            if constexpr (st == K2_S0) {
                wait_vm<0>(fl);
                wait_flag(rs, fa[0], 0, fl);
                WSTAMP(4);
                fl = a_ld1(wsu, fa[1]);
                wd[0] = a_ld4<0>(wdp, l16); wd[1] = a_ld4<1024>(wdp, l16); wd[2] = a_ld4<2048>(wdp, l16); wd[3] = a_ld4<3072>(wdp, l16);
                ld_ob<0>(b[0], ov, ob[0]);
                SB0();
            }
            if constexpr (st == 4) {
                wait_vm<14>(fl);
                wait_flag(rs, fa[1], 0, fl);
                WSTAMP(5);
                ld_ob<0>(b[1], ov, ob[1]);
                SB0();
            }
            if constexpr (st == 5) { landed(wd[0]); landed(wd[1]); landed(wd[2]); landed(wd[3]); }
            if constexpr (st == 10) wait_cnt(lds, 0, c64, 64);
            SFOR(i, 0, n)
                constexpr int ni = i % 5, mi = i / 5;
                if constexpr (st < 4) mfma_ip<st == 0>(acc[mi][ni], B8(wa[st * 5 + ni]), B8(aa[mi >> 1][2 * st + (mi & 1)]));
                else if constexpr (st == 4) mfma_ip<false>(acc[mi][ni], wb[ni], B8(aa[mi >> 1][2 * st + (mi & 1)]));
                else mfma_ip<false>(acc[(st / 5 - 1) * 2 + mi][ni], wb[5 + (st % 5) * 5 + ni], B8(b[st / 5 - 1][2 * (st % 5) + mi]));
                SB0();
                if constexpr (st == 3 && i >= 10 && i < 15) wb[i - 10] = wfr(10 + i);
                if constexpr (st == 4 && i >= 8 && i < 13) wb[i - 3] = wfr(17 + i);
                if constexpr (st >= 5 && st < 9 && i < 5) wb[(st - 3) * 5 + i] = wfr(20 + (st - 3) * 5 + i);
                if constexpr (st == 9 && i == 0) c64 = *(volatile LAS unsigned*)lds;
                if constexpr ((st == 10 || st == 11) && i < 5) *(LAS f32x4*)(wp[0] + (st - 10) * 5120 + i * 64) = acc[st - 10][i];
                if constexpr (st == 12 && i == 4) add_cnt<12>(lds0);
                if constexpr (v >= 0 && v < 10) {
                    u32x4 &x = b[vh][2 * vk], &y = b[vh][2 * vk + 1];
                    if constexpr (i == n - 5) { wait_vm<18 - 2 * v>(x, y); m = max(max(x[0], x[1]), x[2]); }
                    if constexpr (i == n - 4) m = max(max(m, x[3]), y[0]);
                    if constexpr (i == n - 3) m = max(max(m, y[1]), y[2]);
                    if constexpr (i == n - 2) m = max(m, y[3]);
                }
                SB0();
            SEND
            if constexpr (v >= 0 && v < 10) if (__builtin_expect(!__all(m != EMPTY), 0)) {
#pragma clang loop unroll(disable)
                for (int spin = 0; spin < (1 << 20); ++spin) {
                    ld_ob<vk>(b[vh], ov, ob[vh]);
                    wait_vm<0>(b[vh][2 * vk], b[vh][2 * vk + 1]);
                    if (__all(umax4(umax4(0u, b[vh][2 * vk]), b[vh][2 * vk + 1]) != EMPTY)) break;
                }
            }
        SEND
#pragma unroll
        for (int mi = 0; mi < 2; ++mi)
#pragma unroll
            for (int ni = 0; ni < 5; ++ni) { *(LAS f32x4*)(wp[1] + mi * 24576 + ni * 64) = acc[2 + mi][ni]; SB0(); }
        WSTAMP(6);
        asm volatile("s_waitcnt lgkmcnt(0)\n\ts_barrier" ::: "memory");
    } else {
        add_cnt<32>(lds0 + j * 4);
        const int t = tid - 256;
        const LAS char* rp[2][3]; unsigned oo[3];
#pragma unroll
        for (int i = 0; i < 3; ++i) {
            const int id = i < 2 || t < 128 ? i * 256 + t : 1, row = id / 20, cs = id - row * 20, cc = cs ^ ((row >> 1) & 3);
            rp[0][i] = L + L_P0 + id * 16; rp[1][i] = L + L_WO + id * 16 + (id >= 320 ? 24576 - 5120 : 0);
            oo[i] = (unsigned)(((rb * 64 + row) * D_ + cb * 80 + cc * 4) * 2);
            landed(rp[0][i]); landed(rp[1][i]); landed(oo[i]);
        }
        WSTAMP(4);
#pragma unroll
        for (int hf = 0; hf < 2; ++hf) {
            if (hf == 0) { wait_cnt(lds, 12, 4); WSTAMP(5); }
            else { WSTAMP(6); asm volatile("s_barrier" ::: "memory"); }
            f32x4 pv[3][4];
#pragma unroll
            for (int i = 0; i < 3; ++i)
#pragma unroll
                for (int w = 0; w < 4; ++w) pv[i][w] = *(const LAS f32x4*)(rp[hf][i] + w * (hf ? 6144 : 10240));
            __builtin_amdgcn_sched_barrier(0);
#pragma unroll
            for (int i = 0; i < 3; ++i) {
                const f32x4 v = (pv[i][0] + pv[i][1]) + (pv[i][2] + pv[i][3]);
                if (i < 2 || t < 128) __builtin_amdgcn_raw_buffer_store_b64(u32x2{pk2(v[0], v[1]), pk2(v[2], v[3])},
                    __builtin_amdgcn_make_buffer_rsrc((void*)uni64(out), 0, S_ * D_ * 2, 0x00020000), oo[i], hf * (32 * D_ * 2), OUT_AUX);
            }
        }
    }
}

__attribute__((used, noinline)) __device__ void k3_mark() { asm volatile(".Lk2_last:"); }

extern "C" void kernel_launch(void* const* d_in, const int* in_sizes, int n_in, void* d_out, int out_size, void* d_ws, size_t ws_size,
                              hipStream_t stream) {
    const u16* x = (const u16*)d_in[0];
    const u16* wA = (const u16*)d_in[1];
    const u16* woA = (const u16*)d_in[2];
    const u16* wB = (const u16*)d_in[4];
    const u16* woB = (const u16*)d_in[5];
    const u16* gw = (const u16*)d_in[6];
    char* ws = (char*)d_ws;
    (void)hipFuncSetAttribute((const void*)k1_qkv, hipFuncAttributeMaxDynamicSharedMemorySize, LDS_BYTES);
    (void)hipFuncSetAttribute((const void*)k2_attn, hipFuncAttributeMaxDynamicSharedMemorySize, LDS_BYTES);
    k1_qkv<<<256, 512, LDS_BYTES, stream>>>(x, wA, wB, woA, woB, gw, ws);
    k2_attn<<<256, 512, LDS_BYTES, stream>>>(ws, (u16*)d_out);
}
```

```cpp
#define G1_MAP 1
#define OUT_AUX 16
#include <hip/hip_runtime.h>
#include <hip/hip_bf16.h>
#include <stdint.h>

typedef short    bf16x8 __attribute__((ext_vector_type(8)));
typedef short    s16x4  __attribute__((ext_vector_type(4)));
typedef float    f32x2  __attribute__((ext_vector_type(2)));
typedef float    f32x4  __attribute__((ext_vector_type(4)));
typedef float    f32x16 __attribute__((ext_vector_type(16)));
typedef float    f32x32 __attribute__((ext_vector_type(32)));
typedef unsigned u32x2  __attribute__((ext_vector_type(2)));
typedef unsigned u32x4  __attribute__((ext_vector_type(4)));
typedef unsigned u32x8  __attribute__((ext_vector_type(8)));
typedef unsigned u32x32 __attribute__((ext_vector_type(32)));
typedef __bf16   bf2_t  __attribute__((ext_vector_type(2)));
typedef unsigned short u16;
#define LAS __attribute__((address_space(3)))
#define DI __device__ __forceinline__

constexpr int S_ = 2048, D_ = 640;
constexpr size_t IMG     = (size_t)32 * 163840;
constexpr size_t OFF_IQ  = 0;
constexpr size_t OFF_IB  = IMG;
constexpr size_t OFF_IA  = 2 * IMG;
constexpr size_t OFF_O   = 3 * IMG;
constexpr size_t OFF_WOT = 4 * IMG;
constexpr size_t OFF_G   = OFF_WOT + (size_t)1600 * 1024;
constexpr size_t OFF_KVP = OFF_G + 8192;
constexpr size_t OFF_FL  = OFF_KVP + (size_t)256 * 2048;
constexpr int    WS_USED = (int)(OFF_FL + 288 * 128);
constexpr unsigned EMPTY = 0xFFFFFFFFu;
typedef __amdgpu_buffer_rsrc_t rsrc_t;
constexpr int SC1 = 16;

#define CSTAMP(i)
#define PSTAMP(k, i)
#define WSTAMP(i)

DI unsigned pk2(float lo, float hi) { f32x2 v = {lo, hi}; return __builtin_bit_cast(unsigned, __builtin_convertvector(v, bf2_t)); }
DI float bflo(unsigned v) { return __uint_as_float(v << 16); }
DI float bfhi(unsigned v) { return __uint_as_float(v & 0xffff0000u); }
DI int rfl(int x) { return __builtin_amdgcn_readfirstlane(x); }
DI void glds16(const void* sbase, unsigned voff, unsigned lds_base) {
    const unsigned long long a = (unsigned long long)(uintptr_t)sbase;
    const unsigned long long u = (unsigned long long)(unsigned)rfl((int)(unsigned)a) | ((unsigned long long)(unsigned)rfl((int)(unsigned)(a >> 32)) << 32);
    asm volatile("s_mov_b32 m0, %0\n\ts_nop 0\n\tglobal_load_lds_dwordx4 %1, %2" :: "s"(rfl((int)lds_base)), "v"(voff), "s"(u) : "memory");
}
#define MFMA16(a, b, c) __builtin_amdgcn_mfma_f32_16x16x32_bf16((a), (b), (c), 0, 0, 0)
#define MFMA32(a, b, c) __builtin_amdgcn_mfma_f32_32x32x16_bf16((a), (b), (c), 0, 0, 0)
#define B8(x) __builtin_bit_cast(bf16x8, x)
DI u32x2 trd(const LAS char* p) { return __builtin_bit_cast(u32x2, __builtin_amdgcn_ds_read_tr16_b64_v4i16((LAS s16x4*)p)); }
DI float pmax(float t, float c) { return __int_as_float(max(__float_as_int(t), __float_as_int(c))); }
template <int N> DI void wait_vm_bar() { asm volatile("s_waitcnt vmcnt(%0) lgkmcnt(0)\n\ts_barrier" :: "n"(N) : "memory"); }

constexpr int LDS_BYTES = 163840;
constexpr float LOG2E = 1.4426950408889634f;

DI void p1_aux(const u16* __restrict__ x, const u16* __restrict__ woA, const u16* __restrict__ woB, const u16* __restrict__ gate_w,
               char* __restrict__ ws, char* lds, int a) {
    const int tid = threadIdx.x, lane = tid & 63, wave = rfl(tid >> 6);
    const unsigned lds0 = (unsigned)(uintptr_t)lds;
    const int br = a >> 3, u = a & 7;
    if (tid < 144) *(u32x4*)(ws + OFF_FL + (a * 144 + tid) * 16) = u32x4{0u, 0u, 0u, 0u};
    const char* wsrc = (const char*)(br ? woB : woA) + (size_t)u * 102400;
    for (int i = wave; i < 100; i += 8) glds16(wsrc + i * 1024, lane * 16, lds0 + i * 1024);
    const u32x4* xs = (const u32x4*)(x + (size_t)(a * 128 + wave * 16) * D_);
    u32x4 xv[20], gw[5];
#pragma unroll
    for (int i = 0; i < 20; ++i) xv[i] = xs[i * 64 + lane];
#pragma unroll
    for (int i = 0; i < 5; ++i) gw[i] = ((const u32x4*)gate_w)[(i * 64 + lane) % 80];
    {
        const rsrc_t rs = __builtin_amdgcn_make_buffer_rsrc((void*)(ws + OFF_KVP + a * 32768), 0, 32768, 0x00020000);
#pragma unroll
        for (int i = 0; i < 4; ++i) __builtin_amdgcn_raw_buffer_store_b128(u32x4{EMPTY, EMPTY, EMPTY, EMPTY}, rs, tid * 16 + i * 8192, 0, SC1);
    }
    LAS float* part = (LAS float*)(lds + 102400) + wave * 1280;
#pragma unroll
    for (int i = 0; i < 20; ++i) {
        float s = 0.f;
#pragma unroll
        for (int j = 0; j < 4; ++j) s += bflo(xv[i][j]) * bflo(gw[i % 5][j]) + bfhi(xv[i][j]) * bfhi(gw[i % 5][j]);
        part[i * 64 + lane] = s;
    }
    wait_vm_bar<0>();
    {
        float s = 0.f;
#pragma unroll
        for (int j = 0; j < 20; ++j) s += part[lane * 20 + j];
        s += __shfl_xor(s, 1); s += __shfl_xor(s, 2);
        if ((lane & 3) == 0) ((float*)(ws + OFF_G))[a * 128 + wave * 16 + (lane >> 2)] = 1.f / (1.f + __expf(-s));
    }
    const int g = lane >> 4, hh = g & 1, ns = g >> 1, q = (lane >> 2) & 3, p = lane & 3;
    for (int un = wave; un < 100; un += 8) {
        const int e = un / 20, np = un - e * 20, nt = np * 2 + ns;
        const int r0 = e < 4 ? e * 20 + 4 * hh : (2 * hh) * 20 + 16;
        const int r1 = e < 4 ? r0 + 8 : r0 + 20;
        const int ks16 = e < 4 ? (u & 1) * 4 + e : 8 + (u & 1), cbk = nt / 5;
        const u32x2 lo = trd((const LAS char*)lds + (r0 + q) * 1280 + nt * 32 + p * 8);
        const u32x2 hi = trd((const LAS char*)lds + (r1 + q) * 1280 + nt * 32 + p * 8);
        *(u32x4*)(ws + OFF_WOT + (size_t)((cbk * 8 + (u & 6) + br) * 25 + (ks16 >> 1) * 5 + nt - cbk * 5) * 1024 + ((ks16 & 1) * 2 + hh) * 256 + (lane & 15) * 16) = u32x4{lo[0], lo[1], hi[0], hi[1]};
    }
}

#define CPF 64
#define CPF_N 256
#define CPF_M 0
#define G1_TL 3
#define G1_D 3
#define G1_D0 2
#define G1_DT 10
#define G1_DE 0
#define G1_NX 4
constexpr int G1_NW = 10 - G1_NX, G1_HB = 20 - 2 * G1_TL, G1_WOFF = G1_NX * 16384;
constexpr int g1_t(int h) { return h < 2 * G1_HB ? h >> 1 : h < 2 * G1_HB + 2 * G1_TL ? h - G1_HB : h - G1_HB - 2 * G1_TL; }
constexpr int g1_m(int h) { return h < 2 * G1_HB ? h & 1 : h < 2 * G1_HB + 2 * G1_TL ? 0 : 1; }
constexpr int g1_xi(int h) { return h < 2 * G1_HB ? (h >> 1) & 1 : h & 1; }
constexpr int g1_free(int i) { return i < 10 ? (i < G1_NX ? -1 : 2 * (i - G1_NX) + 2) : (i - 10 < G1_NW ? -1 : i - 10 - G1_NW + 1); }
struct G1Plan { int seq[30]; int cnt[22]; int pos[30]; };
constexpr G1Plan g1_plan() {
    G1Plan p = {}; bool done[30] = {}, seen[10] = {}; int ord[30] = {}, need[21] = {}, n = 0;
    for (int b = 0; b < 20; ++b) { const int u = g1_t(2 * b) >> 1; if (!seen[u]) { seen[u] = true; ord[n++] = u; } need[b] = n; ord[n++] = 10 + b; }
    need[20] = 30; n = 0;
    for (int b = 0; b <= 20; ++b) {
        const int ex = b > G1_DT ? b - G1_DT : 0, lim = 3 * (b >> 1) + 1 + (b & 1) + (b == 0 ? G1_D0 : G1_D) + (ex < G1_DE ? ex : G1_DE);
        for (int k = 0; k < 30 && k <= lim; ++k) { const int i = ord[k]; if (!done[i] && g1_free(i) < b) { done[i] = true; p.pos[i] = n; p.seq[n++] = i; } }
        p.cnt[b] = n;
    }
    return p;
}
constexpr G1Plan G1P = g1_plan();
constexpr int g1_wait(int b) { const int x = G1P.pos[g1_t(2 * b) >> 1], w = G1P.pos[10 + b]; return 4 * (G1P.cnt[b] - 1 - (x > w ? x : w)); }
constexpr bool g1_ok() { for (int b = 0; b < 20; ++b) if (g1_wait(b) < 0 || g1_wait(b) > 60) return false; return G1P.cnt[19] == 30 && G1_NX >= G1_TL && G1_NX <= 4; }
static_assert(g1_ok(), "loader plan");
#define G1_RT (G1_MAP == 1 ? 0x20757520 : G1_MAP == 2 ? 0x98654210 : 0x27057250)
#define G1_RC 0
#define G1_RM 0
#define G1_ROT (((((unsigned)G1_RT >> (4 * xcd)) & 15) + G1_RC * ct + G1_RM * mt) % 10)

#define LST_P
#define LST_A
template <int T> struct G1Load {
    template <class FX, class FW> static DI void run(FX&& ldx, FW&& ldw LST_P) {
        asm volatile("s_waitcnt vmcnt(%0)\n\ts_barrier" :: "n"(g1_wait(T)) : "memory");
#pragma unroll
        for (int n = G1P.cnt[T]; n < G1P.cnt[T + 1]; ++n) { const int i = G1P.seq[n]; if (i < 10) ldx(i); else ldw(i - 10); }
        G1Load<T + 1>::run(ldx, ldw LST_A);
    }
};
template <> struct G1Load<20> { template <class FX, class FW> static DI void run(FX&&, FW&& LST_P) {} };

struct G1Ctx {
    f32x4 acc[4][8];
    bf16x8 x[2][4]; u32x2 w[2][4][2];
    const LAS char* xa[2]; const LAS char* wa[8];
    int so[4];
    rsrc_t rB; float sc; f32x2 m[4]; u32x4 o;
    unsigned pf0, pf1;
};
#define SB0() __builtin_amdgcn_sched_barrier(0)
#define G1_SP 12
template <int J, int I> DI void g1_side(G1Ctx& c) {
    constexpr int K = (J - (40 - 2 * G1_TL)) * 16 + I;
    if (CPF && J == 20 && I == 12) { asm volatile("s_waitcnt vmcnt(0)" :: "v"(c.pf0), "v"(c.pf1)); SB0(); }
    if (K > 0 && K <= 8 * G1_SP && (K - 1) % G1_SP == 8) asm volatile("" :: "v"(c.o));
    if (K >= 0 && K < 8 * G1_SP && K % G1_SP < 9) {
        constexpr int U = K < 0 ? 0 : K / G1_SP, e = K < 0 ? 0 : K % G1_SP, ni = (U >> 1) & 3, a = U & 1;
        constexpr int mi = e == 0 ? 0 : e == 1 ? 1 : e == 3 ? 2 : e == 5 ? 3 : -1, ci = e == 2 ? 0 : e == 4 ? 1 : e == 6 ? 2 : e == 7 ? 3 : -1;
        if (mi >= 0) { const f32x4 v = c.acc[2 * a + ((mi & 3) >> 1)][ni]; c.m[mi & 3] = f32x2{v[(mi & 1) * 2], v[(mi & 1) * 2 + 1]} * c.sc; }
        if (ci >= 0) c.o[ci & 3] = pk2(c.m[ci & 3][0], c.m[ci & 3][1]);
        if (e == 8) __builtin_amdgcn_raw_buffer_store_b128(c.o, c.rB, c.so[ni] + a * 2560, 0, SC1);
        SB0();
    }
}
template <int J> struct G1Step {
    static DI void run(G1Ctx& c) {
        constexpr int N = J + 1, t = g1_t(N), xo = ((t >> 1) % G1_NX) * 16384, sl = (N >> 1) % G1_NW, wo = (sl & 3) * 16384 + (N & 1) * 8192, wi = (sl >> 2) * 4;
        constexpr int ps = J & 1, ma = g1_m(J) * 4, xc = g1_xi(J), xn = g1_xi(N);
#define G1_M(i) c.acc[(i) & 3][ma + ((i) >> 2)] = MFMA16(c.x[xc][(i) & 3], B8(((u32x4){c.w[ps][(i) >> 2][0][0], c.w[ps][(i) >> 2][0][1], c.w[ps][(i) >> 2][1][0], c.w[ps][(i) >> 2][1][1]})), \
                c.acc[(i) & 3][ma + ((i) >> 2)]); SB0(); g1_side<J, i>(c);
#define G1_X(i) if (N < 40) c.x[xn][i] = *(const LAS bf16x8*)(c.xa[t & 1] + xo + ((i) >> 1) * 4096 + ((i) & 1) * 1024); SB0();
#define G1_W(i, e) if (N < 40) c.w[ps ^ 1][i][e] = trd(c.wa[wi + (i)] + wo + (e) * 1024); SB0();
        if (ps) {
            G1_M(0) G1_M(1) G1_M(2)
            if (N < 40) asm volatile("s_waitcnt lgkmcnt(0)\n\ts_barrier" ::: "memory");
            SB0();
            G1_X(0) G1_M(3) G1_W(0, 0) G1_M(4) G1_W(0, 1) G1_M(5) G1_X(1) G1_M(6) G1_X(2) G1_M(7) G1_X(3) G1_M(8) G1_W(1, 0) G1_M(9) G1_W(1, 1) G1_M(10)
            G1_W(2, 0) G1_M(11) G1_W(2, 1) G1_M(12) G1_W(3, 0) G1_M(13) G1_W(3, 1) G1_M(14) G1_M(15)
        } else if (xn != xc) {
            G1_M(0) G1_X(0) G1_M(1) G1_W(0, 0) G1_M(2) G1_W(0, 1) G1_M(3) G1_X(1) G1_M(4) G1_X(2) G1_M(5) G1_X(3) G1_M(6) G1_W(1, 0) G1_M(7) G1_W(1, 1)
            G1_M(8) G1_W(2, 0) G1_M(9) G1_W(2, 1) G1_M(10) G1_W(3, 0) G1_M(11) G1_W(3, 1) G1_M(12) G1_M(13) G1_M(14) G1_M(15)
        } else {
            G1_M(0) G1_W(0, 0) G1_M(1) G1_W(0, 1) G1_M(2) G1_W(1, 0) G1_M(3) G1_W(1, 1) G1_M(4) G1_W(2, 0) G1_M(5) G1_W(2, 1) G1_M(6) G1_W(3, 0) G1_M(7) G1_W(3, 1)
            G1_M(8) G1_M(9) G1_M(10) G1_M(11) G1_M(12) G1_M(13) G1_M(14) G1_M(15)
        }
        G1Step<J + 1>::run(c);
    }
};
template <> struct G1Step<40> { static DI void run(G1Ctx&) {} };
template <int K> struct G1Rest { static DI void run(G1Ctx& c) { g1_side<40 - 2 * G1_TL + K / 16, K % 16>(c); G1Rest<K + 1>::run(c); } };
template <> struct G1Rest<8 * G1_SP> { static DI void run(G1Ctx&) {} };

DI void phase1(const u16* __restrict__ x, const u16* __restrict__ wA, const u16* __restrict__ wB, char* __restrict__ ws, char* lds, int bid,
               unsigned pf0, unsigned pf1) {
    const int tid = threadIdx.x, lane = tid & 63, wave = rfl(tid >> 6);
    const unsigned lds0 = (unsigned)(uintptr_t)lds;
    const int xcd = bid & 7, j = (xcd >> 2) * 30 + (bid >> 3);
    const int ct = j >> 2, mt = (xcd & 3) * 4 + (j & 3);
    const int type = rfl(ct / 5), rot = rfl(G1_ROT);
    if (__builtin_expect(wave >= 4, 1)) {
        const int lw = wave - 4;
        unsigned xo[4], wo[2];
#pragma unroll
        for (int i = 0; i < 4; ++i) { const int row = (lw * 4 + i) * 8 + (lane >> 3); xo[i] = (unsigned)((mt * 128 + row) * 1280 + (((lane & 7) ^ ((row >> 1) & 3) ^ ((row >> 2) & 4)) << 4)); }
#pragma unroll
        for (int i = 0; i < 2; ++i) { const int k = (lw * 2 + i) * 4 + (lane >> 4), s16 = lane & 15;
            wo[i] = (unsigned)(k * 3840 + ct * 256 + ((s16 ^ ((k & 3) << 2) ^ ((k >> 2) & 2)) << 4)); }
        auto ldx = [&](int u, unsigned st = 0u) {
            int kk = u + rot; kk -= kk >= 10 ? 10 : 0;
            const unsigned slot = lds0 + (u % G1_NX) * 16384 + lw * 4096;
#pragma unroll
            for (int i = 0; i < 4; ++i) glds16((const char*)x + kk * 128, xo[i], slot + i * 1024);
        };
        auto ldw = [&](int s) {
            int kk = (g1_t(2 * s) >> 1) + rot; kk -= kk >= 10 ? 10 : 0;
            const unsigned slot = lds0 + G1_WOFF + (s % G1_NW) * 16384 + lw * 2048;
#pragma unroll
            for (int e = 0; e < 2; ++e) {
                const char* src = (const char*)(g1_m(2 * s + e) ? wA : wB) + (kk * 64 + (g1_t(2 * s + e) & 1) * 32) * 3840;
                glds16(src, wo[0], slot + e * 8192); glds16(src, wo[1], slot + e * 8192 + 1024);
            }
        };
#pragma unroll
        for (int n = 0; n < G1P.cnt[0]; ++n) { const int i = G1P.seq[n]; if (i < 10) ldx(i); else ldw(i - 10); }
        G1Load<0>::run(ldx, ldw LST_A);
        return;
    }
    const int wm = wave & 1, wn = wave >> 1;
    const int i16 = lane & 15, g = lane >> 4, q = (lane >> 2) & 3, p = lane & 3;
    G1Ctx c;
    c.pf0 = pf0; c.pf1 = pf1;
    const int xr = (i16 >> 3) * 16 + (i16 & 7);
#pragma unroll
    for (int ks = 0; ks < 2; ++ks) { c.xa[ks] = (const LAS char*)lds + (wm * 64 + xr) * 128 + (((ks * 4 + g) ^ ((xr >> 1) & 3) ^ ((xr >> 2) & 4)) << 4); asm volatile("" : "+v"(c.xa[ks])); }
#pragma unroll
    for (int i = 0; i < 8; ++i) { c.wa[i] = (const LAS char*)lds + G1_WOFF + (i >> 2) * 65536 + (8 * g + q) * 256 + (((wn * 4 + (i & 3)) ^ (q << 1) ^ (g & 1)) << 5) + p * 8;
        asm volatile("" : "+v"(c.wa[i])); }
#pragma unroll
    for (int i = 0; i < 4; ++i) { const int cc = (ct - type * 5) * 128 + wn * 64 + i * 16 + i16, h = cc / 20; c.so[i] = h * 163840 + g * 320 + (cc - h * 20) * 16; asm volatile("" : "+v"(c.so[i])); }
    constexpr float QS = 0.22360679774997896f * LOG2E;
    int sci = type == 0 ? __builtin_bit_cast(int, QS) : 0x3F800000;
    asm volatile("" : "+s"(sci));
    c.sc = __int_as_float(sci);
    const int tb = (mt * 4 + wm * 2) * 2560;
    const int sB = tb + (type == 0 ? (int)OFF_IQ : (int)OFF_IB + (type - 1) * 1280), sA = tb + (type == 0 ? (int)OFF_IQ + 1280 : (int)OFF_IA + (type - 1) * 1280);
    c.rB = __builtin_amdgcn_make_buffer_rsrc((void*)(ws + sB), 0, (int)IMG, 0x00020000);
    const rsrc_t rA = __builtin_amdgcn_make_buffer_rsrc((void*)(ws + sA), 0, (int)IMG, 0x00020000);
    {
        const float a0 = type < 2 ? 1e-6f : 0.f;
        f32x4 a4 = {a0, a0, a0, a0};
        asm volatile("" : "+v"(a4));
#pragma unroll
        for (int i = 0; i < 16; ++i) { c.acc[i & 3][i >> 2] = f32x4{0.f, 0.f, 0.f, 0.f}; c.acc[i & 3][4 + (i >> 2)] = a4; }
    }
    __builtin_amdgcn_s_setprio(2);
    asm volatile("s_barrier" ::: "memory");
#pragma unroll
    for (int i = 0; i < 4; ++i) {
        c.x[0][i] = *(const LAS bf16x8*)(c.xa[0] + (i >> 1) * 4096 + (i & 1) * 1024);
        if (i == 0) { c.w[0][0][0] = trd(c.wa[0]); c.w[0][0][1] = trd(c.wa[0] + 1024); }
    }
#pragma unroll
    for (int i = 1; i < 4; ++i) { c.w[0][i][0] = trd(c.wa[i]); c.w[0][i][1] = trd(c.wa[i] + 1024); }
    SB0();
    G1Step<0>::run(c);
    PSTAMP(0, 2);
    G1Rest<(32 * G1_TL < 8 * G1_SP ? 32 * G1_TL : 8 * G1_SP)>::run(c);
    if (type < 2) {
#pragma unroll
        for (int i = 0; i < 4; ++i)
#pragma unroll
            for (int a = 0; a < 2; ++a) {
                u32x4 o;
#pragma unroll
                for (int e = 0; e < 4; ++e) {
                    const f32x2 t = {c.acc[2 * a + (e >> 1)][4 + i][(e & 1) * 2], c.acc[2 * a + (e >> 1)][4 + i][(e & 1) * 2 + 1]};
                    const f32x2 m = __builtin_elementwise_fma(t, f32x2{LOG2E, LOG2E}, f32x2{-1e-6f * LOG2E, -1e-6f * LOG2E});
                    const f32x2 ex = {__builtin_amdgcn_fmed3f(__builtin_amdgcn_exp2f(m[0]), 0.f, 1.f), __builtin_amdgcn_fmed3f(__builtin_amdgcn_exp2f(m[1]), 0.f, 1.f)};
                    const f32x2 r = f32x2{pmax(t[0], 1e-6f), pmax(t[1], 1e-6f)} + ex;
                    o[e] = pk2(r[0], r[1]);
                }
                __builtin_amdgcn_raw_buffer_store_b128(o, rA, c.so[i] + a * 2560, 0, SC1);
            }
    } else {
#pragma unroll
        for (int i = 0; i < 4; ++i)
#pragma unroll
            for (int a = 0; a < 2; ++a) {
                const f32x4 u = c.acc[2 * a][4 + i], v = c.acc[2 * a + 1][4 + i];
                __builtin_amdgcn_raw_buffer_store_b128(u32x4{pk2(u[0], u[1]), pk2(u[2], u[3]), pk2(v[0], v[1]), pk2(v[2], v[3])}, rA, c.so[i] + a * 2560, 0, SC1);
            }
    }
}

__global__ __launch_bounds__(512, 2) void k1_qkv(const u16* __restrict__ x, const u16* __restrict__ wA, const u16* __restrict__ wB,
                                                 const u16* __restrict__ woA, const u16* __restrict__ woB, const u16* __restrict__ gate_w,
                                                 char* __restrict__ ws) {
    extern __shared__ __attribute__((aligned(16))) char lds[];
    int bid = blockIdx.x;
    asm volatile("" : "+s"(bid));
    unsigned pf0, pf1 = 0u;
    {
        unsigned long long pc;
        asm volatile("s_getpc_b64 %1\n.Lk1_pc:\n\tv_lshl_add_u32 %0, %2, 7, %3\n\tv_min_u32 %0, .Lk2_last-.Lk1_pc, %0\n\t"
                     "v_cmp_gt_u32 vcc, %4, %2\n\ts_mov_b64 exec, vcc\n\tglobal_load_dword %0, %0, %1\n\ts_mov_b64 exec, -1"
                     : "=&v"(pf0), "=&s"(pc) : "v"(threadIdx.x), "s"((bid >> 3) * (CPF * 128) + 64), "n"(CPF) : "memory", "vcc");
    }
    if (__builtin_expect(bid < 240, 1)) phase1(x, wA, wB, ws, lds, bid, pf0, pf1);
    else { p1_aux(x, woA, woB, gate_w, ws, lds, bid - 240); asm volatile("" :: "v"(pf0), "v"(pf1)); }
}

#define STR(x) #x
#define XS(x) STR(x)
#define A_E(i) "v_exp_f32 v" XS(i) ", v" XS(i) "\n"
#define A_C(d, a, b) "v_cvt_pk_bf16_f32 v" XS(d) ", v" XS(a) ", v" XS(b) "\n"
#define A_G(a, b, c, d, p0, p1) A_E(a) A_E(b) A_E(c) A_E(d) A_C(p0, a, b) A_C(p1, c, d)
#define A_M(d, a, b, c) "v_mfma_f32_32x32x16_bf16 " d ", " a ", " b ", " c "\n"
#define A_STEP(C0,C1,C2,C3,C4,C5,C6,C7,C8,C9,C10,C11,C12,C13,C14,C15, SN, KM, KT, V1, V0, NKMa, NKMb, NKT, NV1, NV0, OK0, OK1, OV1, OV0, PRIO) \
    "s_waitcnt lgkmcnt(0)\n" \
    A_M(SN, KM, "v[120:123]", "0") \
    "ds_read_b64_tr_b16 " NKMa ", v128 offset:" XS(OK0) "\n" \
    "ds_read_b64_tr_b16 " NKMb ", v128 offset:" XS(OK1) "\n" \
    "ds_read_b64_tr_b16 " NKT ", v129 offset:" XS(OK0) "\n" \
    A_G(C0, C1, C2, C3, 112, 113) \
    A_M("v[64:79]", V1, "v[116:119]", "v[64:79]") \
    "s_mov_b64 exec, s[72:73]\n" \
    "ds_read_b128 " NV1 ", v130 offset:" XS(OV1) "\n" \
    "ds_read_b128 " NV0 ", v130 offset:" XS(OV0) "\n" \
    "s_mov_b64 exec, -1\n" \
    A_G(C4, C5, C6, C7, 114, 115) \
    A_M(SN, KT, "v[124:127]", SN) \
    A_G(C8, C9, C10, C11, 116, 117) \
    A_M("v[64:79]", V0, "v[112:115]", "v[64:79]") \
    "s_setprio " XS(PRIO) "\n" \
    A_G(C12, C13, C14, C15, 118, 119)
#define A_EVEN(OK0, OK1, OV1, OV0, PRIO) A_STEP(32,33,34,35,36,37,38,39,40,41,42,43,44,45,46,47, "v[48:63]", "v[80:83]", "v[84:87]", "v[88:91]", "v[92:95]", \
    "v[96:97]", "v[98:99]", "v[100:101]", "v[104:107]", "v[108:111]", OK0, OK1, OV1, OV0, PRIO)
#define A_ODD(OK0, OK1, OV1, OV0, PRIO) A_STEP(48,49,50,51,52,53,54,55,56,57,58,59,60,61,62,63, "v[32:47]", "v[96:99]", "v[100:103]", "v[104:107]", "v[108:111]", \
    "v[80:81]", "v[82:83]", "v[84:85]", "v[88:91]", "v[92:95]", OK0, OK1, OV1, OV0, PRIO)

#define A_LD(g, off) "global_load_lds_dwordx4 v131, " g " offset:" XS(off) "\n"
#define A_LDA(off) "s_cbranch_scc1 .Lattn_s" XS(off) "\n" A_LD("s[74:75]", off) ".Lattn_s" XS(off) ":\n"
#define A_BODY \
        "s_mov_b32 m0, s78\n s_cmp_eq_u32 s80, 2\n s_nop 3\n" \
        A_EVEN(5120, 5184, 640, 2560, 2)     A_ODD(7680, 7744, 3200, 5120, 1) \
        A_EVEN(10240, 10304, 5760, 7680, 0)  A_ODD(12800, 12864, 8320, 10240, 3) \
        "s_waitcnt vmcnt(0) lgkmcnt(0)\n s_mov_b64 exec, 1\n ds_add_u32 v127, v86\n s_mov_b64 exec, -1\n" \
        A_EVEN(15360, 15424, 10880, 12800, 2) A_LDA(0) A_ODD(17920, 17984, 13440, 15360, 1) A_LDA(1024) \
        A_EVEN(20480, 20544, 16000, 17920, 0) A_LDA(2048) \
        "s_cmp_eq_u32 s80, 1\n s_cbranch_scc0 .Lattn_skip\n s_mov_b32 m0, s79\n s_nop 0\n" \
        A_LD("s[76:77]", 0) A_LD("s[76:77]", 1024) A_LD("s[76:77]", 2048) ".Lattn_skip:\n" \
        "ds_read_b32 v132, v127\n" \
        A_ODD(23040, 23104, 18560, 20480, 3) \
        "v_add_u32 v128, 0x5000, v128\n v_add_u32 v129, 0x5000, v129\n v_add_u32 v130, 0x5000, v130\n" \
        "s_waitcnt lgkmcnt(0)\n s_nop 0\n"
struct AttnRegs { f32x16 S, O; u32x4 KM, P1, QM, KT0, KT1, QT, AD; u32x8 V0, V1; };
#define A_OPERANDS \
        : "+{v[32:47]}"(a.S), "+{v[64:79]}"(a.O), "+{v[80:83]}"(a.KM), "+{v[84:87]}"(a.KT0), "+{v[88:95]}"(a.V0), "+{v[100:103]}"(a.KT1), \
          "+{v[104:111]}"(a.V1), "+{v[116:119]}"(a.P1), "+{v[128:131]}"(a.AD), "={v132}"(cnt) \
        : "{v[120:123]}"(a.QM), "{v[124:127]}"(a.QT), \
          "{s[72:73]}"(vmask), "{s[74:75]}"(ga), "{s[76:77]}"(gb), "{s78}"(ma), "{s79}"(mb), "{s80}"(enb) \
        : "memory", "scc", "s82", "s83", "v48", "v49", "v50", "v51", "v52", "v53", "v54", "v55", "v56", "v57", "v58", "v59", "v60", "v61", "v62", "v63", \
          "v96", "v97", "v98", "v99", "v112", "v113", "v114", "v115"
template <bool FIRST>
DI unsigned attn_chunk(AttnRegs& a, unsigned long long vmask, unsigned ma, unsigned long long ga, unsigned mb, unsigned long long gb, unsigned enb) {
    unsigned cnt;
    if (FIRST) asm volatile("s_call_b64 s[82:83], .Lattn_body\n s_branch .Lattn_done\n .p2align 6\n .Lattn_body:\n" A_BODY "s_setpc_b64 s[82:83]\n .Lattn_done:\n" A_OPERANDS);
    else       asm volatile("s_call_b64 s[82:83], .Lattn_body\n" A_OPERANDS);
    return cnt;
}
DI void wait_cnt(char* lds, int off, unsigned cnt, int n) {
    if (__builtin_expect((unsigned)rfl((int)cnt) < (unsigned)(n * 0x3F80), 0)) {
#pragma clang loop unroll(disable)
        do { __builtin_amdgcn_s_sleep(1); cnt = *(volatile LAS unsigned*)(lds + off); } while ((unsigned)rfl((int)cnt) < (unsigned)(n * 0x3F80));
    }
    asm volatile("" ::: "memory");
}

DI void wait_cnt(char* lds, int off, int n) { wait_cnt(lds, off, *(volatile LAS unsigned*)(lds + off), n); }
template <int OFF> DI void add_cnt(unsigned lds0) {
    asm volatile("s_waitcnt lgkmcnt(0)\n\ts_mov_b64 exec, 1\n\tds_add_u32 %0, %1 offset:%2\n\ts_mov_b64 exec, -1" :: "v"(lds0), "v"(0x3F80u), "n"(OFF) : "memory"); }

DI float xhalf_max(float m) { auto rr = __builtin_amdgcn_permlane32_swap(__float_as_uint(m), __float_as_uint(m), false, false);
    return fmaxf(__uint_as_float(rr[0]), __uint_as_float(rr[1])); }
DI float hi_half(float v) {
    auto rr = __builtin_amdgcn_permlane32_swap(__float_as_uint(v), __float_as_uint(v), false, false);
    return __uint_as_float(rr[1]); }
DI float sum16(float v) { auto rr = __builtin_amdgcn_permlane16_swap(__float_as_uint(v), __float_as_uint(v), false, false);
    return __uint_as_float(rr[0]) + __uint_as_float(rr[1]); }
DI float sum32(float v) { auto rr = __builtin_amdgcn_permlane32_swap(__float_as_uint(v), __float_as_uint(v), false, false);
    return __uint_as_float(rr[0]) + __uint_as_float(rr[1]); }
DI f32x4 sum_rows(f32x4 a, f32x4 b) {
    f32x4 s = a + b;
#pragma unroll
    for (int i = 0; i < 4; ++i) s[i] = sum32(sum16(s[i]));
    return s;
}
DI unsigned long long uni64(const void* p) { const unsigned long long a = (unsigned long long)(uintptr_t)p;
    return (unsigned long long)(unsigned)rfl((int)(unsigned)a) | ((unsigned long long)(unsigned)rfl((int)(unsigned)(a >> 32)) << 32); }
DI unsigned full(unsigned v) { return v < EMPTY ? v : EMPTY - 1u; }
DI unsigned umax4(unsigned m, u32x4 v) { return max(max(max(m, v[0]), max(v[1], v[2])), v[3]); }
DI void scale_o(f32x2 (&s)[6], const f32x16& o, float f) {
#pragma unroll
    for (int i = 0; i < 6; ++i) s[i] = f32x2{o[2 * i], o[2 * i + 1]} * f;
}
DI void store_o(rsrc_t rs, const f32x2 (&s)[6], int rt, int br, int h, int lane) {
    const int rb = (int)OFF_O + (rt * 80 + ((br ? 0 : 4) + (h >> 3)) * 10) * 1024;
    const u32x2 t = {full(pk2(s[4][0], s[4][1])), full(pk2(s[5][0], s[5][1]))};
    __builtin_amdgcn_raw_buffer_store_b128(u32x4{full(pk2(s[0][0], s[0][1])), full(pk2(s[1][0], s[1][1])), full(pk2(s[2][0], s[2][1])), full(pk2(s[3][0], s[3][1]))},
                                           rs, lane * 16, rb + (h & 7) * 1024, SC1);
    __builtin_amdgcn_raw_buffer_store_b64(t, rs, lane < 32 ? lane * 16 : (int)0x7F000000, rb + (8 + ((h >> 2) & 1)) * 1024 + ((h >> 1) & 1) * 512 + (h & 1) * 8, SC1);
}
DI void empty_o(rsrc_t rs, int rt, int h, int lane) {
    const int rb = (int)OFF_O + (rt * 80 + (h >> 3) * 10) * 1024;
    u32x4 e = {EMPTY, EMPTY, EMPTY, EMPTY};
    __builtin_amdgcn_raw_buffer_store_b128(e, rs, lane * 16, rb + (h & 7) * 1024, SC1);
    if (lane < 32) __builtin_amdgcn_raw_buffer_store_b64(u32x2{e[0], e[1]}, rs, lane * 16, rb + (8 + ((h >> 2) & 1)) * 1024 + ((h >> 1) & 1) * 512 + (h & 1) * 8, SC1);
    asm volatile("" :: "v"(e));
}
DI void set_flag(rsrc_t rs, int off) { __builtin_amdgcn_raw_buffer_store_b32(1u, rs, 0, off, SC1); }
DI unsigned get_flag(rsrc_t rs, int voff, int off) { return __builtin_amdgcn_raw_buffer_load_b32(rs, voff, off, SC1); }
DI unsigned a_ld1(unsigned long long sb, unsigned voff) { unsigned v; asm volatile("global_load_dword %0, %1, %2 sc1" : "=v"(v) : "v"(voff), "s"(sb) : "memory"); return v; }
DI u32x4 a_ld4s(unsigned long long sb, unsigned voff) { u32x4 v; asm volatile("global_load_dwordx4 %0, %1, %2 sc1" : "=v"(v) : "v"(voff), "s"(sb) : "memory"); return v; }
template <int OFF> DI u32x4 a_ld4so(unsigned long long sb, unsigned voff) { u32x4 v;
    asm volatile("global_load_dwordx4 %0, %1, %2 offset:%3 sc1" : "=v"(v) : "v"(voff), "s"(sb), "n"(OFF) : "memory"); return v; }
template <int OFF> DI u32x4 a_ld4(unsigned long long sb, unsigned voff) { u32x4 v;
    asm volatile("global_load_dwordx4 %0, %1, %2 offset:%3" : "=v"(v) : "v"(voff), "s"(sb), "n"(OFF) : "memory"); return v; }
template <int I> struct IC { static constexpr int v = I; };
template <int I, int N, class F> DI void sfor(F&& f) { if constexpr (I < N) { f(IC<I>{}); sfor<I + 1, N>(f); } }
#define SFOR(i, a, b) sfor<a, b>([&](auto i##_) __attribute__((always_inline)) { constexpr int i = decltype(i##_)::v;
#define SEND });
template <bool FIRST> DI void mfma_ip(f32x4& c, bf16x8 a, bf16x8 b) {
    if constexpr (FIRST) asm volatile("v_mfma_f32_16x16x32_bf16 %0, %1, %2, 0" : "=&v"(c) : "v"(a), "v"(b));
    else asm volatile("v_mfma_f32_16x16x32_bf16 %0, %1, %2, %0" : "+v"(c) : "v"(a), "v"(b));
}
template <int N> DI void wait_vm(unsigned& x) { asm volatile("s_waitcnt vmcnt(%1)" : "+v"(x) : "n"(N) : "memory"); }
template <int N> DI void wait_vm(u32x4& x, u32x4& y) { asm volatile("s_waitcnt vmcnt(%2)" : "+v"(x), "+v"(y) : "n"(N) : "memory"); }
template <int K0> DI void ld_ob(u32x4 (&b)[10], unsigned vo, const unsigned long long (&ob)[2]) {
    if constexpr (K0 < 5) {
        asm volatile("global_load_dwordx4 %0, %2, %3 offset:%4 sc1\n\tglobal_load_dwordx4 %1, %2, %3 offset:%5 sc1" : "=&v"(b[2 * K0]), "=&v"(b[2 * K0 + 1])
                     : "v"(vo), "s"(ob[K0 >> 2]), "n"(K0 < 4 ? K0 * 2048 - 4096 : 0), "n"(K0 < 4 ? K0 * 2048 - 3840 : 256) : "memory");
        ld_ob<K0 + 1>(b, vo, ob);
    }
}
template <class T> DI void landed(T& v) { asm volatile("" : "+v"(v)); }
DI void wait_flag(rsrc_t rs, int voff, int off, unsigned v) {
    if (__builtin_expect(!__all(v == 1u), 0)) {
#pragma clang loop unroll(disable)
        for (int spin = 0; spin < (1 << 22); ++spin) { if (__all(get_flag(rs, voff, off) == 1u)) break; __builtin_amdgcn_s_sleep(1); }
    }
    __builtin_amdgcn_fence(__ATOMIC_ACQUIRE, "wavefront");
    asm volatile("" ::: "memory");
}

#define K2_W0 8
#define K2_BAR
#define K2_TOUCH 1
#define K2_S0 2
#define FILL_C 2
constexpr int L_SP = 136192, L_K0 = 152320, L_SQ = 153600, L_QA = 1280, L_KVF = 11520, L_WO = 13312;

__global__ __launch_bounds__(512, 2) void k2_attn(char* ws_arg, u16* out_arg) {
    extern __shared__ __attribute__((aligned(16))) char lds[];
    __attribute__((address_space(1))) char* wsg; u16* out;
    asm volatile("s_load_dwordx4 s[20:23], %2, 0x0" : "={s[20:21]}"(wsg), "={s[22:23]}"(out) : "s"(__builtin_amdgcn_kernarg_segment_ptr()));
    int bid = blockIdx.x;
    asm volatile("" : "+s"(bid));
    const int tid = threadIdx.x, lane = tid & 63, wave = rfl(tid >> 6);
    const int h = (bid & 7) * 4 + ((bid >> 3) & 3), qb = bid >> 5;
    const int oq = bid & 7, rb = oq * 4 + ((bid >> 3) & 3), cb = bid >> 5;
    const int r = lane & 31, hh = lane >> 5;
    const unsigned lds0 = (unsigned)(uintptr_t)lds;
    const int wr = (wave & 3) * 2 + (wave >> 2), rt = qb * 8 + wr;
    unsigned long long vmask = 0x000FFFFF000FFFFFull;
    const u32x4 ONES = {0x3F803F80u, 0x3F803F80u, 0x3F803F80u, 0x3F803F80u};
    const f32x16 Z16 = {};
    const int g = lane >> 4, q = (lane >> 2) & 3, p = lane & 3;
    const int tro = (g & 1) * 640 + (p & 1) * 320 + q * 16 + (p >> 1) * 8;
    const LAS char* L = (const LAS char*)lds;
    const int ent = tid < 126 ? tid : 125;
    AttnRegs ar;
    u32x2 c0, c1, c2;
    unsigned aq, ak, aqt, akt, asp, vi[6];
    u32x4 a0 = {0u, 0u, 0u, 0u}, a1 = a0, b0 = ONES, b1 = ONES; float gate;
    {
        if (tid < 12) *(LAS unsigned*)(lds + tid * 4) = 0u;
        const unsigned vc = r == 20 ? 0x3F803F80u : 0u;
        ar.O = Z16; ar.P1 = u32x4{0u, 0u, 0u, 0u}; c0 = u32x2{0x3F80u, 0u}; c1 = c0; c2 = u32x2{0u, 0u};
        ar.V0 = u32x8{vc, vc, vc, vc, vc, vc, vc, vc}; ar.V1 = ar.V0;
        ar.AD = u32x4{lds0 + tro + hh * 128, lds0 + tro + 256, lds0 + 1280 + hh * 320 + r * 16, (unsigned)lane * 16};
        aq = ar.AD[0] + L_SQ + wave * 1280; ak = ar.AD[0] + L_K0; asp = lds0 + L_SP + ((wave * 2 + hh) * 21 + r) * 48;
        aqt = hh ? lds0 + 16 : ar.AD[1] + L_SQ + wave * 1280; akt = ar.AD[1] + L_K0;
        asm volatile("" : "+{v[64:79]}"(ar.O), "+{v[86:87]}"(c0), "+{v[88:95]}"(ar.V0), "+{v[102:103]}"(c1), "+{v[104:111]}"(ar.V1),
                          "+{v[116:119]}"(ar.P1), "+{v[128:131]}"(ar.AD), "+{v[126:127]}"(c2),
                          "+v"(aq), "+v"(ak), "+v"(aqt), "+v"(akt), "+v"(asp), "+{s[72:73]}"(vmask));
        const unsigned hb = h * 163840 + lane * 16;
        unsigned vq = (unsigned)OFF_IQ + hb + rt * 2560, vk = (unsigned)OFF_IB + hb + (wave & 1) * 256;
        unsigned va = (unsigned)OFF_IA + h * 163840 + rt * 2560 + hh * 320 + r * 16, vg = (unsigned)OFF_G + rt * 128 + r * 4;
        unsigned mq = lds0 + L_SQ + wave * 1280, mk = lds0 + L_K0 + (wave & 1) * 256, mi = lds0 + (1 + wave) * 1024;
#pragma unroll
        for (int i = 0; i < 6; ++i) vi[i] = (unsigned)OFF_IB + hb + (1 + i * 8 + wave) * 1024;
        asm volatile("" : "+v"(vq), "+v"(vk), "+v"(va), "+v"(vg), "+v"(vi[0]), "+v"(vi[1]), "+v"(vi[2]), "+v"(vi[3]), "+v"(vi[4]), "+v"(vi[5]),
                          "+v"(a0), "+v"(a1), "+v"(b0), "+v"(b1), "+s"(mq), "+s"(mk), "+s"(mi));
        asm volatile("s_mov_b32 m0, %14\n\ts_waitcnt lgkmcnt(0)\n\tglobal_load_lds_dwordx4 %7, %5\n\tglobal_load_lds_dwordx4 %7, %5 offset:256\n\t"
                     "s_mov_b32 m0, %15\n\ts_nop 0\n\tglobal_load_lds_dwordx4 %8, %5\n\t"
                     "s_mov_b32 m0, %16\n\ts_nop 0\n\tglobal_load_lds_dwordx4 %11, %5\n\t"
                     "s_add_u32 m0, m0, 0x2000\n\ts_nop 0\n\tglobal_load_lds_dwordx4 %12, %5\n\t"
                     "s_add_u32 m0, m0, 0x2000\n\ts_nop 0\n\tglobal_load_lds_dwordx4 %13, %5\n\t"
                     "global_load_dword %4, %10, %5\n\ts_mov_b64 exec, %17\n\t"
                     "global_load_dwordx4 %0, %9, %5\n\tglobal_load_dwordx4 %1, %9, %5 offset:640\n\tglobal_load_dwordx4 %2, %9, %5 offset:1280\n\t"
                     "global_load_dwordx4 %3, %9, %5 offset:1920\n\ts_mov_b64 exec, -1"
                     : "+v"(a0), "+v"(a1), "+v"(b0), "+v"(b1), "=&v"(gate), "+{s[20:21]}"(wsg), "+{s[22:23]}"(out)
                     : "v"(vq), "v"(vk), "v"(va), "v"(vg), "v"(vi[0]), "v"(vi[1]), "v"(vi[2]), "s"(mq), "s"(mk), "s"(mi), "s"(vmask) : "memory", "scc");
    }
    char* const ws = (char*)wsg;
    const rsrc_t rs = __builtin_amdgcn_make_buffer_rsrc((void*)ws, 0, WS_USED, 0x00020000);
    const char* kvb = ws + OFF_IB + (size_t)h * 163840;
    const char* qim = ws + OFF_IQ + (size_t)h * 163840 + qb * 20480;
    const int j = wave & 3, isE = (wave >> 2) ^ 1;
    const char* wop = ws + OFF_WOT + (size_t)((cb * 4 + j) * 50) * 1024;
    const int flA = (int)OFF_FL + (oq * 4 + j) * 128, flv = (lane & 7) * 16;
    const int ov = ((g >> 1) * 1024 + (g & 1) * 512 + (lane & 15) * 16), os = (int)OFF_O + (rb * 160 + j * 10) * 1024;
    unsigned fl = 0u;
    u32x4 kp[8], wa[20], aa[2][10];
    const unsigned long long wsu = uni64(ws);
    unsigned long long kb = uni64(ws + OFF_KVP + 4096 + h * 16384), kb2 = uni64(ws + OFF_KVP + 12288 + h * 16384);
    unsigned long long ob[2][2] = {{uni64(ws + os + 4096), uni64(ws + os + 8192)}, {uni64(ws + os + 81920 + 4096), uni64(ws + os + 81920 + 8192)}};
#define LD_H(A, vo, hf, en, C) asm volatile("s_cmp_lg_u32 %13, 0\n\ts_cbranch_scc0 .Lsk%=\n\t" \
        "global_load_dwordx4 %0, %10, %11 offset:-4096 sc1\n\tglobal_load_dwordx4 %1, %10, %11 offset:-3840 sc1\n\t" \
        "global_load_dwordx4 %2, %10, %11 offset:-2048 sc1\n\tglobal_load_dwordx4 %3, %10, %11 offset:-1792 sc1\n\t" \
        "global_load_dwordx4 %4, %10, %11 sc1\n\tglobal_load_dwordx4 %5, %10, %11 offset:256 sc1\n\t" \
        "global_load_dwordx4 %6, %10, %11 offset:2048 sc1\n\tglobal_load_dwordx4 %7, %10, %11 offset:2304 sc1\n\t" \
        "global_load_dwordx4 %8, %10, %12 sc1\n\tglobal_load_dwordx4 %9, %10, %12 offset:256 sc1\n.Lsk%=:" \
        : C(A[0]), C(A[1]), C(A[2]), C(A[3]), C(A[4]), C(A[5]), C(A[6]), C(A[7]), C(A[8]), C(A[9]) \
        : "v"(vo), "s"(ob[hf][0]), "s"(ob[hf][1]), "s"(en) : "memory", "scc");
#define LD_W4(A, f) asm volatile("s_cmp_lg_u32 %6, 0\n\ts_cbranch_scc0 .Lsk%=\n\t" \
        "global_load_dwordx4 %0, %4, %5\n\tglobal_load_dwordx4 %1, %4, %5 offset:1024\n\t" \
        "global_load_dwordx4 %2, %4, %5 offset:2048\n\tglobal_load_dwordx4 %3, %4, %5 offset:3072\n.Lsk%=:" \
        : "=&v"((A)[0]), "=&v"((A)[1]), "=&v"((A)[2]), "=&v"((A)[3]) : "v"(lane * 16), "s"(uni64(wop + (f) * 1024)), "s"(isE) : "memory", "scc");
#define LD_KP(TAIL) asm volatile("s_cmp_lt_u32 %11, 2\n\ts_cbranch_scc0 .Lsk%=\n\t" \
        "global_load_dwordx4 %0, %8, %9 offset:-4096 sc1\n\tglobal_load_dwordx4 %1, %8, %9 offset:-2048 sc1\n\t" \
        "global_load_dwordx4 %2, %8, %9 sc1\n\tglobal_load_dwordx4 %3, %8, %9 offset:2048 sc1\n\t" \
        "global_load_dwordx4 %4, %8, %10 offset:-4096 sc1\n\tglobal_load_dwordx4 %5, %8, %10 offset:-2048 sc1\n\t" \
        "global_load_dwordx4 %6, %8, %10 sc1\n\tglobal_load_dwordx4 %7, %8, %10 offset:2048 sc1\n" \
        ".Lsk%=:" TAIL \
        : "=&v"(kp[0]), "=&v"(kp[1]), "=&v"(kp[2]), "=&v"(kp[3]), "=&v"(kp[4]), "=&v"(kp[5]), "=&v"(kp[6]), "=&v"(kp[7]) \
        : "v"(ent * 16), "s"(kb), "s"(kb2), "s"(wave) : "memory", "scc")
    asm volatile("" : "+s"(kb), "+s"(kb2), "+s"(ob[0][0]), "+s"(ob[0][1]), "+s"(ob[1][0]), "+s"(ob[1][1]));
    unsigned ma0 = rfl(lds0 + 50176 + wave * 3072), mb0 = rfl(ma0 + 24576); unsigned long long ga0 = uni64(kvb + 50176 + wave * 3072), gb0 = ga0 + 24576;
    asm volatile("" : "+{s[74:75]}"(ga0), "+{s[76:77]}"(gb0), "+{s78}"(ma0), "+{s79}"(mb0));
    asm volatile("s_mov_b32 m0, %4\n\ts_nop 0\n\tglobal_load_lds_dwordx4 %0, %3\n\t"
                 "s_add_u32 m0, m0, 0x2000\n\ts_nop 0\n\tglobal_load_lds_dwordx4 %1, %3\n\t"
                 "s_add_u32 m0, m0, 0x2000\n\ts_nop 0\n\tglobal_load_lds_dwordx4 %2, %3"
                 :: "v"(vi[3]), "v"(vi[4]), "v"(vi[5]), "s"(ws), "s"(lds0 + (25 + wave) * 1024) : "memory", "scc");
    wait_vm_bar<K2_W0>();
    {
        f32x16 kvacc;
        u32x2 t0, t1, t2;
        asm volatile("ds_read_b64_tr_b16 v[96:97], %15\n\tds_read_b64_tr_b16 v[98:99], %15 offset:64\n\t"
                     "ds_read_b64_tr_b16 v[120:121], %14\n\tds_read_b64_tr_b16 v[122:123], %14 offset:64\n\t"
                     "ds_read_b64_tr_b16 v[124:125], %16\n\tds_read_b64_tr_b16 v[100:101], %17\n\t"
                     "ds_read_b64_tr_b16 v[84:85], v129 offset:2560\n\ts_mov_b64 exec, %19\n\tds_read_b128 v[92:95], v130\n\ts_mov_b64 exec, -1\n\t"
                     "ds_read_b64_tr_b16 v[80:81], v128 offset:2560\n\tds_read_b64_tr_b16 v[82:83], v128 offset:2624\n\ts_waitcnt lgkmcnt(6)\n\t"
                     "v_mfma_f32_32x32x16_bf16 v[32:47], v[96:99], v[120:123], 0\n\ts_waitcnt lgkmcnt(4)\n\t"
                     "v_mfma_f32_32x32x16_bf16 v[48:63], v[100:103], v[124:127], v[32:47]\n\t"
                     "s_waitcnt vmcnt(3)\n\t" K2_BAR
                     "v_mfma_f32_32x32x16_bf16 %8, %9, %11, 0\n\tv_mfma_f32_32x32x16_bf16 %8, %10, %12, %8\n\ts_nop 7\n\ts_nop 0\n\t"
                     "v_max3_f32 v96, v48, v49, v50\n\tv_max3_f32 v97, v51, v52, v53\n\tv_max3_f32 v96, v96, v54, v55\n\tv_max3_f32 v97, v97, v56, v57\n\t"
                     "v_max3_f32 v96, v96, v58, v59\n\tv_max3_f32 v97, v97, v60, v61\n\tv_max3_f32 v96, v96, v62, v63\n\tv_max_f32 v96, v96, v97\n\t"
                     "v_mov_b32 v97, v96\n\ts_nop 1\n\tv_permlane32_swap_b32 v96, v97\n\tv_max_f32 v96, v96, v97\n\t"
                     "s_mov_b32 exec_hi, 0\n\tv_cvt_pk_bf16_f32 v126, -v96, 0\n\ts_mov_b32 exec_hi, -1\n\ts_nop 0\n\t"
                     "v_mfma_f32_32x32x16_bf16 v[32:47], v[100:103], v[124:127], v[32:47]"
                     : "=&{v[32:47]}"(ar.S), "=&{v[120:123]}"(ar.QM), "=&{v[124:125]}"(t2), "+{v[126:127]}"(c2), "=&{v[100:101]}"(t1), "=&{v[84:85]}"(t0),
                       "+{v[88:95]}"(ar.V0), "=&{v[80:83]}"(ar.KM), "=&v"(kvacc), "+v"(a0), "+v"(a1), "+v"(b0), "+v"(b1), "+v"(gate)
                     : "v"(aq), "v"(ak), "v"(aqt), "v"(akt), "{v[128:131]}"(ar.AD), "s"(vmask), "{v[102:103]}"(c1)
                     : "memory", "v48", "v49", "v50", "v51", "v52", "v53", "v54", "v55", "v56", "v57", "v58", "v59", "v60", "v61", "v62", "v63",
                       "v96", "v97", "v98", "v99");
        ar.KT0 = u32x4{t0[0], t0[1], c0[0], c0[1]}; ar.KT1 = u32x4{t1[0], t1[1], c1[0], c1[1]}; ar.QT = u32x4{t2[0], t2[1], c2[0], c2[1]};
        if (r <= 20) {
            LAS f32x4* dst = (LAS f32x4*)(uintptr_t)asp;
            dst[0] = f32x4{kvacc[0], kvacc[1], kvacc[2], kvacc[3]};
            dst[1] = f32x4{kvacc[4], kvacc[5], kvacc[6], kvacc[7]};
            dst[2] = f32x4{kvacc[8], kvacc[9], kvacc[10], kvacc[11]};
        }
    }
#pragma unroll
    for (int c = 0; c < 8; ++c) {
        unsigned ma, mb; unsigned long long ga, gb;
        if (c < 3) {
            const int lim = (c == 1 ? L_SP : 163840) - 3072;
            const int oa = (c == 2 ? L_SP : 50176 + c * 49152) + wave * 3072, ob = oa + 24576 < lim ? oa + 24576 : lim;
            ma = lds0 + oa; mb = lds0 + ob; ga = uni64(kvb + oa); gb = uni64(kvb + ob);
        } else {
            ma = lds0 + (c == 7 ? 1024 + j * 3072 : L_WO + ((c - 3) * 8 + wr) * 3072); mb = ma;
            ga = uni64(wop + (c == 7 ? 44 : 20 + (wave >> 2) * 12 + 3 * (c - 3)) * 1024); gb = ga;
        }
        CSTAMP(2 * c);
        unsigned cnt;
        if (c == 0) cnt = attn_chunk<true>(ar, vmask, ma0, ga0, mb0, gb0, 1u);
        else cnt = attn_chunk<false>(ar, vmask, rfl(ma), ga, rfl(mb), gb, c < 3 ? 1u : c < 7 ? 0u : (unsigned)(2 - 2 * isE));
        CSTAMP(2 * c + 1);
        if (c < 7) wait_cnt(lds, 0, cnt, 8 * (c + 1));
        if (c == FILL_C && c != 1) empty_o(rs, rt, h, lane);
        if (c == 0) {
            if (wave < 2) {
                f32x4 s = *(const LAS f32x4*)(L + L_SP + ent * 16);
#pragma unroll
                for (int w = 1; w < 8; ++w) s += *(const LAS f32x4*)(L + L_SP + w * 2016 + ent * 16);
                const u32x4 v = __builtin_bit_cast(u32x4, s);
                if (tid < 126) __builtin_amdgcn_raw_buffer_store_b128(u32x4{full(v[0]), full(v[1]), full(v[2]), full(v[3])}, rs, tid * 16, (int)OFF_KVP + (h * 8 + qb) * 2048, SC1);
            }
            glds16(qim + wr * 2560 + 1280, lane * 16, lds0 + L_QA + wave * 1280);
            glds16(qim + wr * 2560 + 1280 + 256, lane * 16, lds0 + L_QA + wave * 1280 + 256);
        } else if (c == 1) {
            LD_KP("");
            if (FILL_C == 1) empty_o(rs, rt, h, lane);
        } else if (c == 2) {
#pragma unroll
            for (int j = 0; j < 8; ++j) landed(kp[j]);
            if (wave < 2) {
#pragma clang loop unroll(disable)
                for (int spin = 0; spin < (1 << 20); ++spin) {
                    unsigned m = 0u;
#pragma unroll
                    for (int j = 0; j < 8; ++j) m = umax4(m, kp[j]);
                    if (__builtin_expect(__all(m != EMPTY), 1)) break;
                    LD_KP("\n\ts_waitcnt vmcnt(0)");
                }
                f32x4 s = __builtin_bit_cast(f32x4, kp[0]);
#pragma unroll
                for (int j = 1; j < 8; ++j) s += __builtin_bit_cast(f32x4, kp[j]);
                const int e3 = ent / 3;
                *(LAS u32x2*)(lds + L_KVF + e3 * 32 + (ent - e3 * 3) * 8) = u32x2{pk2(s[0], s[1]), pk2(s[2], s[3])};
            }
        } else if (c == 3) {
            const int rr = r <= 20 ? r : 20;
            const u32x4 ka0 = *(const LAS u32x4*)(L + L_KVF + (hh * 21 + rr) * 32);
            const u32x2 ka1 = *(const LAS u32x2*)(L + L_KVF + (hh * 21 + rr) * 32 + 16);
            const LAS char* qa = L + L_QA + wave * 1280 + tro;
            const u32x2 pq0 = trd(qa + hh * 64), pq1 = trd(qa + 128 + hh * 64);
            u32x2 pq2 = trd(qa + 256);
            if (hh) pq2 = u32x2{0u, 0u};
            f32x16 oa = MFMA32(B8(ka0), B8(((u32x4){pq0[0], pq0[1], pq1[0], pq1[1]})), Z16);
            oa = MFMA32(B8(((u32x4){ka1[0], ka1[1], 0u, 0u})), B8(((u32x4){pq2[0], pq2[1], 0u, 0u})), oa);
            const float den = hi_half(oa[8]) + 1e-6f;
            f32x2 sv[6];
            scale_o(sv, oa, gate * __builtin_amdgcn_rcpf(den));
            store_o(rs, sv, rt, 0, h, lane);
            LD_W4(wa, 0) LD_W4(wa + 4, 4)
        } else if (c == 4) {
            if (tid == 0) set_flag(rs, (int)OFF_FL + (qb * 4 + (h >> 3)) * 128 + (h & 7) * 16);
            LD_W4(wa + 8, 8) LD_W4(wa + 12, 12)
        } else if (c == 5) {
            fl = a_ld1(isE ? wsu : uni64(out), isE ? flA + flv : (rb * 64 + lane) * (D_ * 2) + cb * 160);
            LD_W4(wa + 16, 16)
        } else if (c == 6) {
            landed(fl);
            if (wave < 4) wait_flag(rs, flv, flA, fl);
            LD_H(aa[0], ov + 40960, 0, isE, "=&v")
        }
    }
#pragma unroll
    for (int i = 0; i < 10; ++i) { landed(wa[i]); landed(wa[10 + i]); landed(aa[0][i]); }
    u32x4 wd[4];
    f32x16 O = MFMA32(B8(((u32x4){ar.V0[0], ar.V0[1], ar.V0[2], ar.V0[3]})), B8(ar.P1), ar.O);
    landed(O);
    if (lane == 0) set_flag(rs, (int)OFF_FL + (32 + rt * 4 + (h >> 3)) * 128 + (h & 7) * 16);
    LD_H(aa[1], ov + 40960, 1, isE, "=&v")
    WSTAMP(2);
    f32x2 sv[6];
    {
        const float l = hi_half(O[8]);
        scale_o(sv, O, (1.f - gate) * __builtin_amdgcn_rcpf(l));
        const f32x2 t = ((sv[0] + sv[1]) + (sv[2] + sv[3])) + (sv[4] + sv[5]);
        const unsigned long long ok = __builtin_amdgcn_ballot_w64(l >= 1e-18f) & __builtin_amdgcn_ballot_w64(l <= 1.0e30f) &
                                      __builtin_amdgcn_ballot_w64(fabsf(t[0] + t[1]) <= 3.0e38f);
        if (__builtin_expect(ok != ~0ull, 0)) {
            float m = -INFINITY; O = Z16;
            int tl = tid;
            asm volatile("" : "+v"(tl));
            const int r = tl & 31, hh = (tl >> 5) & 1, rc = r < 20 ? r : 19;
            const bf16x8 qm = B8(ar.QM), qt0 = B8(((u32x4){ar.QT[0], ar.QT[1], 0u, 0u}));
            const int ko = (r >> 4) * 640 + ((r >> 2) & 1) * 320 + ((r >> 3) & 1) * 8 + (r & 3) * 2;
            for (int t = 0; t < 64; ++t) {
                const char* tb = kvb + t * 2560;
                unsigned kw[6];
#pragma unroll
                for (int i = 0; i < 6; ++i) {
                    const int d = i < 4 ? 8 * hh + 2 * i : 16 + 2 * (i - 4);
                    kw[i] = (unsigned)*(const u16*)(tb + ko + d * 16) | ((unsigned)*(const u16*)(tb + ko + d * 16 + 16) << 16);
                }
                u32x4 f0 = *(const u32x4*)(tb + 1280 + hh * 320 + rc * 16), f1 = *(const u32x4*)(tb + 1920 + hh * 320 + rc * 16);
                if (r >= 20) { f0 = ONES; f1 = ONES; }
                f32x16 s = MFMA32(B8(((u32x4){kw[0], kw[1], kw[2], kw[3]})), qm, Z16);
                s = MFMA32(B8(((u32x4){kw[4], kw[5], 0u, 0u})), qt0, s);
                float tm = s[0];
#pragma unroll
                for (int i = 1; i < 16; ++i) tm = fmaxf(tm, s[i]);
                tm = xhalf_max(tm);
                const float mn = fmaxf(m, tm);
                const float al = (m == -INFINITY) ? 0.f : __builtin_amdgcn_exp2f(m - mn);
                m = mn;
#pragma unroll
                for (int i = 0; i < 16; ++i) { O[i] *= al; s[i] = __builtin_amdgcn_exp2f(s[i] - mn); }
                u32x4 p0 = {pk2(s[0], s[1]), pk2(s[2], s[3]), pk2(s[4], s[5]), pk2(s[6], s[7])};
                u32x4 p1 = {pk2(s[8], s[9]), pk2(s[10], s[11]), pk2(s[12], s[13]), pk2(s[14], s[15])};
                O = MFMA32(B8(f0), B8(p0), O); O = MFMA32(B8(f1), B8(p1), O);
            }
            scale_o(sv, O, (1.f - gate) * __builtin_amdgcn_rcpf(hi_half(O[8])));
        }
    }
    store_o(rs, sv, rt, 1, h, lane);
    __builtin_amdgcn_s_setprio(0);
    __builtin_amdgcn_sched_barrier(0);
    WSTAMP(3);
    constexpr int L_P0 = L_WO + 98304;
    if (wave < 4) {
        const LAS char* wl[3] = {L + L_WO + j * 6144 + lane * 16, L + L_WO + 49152 + j * 6144 + lane * 16, L + 1024 + j * 3072 + lane * 16};
        LAS char* wp[2] = {(LAS char*)lds + (lane & 15) * 320 + ((g ^ ((lane >> 1) & 3)) << 4) + L_P0 + j * 10240, nullptr};
        wp[1] = wp[0] + (L_WO - L_P0 - j * 4096);
        unsigned fa[2] = {(unsigned)OFF_FL + (32 + rb * 8 + j) * 128 + flv, 0u};
        fa[1] = fa[0] + 512;
        landed(wl[0]); landed(wl[1]); landed(wl[2]); landed(wp[0]); landed(wp[1]); landed(fa[0]); landed(fa[1]);
        auto wfr = [&](int f) -> bf16x8 {
            if (f < 20) return B8(wa[f]);
            if (f < 44) return *(const LAS bf16x8*)(wl[((f - 20) % 12) / 6] + ((((f - 20) % 12) / 3) & 1) * 24576 + ((f - 20) / 12) * 3072 + ((f - 20) % 3) * 1024);
            if (f < 46) return *(const LAS bf16x8*)(wl[2] + (f - 44) * 1024);
            return B8(wd[f - 46]);
        };
        f32x4 acc[4][5];
        u32x4 b[2][10];
        bf16x8 wb[30];
        wait_cnt(lds + j * 4, 32, 1);
        fl = a_ld1(wsu, fa[0]);
        asm volatile("s_waitcnt vmcnt(3)" : "+v"(aa[1][0]), "+v"(aa[1][1]), "+v"(aa[1][2]), "+v"(aa[1][3]), "+v"(aa[1][4]), "+v"(aa[1][5]), "+v"(aa[1][6]),
                     "+v"(aa[1][7]), "+v"(aa[1][8]), "+v"(aa[1][9]) :: "memory");
        SB0();
        unsigned c64 = 0u, m = 0u;
        const unsigned l16 = lane * 16;
        const unsigned long long wdp = uni64(wop + 46 * 1024);
        SFOR(st, 0, 15)
            constexpr int v = st - 4, vh = v < 5 ? 0 : 1, vk = v < 0 ? 0 : v % 5, n = st < 5 ? 20 : 10;
            if constexpr (st == K2_S0) {
                wait_vm<0>(fl);
                wait_flag(rs, fa[0], 0, fl);
                WSTAMP(4);
                fl = a_ld1(wsu, fa[1]);
                wd[0] = a_ld4<0>(wdp, l16); wd[1] = a_ld4<1024>(wdp, l16); wd[2] = a_ld4<2048>(wdp, l16); wd[3] = a_ld4<3072>(wdp, l16);
                ld_ob<0>(b[0], ov, ob[0]);
                SB0();
            }
            if constexpr (st == 4) {
                wait_vm<14>(fl);
                wait_flag(rs, fa[1], 0, fl);
                WSTAMP(5);
                ld_ob<0>(b[1], ov, ob[1]);
                SB0();
            }
            if constexpr (st == 5) { landed(wd[0]); landed(wd[1]); landed(wd[2]); landed(wd[3]); }
            if constexpr (st == 10) wait_cnt(lds, 0, c64, 64);
            SFOR(i, 0, n)
                constexpr int ni = i % 5, mi = i / 5;
                if constexpr (st < 4) mfma_ip<st == 0>(acc[mi][ni], B8(wa[st * 5 + ni]), B8(aa[mi >> 1][2 * st + (mi & 1)]));
                else if constexpr (st == 4) mfma_ip<false>(acc[mi][ni], wb[ni], B8(aa[mi >> 1][2 * st + (mi & 1)]));
                else mfma_ip<false>(acc[(st / 5 - 1) * 2 + mi][ni], wb[5 + (st % 5) * 5 + ni], B8(b[st / 5 - 1][2 * (st % 5) + mi]));
                SB0();
                if constexpr (st == 3 && i >= 10 && i < 15) wb[i - 10] = wfr(10 + i);
                if constexpr (st == 4 && i >= 8 && i < 13) wb[i - 3] = wfr(17 + i);
                if constexpr (st >= 5 && st < 9 && i < 5) wb[(st - 3) * 5 + i] = wfr(20 + (st - 3) * 5 + i);
                if constexpr (st == 9 && i == 0) c64 = *(volatile LAS unsigned*)lds;
                if constexpr ((st == 10 || st == 11) && i < 5) *(LAS f32x4*)(wp[0] + (st - 10) * 5120 + i * 64) = acc[st - 10][i];
                if constexpr (st == 12 && i == 4) add_cnt<12>(lds0);
                if constexpr (v >= 0 && v < 10) {
                    u32x4 &x = b[vh][2 * vk], &y = b[vh][2 * vk + 1];
                    if constexpr (i == n - 5) { wait_vm<18 - 2 * v>(x, y); m = max(max(x[0], x[1]), x[2]); }
                    if constexpr (i == n - 4) m = max(max(m, x[3]), y[0]);
                    if constexpr (i == n - 3) m = max(max(m, y[1]), y[2]);
                    if constexpr (i == n - 2) m = max(m, y[3]);
                }
                SB0();
            SEND
            if constexpr (v >= 0 && v < 10) if (__builtin_expect(!__all(m != EMPTY), 0)) {
#pragma clang loop unroll(disable)
                for (int spin = 0; spin < (1 << 20); ++spin) {
                    ld_ob<vk>(b[vh], ov, ob[vh]);
                    wait_vm<0>(b[vh][2 * vk], b[vh][2 * vk + 1]);
                    if (__all(umax4(umax4(0u, b[vh][2 * vk]), b[vh][2 * vk + 1]) != EMPTY)) break;
                }
            }
        SEND
#pragma unroll
        for (int mi = 0; mi < 2; ++mi)
#pragma unroll
            for (int ni = 0; ni < 5; ++ni) { *(LAS f32x4*)(wp[1] + mi * 24576 + ni * 64) = acc[2 + mi][ni]; SB0(); }
        WSTAMP(6);
        asm volatile("s_waitcnt lgkmcnt(0)\n\ts_barrier" ::: "memory");
    } else {
        add_cnt<32>(lds0 + j * 4);
        const int t = tid - 256;
        const LAS char* rp[2][3]; unsigned oo[3];
#pragma unroll
        for (int i = 0; i < 3; ++i) {
            const int id = i < 2 || t < 128 ? i * 256 + t : 1, row = id / 20, cs = id - row * 20, cc = cs ^ ((row >> 1) & 3);
            rp[0][i] = L + L_P0 + id * 16; rp[1][i] = L + L_WO + id * 16 + (id >= 320 ? 24576 - 5120 : 0);
            oo[i] = (unsigned)(((rb * 64 + row) * D_ + cb * 80 + cc * 4) * 2);
            landed(rp[0][i]); landed(rp[1][i]); landed(oo[i]);
        }
        WSTAMP(4);
#pragma unroll
        for (int hf = 0; hf < 2; ++hf) {
            if (hf == 0) { wait_cnt(lds, 12, 4); WSTAMP(5); }
            else { WSTAMP(6); asm volatile("s_barrier" ::: "memory"); }
            f32x4 pv[3][4];
#pragma unroll
            for (int i = 0; i < 3; ++i)
#pragma unroll
                for (int w = 0; w < 4; ++w) pv[i][w] = *(const LAS f32x4*)(rp[hf][i] + w * (hf ? 6144 : 10240));
            __builtin_amdgcn_sched_barrier(0);
#pragma unroll
            for (int i = 0; i < 3; ++i) {
                const f32x4 v = (pv[i][0] + pv[i][1]) + (pv[i][2] + pv[i][3]);
                if (i < 2 || t < 128) __builtin_amdgcn_raw_buffer_store_b64(u32x2{pk2(v[0], v[1]), pk2(v[2], v[3])},
                    __builtin_amdgcn_make_buffer_rsrc((void*)uni64(out), 0, S_ * D_ * 2, 0x00020000), oo[i], hf * (32 * D_ * 2), OUT_AUX);
            }
        }
    }
}

__attribute__((used, noinline)) __device__ void k3_mark() { asm volatile(".Lk2_last:"); }

extern "C" void kernel_launch(void* const* d_in, const int* in_sizes, int n_in, void* d_out, int out_size, void* d_ws, size_t ws_size,
                              hipStream_t stream) {
    const u16* x = (const u16*)d_in[0];
    const u16* wA = (const u16*)d_in[1];
    const u16* woA = (const u16*)d_in[2];
    const u16* wB = (const u16*)d_in[4];
    const u16* woB = (const u16*)d_in[5];
    const u16* gw = (const u16*)d_in[6];
    char* ws = (char*)d_ws;
    (void)hipFuncSetAttribute((const void*)k1_qkv, hipFuncAttributeMaxDynamicSharedMemorySize, LDS_BYTES);
    (void)hipFuncSetAttribute((const void*)k2_attn, hipFuncAttributeMaxDynamicSharedMemorySize, LDS_BYTES);
    k1_qkv<<<256, 512, LDS_BYTES, stream>>>(x, wA, wB, woA, woB, gw, ws);
    k2_attn<<<256, 512, LDS_BYTES, stream>>>(ws, (u16*)d_out);
}
```

```cpp
#define G1_MAP 1
#define OUT_AUX 16
#include <hip/hip_runtime.h>
#include <hip/hip_bf16.h>
#include <stdint.h>

typedef short    bf16x8 __attribute__((ext_vector_type(8)));
typedef short    s16x4  __attribute__((ext_vector_type(4)));
typedef float    f32x2  __attribute__((ext_vector_type(2)));
typedef float    f32x4  __attribute__((ext_vector_type(4)));
typedef float    f32x16 __attribute__((ext_vector_type(16)));
typedef float    f32x32 __attribute__((ext_vector_type(32)));
typedef unsigned u32x2  __attribute__((ext_vector_type(2)));
typedef unsigned u32x4  __attribute__((ext_vector_type(4)));
typedef unsigned u32x8  __attribute__((ext_vector_type(8)));
typedef unsigned u32x32 __attribute__((ext_vector_type(32)));
typedef __bf16   bf2_t  __attribute__((ext_vector_type(2)));
typedef __bf16   bf8_t  __attribute__((ext_vector_type(8)));
typedef __bf16   bf16_t __attribute__((ext_vector_type(16)));
typedef unsigned short u16;
#define LAS __attribute__((address_space(3)))
#define DI __device__ __forceinline__

constexpr int S_ = 2048, D_ = 640;
constexpr size_t IMG     = (size_t)32 * 163840;
constexpr size_t OFF_IQ  = 0;
constexpr size_t OFF_IB  = IMG;
constexpr size_t OFF_IA  = 2 * IMG;
constexpr size_t OFF_O   = 3 * IMG;
constexpr size_t OFF_WOT = 4 * IMG;
constexpr size_t OFF_G   = OFF_WOT + (size_t)1600 * 1024;
constexpr size_t OFF_KVP = OFF_G + 8192;
constexpr size_t OFF_FL  = OFF_KVP + (size_t)256 * 2048;
#define FLS 128
#define FLI 16
constexpr int    WS_USED = (int)(OFF_FL + 288 * FLS);
constexpr unsigned EMPTY = 0xFFFFFFFFu;
typedef __amdgpu_buffer_rsrc_t rsrc_t;
constexpr int SC1 = 16;

#define CSTAMP(i)
#define PSTAMP(k, i)
#define WSTAMP(i)

DI unsigned pk2(float lo, float hi) { f32x2 v = {lo, hi}; return __builtin_bit_cast(unsigned, __builtin_convertvector(v, bf2_t)); }
DI float bflo(unsigned v) { return __uint_as_float(v << 16); }
DI float bfhi(unsigned v) { return __uint_as_float(v & 0xffff0000u); }
DI int rfl(int x) { return __builtin_amdgcn_readfirstlane(x); }
DI void glds16(const void* sbase, unsigned voff, unsigned lds_base) {
    const unsigned long long a = (unsigned long long)(uintptr_t)sbase;
    const unsigned long long u = (unsigned long long)(unsigned)rfl((int)(unsigned)a) | ((unsigned long long)(unsigned)rfl((int)(unsigned)(a >> 32)) << 32);
    asm volatile("s_mov_b32 m0, %0\n\ts_nop 0\n\tglobal_load_lds_dwordx4 %1, %2" :: "s"(rfl((int)lds_base)), "v"(voff), "s"(u) : "memory");
}
#define MFMA16(a, b, c) __builtin_amdgcn_mfma_f32_16x16x32_bf16((a), (b), (c), 0, 0, 0)
#define MFMA32(a, b, c) __builtin_amdgcn_mfma_f32_32x32x16_bf16((a), (b), (c), 0, 0, 0)
#define B8(x) __builtin_bit_cast(bf16x8, x)
DI u32x2 trd(const LAS char* p) { return __builtin_bit_cast(u32x2, __builtin_amdgcn_ds_read_tr16_b64_v4i16((LAS s16x4*)p)); }
DI float pmax(float t, float c) { return __int_as_float(max(__float_as_int(t), __float_as_int(c))); }
template <int N> DI void wait_vm_bar() { asm volatile("s_waitcnt vmcnt(%0) lgkmcnt(0)\n\ts_barrier" :: "n"(N) : "memory"); }

constexpr int LDS_BYTES = 163840;
constexpr float LOG2E = 1.4426950408889634f;

DI void p1_aux(const u16* __restrict__ x, const u16* __restrict__ woA, const u16* __restrict__ woB, const u16* __restrict__ gate_w,
               char* __restrict__ ws, char* lds, int a) {
    const int tid = threadIdx.x, lane = tid & 63, wave = rfl(tid >> 6);
    const unsigned lds0 = (unsigned)(uintptr_t)lds;
    const int br = a >> 3, u = a & 7;
    if (tid < 144) *(u32x4*)(ws + OFF_FL + (a * 18 + (tid >> 3)) * FLS + (tid & 7) * FLI) = u32x4{0u, 0u, 0u, 0u};
    const char* wsrc = (const char*)(br ? woB : woA) + (size_t)u * 102400;
    for (int i = wave; i < 100; i += 8) glds16(wsrc + i * 1024, lane * 16, lds0 + i * 1024);
    const u32x4* xs = (const u32x4*)(x + (size_t)(a * 128 + wave * 16) * D_);
    u32x4 xv[20], gw[5];
#pragma unroll
    for (int i = 0; i < 20; ++i) xv[i] = xs[i * 64 + lane];
#pragma unroll
    for (int i = 0; i < 5; ++i) gw[i] = ((const u32x4*)gate_w)[(i * 64 + lane) % 80];
    {
        const rsrc_t rs = __builtin_amdgcn_make_buffer_rsrc((void*)(ws + OFF_KVP + a * 32768), 0, 32768, 0x00020000);
#pragma unroll
        for (int i = 0; i < 4; ++i) __builtin_amdgcn_raw_buffer_store_b128(u32x4{EMPTY, EMPTY, EMPTY, EMPTY}, rs, tid * 16 + i * 8192, 0, SC1);
    }
    LAS float* part = (LAS float*)(lds + 102400) + wave * 1280;
#pragma unroll
    for (int i = 0; i < 20; ++i) {
        float s = 0.f;
#pragma unroll
        for (int j = 0; j < 4; ++j) s += bflo(xv[i][j]) * bflo(gw[i % 5][j]) + bfhi(xv[i][j]) * bfhi(gw[i % 5][j]);
        part[i * 64 + lane] = s;
    }
    wait_vm_bar<0>();
    {
        float s = 0.f;
#pragma unroll
        for (int j = 0; j < 20; ++j) s += part[lane * 20 + j];
        s += __shfl_xor(s, 1); s += __shfl_xor(s, 2);
        if ((lane & 3) == 0) ((float*)(ws + OFF_G))[a * 128 + wave * 16 + (lane >> 2)] = 1.f / (1.f + __expf(-s));
    }
    const int g = lane >> 4, hh = g & 1, ns = g >> 1, q = (lane >> 2) & 3, p = lane & 3;
    for (int un = wave; un < 100; un += 8) {
        const int e = un / 20, np = un - e * 20, nt = np * 2 + ns;
        const int r0 = e < 4 ? e * 20 + 4 * hh : (2 * hh) * 20 + 16;
        const int r1 = e < 4 ? r0 + 8 : r0 + 20;
        const int ks16 = e < 4 ? (u & 1) * 4 + e : 8 + (u & 1), cbk = nt / 5;
        const u32x2 lo = trd((const LAS char*)lds + (r0 + q) * 1280 + nt * 32 + p * 8);
        const u32x2 hi = trd((const LAS char*)lds + (r1 + q) * 1280 + nt * 32 + p * 8);
        *(u32x4*)(ws + OFF_WOT + (size_t)((cbk * 8 + (u & 6) + br) * 25 + (ks16 >> 1) * 5 + nt - cbk * 5) * 1024 + ((ks16 & 1) * 2 + hh) * 256 + (lane & 15) * 16) = u32x4{lo[0], lo[1], hi[0], hi[1]};
    }
}

#define CPF 64
#define CPF_N 256
#define CPF_M 0
#define G1_TL 3
#define G1_D 3
#define G1_D0 2
#define G1_DT 10
#define G1_DE 0
#define G1_NX 4
constexpr int G1_NW = 10 - G1_NX, G1_HB = 20 - 2 * G1_TL, G1_WOFF = G1_NX * 16384;
constexpr int g1_t(int h) { return h < 2 * G1_HB ? h >> 1 : h < 2 * G1_HB + 2 * G1_TL ? h - G1_HB : h - G1_HB - 2 * G1_TL; }
constexpr int g1_m(int h) { return h < 2 * G1_HB ? h & 1 : h < 2 * G1_HB + 2 * G1_TL ? 0 : 1; }
constexpr int g1_xi(int h) { return h < 2 * G1_HB ? (h >> 1) & 1 : h & 1; }
constexpr int g1_free(int i) { return i < 10 ? (i < G1_NX ? -1 : 2 * (i - G1_NX) + 2) : (i - 10 < G1_NW ? -1 : i - 10 - G1_NW + 1); }
struct G1Plan { int seq[30]; int cnt[22]; int pos[30]; };
constexpr G1Plan g1_plan() {
    G1Plan p = {}; bool done[30] = {}, seen[10] = {}; int ord[30] = {}, need[21] = {}, n = 0;
    for (int b = 0; b < 20; ++b) { const int u = g1_t(2 * b) >> 1; if (!seen[u]) { seen[u] = true; ord[n++] = u; } need[b] = n; ord[n++] = 10 + b; }
    need[20] = 30; n = 0;
    for (int b = 0; b <= 20; ++b) {
        const int ex = b > G1_DT ? b - G1_DT : 0, lim = 3 * (b >> 1) + 1 + (b & 1) + (b == 0 ? G1_D0 : G1_D) + (ex < G1_DE ? ex : G1_DE);
        for (int k = 0; k < 30 && k <= lim; ++k) { const int i = ord[k]; if (!done[i] && g1_free(i) < b) { done[i] = true; p.pos[i] = n; p.seq[n++] = i; } }
        p.cnt[b] = n;
    }
    return p;
}
constexpr G1Plan G1P = g1_plan();
constexpr int g1_wait(int b) { const int x = G1P.pos[g1_t(2 * b) >> 1], w = G1P.pos[10 + b]; return 4 * (G1P.cnt[b] - 1 - (x > w ? x : w)); }
constexpr bool g1_ok() { for (int b = 0; b < 20; ++b) if (g1_wait(b) < 0 || g1_wait(b) > 60) return false; return G1P.cnt[19] == 30 && G1_NX >= G1_TL && G1_NX <= 4; }
static_assert(g1_ok(), "loader plan");
#define G1_RT (G1_MAP == 1 ? 0x20757520 : G1_MAP == 2 ? 0x98654210 : 0x27057250)
#define G1_LP 3
#define STR(x) #x
#define XS(x) STR(x)
#define G1_RC 0
#define G1_RM 0
#define G1_ROT (((((unsigned)G1_RT >> (4 * xcd)) & 15) + G1_RC * ct + G1_RM * mt) % 10)

#define LST_P
#define LST_A
template <int T> struct G1Load {
    template <class FX, class FW> static DI void run(FX&& ldx, FW&& ldw LST_P) {
        asm volatile("s_waitcnt vmcnt(%0)\n\ts_barrier" :: "n"(g1_wait(T)) : "memory");
#pragma unroll
        for (int n = G1P.cnt[T]; n < G1P.cnt[T + 1]; ++n) { const int i = G1P.seq[n]; if (i < 10) ldx(i); else ldw(i - 10); }
        G1Load<T + 1>::run(ldx, ldw LST_A);
    }
};
template <> struct G1Load<20> { template <class FX, class FW> static DI void run(FX&&, FW&& LST_P) {} };

struct G1Ctx {
    f32x4 acc[4][8];
    bf16x8 x[2][4]; u32x2 w[2][4][2];
    const LAS char* xa[2]; const LAS char* wa[8];
    int so[4];
    rsrc_t rB; float sc; f32x2 m[4]; u32x4 o;
    unsigned pf0, pf1;
};
#define SB0() __builtin_amdgcn_sched_barrier(0)
#define G1_SP 12
template <int J, int I> DI void g1_side(G1Ctx& c) {
    constexpr int K = (J - (40 - 2 * G1_TL)) * 16 + I;
    if (CPF && J == 20 && I == 12) { asm volatile("s_waitcnt vmcnt(0)" :: "v"(c.pf0), "v"(c.pf1)); SB0(); }
    if (K > 0 && K <= 8 * G1_SP && (K - 1) % G1_SP == 8) asm volatile("" :: "v"(c.o));
    if (K >= 0 && K < 8 * G1_SP && K % G1_SP < 9) {
        constexpr int U = K < 0 ? 0 : K / G1_SP, e = K < 0 ? 0 : K % G1_SP, ni = (U >> 1) & 3, a = U & 1;
        constexpr int mi = e == 0 ? 0 : e == 1 ? 1 : e == 3 ? 2 : e == 5 ? 3 : -1, ci = e == 2 ? 0 : e == 4 ? 1 : e == 6 ? 2 : e == 7 ? 3 : -1;
        if (mi >= 0) { const f32x4 v = c.acc[2 * a + ((mi & 3) >> 1)][ni]; c.m[mi & 3] = f32x2{v[(mi & 1) * 2], v[(mi & 1) * 2 + 1]} * c.sc; }
        if (ci >= 0) c.o[ci & 3] = pk2(c.m[ci & 3][0], c.m[ci & 3][1]);
        if (e == 8) __builtin_amdgcn_raw_buffer_store_b128(c.o, c.rB, c.so[ni] + a * 2560, 0, SC1);
        SB0();
    }
}
template <int J> struct G1Step {
    static DI void run(G1Ctx& c) {
        constexpr int N = J + 1, t = g1_t(N), xo = ((t >> 1) % G1_NX) * 16384, sl = (N >> 1) % G1_NW, wo = (sl & 3) * 16384 + (N & 1) * 8192, wi = (sl >> 2) * 4;
        constexpr int ps = J & 1, ma = g1_m(J) * 4, xc = g1_xi(J), xn = g1_xi(N);
#define G1_M(i) c.acc[(i) & 3][ma + ((i) >> 2)] = MFMA16(c.x[xc][(i) & 3], B8(((u32x4){c.w[ps][(i) >> 2][0][0], c.w[ps][(i) >> 2][0][1], c.w[ps][(i) >> 2][1][0], c.w[ps][(i) >> 2][1][1]})), \
                c.acc[(i) & 3][ma + ((i) >> 2)]); SB0(); g1_side<J, i>(c);
#define G1_X(i) if (N < 40) c.x[xn][i] = *(const LAS bf16x8*)(c.xa[t & 1] + xo + ((i) >> 1) * 4096 + ((i) & 1) * 1024); SB0();
#define G1_W(i, e) if (N < 40) c.w[ps ^ 1][i][e] = trd(c.wa[wi + (i)] + wo + (e) * 1024); SB0();
        if (ps) {
            G1_M(0) G1_M(1) G1_M(2)
            if (N < 40) asm volatile("s_waitcnt lgkmcnt(0)\n\ts_barrier" ::: "memory");
            SB0();
            G1_X(0) G1_M(3) G1_W(0, 0) G1_M(4) G1_W(0, 1) G1_M(5) G1_X(1) G1_M(6) G1_X(2) G1_M(7) G1_X(3) G1_M(8) G1_W(1, 0) G1_M(9) G1_W(1, 1) G1_M(10)
            G1_W(2, 0) G1_M(11) G1_W(2, 1) G1_M(12) G1_W(3, 0) G1_M(13) G1_W(3, 1) G1_M(14) G1_M(15)
        } else if (xn != xc) {
            G1_M(0) G1_X(0) G1_M(1) G1_W(0, 0) G1_M(2) G1_W(0, 1) G1_M(3) G1_X(1) G1_M(4) G1_X(2) G1_M(5) G1_X(3) G1_M(6) G1_W(1, 0) G1_M(7) G1_W(1, 1)
            G1_M(8) G1_W(2, 0) G1_M(9) G1_W(2, 1) G1_M(10) G1_W(3, 0) G1_M(11) G1_W(3, 1) G1_M(12) G1_M(13) G1_M(14) G1_M(15)
        } else {
            G1_M(0) G1_W(0, 0) G1_M(1) G1_W(0, 1) G1_M(2) G1_W(1, 0) G1_M(3) G1_W(1, 1) G1_M(4) G1_W(2, 0) G1_M(5) G1_W(2, 1) G1_M(6) G1_W(3, 0) G1_M(7) G1_W(3, 1)
            G1_M(8) G1_M(9) G1_M(10) G1_M(11) G1_M(12) G1_M(13) G1_M(14) G1_M(15)
        }
        G1Step<J + 1>::run(c);
    }
};
template <> struct G1Step<40> { static DI void run(G1Ctx&) {} };
template <int K> struct G1Rest { static DI void run(G1Ctx& c) { g1_side<40 - 2 * G1_TL + K / 16, K % 16>(c); G1Rest<K + 1>::run(c); } };
template <> struct G1Rest<8 * G1_SP> { static DI void run(G1Ctx&) {} };

typedef __attribute__((address_space(1))) char* gptr;
template <bool LOADER> DI void phase1(char* __restrict__ ws, char* lds, int bid, unsigned pf0, unsigned pf1) {
    gptr xg, wAg, wBg;
    if (LOADER) asm volatile("s_setprio " XS(G1_LP) "\n\ts_load_dwordx4 s[40:43], %3, 0x0\n\ts_load_dwordx2 s[44:45], %3, 0x10"
                             : "={s[40:41]}"(xg), "={s[42:43]}"(wAg), "={s[44:45]}"(wBg) : "s"(__builtin_amdgcn_kernarg_segment_ptr()));
    const int tid = threadIdx.x, lane = tid & 63, wave = rfl(tid >> 6);
    const unsigned lds0 = (unsigned)(uintptr_t)(LAS char*)lds;
    const int xcd = bid & 7, j = (xcd >> 2) * 30 + (bid >> 3);
    const int ct = j >> 2, mt = (xcd & 3) * 4 + (j & 3);
    const int type = rfl(ct / 5), rot = rfl(G1_ROT);
    if (LOADER) {
        const int lw = wave - 4;
        unsigned xo[4], wo[2];
#pragma unroll
        for (int i = 0; i < 4; ++i) { const int row = (lw * 4 + i) * 8 + (lane >> 3); xo[i] = (unsigned)((mt * 128 + row) * 1280 + (((lane & 7) ^ ((row >> 1) & 3) ^ ((row >> 2) & 4)) << 4)); }
#pragma unroll
        for (int i = 0; i < 2; ++i) { const int k = (lw * 2 + i) * 4 + (lane >> 4), s16 = lane & 15;
            wo[i] = (unsigned)(k * 3840 + ct * 256 + ((s16 ^ ((k & 3) << 2) ^ ((k >> 2) & 2)) << 4)); }
        unsigned rx = rot * 128, rw = rot * (64 * 3840);
        asm volatile("s_waitcnt lgkmcnt(0)" : "+{s[40:41]}"(xg), "+{s[42:43]}"(wAg), "+{s[44:45]}"(wBg), "+s"(rx), "+s"(rw)
                     : "v"(xo[0]), "v"(xo[1]), "v"(xo[2]), "v"(xo[3]), "v"(wo[0]), "v"(wo[1]));
        const char* x = (const char*)xg; const char* wA = (const char*)wAg; const char* wB = (const char*)wBg;
        auto ldx = [&](int u, unsigned st = 0u) {
            int kk = u + rot; kk -= kk >= 10 ? 10 : 0;
            const unsigned slot = lds0 + (u % G1_NX) * 16384 + lw * 4096;
#pragma unroll
            for (int i = 0; i < 4; ++i) glds16(x + (u == 0 ? rx : (unsigned)kk * 128), xo[i], slot + i * 1024);
        };
        auto ldw = [&](int s) {
            int kk = (g1_t(2 * s) >> 1) + rot; kk -= kk >= 10 ? 10 : 0;
            const unsigned slot = lds0 + G1_WOFF + (s % G1_NW) * 16384 + lw * 2048;
#pragma unroll
            for (int e = 0; e < 2; ++e) {
                const char* src = (g1_m(2 * s + e) ? wA : wB) + (s == 0 ? rw : (unsigned)(kk * 64 + (g1_t(2 * s + e) & 1) * 32) * 3840);
                glds16(src, wo[0], slot + e * 8192); glds16(src, wo[1], slot + e * 8192 + 1024);
            }
        };
#pragma unroll
        for (int n = 0; n < G1P.cnt[0]; ++n) { const int i = G1P.seq[n]; if (i < 10) ldx(i); else ldw(i - 10); }
        __builtin_amdgcn_s_setprio(0);
        G1Load<0>::run(ldx, ldw LST_A);
        return;
    }
    const int wm = wave & 1, wn = wave >> 1;
    const int i16 = lane & 15, g = lane >> 4, q = (lane >> 2) & 3, p = lane & 3;
    G1Ctx c;
    c.pf0 = pf0; c.pf1 = pf1;
    const int xr = (i16 >> 3) * 16 + (i16 & 7);
#pragma unroll
    for (int ks = 0; ks < 2; ++ks) { c.xa[ks] = (const LAS char*)lds + (wm * 64 + xr) * 128 + (((ks * 4 + g) ^ ((xr >> 1) & 3) ^ ((xr >> 2) & 4)) << 4); asm volatile("" : "+v"(c.xa[ks])); }
#pragma unroll
    for (int i = 0; i < 8; ++i) { c.wa[i] = (const LAS char*)lds + G1_WOFF + (i >> 2) * 65536 + (8 * g + q) * 256 + (((wn * 4 + (i & 3)) ^ (q << 1) ^ (g & 1)) << 5) + p * 8;
        asm volatile("" : "+v"(c.wa[i])); }
#pragma unroll
    for (int i = 0; i < 4; ++i) { const int cc = (ct - type * 5) * 128 + wn * 64 + i * 16 + i16, h = cc / 20; c.so[i] = h * 163840 + g * 320 + (cc - h * 20) * 16; asm volatile("" : "+v"(c.so[i])); }
    constexpr float QS = 0.22360679774997896f * LOG2E;
    int sci = type == 0 ? __builtin_bit_cast(int, QS) : 0x3F800000;
    asm volatile("" : "+s"(sci));
    c.sc = __int_as_float(sci);
    const int tb = (mt * 4 + wm * 2) * 2560;
    const int sB = tb + (type == 0 ? (int)OFF_IQ : (int)OFF_IB + (type - 1) * 1280), sA = tb + (type == 0 ? (int)OFF_IQ + 1280 : (int)OFF_IA + (type - 1) * 1280);
    c.rB = __builtin_amdgcn_make_buffer_rsrc((void*)(ws + sB), 0, (int)IMG, 0x00020000);
    const rsrc_t rA = __builtin_amdgcn_make_buffer_rsrc((void*)(ws + sA), 0, (int)IMG, 0x00020000);
    {
        const float a0 = type < 2 ? 1e-6f : 0.f;
        f32x4 a4 = {a0, a0, a0, a0};
        asm volatile("" : "+v"(a4));
#pragma unroll
        for (int i = 0; i < 16; ++i) { c.acc[i & 3][i >> 2] = f32x4{0.f, 0.f, 0.f, 0.f}; c.acc[i & 3][4 + (i >> 2)] = a4; }
    }
    __builtin_amdgcn_s_setprio(2);
    asm volatile("s_barrier" ::: "memory");
#pragma unroll
    for (int i = 0; i < 4; ++i) {
        c.x[0][i] = *(const LAS bf16x8*)(c.xa[0] + (i >> 1) * 4096 + (i & 1) * 1024);
        if (i == 0) { c.w[0][0][0] = trd(c.wa[0]); c.w[0][0][1] = trd(c.wa[0] + 1024); }
    }
#pragma unroll
    for (int i = 1; i < 4; ++i) { c.w[0][i][0] = trd(c.wa[i]); c.w[0][i][1] = trd(c.wa[i] + 1024); }
    SB0();
    G1Step<0>::run(c);
    PSTAMP(0, 2);
    G1Rest<(32 * G1_TL < 8 * G1_SP ? 32 * G1_TL : 8 * G1_SP)>::run(c);
    if (type < 2) {
#pragma unroll
        for (int i = 0; i < 4; ++i)
#pragma unroll
            for (int a = 0; a < 2; ++a) {
                u32x4 o;
#pragma unroll
                for (int e = 0; e < 4; ++e) {
                    const f32x2 t = {c.acc[2 * a + (e >> 1)][4 + i][(e & 1) * 2], c.acc[2 * a + (e >> 1)][4 + i][(e & 1) * 2 + 1]};
                    const f32x2 m = __builtin_elementwise_fma(t, f32x2{LOG2E, LOG2E}, f32x2{-1e-6f * LOG2E, -1e-6f * LOG2E});
                    const f32x2 ex = {__builtin_amdgcn_fmed3f(__builtin_amdgcn_exp2f(m[0]), 0.f, 1.f), __builtin_amdgcn_fmed3f(__builtin_amdgcn_exp2f(m[1]), 0.f, 1.f)};
                    const f32x2 r = f32x2{pmax(t[0], 1e-6f), pmax(t[1], 1e-6f)} + ex;
                    o[e] = pk2(r[0], r[1]);
                }
                __builtin_amdgcn_raw_buffer_store_b128(o, rA, c.so[i] + a * 2560, 0, SC1);
            }
    } else {
#pragma unroll
        for (int i = 0; i < 4; ++i)
#pragma unroll
            for (int a = 0; a < 2; ++a) {
                const f32x4 u = c.acc[2 * a][4 + i], v = c.acc[2 * a + 1][4 + i];
                __builtin_amdgcn_raw_buffer_store_b128(u32x4{pk2(u[0], u[1]), pk2(u[2], u[3]), pk2(v[0], v[1]), pk2(v[2], v[3])}, rA, c.so[i] + a * 2560, 0, SC1);
            }
    }
}

__global__ __launch_bounds__(512, 2) void k1_qkv(const u16* __restrict__ x, const u16* __restrict__ wA, const u16* __restrict__ wB,
                                                 const u16* __restrict__ woA, const u16* __restrict__ woB, const u16* __restrict__ gate_w,
                                                 char* __restrict__ ws) {
    extern __shared__ __attribute__((aligned(16))) char lds[];
    int bid = blockIdx.x;
    asm volatile("" : "+s"(bid));
    unsigned long long pc;
    asm volatile("s_getpc_b64 %0\n.Lk1_pc:" : "=s"(pc));
    auto cpf = [&]() { unsigned v;
        asm volatile("v_lshl_add_u32 %0, %2, 7, %3\n\tv_min_u32 %0, .Lk2_last-.Lk1_pc, %0\n\t"
                     "v_cmp_gt_u32 vcc, %4, %2\n\ts_mov_b64 exec, vcc\n\tglobal_load_dword %0, %0, %1\n\ts_mov_b64 exec, -1"
                     : "=&v"(v) : "s"(pc), "v"(threadIdx.x), "s"((bid >> 3) * (CPF * 128) + 64), "n"(CPF) : "memory", "vcc");
        return v; };
    const unsigned pf = cpf();
    if (__builtin_expect(bid < 240, 1)) {
        if (__builtin_expect(rfl(threadIdx.x) >= 256, 1)) phase1<true>(ws, lds, bid, 0u,
 0u
 );
        else { phase1<false>(ws, lds, bid, pf, 0u); __builtin_amdgcn_s_waitcnt(0xC07F); }
    } else {
        p1_aux(x, woA, woB, gate_w, ws, lds, bid - 240); asm volatile("" :: "v"(pf));
        __builtin_amdgcn_s_waitcnt(0xC07F);
    }
}

#define A_E(i) "v_exp_f32 v" XS(i) ", v" XS(i) "\n"
#define A_G(a, b, c, d) A_E(a) A_E(b) A_E(c) A_E(d)
#define A_M(d, a, b, c) "v_mfma_f32_32x32x16_bf16 " d ", " a ", " b ", " c "\n"
#define A_S(a, b) "v_smfmac_f32_32x32x32_bf16 v[64:79], " a ", " b ", v133\n"
#define A_MT(d, a) "v_mfma_f32_32x32x8_bf16 " d ", " a ", v[124:125], " d "\n"
#define A_PAT 0
#define A_IP 0
#define A_INC "v_add_u32 v129, v126, v129\n"
#define A_INC0 A_INC
#define A_INC1
#define A_KK(NKMa, NKMb, NKT, OK0, OK1) \
    A_INC0 "ds_read_b64_tr_b16 " NKMa ", v128 offset:" XS(OK0) "\n" \
    "ds_read_b64_tr_b16 " NKMb ", v128 offset:" XS(OK1) "\n" \
    "ds_read_b64_tr_b16 " NKT ", v129\n"
#define A_VV(NV1, NV0, OV1, OV0) \
    "s_mov_b64 exec, s[72:73]\n" \
    "ds_read_b128 " NV1 ", v130 offset:" XS(OV1) "\n" \
    "ds_read_b128 " NV0 ", v130 offset:" XS(OV0) "\n" \
    "s_mov_b64 exec, -1\n"
#define A_STEP(C0,C1,C2,C3,C4,C5,C6,C7,C8,C9,C10,C11,C12,C13,C14,C15, SN, P1, P0, KM, KT, V1, V0, NKMa, NKMb, NKT, NV1, NV0, OK0, OK1, OV1, OV0, PRIO) \
    A_S(V1, P1) A_G(C0, C1, C2, C3) A_VV(NV1, NV0, OV1, OV0) \
    A_M(SN, KM, "v[120:123]", "0") A_INC1 A_G(C4, C5, C6, C7) A_KK(NKMa, NKMb, NKT, OK0, OK1) \
    A_MT(SN, KT) A_G(C8, C9, C10, C11) \
    A_S(V0, P0) A_G(C12, C13, C14, C15) \
    "s_setprio " XS(PRIO) "\n s_waitcnt lgkmcnt(0)\n"
#define A_EVEN(OK0, OK1, OV1, OV0, PRIO) A_STEP(32,33,34,35,36,37,38,39,40,41,42,43,44,45,46,47, "v[48:63]", "v[56:63]", "v[32:39]", \
    "v[80:83]", "v[84:85]", "v[88:91]", "v[92:95]", "v[96:97]", "v[98:99]", "v[100:101]", "v[104:107]", "v[108:111]", OK0, OK1, OV1, OV0, PRIO)
#define A_ODD(OK0, OK1, OV1, OV0, PRIO) A_STEP(48,49,50,51,52,53,54,55,56,57,58,59,60,61,62,63, "v[32:47]", "v[40:47]", "v[48:55]", \
    "v[96:99]", "v[100:101]", "v[104:107]", "v[108:111]", "v[80:81]", "v[82:83]", "v[84:85]", "v[88:91]", "v[92:95]", OK0, OK1, OV1, OV0, PRIO)

#define A_LD(g, off) "global_load_lds_dwordx4 v131, " g " offset:" XS(off) "\n"
#define A_LDA(L, off) "s_cbranch_scc1 .Lattn_s" L XS(off) "\n" A_LD("s[74:75]", off) ".Lattn_s" L XS(off) ":\n"
#define HP 3
#define A_HINT "s_mov_b64 exec, s[88:89]\n global_store_dword v127, v86, s[76:77] sc1\n s_mov_b64 exec, -1\n"
#define A_H1
#define A_H2
#define A_H3 A_HINT
#define A_H4
#define A_BODY(L, p0, p1, p2, p3, p4, p5, p6, p7) \
        ".p2align 6\n .Lattn_body" L ":\n" \
        "s_cmp_eq_u32 s80, 3\n s_waitcnt lgkmcnt(0)\n s_nop 3\n" \
        A_EVEN(5120, 5184, 640, 2560, p0)     A_ODD(7680, 7744, 3200, 5120, p1) \
        A_EVEN(10240, 10304, 5760, 7680, p2)  A_ODD(12800, 12864, 8320, 10240, p3) \
        "s_cbranch_scc1 .Lattn_ret" L "\n .Lattn_mid" L ":\n s_mov_b32 m0, s78\n s_cmp_eq_u32 s80, 2\n" \
        "s_waitcnt vmcnt(0) lgkmcnt(0)\n s_mov_b64 exec, 1\n ds_add_u32 v127, v86\n s_mov_b64 exec, -1\n" \
        A_H4 A_EVEN(15360, 15424, 10880, 12800, p4) A_LDA(L, 0) A_H3 A_ODD(17920, 17984, 13440, 15360, p5) A_LDA(L, 1024) A_H2 \
        A_EVEN(20480, 20544, 16000, 17920, p6) A_LDA(L, 2048) A_H1 \
        "s_cmp_eq_u32 s80, 1\n s_cbranch_scc0 .Lattn_skip" L "\n s_mov_b32 m0, s79\n s_nop 0\n" \
        A_LD("s[76:77]", 0) A_LD("s[76:77]", 1024) A_LD("s[76:77]", 2048) ".Lattn_skip" L ":\n" \
        "ds_read_b32 v132, v127\n" \
        A_ODD(23040, 23104, 18560, 20480, p7) \
        "v_add_u32 v128, 0x5000, v128\n v_add_u32 v130, 0x5000, v130\n" \
        ".Lattn_ret" L ":\n s_nop 1\n s_setpc_b64 s[82:83]\n"
#define A_PE 2, 1, 0, 2, 1, 0, 0, 3
#define A_PL 1, 3, 2, 1, 0, 3, 2, 1
#define A_BODY_(...) A_BODY(__VA_ARGS__)
struct AttnRegs { f32x16 S, O; u32x4 KM, QM, KT0, KT1, QT, AD; u32x8 V0, V1, P1; unsigned IX; unsigned long long BA; };
#define A_OPERANDS \
        : "+{v[32:47]}"(a.S), "+{v[64:79]}"(a.O), "+{v[80:83]}"(a.KM), "+{v[84:87]}"(a.KT0), "+{v[88:95]}"(a.V0), "+{v[100:103]}"(a.KT1), \
          "+{v[104:111]}"(a.V1), "+{v[56:63]}"(a.P1), "+{v[128:131]}"(a.AD), "={v132}"(cnt) \
        : "{v[120:123]}"(a.QM), "{v[124:127]}"(a.QT), "{v133}"(a.IX), \
          "{s[72:73]}"(vmask), "{s[74:75]}"(ga), "{s[76:77]}"(gb), "{s78}"(ma), "{s79}"(mb), "{s80}"(enb), "{s[84:85]}"(a.BA), "{s[88:89]}"(hm) \
        : "memory", "scc", "s82", "s83", "v48", "v49", "v50", "v51", "v52", "v53", "v54", "v55", \
          "v96", "v97", "v98", "v99"
template <int FIRST>
DI unsigned attn_chunk(AttnRegs& a, unsigned long long vmask, unsigned ma, unsigned long long ga, unsigned mb, unsigned long long gb, unsigned enb,
                       unsigned long long hm = 0ull) {
    unsigned cnt;
    if (FIRST == 1) asm volatile("s_swappc_b64 s[82:83], s[84:85]\n s_branch .Lattn_done\n" A_BODY_("E", A_PE) A_BODY_("L", A_PL) ".Lattn_done:\n" A_OPERANDS);
    else if (FIRST == 2) asm volatile("s_add_u32 s86, s84, .Lattn_midE-.Lattn_bodyE\n s_addc_u32 s87, s85, 0\n s_swappc_b64 s[82:83], s[86:87]\n" A_OPERANDS, "s86", "s87");
    else       asm volatile("s_swappc_b64 s[82:83], s[84:85]\n" A_OPERANDS);
    return cnt;
}
DI void wait_cnt(char* lds, int off, unsigned cnt, int n) {
    if (__builtin_expect((unsigned)rfl((int)cnt) < (unsigned)(n * 0x3F80), 0)) {
#pragma clang loop unroll(disable)
        do { __builtin_amdgcn_s_sleep(1); cnt = *(volatile LAS unsigned*)(lds + off); } while ((unsigned)rfl((int)cnt) < (unsigned)(n * 0x3F80));
    }
    asm volatile("" ::: "memory");
}

DI void wait_cnt(char* lds, int off, int n) { wait_cnt(lds, off, *(volatile LAS unsigned*)(lds + off), n); }
template <int OFF> DI void add_cnt(unsigned lds0) {
    asm volatile("s_waitcnt lgkmcnt(0)\n\ts_mov_b64 exec, 1\n\tds_add_u32 %0, %1 offset:%2\n\ts_mov_b64 exec, -1" :: "v"(lds0), "v"(0x3F80u), "n"(OFF) : "memory"); }

DI float xhalf_max(float m) { auto rr = __builtin_amdgcn_permlane32_swap(__float_as_uint(m), __float_as_uint(m), false, false);
    return fmaxf(__uint_as_float(rr[0]), __uint_as_float(rr[1])); }
DI float hi_half(float v) {
    auto rr = __builtin_amdgcn_permlane32_swap(__float_as_uint(v), __float_as_uint(v), false, false);
    return __uint_as_float(rr[1]); }
DI float sum16(float v) { auto rr = __builtin_amdgcn_permlane16_swap(__float_as_uint(v), __float_as_uint(v), false, false);
    return __uint_as_float(rr[0]) + __uint_as_float(rr[1]); }
DI float sum32(float v) { auto rr = __builtin_amdgcn_permlane32_swap(__float_as_uint(v), __float_as_uint(v), false, false);
    return __uint_as_float(rr[0]) + __uint_as_float(rr[1]); }
DI f32x4 sum_rows(f32x4 a, f32x4 b) {
    f32x4 s = a + b;
#pragma unroll
    for (int i = 0; i < 4; ++i) s[i] = sum32(sum16(s[i]));
    return s;
}
DI unsigned long long uni64(const void* p) { const unsigned long long a = (unsigned long long)(uintptr_t)p;
    return (unsigned long long)(unsigned)rfl((int)(unsigned)a) | ((unsigned long long)(unsigned)rfl((int)(unsigned)(a >> 32)) << 32); }
DI unsigned full(unsigned v) { return v < EMPTY ? v : EMPTY - 1u; }
DI unsigned umax4(unsigned m, u32x4 v) { return max(max(max(m, v[0]), max(v[1], v[2])), v[3]); }
#define NOPK 3
DI float fadd1(float a, float b) { float r = a + b; asm("" : "+v"(r)); return r; }
DI float fmul1(float a, float b) { float r = a * b; asm("" : "+v"(r)); return r; }
template <bool U> DI f32x4 add4(f32x4 a, f32x4 b) {
    if constexpr (U) return f32x4{fadd1(a[0], b[0]), fadd1(a[1], b[1]), fadd1(a[2], b[2]), fadd1(a[3], b[3])};
    else return a + b;
}
template <bool U> DI f32x2 add2(f32x2 a, f32x2 b) {
    if constexpr (U) return f32x2{fadd1(a[0], b[0]), fadd1(a[1], b[1])};
    else return a + b;
}
DI void scale_o(f32x2 (&s)[6], const f32x16& o, float f) {
#pragma unroll
    for (int i = 0; i < 6; ++i) {
        if constexpr (NOPK & 2) s[i] = f32x2{fmul1(o[2 * i], f), fmul1(o[2 * i + 1], f)};
        else s[i] = f32x2{o[2 * i], o[2 * i + 1]} * f;
    }
}
DI void store_o(rsrc_t rs, const f32x2 (&s)[6], int rt, int br, int h, int lane) {
    const int rb = (int)OFF_O + (rt * 80 + ((br ? 0 : 4) + (h >> 3)) * 10) * 1024;
    const u32x2 t = {full(pk2(s[4][0], s[4][1])), full(pk2(s[5][0], s[5][1]))};
    __builtin_amdgcn_raw_buffer_store_b128(u32x4{full(pk2(s[0][0], s[0][1])), full(pk2(s[1][0], s[1][1])), full(pk2(s[2][0], s[2][1])), full(pk2(s[3][0], s[3][1]))},
                                           rs, lane * 16, rb + (h & 7) * 1024, SC1);
    __builtin_amdgcn_raw_buffer_store_b64(t, rs, lane < 32 ? lane * 16 : (int)0x7F000000, rb + (8 + ((h >> 2) & 1)) * 1024 + ((h >> 1) & 1) * 512 + (h & 1) * 8, SC1);
}
DI void empty_o(rsrc_t rs, int rt, int h, int lane) {
    const int rb = (int)OFF_O + (rt * 80 + (h >> 3) * 10) * 1024;
    u32x4 e = {EMPTY, EMPTY, EMPTY, EMPTY};
    __builtin_amdgcn_raw_buffer_store_b128(e, rs, lane * 16, rb + (h & 7) * 1024, SC1);
    if (lane < 32) __builtin_amdgcn_raw_buffer_store_b64(u32x2{e[0], e[1]}, rs, lane * 16, rb + (8 + ((h >> 2) & 1)) * 1024 + ((h >> 1) & 1) * 512 + (h & 1) * 8, SC1);
    asm volatile("" :: "v"(e));
}
DI void set_flag(rsrc_t rs, int off) { __builtin_amdgcn_raw_buffer_store_b32(0x3F80u, rs, 0, off, SC1); }
DI unsigned get_flag(rsrc_t rs, int voff, int off) { return __builtin_amdgcn_raw_buffer_load_b32(rs, voff, off, SC1); }
DI unsigned a_ld1(unsigned long long sb, unsigned voff) { unsigned v; asm volatile("global_load_dword %0, %1, %2 sc1" : "=v"(v) : "v"(voff), "s"(sb) : "memory"); return v; }
DI u32x4 a_ld4s(unsigned long long sb, unsigned voff) { u32x4 v; asm volatile("global_load_dwordx4 %0, %1, %2 sc1" : "=v"(v) : "v"(voff), "s"(sb) : "memory"); return v; }
template <int OFF> DI u32x4 a_ld4so(unsigned long long sb, unsigned voff) { u32x4 v;
    asm volatile("global_load_dwordx4 %0, %1, %2 offset:%3 sc1" : "=v"(v) : "v"(voff), "s"(sb), "n"(OFF) : "memory"); return v; }
template <int OFF> DI u32x4 a_ld4(unsigned long long sb, unsigned voff) { u32x4 v;
    asm volatile("global_load_dwordx4 %0, %1, %2 offset:%3" : "=v"(v) : "v"(voff), "s"(sb), "n"(OFF) : "memory"); return v; }
template <int I> struct IC { static constexpr int v = I; };
template <int I, int N, class F> DI void sfor(F&& f) { if constexpr (I < N) { f(IC<I>{}); sfor<I + 1, N>(f); } }
#define SFOR(i, a, b) sfor<a, b>([&](auto i##_) __attribute__((always_inline)) { constexpr int i = decltype(i##_)::v;
#define SEND });
template <bool FIRST> DI void mfma_ip(f32x4& c, bf16x8 a, bf16x8 b) {
    if constexpr (FIRST) asm volatile("v_mfma_f32_16x16x32_bf16 %0, %1, %2, 0" : "=&v"(c) : "v"(a), "v"(b));
    else asm volatile("v_mfma_f32_16x16x32_bf16 %0, %1, %2, %0" : "+v"(c) : "v"(a), "v"(b));
}
template <int N> DI void wait_vm(unsigned& x) { asm volatile("s_waitcnt vmcnt(%1)" : "+v"(x) : "n"(N) : "memory"); }
template <int N> DI void wait_vm(u32x4& x, u32x4& y) { asm volatile("s_waitcnt vmcnt(%2)" : "+v"(x), "+v"(y) : "n"(N) : "memory"); }
template <int K0, int K1 = 5> DI void ld_ob(u32x4 (&b)[10], unsigned vo, const unsigned long long (&ob)[2]) {
    if constexpr (K0 < K1) {
        asm volatile("global_load_dwordx4 %0, %2, %3 offset:%4 sc1\n\tglobal_load_dwordx4 %1, %2, %3 offset:%5 sc1" : "=&v"(b[2 * K0]), "=&v"(b[2 * K0 + 1])
                     : "v"(vo), "s"(ob[K0 >> 2]), "n"(K0 < 4 ? K0 * 2048 - 4096 : 0), "n"(K0 < 4 ? K0 * 2048 - 3840 : 256) : "memory");
        ld_ob<K0 + 1, K1>(b, vo, ob);
    }
}
template <int N> DI void ld_o1(u32x4 (&b)[10], unsigned vo, const unsigned long long (&ob)[2]) {
    asm volatile("global_load_dwordx4 %0, %1, %2 offset:%3 sc1" : "=v"(b[N]) : "v"(vo), "s"(ob[N >> 3]), "n"((N < 8 ? (N >> 1) * 2048 - 4096 : 0) + (N & 1) * 256) : "memory");
}
template <class T> DI void landed(T& v) { asm volatile("" : "+v"(v)); }
DI void wait_flag(rsrc_t rs, int voff, int off, unsigned v) {
    if (__builtin_expect(!__all(v == 0x3F80u), 0)) {
#pragma clang loop unroll(disable)
        for (int spin = 0; spin < (1 << 22); ++spin) { if (__all(get_flag(rs, voff, off) == 0x3F80u)) break; __builtin_amdgcn_s_sleep(1); }
    }
    __builtin_amdgcn_fence(__ATOMIC_ACQUIRE, "wavefront");
    asm volatile("" ::: "memory");
}

#define K2_W0 8
#define K2_BAR
#define K2_TOUCH 1
#define E1 4
#define E3 4
#define F0E 1
#define EP 0
#define SP0 2
#define SP1 4
static_assert(13 * SP0 < 40 && 9 * SP1 < 50, "");
#define SPA 3
#define SPL 2
#define SPL0 1
static_assert(SPL >= 2 && SPL <= 5 && SPL0 == 1, "");
#define LP 1
constexpr DI int hs_k(int G) { const int s = G < 10 ? SPL0 : SPL, h = G % 10; return h < 2 * (5 - s) ? h / 2 : 5 - s + (h - 2 * (5 - s)) % s; }
constexpr DI int hs_m(int G) { const int s = G < 10 ? SPL0 : SPL, h = G % 10; return h < 2 * (5 - s) ? h % 2 : (h - 2 * (5 - s)) / s; }
constexpr DI int grp_q(int g) { return (g == 0 ? 10 - SPL0 : g == 1 ? 10 : 20 - SPL) * 5 + 3; }
#define K2_S0 2
#define KVW 2
#define FILL_C 2
constexpr int L_SP = 136192, L_K0 = 152320, L_SQ = 153600, L_QA = 1280, L_KVF = 11520, L_WO = 13312;

__global__ __launch_bounds__(512, 2) void k2_attn(char* ws_arg, u16* out_arg) {
    extern __shared__ __attribute__((aligned(16))) char lds[];
    __attribute__((address_space(1))) char* wsg; u16* out;
    asm volatile("s_load_dwordx4 s[20:23], %2, 0x0" : "={s[20:21]}"(wsg), "={s[22:23]}"(out) : "s"(__builtin_amdgcn_kernarg_segment_ptr()));
    int bid = blockIdx.x;
    asm volatile("" : "+s"(bid));
    const int tid = threadIdx.x, wave = rfl(tid >> 6);
    const int h = (bid & 7) * 4 + ((bid >> 3) & 3), qb = bid >> 5;
    const int oq = bid & 7, rb = oq * 4 + ((bid >> 3) & 3), cb = bid >> 5;
    const unsigned lds0 = (unsigned)(uintptr_t)(LAS char*)lds;
    const int wr = (wave & 3) * 2 + (wave >> 2), rt = qb * 8 + wr;
    unsigned long long vmask = 0x000FFFFF000FFFFFull;
    const u32x4 ONES = {0x3F803F80u, 0x3F803F80u, 0x3F803F80u, 0x3F803F80u};
    const f32x16 Z16 = {};
    AttnRegs ar;
    u32x2 c0, c1, c2;
    unsigned aq, ak, aqt, akt, asp, vi, va, vg;
    u32x4 a0, a1, b0, b1; float gate;
    const unsigned mi = lds0 + (1 + wave) * 1024;
    const unsigned hb = h * 163840, mq = lds0 + L_SQ + wave * 1280, mk = lds0 + L_SP + wave * 2016;
    {
        const unsigned t16 = tid * 16;
        unsigned vq = t16 + ((unsigned)OFF_IQ + hb + rt * 2560 - wave * 1024), vk = t16 + ((unsigned)OFF_IB + hb - wave * 1024);
        vi = t16 + ((unsigned)OFF_IB + hb + 1024);
        asm volatile("s_mov_b32 m0, %5\n\tv_add_u32 v96, 0x2000, %4\n\tv_add_u32 v97, 0x4000, %4\n\ts_waitcnt lgkmcnt(0)\n\t"
                     "global_load_lds_dwordx4 %2, %0\n\tglobal_load_lds_dwordx4 %2, %0 offset:256\n\t"
                     "s_mov_b32 m0, %6\n\ts_nop 0\n\tglobal_load_lds_dwordx4 %3, %0\n\tglobal_load_lds_dwordx4 %3, %0 offset:256\n\t"
                     "s_mov_b32 m0, %7\n\ts_nop 0\n\tglobal_load_lds_dwordx4 %4, %0\n\t"
                     "s_add_u32 m0, m0, 0x2000\n\ts_nop 0\n\tglobal_load_lds_dwordx4 v96, %0\n\t"
                     "s_add_u32 m0, m0, 0x2000\n\ts_nop 0\n\tglobal_load_lds_dwordx4 v97, %0"
                     : "+{s[20:21]}"(wsg), "+{s[22:23]}"(out)
                     : "v"(vq), "v"(vk), "v"(vi), "s"(mq), "s"(mk), "s"(mi) : "memory", "scc", "v96", "v97");
        SB0();
    }
    const int lane = tid & 63, r = lane & 31, hh = lane >> 5;
    const int g = lane >> 4, q = (lane >> 2) & 3, p = lane & 3;
    const int tro = (g & 1) * 640 + (p & 1) * 320 + q * 16 + (p >> 1) * 8;
    const int trk = (g & 1) * 640 + (p >> 1) * 320 + q * 16 + (p & 1) * 8;
    const LAS char* L = (const LAS char*)lds;
    const int ent = min(tid & 127, 125);
    {
        va = (unsigned)OFF_IA + hb + rt * 2560 + hh * 320 + r * 16; vg = (unsigned)OFF_G + rt * 128 + r * 4;
        asm volatile("s_getpc_b64 s[84:85]\n.Lattn_pc:\n\ts_mul_i32 s82, %1, .Lattn_bodyL-.Lattn_bodyE\n\ts_add_u32 s82, s82, .Lattn_bodyE-.Lattn_pc\n\t"
                     "s_add_u32 s84, s84, s82\n\ts_addc_u32 s85, s85, 0" : "={s[84:85]}"(ar.BA) : "s"(wave >> 2) : "s82", "scc");
        *(LAS unsigned*)(uintptr_t)((lane < 4 ? mk + 1280 : lds0 - 16) + lane * 4) = (lane >> 1) == 8 ? 0x3F803F80u : 0u;
        const unsigned vc = r == 20 ? 0x3F803F80u : 0u;
        u32x2 z = {0u, 0u}, o2 = {0x3F803F80u, 0x3F803F80u}, v2c = {vc, vc};
        asm volatile("" : "+v"(z), "+v"(o2), "+v"(v2c));
        const u32x4 z4 = {z[0], z[1], z[0], z[1]};
        a0 = z4; a1 = z4; b0 = u32x4{o2[0], o2[1], o2[0], o2[1]}; b1 = b0;
        ar.O = MFMA32(B8(z4), B8(z4), Z16);
        ar.P1 = u32x8{z[0], z[1], z[0], z[1], z[0], z[1], z[0], z[1]}; ar.IX = 0xDDDDDDDDu; c0 = u32x2{0x3F80u, 0u}; c1 = c0; c2 = u32x2{hh ? 0u : 2560u, z[1]};
        ar.V0 = u32x8{v2c[0], v2c[1], v2c[0], v2c[1], v2c[0], v2c[1], v2c[0], v2c[1]}; ar.V1 = ar.V0;
        ar.AD = u32x4{lds0 + trk + hh * 128, lds0 + (hh ? 48 : trk + 2816), lds0 + 1280 + hh * 320 + r * 16, (unsigned)lane * 16};
        aq = tro + hh * 128 + mq; ak = trk + hh * 128 + mk; asp = mk + (hh * 21 + r) * 48;
        aqt = hh ? mk + 1280 : tro + 256 + mq; akt = hh ? lds0 + 48 : trk + 256 + mk;
        asm volatile("" : "+{v[64:79]}"(ar.O), "+{v[86:87]}"(c0), "+{v[88:95]}"(ar.V0), "+{v[102:103]}"(c1), "+{v[104:111]}"(ar.V1),
                          "+{v[56:63]}"(ar.P1), "+{v133}"(ar.IX), "+{v[128:131]}"(ar.AD), "+{v[126:127]}"(c2),
                          "+v"(aq), "+v"(ak), "+v"(aqt), "+v"(akt), "+v"(asp), "+{s[72:73]}"(vmask),
                          "+v"(a0), "+v"(a1), "+v"(b0), "+v"(b1), "+v"(va), "+v"(vg));
    }
    char* const ws = (char*)wsg;
    const rsrc_t rs = __builtin_amdgcn_make_buffer_rsrc((void*)ws, 0, WS_USED, 0x00020000);
    const char* kvb = ws + OFF_IB + (size_t)h * 163840;
    const char* qim = ws + OFF_IQ + (size_t)h * 163840 + qb * 20480;
    const int j = wave & 3, isE = (wave >> 2) ^ 1;
    const char* wop = ws + OFF_WOT + (size_t)((cb * 4 + j) * 50) * 1024;
    const int flA = (int)OFF_FL + (oq * 4 + j) * FLS, flv = (lane & 7) * FLI;
    const int ov = ((g >> 1) * 1024 + (g & 1) * 512 + (lane & 15) * 16), os = (int)OFF_O + (rb * 160 + j * 10) * 1024;
    unsigned fl = 0u;
    u32x4 kp[8], wa[20], aa[2][10];
    const unsigned long long wsu = uni64(ws);
    unsigned long long kb = uni64(ws + OFF_KVP + 4096 + h * 16384), kb2 = uni64(ws + OFF_KVP + 12288 + h * 16384);
    unsigned long long ob[2][2] = {{uni64(ws + os + 4096), uni64(ws + os + 8192)}, {uni64(ws + os + 81920 + 4096), uni64(ws + os + 81920 + 8192)}};
#define LD_H(A, vo, hf, en, C) asm volatile("s_cmp_lg_u32 %13, 0\n\ts_cbranch_scc0 .Lsk%=\n\t" \
        "global_load_dwordx4 %0, %10, %11 offset:-4096 sc1\n\tglobal_load_dwordx4 %1, %10, %11 offset:-3840 sc1\n\t" \
        "global_load_dwordx4 %2, %10, %11 offset:-2048 sc1\n\tglobal_load_dwordx4 %3, %10, %11 offset:-1792 sc1\n\t" \
        "global_load_dwordx4 %4, %10, %11 sc1\n\tglobal_load_dwordx4 %5, %10, %11 offset:256 sc1\n\t" \
        "global_load_dwordx4 %6, %10, %11 offset:2048 sc1\n\tglobal_load_dwordx4 %7, %10, %11 offset:2304 sc1\n\t" \
        "global_load_dwordx4 %8, %10, %12 sc1\n\tglobal_load_dwordx4 %9, %10, %12 offset:256 sc1\n.Lsk%=:" \
        : C(A[0]), C(A[1]), C(A[2]), C(A[3]), C(A[4]), C(A[5]), C(A[6]), C(A[7]), C(A[8]), C(A[9]) \
        : "v"(vo), "s"(ob[hf][0]), "s"(ob[hf][1]), "s"(en) : "memory", "scc");
#define LD_W4(A, f) asm volatile("s_cmp_lg_u32 %6, 0\n\ts_cbranch_scc0 .Lsk%=\n\t" \
        "global_load_dwordx4 %0, %4, %5\n\tglobal_load_dwordx4 %1, %4, %5 offset:1024\n\t" \
        "global_load_dwordx4 %2, %4, %5 offset:2048\n\tglobal_load_dwordx4 %3, %4, %5 offset:3072\n.Lsk%=:" \
        : "=&v"((A)[0]), "=&v"((A)[1]), "=&v"((A)[2]), "=&v"((A)[3]) : "v"(lane * 16), "s"(uni64(wop + (f) * 1024)), "s"(isE) : "memory", "scc");
#define LD_KP(TAIL) asm volatile("s_cmp_lt_u32 %11, 2\n\ts_cbranch_scc0 .Lsk%=\n\t" \
        "global_load_dwordx4 %0, %8, %9 offset:-4096 sc1\n\tglobal_load_dwordx4 %1, %8, %9 offset:-2048 sc1\n\t" \
        "global_load_dwordx4 %2, %8, %9 sc1\n\tglobal_load_dwordx4 %3, %8, %9 offset:2048 sc1\n\t" \
        "global_load_dwordx4 %4, %8, %10 offset:-4096 sc1\n\tglobal_load_dwordx4 %5, %8, %10 offset:-2048 sc1\n\t" \
        "global_load_dwordx4 %6, %8, %10 sc1\n\tglobal_load_dwordx4 %7, %8, %10 offset:2048 sc1\n" \
        ".Lsk%=:" TAIL \
        : "=&v"(kp[0]), "=&v"(kp[1]), "=&v"(kp[2]), "=&v"(kp[3]), "=&v"(kp[4]), "=&v"(kp[5]), "=&v"(kp[6]), "=&v"(kp[7]) \
        : "v"(ent * 16), "s"(kb), "s"(kb2), "s"(wave ^ KVW) : "memory", "scc")
    asm volatile("" : "+s"(kb), "+s"(kb2), "+s"(ob[0][0]), "+s"(ob[0][1]), "+s"(ob[1][0]), "+s"(ob[1][1]));
    unsigned ma0 = rfl(lds0 + 50176 + wave * 3072), mb0 = rfl(ma0 + 24576); unsigned long long ga0 = uni64(kvb + 50176 + wave * 3072), gb0 = ga0 + 24576;
    asm volatile("" : "+{s[74:75]}"(ga0), "+{s[76:77]}"(gb0), "+{s78}"(ma0), "+{s79}"(mb0));
    {
        u32x2 t0, t1, t2;
        asm volatile("s_add_u32 m0, %23, 0x6000\n\tv_add_u32 v96, 0x6000, %22\n\tglobal_load_lds_dwordx4 v96, %19\n\t"
                     "s_add_u32 m0, m0, 0x2000\n\tv_add_u32 v97, 0x8000, %22\n\tglobal_load_lds_dwordx4 v97, %19\n\t"
                     "s_add_u32 m0, m0, 0x2000\n\tv_add_u32 v98, 0xA000, %22\n\tglobal_load_lds_dwordx4 v98, %19\n\t"
                     "s_waitcnt vmcnt(6)\n\t"
                     "ds_read_b64_tr_b16 v[96:97], %14\n\tds_read_b64_tr_b16 v[98:99], %14 offset:64\n\t"
                     "ds_read_b64_tr_b16 v[120:121], %13\n\tds_read_b64_tr_b16 v[122:123], %13 offset:64\n\t"
                     "ds_read_b64_tr_b16 v[124:125], %15\n\tds_read_b64_tr_b16 v[100:101], %16\n\ts_waitcnt lgkmcnt(2)\n\t"
                     "v_mfma_f32_32x32x16_bf16 v[32:47], v[96:99], v[120:123], 0\n\ts_waitcnt lgkmcnt(0)\n\t"
                     "v_mfma_f32_32x32x8_bf16 v[136:151], v[100:101], v[124:125], v[32:47]\n\t"
                     "global_load_dword %12, %21, %19\n\ts_mov_b64 exec, %18\n\t"
                     "global_load_dwordx4 %8, %20, %19\n\tglobal_load_dwordx4 %9, %20, %19 offset:640\n\tglobal_load_dwordx4 %10, %20, %19 offset:1280\n\t"
                     "global_load_dwordx4 %11, %20, %19 offset:1920\n\ts_mov_b64 exec, -1\n\ts_nop 4\n\t"
                     "v_max3_f32 v96, v136, v137, v138\n\tv_max3_f32 v97, v139, v140, v141\n\tv_max3_f32 v96, v96, v142, v143\n\tv_max3_f32 v97, v97, v144, v145\n\t"
                     "v_max3_f32 v96, v96, v146, v147\n\tv_max3_f32 v97, v97, v148, v149\n\tv_max3_f32 v96, v96, v150, v151\n\tv_max_f32 v96, v96, v97\n\t"
                     "v_mov_b32 v97, v96\n\ts_nop 1\n\tv_permlane32_swap_b32 v96, v97\n\tv_max_f32 v96, v96, v97\n\t"
                     "s_mov_b32 exec_lo, 0\n\tv_cvt_pk_bf16_f32 v124, -v96, 0\n\ts_mov_b32 exec_lo, -1\n\ts_nop 0\n\t"
                     "v_mfma_f32_32x32x8_bf16 v[32:47], v[100:101], v[124:125], v[32:47]\n\t"
                     "s_waitcnt vmcnt(8)\n\ts_barrier\n\t"
                     "ds_read_b64_tr_b16 v[80:81], v128 offset:2560\n\tds_read_b64_tr_b16 v[82:83], v128 offset:2624\n\t"
                     "ds_read_b64_tr_b16 v[84:85], v129\n\ts_mov_b64 exec, %18\n\tds_read_b128 v[92:95], v130\n\ts_mov_b64 exec, -1"
                     : "=&{v[32:47]}"(ar.S), "=&{v[120:123]}"(ar.QM), "=&{v[124:125]}"(t2), "+{v[126:127]}"(c2), "=&{v[100:101]}"(t1), "=&{v[84:85]}"(t0),
                       "+{v[88:95]}"(ar.V0), "=&{v[80:83]}"(ar.KM), "+v"(a0), "+v"(a1), "+v"(b0), "+v"(b1), "=&v"(gate)
                     : "v"(aq), "v"(ak), "v"(aqt), "v"(akt), "{v[128:131]}"(ar.AD), "s"(vmask), "s"(wsg), "v"(va), "v"(vg), "v"(vi), "s"(mi), "{v[102:103]}"(c1)
                     : "memory", "scc", "v136", "v137", "v138", "v139", "v140", "v141", "v142", "v143", "v144", "v145", "v146", "v147", "v148", "v149", "v150", "v151",
                       "v96", "v97", "v98", "v99");
        ar.KT0 = u32x4{t0[0], t0[1], c0[0], c0[1]}; ar.KT1 = u32x4{t1[0], t1[1], c1[0], c1[1]}; ar.QT = u32x4{t2[0], t2[1], c2[0], c2[1]};
    }
#pragma unroll
    for (int c = 0; c < 8; ++c) {
        unsigned ma, mb; unsigned long long ga, gb;
        if (c < 3) {
            const int lim = (c == 1 ? L_SP : 163840) - 3072;
            const int oa = (c == 2 ? L_SP : 50176 + c * 49152) + wave * 3072, ob = oa + 24576 < lim ? oa + 24576 : lim;
            ma = lds0 + oa; mb = lds0 + ob; ga = uni64(kvb + oa); gb = uni64(kvb + ob);
        } else {
            ma = lds0 + (c == 7 ? 1024 + j * 3072 : L_WO + ((c - 3) * 8 + wr) * 3072); mb = ma;
            ga = uni64(wop + (c == 7 ? 44 : 20 + (wave >> 2) * 12 + 3 * (c - 3)) * 1024);
            gb = c == 7 ? uni64(ws + OFF_FL + (32 + rt * 4 + (h >> 3)) * FLS + (h & 7) * FLI) : ga;
        }
        CSTAMP(2 * c);
        unsigned cnt;
        if (c == 0) {
            attn_chunk<1>(ar, vmask, ma0, ga0, mb0, gb0, 3u);
            asm volatile("s_waitcnt vmcnt(0)" : "+v"(a0), "+v"(a1), "+v"(b0), "+v"(b1), "+v"(gate) :: "memory");
            const f32x16 kvacc = MFMA32(B8(a1), B8(b1), MFMA32(B8(a0), B8(b0), Z16));
            if (r <= 20) {
                LAS f32x4* dst = (LAS f32x4*)(uintptr_t)asp;
                dst[0] = f32x4{kvacc[0], kvacc[1], kvacc[2], kvacc[3]};
                dst[1] = f32x4{kvacc[4], kvacc[5], kvacc[6], kvacc[7]};
                dst[2] = f32x4{kvacc[8], kvacc[9], kvacc[10], kvacc[11]};
            }
            cnt = attn_chunk<2>(ar, vmask, ma0, ga0, mb0, gb0, 1u);
        } else cnt = attn_chunk<0>(ar, vmask, rfl(ma), ga, rfl(mb), gb, c < 3 ? 1u : c < 7 ? 0u : (unsigned)(2 - 2 * isE), c == 7 ? 1ull : 0ull);
        CSTAMP(2 * c + 1);
        if (c < 7) wait_cnt(lds, 0, cnt, 8 * (c + 1));
        if (c == FILL_C && c != 1) empty_o(rs, rt, h, lane);
        if (c == 0) {
            if (wave < 2) {
                f32x4 s = *(const LAS f32x4*)(L + L_SP + ent * 16);
#pragma unroll
                for (int w = 1; w < 8; ++w) s = add4<(NOPK & 1) != 0>(s, *(const LAS f32x4*)(L + L_SP + w * 2016 + ent * 16));
                const u32x4 v = __builtin_bit_cast(u32x4, s);
                if (tid < 126) __builtin_amdgcn_raw_buffer_store_b128(u32x4{full(v[0]), full(v[1]), full(v[2]), full(v[3])}, rs, tid * 16, (int)OFF_KVP + (h * 8 + qb) * 2048, SC1);
            }
            glds16(qim + wr * 2560 + 1280, lane * 16, lds0 + L_QA + wave * 1280);
            glds16(qim + wr * 2560 + 1280 + 256, lane * 16, lds0 + L_QA + wave * 1280 + 256);
        } else if (c == 1) {
            LD_KP("");
            if (FILL_C == 1) empty_o(rs, rt, h, lane);
        } else if (c == 2) {
#pragma unroll
            for (int j = 0; j < 8; ++j) landed(kp[j]);
            if ((wave ^ KVW) < 2) {
#pragma clang loop unroll(disable)
                for (int spin = 0; spin < (1 << 20); ++spin) {
                    unsigned m = 0u;
#pragma unroll
                    for (int j = 0; j < 8; ++j) m = umax4(m, kp[j]);
                    if (__builtin_expect(__all(m != EMPTY), 1)) break;
                    LD_KP("\n\ts_waitcnt vmcnt(0)");
                }
                f32x4 s = __builtin_bit_cast(f32x4, kp[0]);
#pragma unroll
                for (int j = 1; j < 8; ++j) s = add4<(NOPK & 1) != 0>(s, __builtin_bit_cast(f32x4, kp[j]));
                const int e3 = ent / 3;
                *(LAS u32x2*)(lds + L_KVF + e3 * 32 + (ent - e3 * 3) * 8) = u32x2{pk2(s[0], s[1]), pk2(s[2], s[3])};
            }
        } else if (c == 3) {
            const int rr = r <= 20 ? r : 20;
            const u32x4 ka0 = *(const LAS u32x4*)(L + L_KVF + (hh * 21 + rr) * 32);
            const u32x2 ka1 = *(const LAS u32x2*)(L + L_KVF + (hh * 21 + rr) * 32 + 16);
            const LAS char* qa = L + L_QA + wave * 1280 + tro;
            const u32x2 pq0 = trd(qa + hh * 64), pq1 = trd(qa + 128 + hh * 64);
            u32x2 pq2 = trd(qa + 256);
            if (hh) pq2 = u32x2{0u, 0u};
            f32x16 oa = MFMA32(B8(ka0), B8(((u32x4){pq0[0], pq0[1], pq1[0], pq1[1]})), Z16);
            oa = MFMA32(B8(((u32x4){ka1[0], ka1[1], 0u, 0u})), B8(((u32x4){pq2[0], pq2[1], 0u, 0u})), oa);
            const float den = hi_half(oa[8]) + 1e-6f;
            f32x2 sv[6];
            scale_o(sv, oa, gate * __builtin_amdgcn_rcpf(den));
            store_o(rs, sv, rt, 0, h, lane);
            LD_W4(wa, 0) LD_W4(wa + 4, 4)
        } else if (c == 4) {
            if (tid == 0) set_flag(rs, (int)OFF_FL + (qb * 4 + (h >> 3)) * FLS + (h & 7) * FLI);
            LD_W4(wa + 8, 8) LD_W4(wa + 12, 12)
        } else if (c == 5) {
            fl = a_ld1(isE ? wsu : uni64(out), isE ? flA + flv : (rb * 64 + lane) * (D_ * 2) + cb * 160);
            LD_W4(wa + 16, 16)
        } else if (c == 6) {
            landed(fl);
            if (wave < 4) wait_flag(rs, flv, flA, fl);
            LD_H(aa[0], ov + 40960, 0, isE, "=&v")
        }
    }
#pragma unroll
    for (int i = 0; i < 10; ++i) { landed(wa[i]); landed(wa[10 + i]); landed(aa[0][i]); }
    u32x4 wd[4];
    f32x16 O = __builtin_amdgcn_smfmac_f32_32x32x32_bf16(__builtin_bit_cast(bf8_t, ((u32x4){ar.V0[0], ar.V0[1], ar.V0[2], ar.V0[3]})),
                                                         __builtin_bit_cast(bf16_t, ar.P1), ar.O, (int)ar.IX, 0, 0);
    landed(O);
    if (HP == 0 && lane == 0) set_flag(rs, (int)OFF_FL + (32 + rt * 4 + (h >> 3)) * FLS + (h & 7) * FLI);
    LD_H(aa[1], ov + 40960, 1, isE, "=&v")
    landed(O);
    WSTAMP(2);
    f32x2 sv[6];
    {
        const float l = hi_half(O[8]);
        scale_o(sv, O, (1.f - gate) * __builtin_amdgcn_rcpf(l));
        constexpr bool U = (NOPK & 2) != 0;
        const f32x2 t = add2<U>(add2<U>(add2<U>(sv[0], sv[1]), add2<U>(sv[2], sv[3])), add2<U>(sv[4], sv[5]));
        const unsigned long long ok = __builtin_amdgcn_ballot_w64(l >= 1e-18f) & __builtin_amdgcn_ballot_w64(l <= 1.0e30f) &
                                      __builtin_amdgcn_ballot_w64(fabsf(t[0] + t[1]) <= 3.0e38f);
        if (__builtin_expect(ok != ~0ull, 0)) {
            float m = -INFINITY; O = Z16;
            int tl = tid;
            asm volatile("" : "+v"(tl));
            const int r = tl & 31, hh = (tl >> 5) & 1, rc = r < 20 ? r : 19;
            const bf16x8 qm = B8(ar.QM), qt0 = B8(((u32x4){hh ? 0u : ar.QT[0], ar.QT[1], 0u, 0u}));
            const int ko = (r >> 4) * 640 + ((r >> 2) & 1) * 320 + ((r >> 3) & 1) * 8 + (r & 3) * 2;
            for (int t = 0; t < 64; ++t) {
                const char* tb = kvb + t * 2560;
                unsigned kw[6];
#pragma unroll
                for (int i = 0; i < 6; ++i) {
                    const int d = i < 4 ? 8 * hh + 2 * i : 16 + 2 * (i - 4);
                    kw[i] = (unsigned)*(const u16*)(tb + ko + d * 16) | ((unsigned)*(const u16*)(tb + ko + d * 16 + 16) << 16);
                }
                u32x4 f0 = *(const u32x4*)(tb + 1280 + hh * 320 + rc * 16), f1 = *(const u32x4*)(tb + 1920 + hh * 320 + rc * 16);
                if (r >= 20) { f0 = ONES; f1 = ONES; }
                f32x16 s = MFMA32(B8(((u32x4){kw[0], kw[1], kw[2], kw[3]})), qm, Z16);
                s = MFMA32(B8(((u32x4){kw[4], kw[5], 0u, 0u})), qt0, s);
                float tm = s[0];
#pragma unroll
                for (int i = 1; i < 16; ++i) tm = fmaxf(tm, s[i]);
                tm = xhalf_max(tm);
                const float mn = fmaxf(m, tm);
                const float al = (m == -INFINITY) ? 0.f : __builtin_amdgcn_exp2f(m - mn);
                m = mn;
#pragma unroll
                for (int i = 0; i < 16; ++i) { O[i] *= al; s[i] = __builtin_amdgcn_exp2f(s[i] - mn); }
                u32x4 p0 = {pk2(s[0], s[1]), pk2(s[2], s[3]), pk2(s[4], s[5]), pk2(s[6], s[7])};
                u32x4 p1 = {pk2(s[8], s[9]), pk2(s[10], s[11]), pk2(s[12], s[13]), pk2(s[14], s[15])};
                O = MFMA32(B8(f0), B8(p0), O); O = MFMA32(B8(f1), B8(p1), O);
            }
            scale_o(sv, O, (1.f - gate) * __builtin_amdgcn_rcpf(hi_half(O[8])));
        }
    }
    store_o(rs, sv, rt, 1, h, lane);
    __builtin_amdgcn_s_setprio(0);
    __builtin_amdgcn_sched_barrier(0);
    WSTAMP(3);
    constexpr int L_P0 = L_WO + 98304;
    if (wave < 4) {
        unsigned z0 = 0u;
        asm volatile("" : "+s"(z0));
        const int ln = (int)__builtin_amdgcn_mbcnt_hi(~0u, __builtin_amdgcn_mbcnt_lo(~0u, z0));
        const LAS char* wl[3] = {L + L_WO + j * 6144 + lane * 16, L + L_WO + 49152 + j * 6144 + lane * 16, L + 1024 + j * 3072 + lane * 16};
        LAS char* wp[2] = {(LAS char*)lds + (ln & 15) * 320 + (((ln >> 4) ^ ((ln >> 1) & 3)) << 4) + L_P0 + j * 10240, nullptr};
        wp[1] = wp[0] + (L_WO - L_P0 - j * 4096);
        unsigned fa[2] = {(unsigned)OFF_FL + (32 + rb * 8 + j) * FLS + (ln & 7) * FLI, 0u};
        fa[1] = fa[0] + 4 * FLS;
        landed(wl[0]); landed(wl[1]); landed(wp[0]); landed(wl[2]); landed(wp[1]); landed(fa[0]); landed(fa[1]);
        auto wfr = [&](int f) -> bf16x8 {
            if (f < 20) return B8(wa[f]);
            if (f < 44) return *(const LAS bf16x8*)(wl[((f - 20) % 12) / 6] + ((((f - 20) % 12) / 3) & 1) * 24576 + ((f - 20) / 12) * 3072 + ((f - 20) % 3) * 1024);
            if (f < 46 + EP) return *(const LAS bf16x8*)(wl[2] + (f - 44) * 1024);
            return B8(wd[f - 46]);
        };
        f32x4 acc[4][5];
        u32x4 b[2][10];
        bf16x8 wb[30];
        wait_cnt(lds + j * 4, 32, 1);
        fl = a_ld1(wsu, fa[0]);
        asm volatile("s_waitcnt vmcnt(3)" : "+v"(aa[1][0]), "+v"(aa[1][1]), "+v"(aa[1][2]), "+v"(aa[1][3]), "+v"(aa[1][4]), "+v"(aa[1][5]), "+v"(aa[1][6]),
                     "+v"(aa[1][7]), "+v"(aa[1][8]), "+v"(aa[1][9]) :: "memory");
        SB0();
        unsigned c64 = 0u, m = 0u;
        const unsigned l16 = lane * 16;
        const unsigned long long wdp = uni64(wop + 46 * 1024);
        SFOR(st, 0, 15)
            constexpr int n = st < 5 ? 20 : 10;
            if constexpr (st == K2_S0) {
                wait_vm<0>(fl);
                wait_flag(rs, fa[0], 0, fl);
                WSTAMP(4);
                fl = a_ld1(wsu, fa[1]);
                SB0();
            }
            if constexpr (st == 4) {
                wait_vm<14>(fl);
                wait_flag(rs, fa[1], 0, fl);
                WSTAMP(5);
                SB0();
            }
            SFOR(i, 0, n)
                constexpr int ni = i % 5, mi = i / 5, Q = st < 5 ? (st - 5) * 20 + i : (st - 5) * 10 + i, G = Q < 0 ? -((4 - Q) / 5) : Q / 5;
                constexpr int v = G >= -1 && G < 19 && hs_m(G + 1) == 0 ? (G + 1) / 10 * 5 + hs_k(G + 1) : -1;
                constexpr int wk = G >= -2 && G < 8 && hs_m(G + 2) == 0 ? hs_k(G + 2) : -1;
                constexpr int vh = v < 5 ? 0 : 1, vk = v < 0 ? 0 : v % 5;
                if constexpr (st < 4) mfma_ip<st == 0>(acc[mi][ni], B8(wa[st * 5 + ni]), B8(aa[mi >> 1][2 * st + (mi & 1)]));
                else if constexpr (st == 4) mfma_ip<false>(acc[mi][ni], wb[ni], B8(aa[mi >> 1][2 * st + (mi & 1)]));
                else mfma_ip<false>(acc[G / 10 * 2 + hs_m(G)][ni], wb[5 + hs_k(G) * 5 + ni], B8(b[G / 10][2 * hs_k(G) + hs_m(G)]));
                SB0();
                constexpr int g0 = (st - 2) * 20 + i, g1 = st < 5 ? (st - 4) * 20 + i : (st - 3) * 10 + i;
                if constexpr (st >= 2 && st < 4 && g0 % SP0 == 0 && g0 / SP0 < 14) {
                    if constexpr (g0 / SP0 < 10) ld_o1<(g0 / SP0) % 10>(b[0], ov, ob[0]); else wd[(g0 / SP0 - 10) & 3] = a_ld4<((g0 / SP0 - 10) & 3) * 1024>(wdp, l16);
                }
                if constexpr (g1 >= 0 && g1 % SP1 == 0 && g1 / SP1 < 10) ld_o1<(g1 < 0 ? 0 : g1 / SP1) % 10>(b[1], ov, ob[1]);
                if constexpr (st == 3 && i >= 10 && i < 15) wb[i - 10] = wfr(10 + i);
                if constexpr (wk >= 0 && (wk < 4 || ni == 0)) wb[5 + wk * 5 + ni] = wfr(25 + wk * 5 + ni);
                if constexpr (v == 4 && ni == 4) {
                    wait_vm<(g1 / SP1 + 1 < 10 ? g1 / SP1 + 1 : 10)>(wd[0], wd[1]); landed(wd[2]); landed(wd[3]);
                    wb[26] = wfr(46); wb[27] = wfr(47); wb[28] = wfr(48); wb[29] = wfr(49);
                }
                if constexpr (Q == grp_q(0) - 10) c64 = *(volatile LAS unsigned*)lds;
                if constexpr (Q == grp_q(0)) wait_cnt(lds, 0, c64, 64);
                SFOR(gr, 0, 3)
                    constexpr int wq = Q - grp_q(gr);
                    if constexpr (wq >= 0 && wq < 5) *(LAS f32x4*)(wp[gr >> 1] + (gr & 1) * 5120 + (wq % 5) * 64) = acc[gr][wq % 5];
                    if constexpr (wq == 5 + SPA && gr < 2 && (gr || SPL0 > 1)) add_cnt<12 + gr * 4>(lds0);
                SEND
                if constexpr (v >= 0) {
                    u32x4 &x = b[vh][2 * vk], &y = b[vh][2 * vk + 1];
                    if constexpr (ni == 0) { wait_vm<(v < 5 ? 12 + (g1 / SP1 + 1 < 10 ? g1 / SP1 + 1 : 10) : 18) - 2 * v>(x, y); m = max(max(x[0], x[1]), x[2]); }
                    if constexpr (ni == 1) m = max(max(m, x[3]), y[0]);
                    if constexpr (ni == 2) m = max(max(m, y[1]), y[2]);
                    if constexpr (ni == 3) m = max(m, y[3]);
                }
                SB0();
                if constexpr (v >= 0 && ni == 4) if (__builtin_expect(!__all(m != EMPTY), 0)) {
#pragma clang loop unroll(disable)
                    for (int spin = 0; spin < (1 << 20); ++spin) {
                        ld_ob<vk>(b[vh], ov, ob[vh]);
                        wait_vm<0>(b[vh][2 * vk], b[vh][2 * vk + 1]);
                        if (__all(umax4(umax4(0u, b[vh][2 * vk]), b[vh][2 * vk + 1]) != EMPTY)) break;
                    }
                }
            SEND
        SEND
        asm volatile("s_nop 3"); SB0();
#pragma unroll
        for (int ni = 0; ni < 5; ++ni) { *(LAS f32x4*)(wp[1] + 24576 + ni * 64) = acc[3][ni]; SB0(); }
        WSTAMP(6);
        asm volatile("s_waitcnt lgkmcnt(0)\n\ts_barrier" ::: "memory");
    } else {
        add_cnt<32>(lds0 + j * 4);
        int t0 = (wave - 4) * 64;
        asm volatile("" : "+s"(t0));
        const int t = (int)__builtin_amdgcn_mbcnt_hi(~0u, __builtin_amdgcn_mbcnt_lo(~0u, (unsigned)t0));
        const LAS char* rq[2][2]; unsigned oq2[2];
#pragma unroll
        for (int i = 0; i < 2; ++i) {
            const int id = (t >> 6) * 80 + (i == 0 ? t & 63 : 64 + (t & 15)), row = id / 20, cs = id - row * 20, cc = cs ^ ((row >> 1) & 3);
            const int x = i ? (t >> 4) & 1 : 0;
            rq[0][i] = L + L_P0 + id * 16 + x * 5120; rq[1][i] = L + L_WO + id * 16 + x * 24576;
            oq2[i] = (unsigned)(((rb * 64 + x * 16 + row) * D_ + cb * 80 + cc * 4) * 2);
            landed(rq[0][i]); landed(rq[1][i]); landed(oq2[i]);
        }
        const rsrc_t ro = __builtin_amdgcn_make_buffer_rsrc((void*)uni64(out), 0, S_ * D_ * 2, 0x00020000);
        WSTAMP(4);
        if (LP) __builtin_amdgcn_s_setprio(LP);
#pragma unroll
        for (int hf = 0; hf < 2; ++hf) {
            if (hf == 0) { wait_cnt(lds, 16, 4); WSTAMP(5); }
            else { WSTAMP(6); asm volatile("s_barrier" ::: "memory"); }
            f32x4 pv[3][4];
#pragma unroll
            for (int i = 0; i < 3; ++i)
#pragma unroll
                for (int w = 0; w < 4; ++w) pv[i][w] = *(const LAS f32x4*)(rq[hf][i >> 1] + (hf ? (i & 1) * 24576 + w * 6144 : (i & 1) * 5120 + w * 10240));
            __builtin_amdgcn_sched_barrier(0);
#pragma unroll
            for (int i = 0; i < 3; ++i) {
                const f32x4 v = (pv[i][0] + pv[i][1]) + (pv[i][2] + pv[i][3]);
                if (i < 2 || (t & 32) == 0)
                    __builtin_amdgcn_raw_buffer_store_b64(u32x2{pk2(v[0], v[1]), pk2(v[2], v[3])}, ro, oq2[i >> 1], (hf * 2 + (i & 1)) * (16 * D_ * 2), OUT_AUX);
            }
        }
    }
}

__attribute__((used, noinline)) __device__ void k3_mark() { asm volatile(".Lk2_last:"); }

extern "C" void kernel_launch(void* const* d_in, const int* in_sizes, int n_in, void* d_out, int out_size, void* d_ws, size_t ws_size,
                              hipStream_t stream) {
    const u16* x = (const u16*)d_in[0];
    const u16* wA = (const u16*)d_in[1];
    const u16* woA = (const u16*)d_in[2];
    const u16* wB = (const u16*)d_in[4];
    const u16* woB = (const u16*)d_in[5];
    const u16* gw = (const u16*)d_in[6];
    char* ws = (char*)d_ws;
    (void)hipFuncSetAttribute((const void*)k1_qkv, hipFuncAttributeMaxDynamicSharedMemorySize, LDS_BYTES);
    (void)hipFuncSetAttribute((const void*)k2_attn, hipFuncAttributeMaxDynamicSharedMemorySize, LDS_BYTES);
    k1_qkv<<<256, 512, LDS_BYTES, stream>>>(x, wA, wB, woA, woB, gw, ws);
    k2_attn<<<256, 512, LDS_BYTES, stream>>>(ws, (u16*)d_out);
}
```
